```python
import torch

def _softmax(x, axis=-1):
    return torch.softmax(x, dim=axis)

def _gelu(x):
    return torch.nn.functional.gelu(x, approximate='tanh')

def _silu(x):
    return torch.nn.functional.silu(x)

def _sigmoid(x):
    return torch.sigmoid(x)

def _layernorm(x, axis=-1, eps=1e-05):
    m = x.mean(dim=axis, keepdim=True)
    v = x.var(dim=axis, keepdim=True, unbiased=False)
    return (x - m) / torch.sqrt(v + eps)

def _scatter_add(tgt, idx, src, axis=0):
    return tgt.clone().index_add_(axis, idx, src)

def _segment_sum(src, seg, n):
    out = torch.zeros((n,) + src.shape[1:], dtype=src.dtype)
    return out.index_add_(0, seg, src)

def _pad(x, pw):
    flat = tuple((v for pair in reversed(pw) for v in pair))
    return torch.nn.functional.pad(x, flat)
kernel_name = 'adaLN_cond__grouped_kv_mqa__swiglu_ffn__prompt'

def setup_inputs():

    _w = lambda a: (lambda t: t.to(torch.bfloat16) if t.is_floating_point() else t)(torch.as_tensor(a))

    def _A_setup():
        B, S, D, H = (1, 2048, 576, 32)
        Dh = D // H
        return {'x': _w((lambda *s: torch.randn(*s, dtype=torch.bfloat16) * 0.05)(B, S, D)), 'wq': _w((lambda *s: torch.randn(*s, dtype=torch.bfloat16) * 0.05)(D, H * Dh)), 'wk': _w((lambda *s: torch.randn(*s, dtype=torch.bfloat16) * 0.05)(D, Dh)), 'wv': _w((lambda *s: torch.randn(*s, dtype=torch.bfloat16) * 0.05)(D, Dh)), 'wo': _w((lambda *s: torch.randn(*s, dtype=torch.bfloat16) * 0.05)(H * Dh, D))}
    _A_in = _A_setup()

    def _B_setup():
        B, S, D, F = (1, 2048, 576, 4608)
        return {'x': _w((lambda *s: torch.randn(*s, dtype=torch.bfloat16) * 0.05)(B, S, D)), 'w_gate': _w((lambda *s: torch.randn(*s, dtype=torch.bfloat16) * 0.05)(D, F)), 'w_up': _w((lambda *s: torch.randn(*s, dtype=torch.bfloat16) * 0.05)(D, F)), 'w_down': _w((lambda *s: torch.randn(*s, dtype=torch.bfloat16) * 0.05)(F, D))}
    _B_in = _B_setup()
    return {'A_x': _A_in['x'], 'A_wq': _A_in['wq'], 'A_wk': _A_in['wk'], 'A_wv': _A_in['wv'], 'A_wo': _A_in['wo'], 'B_x': _B_in['x'], 'B_w_gate': _B_in['w_gate'], 'B_w_up': _B_in['w_up'], 'B_w_down': _B_in['w_down'], 'c': _w((lambda *s: torch.randn(*s, dtype=torch.bfloat16) * 0.1)(1, 576) * 8.0), 'w_mod': _w((lambda *s: torch.randn(*s, dtype=torch.bfloat16) * 0.1)(576, 6 * 576) * 576 ** (-0.5) * 8.0)}

def reference(A_x, A_wq, A_wk, A_wv, A_wo, B_x, B_w_gate, B_w_up, B_w_down, c, w_mod):

    def _A_ref(x, wq, wk, wv, wo):
        B, S, D = x.shape
        H = 32
        Dh = D // H
        q = (lambda x, *d: x.permute(*d))((x @ wq).reshape(B, S, H, Dh), 0, 2, 1, 3)
        k = x @ wk
        v = x @ wv
        scores = torch.einsum('bhqd,bkd->bhqk', q, k) * Dh ** (-0.5)
        p = _softmax(scores, axis=-1)
        o = torch.einsum('bhqk,bkd->bhqd', p, v)
        return (lambda x, *d: x.permute(*d))(o, 0, 2, 1, 3).reshape(B, S, H * Dh) @ wo

    def _B_ref(x, w_gate, w_up, w_down):
        g = _silu(x @ w_gate)
        u = x @ w_up
        return g * u @ w_down
    x = A_x
    mod = (_silu(c) @ w_mod).reshape(c.shape[0], 6, 1, -1)
    sa, ca, ga, sb, cb, gb = (mod[:, 0].clone(), mod[:, 1].clone(), mod[:, 2].clone(), mod[:, 3].clone(), mod[:, 4].clone(), mod[:, 5].clone())
    h = _layernorm(x, axis=-1) * (1 + ca) + sa
    x = x + ga * _A_ref(h, A_wq, A_wk, A_wv, A_wo)
    h = _layernorm(x, axis=-1) * (1 + cb) + sb
    x = x + gb * _B_ref(h, B_w_gate, B_w_up, B_w_down)
    return x
```

```cpp
#include <hip/hip_runtime.h>
#include <hip/hip_bf16.h>
#include <stdint.h>

typedef __attribute__((ext_vector_type(8))) short bf16x8;
typedef __attribute__((ext_vector_type(4))) short s16x4;
typedef __attribute__((ext_vector_type(16))) float f32x16;
typedef __attribute__((ext_vector_type(4))) float f32x4;
typedef __attribute__((ext_vector_type(2))) float f32x2;
typedef __attribute__((ext_vector_type(4))) unsigned u32x4;
typedef __attribute__((ext_vector_type(2))) unsigned u32x2;
typedef __attribute__((ext_vector_type(2))) __bf16 bf2_t;
typedef unsigned short u16;
typedef __attribute__((address_space(3))) s16x4* lds_s16x4_p;

#define DEV __device__ __forceinline__

constexpr int S_ = 2048, D_ = 576, F_ = 4608;

constexpr size_t WS_MOD   = 0;
constexpr size_t WS_STATS = 16384;
constexpr size_t WS_KMAX  = 32768;
constexpr size_t WS_WQKV  = 65536;
constexpr size_t WS_WO    = WS_WQKV + 40 * 18 * 1024;
constexpr size_t WS_Q     = WS_WO + 36 * 18 * 1024;
constexpr size_t WS_KM    = WS_Q + (size_t)S_ * D_ * 2;
constexpr size_t WS_K2    = WS_KM + 64 * 1024;
constexpr size_t WS_VI    = WS_K2 + 64 * 128;
constexpr size_t WS_H2    = WS_VI + 64 * 1280;
constexpr size_t WS_X1    = WS_H2 + (size_t)S_ * D_ * 2;
constexpr size_t WS_W1T   = WS_X1 + (size_t)S_ * D_ * 2;
constexpr size_t WS_WDT   = WS_W1T + (size_t)288 * 36 * 1024;
constexpr size_t WS_ACT   = WS_WDT + (size_t)36 * 144 * 1024;
constexpr size_t WS_PART  = WS_ACT + (size_t)128 * 144 * 1024;

DEV float bl(unsigned v) { return __uint_as_float(v << 16); }
DEV float bh(unsigned v) { return __uint_as_float(v & 0xffff0000u); }
DEV unsigned pk2(float a, float b) {
  f32x2 v = {a, b};
  bf2_t r = __builtin_convertvector(v, bf2_t);
  return __builtin_bit_cast(unsigned, r);
}
DEV float rbf(float a) { return bl(pk2(a, a)); }
template <int CTRL> DEV float dppf(float v) {
  return __int_as_float(__builtin_amdgcn_update_dpp(0, __float_as_int(v), CTRL, 0xf, 0xf, true));
}
DEV float sum16(float v) {
  v += dppf<0xB1>(v); v += dppf<0x4E>(v); v += dppf<0x124>(v); v += dppf<0x128>(v);
  return v;
}
DEV float max16(float v) {
  v = fmaxf(v, dppf<0xB1>(v)); v = fmaxf(v, dppf<0x4E>(v)); v = fmaxf(v, dppf<0x124>(v)); v = fmaxf(v, dppf<0x128>(v));
  return v;
}
DEV f32x16 mfma32(bf16x8 a, bf16x8 b, f32x16 c) { return __builtin_amdgcn_mfma_f32_32x32x16_bf16(a, b, c, 0, 0, 0); }
DEV f32x4 mfma16(bf16x8 a, bf16x8 b, f32x4 c) { return __builtin_amdgcn_mfma_f32_16x16x32_bf16(a, b, c, 0, 0, 0); }
DEV bf16x8 asb(u32x4 v) { return __builtin_bit_cast(bf16x8, v); }
#define RAW_BARRIER() do { asm volatile("s_waitcnt lgkmcnt(0)" ::: "memory"); __builtin_amdgcn_s_barrier(); asm volatile("" ::: "memory"); } while (0)
#define WAIT_VM(N) asm volatile("s_waitcnt vmcnt(%0)" :: "n"(N) : "memory")

DEV u32x4 piece_transpose(u32x4 v, char* stg, int lane) {
  *(u32x4*)(stg + lane * 16) = v;
  asm volatile("" ::: "memory");
  char* a = stg + ((lane & 15) >> 2) * 128 + (lane >> 4) * 32 + (lane & 3) * 8;
  s16x4 r0 = __builtin_amdgcn_ds_read_tr16_b64_v4i16((lds_s16x4_p)a);
  s16x4 r1 = __builtin_amdgcn_ds_read_tr16_b64_v4i16((lds_s16x4_p)(a + 512));
  asm volatile("" ::: "memory");
  u32x2 x0 = __builtin_bit_cast(u32x2, r0), x1 = __builtin_bit_cast(u32x2, r1);
  u32x4 o = {x0[0], x0[1], x1[0], x1[1]};
  return o;
}

constexpr int K1_GEMV = 216, K1_STATS = 86, K1_PT = 108, K1_KV = 12;
__global__ __launch_bounds__(384) void k1_prep(const u16* __restrict__ x, const u16* __restrict__ wq, const u16* __restrict__ wk,
                                               const u16* __restrict__ wv, const u16* __restrict__ wo, const u16* __restrict__ c,
                                               const u16* __restrict__ wmod, char* __restrict__ ws) {
  __shared__ __attribute__((aligned(16))) char sm[6 * 1024];
  const int tid = threadIdx.x, lane = tid & 63, w = tid >> 6;
  int bi = blockIdx.x;
  if (bi < K1_GEMV) {
    const int cg = (bi & 7) * 27 + (bi >> 3);
    const int half = tid & 1, rs = tid >> 1;
    u32x4 v[3]; float cv[3];
#pragma unroll
    for (int i = 0; i < 3; ++i) v[i] = *(const u32x4*)(wmod + (size_t)(rs + 192 * i) * 3456 + cg * 16 + half * 8);
#pragma unroll
    for (int i = 0; i < 3; ++i) {
      float t = bl(c[rs + 192 * i]);
      cv[i] = rbf(t / (1.f + __expf(-t)));
    }
    float a[8];
#pragma unroll
    for (int j = 0; j < 4; ++j) {
      a[2 * j] = cv[0] * bl(v[0][j]) + cv[1] * bl(v[1][j]) + cv[2] * bl(v[2][j]);
      a[2 * j + 1] = cv[0] * bh(v[0][j]) + cv[1] * bh(v[1][j]) + cv[2] * bh(v[2][j]);
    }
#pragma unroll
    for (int j = 0; j < 8; ++j) { a[j] += dppf<0x4E>(a[j]); a[j] += dppf<0x124>(a[j]); a[j] += dppf<0x128>(a[j]); }
    float* red = (float*)sm;
    if ((lane & 15) < 2) {
      float* p = red + (tid >> 4) * 16 + half * 8;
      *(f32x4*)p = (f32x4){a[0], a[1], a[2], a[3]};
      *(f32x4*)(p + 4) = (f32x4){a[4], a[5], a[6], a[7]};
    }
    __syncthreads();
    if (tid < 16) {
      float s = 0.f;
#pragma unroll
      for (int r = 0; r < 24; ++r) s += red[r * 16 + tid];
      ((float*)(ws + WS_MOD))[cg * 16 + tid] = rbf(s);
    }
    return;
  }
  bi -= K1_GEMV;
  if (bi < K1_STATS) {
    const int wv_ = bi * 6 + w;
    if (wv_ >= 512) return;
    const int row = wv_ * 4 + (lane >> 4), i = lane & 15;
    const u16* xr = x + (size_t)row * D_;
    u32x4 v[5];
#pragma unroll
    for (int j = 0; j < 4; ++j) v[j] = *(const u32x4*)(xr + (i + 16 * j) * 8);
    v[4] = *(const u32x4*)(xr + (64 + (i & 7)) * 8);
    const float wl = i < 8 ? 1.f : 0.f;
    float s = 0.f, s4 = 0.f;
#pragma unroll
    for (int j = 0; j < 4; ++j)
#pragma unroll
      for (int e = 0; e < 4; ++e) s += bl(v[j][e]) + bh(v[j][e]);
#pragma unroll
    for (int e = 0; e < 4; ++e) s4 += bl(v[4][e]) + bh(v[4][e]);
    s = sum16(i < 8 ? s + s4 : s);
    const float mu = s * (1.f / D_);
    float q = 0.f, q4 = 0.f;
#pragma unroll
    for (int j = 0; j < 4; ++j)
#pragma unroll
      for (int e = 0; e < 4; ++e) { float a = bl(v[j][e]) - mu, b = bh(v[j][e]) - mu; q += a * a + b * b; }
#pragma unroll
    for (int e = 0; e < 4; ++e) { float a = bl(v[4][e]) - mu, b = bh(v[4][e]) - mu; q4 += a * a + b * b; }
    (void)wl;
    q = sum16(i < 8 ? q + q4 : q);
    if (i == 0) ((f32x2*)(ws + WS_STATS))[row] = (f32x2){mu, rsqrtf(q * (1.f / D_) + 1e-5f)};
    return;
  }
  bi -= K1_STATS;
  if (bi < K1_PT) {
    const int pc = bi * 6 + w;
    const int ro = pc / 9, cb = pc % 9;
    const int j = lane >> 3, P = ro >> 2, g = ro & 3;
    const int rq = ro * 8 + j;
    int rwo;
    if (P < 16) rwo = (2 * P + (g >> 1)) * 18 + 8 * (j >> 2) + 4 * (g & 1) + (j & 3);
    else rwo = (16 * (P - 16) + 4 * g + (j >> 1)) * 18 + 16 + (j & 1);
    u32x4 a = *(const u32x4*)(wq + (size_t)rq * D_ + cb * 64 + (lane & 7) * 8);
    u32x4 b = *(const u32x4*)(wo + (size_t)rwo * D_ + cb * 64 + (lane & 7) * 8);
    char* stg = sm + w * 1024;
    const size_t doff = ((size_t)(cb * 4 + (lane >> 4)) * 18 + P) * 1024 + (g * 16 + (lane & 15)) * 16;
    a = piece_transpose(a, stg, lane);
    *(u32x4*)(ws + WS_WQKV + doff) = a;
    b = piece_transpose(b, stg, lane);
    *(u32x4*)(ws + WS_WO + doff) = b;
    return;
  }
  bi -= K1_PT;
  {
    const int fr = bi * 6 + w;
    const int gi = fr / 18, P = fr % 18, n = lane & 15, g = lane >> 4;
    const u16* src; int col; bool z = false;
    if (gi == 0) { src = wk; col = n; }
    else if (gi == 1) { src = wv; col = n; }
    else { src = (n < 2) ? wk : wv; col = 16 + (n & 1); z = n >= 4; }
    unsigned e[8];
#pragma unroll
    for (int j = 0; j < 8; ++j) e[j] = src[(32 * P + 8 * g + j) * 18 + col];
    u32x4 o = {e[0] | (e[1] << 16), e[2] | (e[3] << 16), e[4] | (e[5] << 16), e[6] | (e[7] << 16)};
    if (z) o = (u32x4){0, 0, 0, 0};
    *(u32x4*)(ws + WS_WQKV + ((size_t)(36 + gi) * 18 + P) * 1024 + lane * 16) = o;
  }
}

constexpr float QSCALE = 0.23570226039551584f * 1.4426950408889634f;
__global__ __launch_bounds__(640) void k2_qkv(const u16* __restrict__ x, char* __restrict__ ws) {
  extern __shared__ __attribute__((aligned(16))) char sm[];
  const int tid = threadIdx.x, lane = tid & 63, w = __builtin_amdgcn_readfirstlane(tid >> 6);
  const int bi = blockIdx.x;
  const int tb = (bi & 7) * 4 + ((bi >> 3) & 3), cb = bi >> 5;
  const int grp = w % 5, kh = w / 5, ng = cb * 5 + grp;
  u32x4 xv[8];
  const int kc = tid % 72, rs = tid / 72;
  if (tid < 576) {
#pragma unroll
    for (int i = 0; i < 8; ++i) xv[i] = *(const u32x4*)(x + (size_t)(tb * 64 + 8 * i) * D_ + tid * 8);
  }
  u32x4 af[9];
#pragma unroll
  for (int p = 0; p < 9; ++p) af[p] = *(const u32x4*)(ws + WS_WQKV + ((size_t)ng * 18 + kh * 9 + p) * 1024 + lane * 16);
  if (tid < 576) {
    const float* mod = (const float*)(ws + WS_MOD);
    f32x4 s0 = *(const f32x4*)(mod + kc * 8), s1 = *(const f32x4*)(mod + kc * 8 + 4);
    f32x4 c0 = *(const f32x4*)(mod + 576 + kc * 8), c1 = *(const f32x4*)(mod + 576 + kc * 8 + 4);
    float A[8], B[8];
#pragma unroll
    for (int e = 0; e < 4; ++e) { A[e] = rbf(1.f + c0[e]); A[4 + e] = rbf(1.f + c1[e]); B[e] = s0[e]; B[4 + e] = s1[e]; }
#pragma unroll
    for (int i = 0; i < 8; ++i) {
      const int tok = 8 * i + rs;
      f32x2 st = ((const f32x2*)(ws + WS_STATS))[tb * 64 + tok];
      const float r = st[1], nm = -st[0] * st[1];
      u32x4 o;
#pragma unroll
      for (int e = 0; e < 4; ++e) {
        float a = fmaf(bl(xv[i][e]), r, nm), b = fmaf(bh(xv[i][e]), r, nm);
        o[e] = pk2(fmaf(a, A[2 * e], B[2 * e]), fmaf(b, A[2 * e + 1], B[2 * e + 1]));
      }
      *(u32x4*)(sm + (((tok >> 4) * 72 + kc) * 16 + ((tok & 15) ^ (kc & 7))) * 16) = o;
    }
  }
  __syncthreads();
  const int t16 = lane & 15, g = lane >> 4;
  f32x4 acc[4] = {};
  const bool swp = (ng == 37) || (ng == 39);
  if (!swp) {
#pragma unroll
    for (int p = 0; p < 9; ++p) {
      const int kk = (kh * 9 + p) * 4 + g;
#pragma unroll
      for (int tg = 0; tg < 4; ++tg) {
        u32x4 b = *(const u32x4*)(sm + ((tg * 72 + kk) * 16 + (t16 ^ (kk & 7))) * 16);
        acc[tg] = mfma16(asb(af[p]), asb(b), acc[tg]);
      }
    }
  } else {
#pragma unroll
    for (int p = 0; p < 9; ++p) {
      const int kk = (kh * 9 + p) * 4 + g;
#pragma unroll
      for (int tg = 0; tg < 4; ++tg) {
        u32x4 b = *(const u32x4*)(sm + ((tg * 72 + kk) * 16 + (t16 ^ (kk & 7))) * 16);
        acc[tg] = mfma16(asb(b), asb(af[p]), acc[tg]);
      }
    }
  }
  char* ex = sm + 73728;
  {
    f32x4 s0 = kh ? acc[0] : acc[2], s1 = kh ? acc[1] : acc[3];
    *(f32x4*)(ex + w * 2048 + lane * 16) = s0;
    *(f32x4*)(ex + w * 2048 + 1024 + lane * 16) = s1;
  }
  __syncthreads();
  const int ow = kh ? w - 5 : w + 5;
  f32x4 r[2];
  r[0] = (kh ? acc[2] : acc[0]) + *(const f32x4*)(ex + ow * 2048 + lane * 16);
  r[1] = (kh ? acc[3] : acc[1]) + *(const f32x4*)(ex + ow * 2048 + 1024 + lane * 16);
  const int tile = tb * 2 + kh;
  if (ng < 36) {
#pragma unroll
    for (int s = 0; s < 2; ++s) {
      u32x2 o = {pk2(r[s][0] * QSCALE, r[s][1] * QSCALE), pk2(r[s][2] * QSCALE, r[s][3] * QSCALE)};
      *(u32x2*)(ws + WS_Q + ((size_t)(tile * 32 + s * 16 + t16) * D_ + ng * 16 + g * 4) * 2) = o;
    }
  } else if (ng == 36) {
    float mx = 0.f;
#pragma unroll
    for (int s = 0; s < 2; ++s) {
      u32x2 o = {pk2(r[s][0], r[s][1]), pk2(r[s][2], r[s][3])};
      *(u32x2*)(ws + WS_KM + tile * 1024 + ((g >> 1) * 32 + s * 16 + t16) * 16 + (g & 1) * 8) = o;
      float n = bl(o[0]) * bl(o[0]) + bh(o[0]) * bh(o[0]) + bl(o[1]) * bl(o[1]) + bh(o[1]) * bh(o[1]);
      n += __shfl_xor(n, 16); n += __shfl_xor(n, 32);
      mx = (n > mx || n != n) ? n : mx;
    }
    const bool bad = __any(mx != mx);
    mx = max16(mx);
    if (lane == 0) ((float*)(ws + WS_KMAX))[tile] = bad ? __int_as_float(0x7fc00000) : mx;
  } else if (ng == 37) {
#pragma unroll
    for (int s = 0; s < 2; ++s) {
      u32x2 o = {pk2(r[s][0], r[s][1]), pk2(r[s][2], r[s][3])};
      *(u32x2*)(ws + WS_VI + tile * 1280 + (s * 2 + (g & 1)) * 320 + t16 * 16 + (g >> 1) * 8) = o;
    }
  } else if (ng == 38) {
    float mx = 0.f;
#pragma unroll
    for (int s = 0; s < 2; ++s) {
      unsigned o = pk2(r[s][0], r[s][1]);
      if (g == 0) *(unsigned*)(ws + WS_K2 + (tile >> 2) * 512 + (s * 16 + t16) * 16 + (tile & 3) * 4) = o;
      float n = bl(o) * bl(o) + bh(o) * bh(o);
      mx = (n > mx || n != n) ? n : mx;
    }
    const bool bad = __any(g == 0 && mx != mx);
    mx = max16(mx);
    if (lane == 0) ((float*)(ws + WS_KMAX))[64 + tile] = bad ? __int_as_float(0x7fc00000) : mx;
    if (lane < 8) *(u32x4*)(ws + WS_VI + tile * 1280 + (lane >> 1) * 320 + (18 + (lane & 1)) * 16) =
        (u32x4){0x3f803f80u, 0x3f803f80u, 0x3f803f80u, 0x3f803f80u};
  } else {
#pragma unroll
    for (int s = 0; s < 2; ++s) {
      u32x2 o = {pk2(r[s][0], r[s][1]), pk2(r[s][2], r[s][3])};
      if (t16 == 2 || t16 == 3)
        *(u32x2*)(ws + WS_VI + tile * 1280 + (s * 2 + (g & 1)) * 320 + (14 + t16) * 16 + (g >> 1) * 8) = o;
    }
  }
}

constexpr int L3_QM = 0;
constexpr int L3_Q2 = 8192;
constexpr int L3_NM = L3_Q2 + 32768 + 512;
constexpr int L3_MX = L3_NM + 1024;
constexpr int L3_PART = L3_MX + 8192;
constexpr int L3_PL = L3_PART + 32768;
constexpr int L3_OF = L3_PL + 8192;
constexpr int L3_STG = L3_OF + 9216;
constexpr int L3_ST = L3_STG + 8192;
constexpr int L3_FLAG = L3_ST + 512;
constexpr int L3_SIZE = L3_FLAG + 64;

struct PtSrc { const u16* src; char* dst; bool ok; };
DEV PtSrc pt_addr(int pc, int lane, const u16* wg, const u16* wu, const u16* wd, char* ws) {
  PtSrc r;
  r.ok = pc < 15552;
  const int j = lane >> 3, q = lane >> 4, i = lane & 15;
  if (pc < 10368) {
    const int mat = pc >= 5184, p = pc - mat * 5184;
    const int ro = p / 72, cb = p % 72;
    r.src = (mat ? wu : wg) + (size_t)(ro * 8 + j) * F_ + cb * 64 + (lane & 7) * 8;
    r.dst = ws + WS_W1T + ((size_t)(cb * 4 + q) * 36 + (ro >> 1)) * 1024 + ((ro & 1) * 32 + mat * 16 + i) * 16;
  } else {
    const int p = r.ok ? pc - 10368 : 0;
    const int ro = p / 9, cb = p % 9, P = ro >> 2, g = ro & 3;
    r.src = wd + (size_t)(32 * P + 16 * (g >> 1) + 4 * (g & 1) + (j & 3) + 8 * (j >> 2)) * D_ + cb * 64 + (lane & 7) * 8;
    r.dst = ws + WS_WDT + ((size_t)(cb * 4 + q) * 144 + P) * 1024 + (g * 16 + i) * 16;
  }
  return r;
}

template <bool SAFE>
DEV void attn_tiles(const u32x4 (&km)[8], const u32x4& k2, const u32x4 (&vf)[8][2], char* sm, int ct, int w, int lane) {
  const u32x4 qm = *(const u32x4*)(sm + L3_QM + ct * 1024 + lane * 16);
  const char* q2p = sm + L3_Q2 + ct * 4096 + lane * 16;
  f32x16 c0;
  float nm = 0.f;
  if (SAFE) nm = *(const float*)(sm + L3_NM + ct * 128 + (lane & 31) * 4);
#pragma unroll
  for (int i = 0; i < 16; ++i) c0[i] = nm;
  f32x16 O = {};
#pragma unroll
  for (int t = 0; t < 8; ++t) {
    f32x16 s = mfma32(asb(km[t]), asb(qm), c0);
    s = mfma32(asb(k2), asb(*(const u32x4*)(q2p + (t & 3) * 1024 + (t < 4 ? 512 : 0))), s);
    u32x4 p0, p1;
#pragma unroll
    for (int i = 0; i < 4; ++i) {
      p0[i] = pk2(__builtin_amdgcn_exp2f(s[2 * i]), __builtin_amdgcn_exp2f(s[2 * i + 1]));
      p1[i] = pk2(__builtin_amdgcn_exp2f(s[8 + 2 * i]), __builtin_amdgcn_exp2f(s[8 + 2 * i + 1]));
    }
    O = mfma32(asb(vf[t][0]), asb(p0), O);
    O = mfma32(asb(vf[t][1]), asb(p1), O);
  }
  char* pp = sm + L3_PART + (ct & 1) * 16384 + w * 2048 + lane * 16;
  *(f32x4*)pp = (f32x4){O[0], O[1], O[2], O[3]};
  *(f32x4*)(pp + 1024) = (f32x4){O[4], O[5], O[6], O[7]};
  *(f32x2*)(sm + L3_PL + (ct & 1) * 4096 + w * 512 + lane * 8) = (f32x2){O[8], O[9]};
}

DEV void attn_reduce(char* sm, int ct, int w, int lane) {
  const int c = lane & 31, h = lane >> 5, tok = c & 7, hh = c >> 3;
  const char* pl = sm + L3_PL + (ct & 1) * 4096;
  if (w >= 4) {
    const int sub = w - 4, ch = sub >> 1, hf = sub & 1;
    const char* pp = sm + L3_PART + (ct & 1) * 16384 + ch * 1024 + lane * 16 + hf * 8;
    f32x2 a = *(const f32x2*)pp;
    float l = *(const float*)(pl + (32 + c) * 8);
#pragma unroll
    for (int k = 1; k < 8; ++k) { a += *(const f32x2*)(pp + k * 2048); l += *(const float*)(pl + k * 512 + (32 + c) * 8); }
    const float r = __builtin_amdgcn_rcpf(l);
    *(unsigned*)(sm + L3_OF + ((2 * ct + (hh >> 1)) * 4 + 2 * (hh & 1) + h) * 128 + tok * 16 + ch * 8 + hf * 4) = pk2(a[0] * r, a[1] * r);
  } else if (w == (ct & 3)) {
    f32x2 a = *(const f32x2*)(pl + lane * 8);
#pragma unroll
    for (int k = 1; k < 8; ++k) a += *(const f32x2*)(pl + k * 512 + lane * 8);
    const float l = __shfl_xor(a[0], 32);
    const float r = __builtin_amdgcn_rcpf(l);
    if (h == 0) *(unsigned*)(sm + L3_OF + ((16 + (ct >> 2)) * 4 + (ct & 3)) * 128 + tok * 16 + hh * 4) = pk2(a[0] * r, a[1] * r);
  }
}

DEV void oproj_step(f32x4 (&acc)[5], const char* sm, const char* wo, int P, int w, int lane) {
  const u32x4 b = *(const u32x4*)(sm + L3_OF + (P * 4 + (lane >> 4)) * 128 + (lane & 7) * 16);
#pragma unroll
  for (int i = 0; i < 4; ++i) {
    const u32x4 a = *(const u32x4*)(wo + ((size_t)(w + 8 * i) * 18 + P) * 1024 + lane * 16);
    acc[i] = mfma16(asb(a), asb(b), acc[i]);
  }
  {
    const u32x4 a = *(const u32x4*)(wo + ((size_t)((w & 3) + 32) * 18 + P) * 1024 + lane * 16);
    acc[4] = mfma16(asb(a), asb(b), acc[4]);
  }
}

template <bool SAFE>
DEV void attn_main(const u32x4 (&km)[8], const u32x4& k2, const u32x4 (&vf)[8][2], f32x4 (&acc)[5], char* sm,
                   const u16* wg, const u16* wu, const u16* wd, char* ws, int w, int lane) {
  const char* wo = ws + WS_WO;
  const int gw = blockIdx.x * 8 + w;
#pragma unroll 1
  for (int ct = 0; ct < 8; ++ct) {
    PtSrc ps = pt_addr(ct * 2048 + gw, lane, wg, wu, wd, ws);
    u32x4 pv = *(const u32x4*)ps.src;
    if (ct >= 1) attn_reduce(sm, ct - 1, w, lane);
    if (ct >= 2) {
      oproj_step(acc, sm, wo, 2 * (ct - 2), w, lane);
      oproj_step(acc, sm, wo, 2 * (ct - 2) + 1, w, lane);
      if (ct == 5) oproj_step(acc, sm, wo, 16, w, lane);
    }
    attn_tiles<SAFE>(km, k2, vf, sm, ct, w, lane);
    pv = piece_transpose(pv, sm + L3_STG + w * 1024, lane);
    if (ps.ok) *(u32x4*)ps.dst = pv;
    __syncthreads();
  }
  attn_reduce(sm, 7, w, lane);
  oproj_step(acc, sm, wo, 12, w, lane);
  oproj_step(acc, sm, wo, 13, w, lane);
  __syncthreads();
  oproj_step(acc, sm, wo, 14, w, lane);
  oproj_step(acc, sm, wo, 15, w, lane);
  oproj_step(acc, sm, wo, 17, w, lane);
}

__global__ __launch_bounds__(512, 2) void k3_attn(const u16* __restrict__ x, const u16* __restrict__ wg, const u16* __restrict__ wu,
                                                  const u16* __restrict__ wd, char* __restrict__ ws) {
  extern __shared__ __attribute__((aligned(16))) char sm[];
  const int tid = threadIdx.x, lane = tid & 63, w = __builtin_amdgcn_readfirstlane(tid >> 6);
  const int t0 = blockIdx.x * 8;
  u32x4 km[8], vf[8][2];
  u32x4 k2 = *(const u32x4*)(ws + WS_K2 + w * 1024 + lane * 16);
  {
    const int r = lane & 31, h = lane >> 5, rv = r < 18 ? r : 18;
#pragma unroll
    for (int t = 0; t < 8; ++t) {
      const int tile = w * 8 + t;
      km[t] = *(const u32x4*)(ws + WS_KM + tile * 1024 + lane * 16);
      vf[t][0] = *(const u32x4*)(ws + WS_VI + tile * 1280 + h * 320 + rv * 16);
      vf[t][1] = *(const u32x4*)(ws + WS_VI + tile * 1280 + 640 + h * 320 + rv * 16);
    }
  }
  if (tid == 0) *(int*)(sm + L3_FLAG) = 0;
  __syncthreads();
  if (tid < 256) {
    const int ct = tid >> 5, c = tid & 31, tok = c & 7, head = ct * 4 + (c >> 3);
    const unsigned* qp = (const unsigned*)(ws + WS_Q + ((size_t)(t0 + tok) * D_ + head * 18) * 2);
    unsigned q[9];
#pragma unroll
    for (int i = 0; i < 9; ++i) q[i] = qp[i];
    const float* kmx = (const float*)(ws + WS_KMAX);
    float ka = kmx[lane], kb = kmx[64 + lane];
    const bool nanv = __any((ka != ka) || (kb != kb));
    ka = max16(ka); kb = max16(kb);
    ka = fmaxf(ka, __shfl_xor(ka, 16)); ka = fmaxf(ka, __shfl_xor(ka, 32));
    kb = fmaxf(kb, __shfl_xor(kb, 16)); kb = fmaxf(kb, __shfl_xor(kb, 32));
    float n = 0.f;
#pragma unroll
    for (int i = 0; i < 9; ++i) n += bl(q[i]) * bl(q[i]) + bh(q[i]) * bh(q[i]);
    *(u32x4*)(sm + L3_QM + ct * 1024 + c * 16) = (u32x4){q[0], q[1], q[2], q[3]};
    *(u32x4*)(sm + L3_QM + ct * 1024 + 512 + c * 16) = (u32x4){q[4], q[5], q[6], q[7]};
    {
      char* q2p = sm + L3_Q2 + ct * 4096 + c * 16;
      const u32x4 z = {0u, 0u, 0u, 0u};
      *(u32x4*)(q2p) = z; *(u32x4*)(q2p + 1024) = z; *(u32x4*)(q2p + 2048) = z; *(u32x4*)(q2p + 3072) = z;
      if (ct == 7) *(u32x4*)(q2p + 4096) = z;
      *(u32x4*)(q2p + 512) = (u32x4){q[8], 0u, 0u, 0u};
      *(u32x4*)(q2p + 1536) = (u32x4){0u, q[8], 0u, 0u};
      *(u32x4*)(q2p + 2560) = (u32x4){0u, 0u, q[8], 0u};
      *(u32x4*)(q2p + 3584) = (u32x4){0u, 0u, 0u, q[8]};
    }
    const bool unsafe = nanv || !(n * (ka + kb) <= 3600.f);
    if (unsafe) *(int*)(sm + L3_FLAG) = 1;
  }
  __syncthreads();
  f32x4 acc[5] = {};
  const int flag = __builtin_amdgcn_readfirstlane(*(volatile int*)(sm + L3_FLAG));
  if (flag == 0) {
    attn_main<false>(km, k2, vf, acc, sm, wg, wu, wd, ws, w, lane);
  } else {
#pragma unroll 1
    for (int ct = 0; ct < 8; ++ct) {
      const u32x4 qm = *(const u32x4*)(sm + L3_QM + ct * 1024 + lane * 16);
      const char* q2p = sm + L3_Q2 + ct * 4096 + lane * 16;
      float m = -__builtin_inff();
#pragma unroll
      for (int t = 0; t < 8; ++t) {
        f32x16 s = {};
        s = mfma32(asb(km[t]), asb(qm), s);
        s = mfma32(asb(k2), asb(*(const u32x4*)(q2p + (t & 3) * 1024 + (t < 4 ? 512 : 0))), s);
#pragma unroll
        for (int i = 0; i < 16; ++i) m = fmaxf(m, s[i]);
      }
      m = fmaxf(m, __shfl_xor(m, 32));
      if (lane < 32) *(float*)(sm + L3_MX + w * 1024 + ct * 128 + lane * 4) = m;
    }
    __syncthreads();
    if (tid < 256) {
      float m = *(const float*)(sm + L3_MX + tid * 4);
#pragma unroll
      for (int k = 1; k < 8; ++k) m = fmaxf(m, *(const float*)(sm + L3_MX + k * 1024 + tid * 4));
      *(float*)(sm + L3_NM + tid * 4) = -m;
    }
    __syncthreads();
    attn_main<true>(km, k2, vf, acc, sm, wg, wu, wd, ws, w, lane);
  }
  const int tok = lane & 7, g = lane >> 4;
  const bool act = (lane & 8) == 0;
  const float* mod = (const float*)(ws + WS_MOD);
  const int ngr = w < 4 ? 5 : 4;
  float s = 0.f;
#pragma unroll
  for (int i = 0; i < 5; ++i) {
    if (i < ngr) {
      const int n = (w + 8 * i) * 16 + g * 4;
      const u32x2 xv = *(const u32x2*)(x + (size_t)(t0 + tok) * D_ + n);
      const f32x4 ga = *(const f32x4*)(mod + 2 * 576 + n);
      acc[i][0] = fmaf(ga[0], acc[i][0], bl(xv[0]));
      acc[i][1] = fmaf(ga[1], acc[i][1], bh(xv[0]));
      acc[i][2] = fmaf(ga[2], acc[i][2], bl(xv[1]));
      acc[i][3] = fmaf(ga[3], acc[i][3], bh(xv[1]));
      if (act) *(u32x2*)(ws + WS_X1 + ((size_t)(t0 + tok) * D_ + n) * 2) = (u32x2){pk2(acc[i][0], acc[i][1]), pk2(acc[i][2], acc[i][3])};
      s += (acc[i][0] + acc[i][1]) + (acc[i][2] + acc[i][3]);
    }
  }
  s += __shfl_xor(s, 16); s += __shfl_xor(s, 32);
  const float lm = s * (w < 4 ? (1.f / 80.f) : (1.f / 64.f));
  float m2 = 0.f;
#pragma unroll
  for (int i = 0; i < 5; ++i)
    if (i < ngr) {
#pragma unroll
      for (int e = 0; e < 4; ++e) { const float d = acc[i][e] - lm; m2 += d * d; }
    }
  m2 += __shfl_xor(m2, 16); m2 += __shfl_xor(m2, 32);
  if (lane < 8) *(f32x2*)(sm + L3_ST + (w * 8 + lane) * 8) = (f32x2){lm, m2};
  __syncthreads();
  float mu = 0.f;
  f32x2 st[8];
#pragma unroll
  for (int k = 0; k < 8; ++k) { st[k] = *(const f32x2*)(sm + L3_ST + (k * 8 + tok) * 8); mu += st[k][0] * (k < 4 ? 80.f : 64.f); }
  mu *= (1.f / D_);
  float var = 0.f;
#pragma unroll
  for (int k = 0; k < 8; ++k) { const float d = st[k][0] - mu; var += st[k][1] + d * d * (k < 4 ? 80.f : 64.f); }
  const float rs = rsqrtf(var * (1.f / D_) + 1e-5f);
  const int tk = t0 + tok;
#pragma unroll
  for (int i = 0; i < 5; ++i) {
    if (i < ngr) {
      const int grp = w + 8 * i, n = grp * 16 + g * 4;
      const f32x4 sb = *(const f32x4*)(mod + 3 * 576 + n);
      const f32x4 cb = *(const f32x4*)(mod + 4 * 576 + n);
      float o[4];
#pragma unroll
      for (int e = 0; e < 4; ++e) o[e] = fmaf((acc[i][e] - mu) * rs, rbf(1.f + cb[e]), sb[e]);
      if (act) *(u32x2*)(ws + WS_H2 + ((size_t)(tk >> 5) * 36 + grp) * 1024 + ((g >> 1) * 32 + (tk & 31)) * 16 + (g & 1) * 8) =
          (u32x2){pk2(o[0], o[1]), pk2(o[2], o[3])};
    }
  }
}

DEV void glds16(const char* g, char* l) {
  __builtin_amdgcn_global_load_lds((const unsigned*)g, (unsigned*)l, 16, 0, 0);
}
DEV u32x4 swiglu(const f32x16& a) {
  u32x4 o;
#pragma unroll
  for (int i = 0; i < 4; ++i) {
    float r[2];
#pragma unroll
    for (int e = 0; e < 2; ++e) {
      const float gt = a[2 * i + e], up = a[8 + 2 * i + e];
      const float sg = __builtin_amdgcn_rcpf(1.f + __builtin_amdgcn_exp2f(gt * -1.4426950408889634f));
      r[e] = gt * sg * up;
    }
    o[i] = pk2(r[0], r[1]);
  }
  return o;
}

__global__ __launch_bounds__(512, 2) void k5_gemm1(char* __restrict__ ws) {
  extern __shared__ __attribute__((aligned(16))) char sm[];
  const int tid = threadIdx.x, lane = tid & 63, w = __builtin_amdgcn_readfirstlane(tid >> 6);
  const int xcd = blockIdx.x & 7, bi = blockIdx.x >> 3;
  const int panel = (xcd & 1) * 4 + (bi >> 3), fs = (xcd >> 1) * 8 + (bi & 7);
  const int tg32 = panel * 8 + w;
  const char* wsrc = ws + WS_W1T + (size_t)fs * 9 * 36864;
  auto issue = [&](int u) {
    char* dst = sm + (u & 3) * 36864 + w * 4608;
#pragma unroll
    for (int i = 0; i < 5; ++i) {
      const int o = w * 4608 + i * 1024 + lane * 16;
      const char* src;
      if (u < 3) src = wsrc + (o / 12288) * 36864 + u * 12288 + (o % 12288);
      else src = wsrc + u * 36864 + o;
      if (i < 4 || lane < 32) glds16(src, dst + i * 1024);
    }
  };
  u32x4 bfr[36];
  const char* hsrc = ws + WS_H2 + (size_t)tg32 * 36864 + lane * 16;
  issue(0);
#pragma unroll
  for (int k = 0; k < 12; ++k) bfr[k] = *(const u32x4*)(hsrc + k * 1024);
  issue(1);
#pragma unroll
  for (int k = 12; k < 24; ++k) bfr[k] = *(const u32x4*)(hsrc + k * 1024);
  issue(2);
#pragma unroll
  for (int k = 24; k < 36; ++k) bfr[k] = *(const u32x4*)(hsrc + k * 1024);
  char* adst = ws + WS_ACT + (size_t)(tg32 * 2 + ((lane >> 4) & 1)) * 147456 + ((lane >> 5) * 16 + (lane & 15)) * 16;
  auto store_act = [&](int gl, const f32x16& a) {
    const int G = fs * 9 + gl;
    *(u32x4*)(adst + (G >> 1) * 1024 + (G & 1) * 512) = swiglu(a);
  };
  f32x16 a0 = {}, a1 = {}, a2 = {};
#pragma unroll
  for (int u = 0; u < 3; ++u) {
    if (u == 0) WAIT_VM(46); else if (u == 1) WAIT_VM(34); else WAIT_VM(22);
    RAW_BARRIER();
    issue(u + 3);
    const char* l = sm + (u & 3) * 36864 + lane * 16;
#pragma unroll
    for (int k = 0; k < 12; ++k) {
      a0 = mfma32(asb(*(const u32x4*)(l + k * 1024)), asb(bfr[u * 12 + k]), a0);
      a1 = mfma32(asb(*(const u32x4*)(l + (12 + k) * 1024)), asb(bfr[u * 12 + k]), a1);
      a2 = mfma32(asb(*(const u32x4*)(l + (24 + k) * 1024)), asb(bfr[u * 12 + k]), a2);
    }
  }
  store_act(0, a0); store_act(1, a1); store_act(2, a2);
#pragma unroll
  for (int u = 3; u < 9; ++u) {
    if (u == 3) WAIT_VM(13); else if (u == 4) WAIT_VM(14); else if (u == 5) WAIT_VM(15);
    else if (u == 6) WAIT_VM(13); else if (u == 7) WAIT_VM(8); else WAIT_VM(3);
    RAW_BARRIER();
    if (u + 3 < 9) issue(u + 3);
    const char* l = sm + (u & 3) * 36864 + lane * 16;
    f32x16 a = {};
#pragma unroll
    for (int k = 0; k < 36; ++k) a = mfma32(asb(*(const u32x4*)(l + k * 1024)), asb(bfr[k]), a);
    store_act(u, a);
  }
}

__global__ __launch_bounds__(512, 2) void k6_gemm2(char* __restrict__ ws) {
  extern __shared__ __attribute__((aligned(16))) char sm[];
  const int tid = threadIdx.x, lane = tid & 63, w = __builtin_amdgcn_readfirstlane(tid >> 6);
  const int xcd = blockIdx.x & 7, bi = blockIdx.x >> 3;
  const int kq = xcd >> 1, tb = (xcd & 1) * 8 + (bi >> 2), nb = bi & 3;
  const int tq = w & 3, kh = w >> 2;
  const char* wsrc = ws + WS_WDT + ((size_t)nb * 9 * 144 + kq * 36) * 1024;
  int soff[5];
#pragma unroll
  for (int i = 0; i < 5; ++i) {
    const int o = w * 4608 + i * 1024 + lane * 16;
    const int f = o >> 10, hg = f >> 1, pp = f & 1, k_ = hg / 9, g_ = hg % 9;
    soff[i] = (g_ * 144 + k_ * 18 + pp) * 1024 + (o & 1023);
  }
  auto issue = [&](int u) {
    char* dst = sm + (u & 3) * 36864 + w * 4608;
#pragma unroll
    for (int i = 0; i < 5; ++i)
      if (i < 4 || lane < 32) glds16(wsrc + soff[i] + u * 2048, dst + i * 1024);
  };
  const char* asrc = ws + WS_ACT + ((size_t)(tb * 8 + tq * 2) * 144 + kq * 36 + kh * 18) * 1024 + lane * 16;
  u32x4 bq[3][4];
  auto loadb = [&](int u) {
#pragma unroll
    for (int t = 0; t < 2; ++t)
#pragma unroll
      for (int pp = 0; pp < 2; ++pp) bq[u % 3][t * 2 + pp] = *(const u32x4*)(asrc + (size_t)t * 147456 + (u * 2 + pp) * 1024);
  };
  issue(0); loadb(0); issue(1); loadb(1); issue(2);
  f32x4 acc[9][2] = {};
#pragma unroll
  for (int u = 0; u < 9; ++u) {
    if (u == 0) WAIT_VM(18); else if (u < 7) WAIT_VM(18); else if (u == 7) WAIT_VM(13); else WAIT_VM(4);
    RAW_BARRIER();
    if (u + 2 < 9) loadb(u + 2);
    if (u + 3 < 9) issue(u + 3);
    const char* l = sm + (u & 3) * 36864 + kh * 18432 + lane * 16;
#pragma unroll
    for (int pp = 0; pp < 2; ++pp)
#pragma unroll
      for (int g = 0; g < 9; ++g) {
        const u32x4 a = *(const u32x4*)(l + (g * 2 + pp) * 1024);
        acc[g][0] = mfma16(asb(a), asb(bq[u % 3][pp]), acc[g][0]);
        acc[g][1] = mfma16(asb(a), asb(bq[u % 3][2 + pp]), acc[g][1]);
      }
  }
  RAW_BARRIER();
#pragma unroll
  for (int g = 0; g < 9; ++g) *(f32x4*)(sm + (w * 9 + g) * 1024 + lane * 16) = kh ? acc[g][0] : acc[g][1];
  RAW_BARRIER();
  char* pdst = ws + WS_PART + (((size_t)kq * 128 + tb * 8 + tq * 2 + kh) * 36 + nb * 9) * 1024 + lane * 16;
#pragma unroll
  for (int g = 0; g < 9; ++g) {
    const f32x4 o = (kh ? acc[g][1] : acc[g][0]) + *(const f32x4*)(sm + ((w ^ 4) * 9 + g) * 1024 + lane * 16);
    *(f32x4*)(pdst + g * 1024) = o;
  }
}

__global__ __launch_bounds__(256) void k7_reduce(const char* __restrict__ ws, u16* __restrict__ out) {
  const int t = blockIdx.x * 256 + threadIdx.x;
  const int lane = t & 63, fr = t >> 6, ng = fr % 36, tg = fr / 36;
  const char* p = ws + WS_PART + (size_t)t * 16;
  f32x4 s = *(const f32x4*)p;
#pragma unroll
  for (int k = 1; k < 4; ++k) s += *(const f32x4*)(p + (size_t)k * 128 * 36 * 1024);
  const int tok = tg * 16 + (lane & 15), n = ng * 16 + (lane >> 4) * 4;
  const u32x2 xv = *(const u32x2*)(ws + WS_X1 + ((size_t)tok * D_ + n) * 2);
  const f32x4 gb = *(const f32x4*)(ws + WS_MOD + (5 * 576 + n) * 4);
  u32x2 o = {pk2(fmaf(gb[0], s[0], bl(xv[0])), fmaf(gb[1], s[1], bh(xv[0]))), pk2(fmaf(gb[2], s[2], bl(xv[1])), fmaf(gb[3], s[3], bh(xv[1])))};
  *(u32x2*)(out + (size_t)tok * D_ + n) = o;
}

extern "C" void kernel_launch(void* const* d_in, const int* in_sizes, int n_in, void* d_out, int out_size, void* d_ws,
                              size_t ws_size, hipStream_t stream) {
  const u16* x = (const u16*)d_in[0];
  const u16* wq = (const u16*)d_in[1];
  const u16* wk = (const u16*)d_in[2];
  const u16* wv = (const u16*)d_in[3];
  const u16* wo = (const u16*)d_in[4];
  const u16* wg = (const u16*)d_in[6];
  const u16* wu = (const u16*)d_in[7];
  const u16* wd = (const u16*)d_in[8];
  const u16* c = (const u16*)d_in[9];
  const u16* wmod = (const u16*)d_in[10];
  char* ws = (char*)d_ws;
  (void)hipFuncSetAttribute((const void*)k2_qkv, hipFuncAttributeMaxDynamicSharedMemorySize, 94208);
  (void)hipFuncSetAttribute((const void*)k3_attn, hipFuncAttributeMaxDynamicSharedMemorySize, L3_SIZE);
  (void)hipFuncSetAttribute((const void*)k5_gemm1, hipFuncAttributeMaxDynamicSharedMemorySize, 147456);
  (void)hipFuncSetAttribute((const void*)k6_gemm2, hipFuncAttributeMaxDynamicSharedMemorySize, 147456);
  k1_prep<<<K1_GEMV + K1_STATS + K1_PT + K1_KV, 384, 0, stream>>>(x, wq, wk, wv, wo, c, wmod, ws);
  k2_qkv<<<256, 640, 94208, stream>>>(x, ws);
  k3_attn<<<256, 512, L3_SIZE, stream>>>(x, wg, wu, wd, ws);
  k5_gemm1<<<256, 512, 147456, stream>>>(ws);
  k6_gemm2<<<256, 512, 147456, stream>>>(ws);
  k7_reduce<<<128 * 36 * 64 / 256, 256, 0, stream>>>(ws, (u16*)d_out);
}
```

```cpp
#include <hip/hip_runtime.h>
#include <hip/hip_bf16.h>
#include <stdint.h>
#pragma clang diagnostic ignored "-Wint-to-pointer-cast"

typedef __attribute__((ext_vector_type(8))) short bf16x8;
typedef __attribute__((ext_vector_type(4))) short s16x4;
typedef __attribute__((ext_vector_type(16))) float f32x16;
typedef __attribute__((ext_vector_type(4))) float f32x4;
typedef __attribute__((ext_vector_type(2))) float f32x2;
typedef __attribute__((ext_vector_type(4))) unsigned u32x4;
typedef __attribute__((ext_vector_type(2))) unsigned u32x2;
typedef __attribute__((ext_vector_type(2))) __bf16 bf2_t;
typedef unsigned short u16;
typedef __attribute__((address_space(3))) s16x4* lds_s16x4_p;

#define DEV __device__ __forceinline__
#define STAMP(i)
#define STAMP_R(i)
#define STAMP_DUMP(kid)
#define LDSZ(x) (x)

constexpr int S_ = 2048, D_ = 576, F_ = 4608;

constexpr size_t WS_EP    = 0;
constexpr size_t WS_CG    = 1024;
constexpr size_t WS_MG    = 9216;
constexpr size_t WS_WF    = 18432;
constexpr size_t WS_FL    = 32768;
constexpr size_t WS_FLAG  = 65536;
constexpr size_t WS_WO    = 81920;
constexpr size_t WS_Q     = WS_WO + 36 * 18 * 1024;
constexpr size_t WS_KM    = WS_Q + (size_t)S_ * D_ * 4;
constexpr size_t WS_K2    = WS_KM + 64 * 1024;
constexpr size_t WS_VI    = WS_K2 + 64 * 256;
constexpr size_t WS_H2    = WS_VI + 64 * 1280;
constexpr size_t WS_X1    = WS_H2 + (size_t)S_ * D_ * 2;
constexpr size_t WS_W1T   = WS_X1 + (size_t)S_ * D_ * 2;
constexpr size_t WS_WDT   = WS_W1T + (size_t)288 * 36 * 1024;
constexpr size_t WS_ACT   = WS_WDT + (size_t)36 * 144 * 1024;
constexpr size_t WS_PART  = WS_ACT + (size_t)128 * 144 * 1024;

DEV float bl(unsigned v) { return __uint_as_float(v << 16); }
DEV float bh(unsigned v) { return __uint_as_float(v & 0xffff0000u); }
DEV unsigned pk2(float a, float b) {
  f32x2 v = {a, b};
  bf2_t r = __builtin_convertvector(v, bf2_t);
  return __builtin_bit_cast(unsigned, r);
}
DEV float rbf(float a) { return bl(pk2(a, a)); }
template <int CTRL> DEV float dppf(float v) {
  return __int_as_float(__builtin_amdgcn_update_dpp(0, __float_as_int(v), CTRL, 0xf, 0xf, true));
}
DEV float rdl(float v, int l) { return __int_as_float(__builtin_amdgcn_readlane(__float_as_int(v), l)); }
DEV float sum16(float v) {
  v += dppf<0xB1>(v); v += dppf<0x4E>(v); v += dppf<0x124>(v); v += dppf<0x128>(v);
  return v;
}
DEV float max16(float v) {
  v = fmaxf(v, dppf<0xB1>(v)); v = fmaxf(v, dppf<0x4E>(v)); v = fmaxf(v, dppf<0x124>(v)); v = fmaxf(v, dppf<0x128>(v));
  return v;
}
DEV f32x16 mfma32(bf16x8 a, bf16x8 b, f32x16 c) { return __builtin_amdgcn_mfma_f32_32x32x16_bf16(a, b, c, 0, 0, 0); }
DEV f32x4 mfma16(bf16x8 a, bf16x8 b, f32x4 c) { return __builtin_amdgcn_mfma_f32_16x16x32_bf16(a, b, c, 0, 0, 0); }
DEV bf16x8 asb(u32x4 v) { return __builtin_bit_cast(bf16x8, v); }
#define RAW_BARRIER() do { asm volatile("s_waitcnt lgkmcnt(0)" ::: "memory"); __builtin_amdgcn_s_barrier(); asm volatile("" ::: "memory"); } while (0)
#define WAIT_VM(N) asm volatile("s_waitcnt vmcnt(%0)" :: "n"(N) : "memory")

#define CODE_PF(op, dst, cons, voff, BEG, END) do { unsigned long long pc_; unsigned t_; \
  asm volatile("s_getpc_b64 %1\n" BEG ":\nv_min_u32 %2, " END "-" BEG "-16, %3\n" op " %0, %2, %1" \
               : cons(dst), "=&s"(pc_), "=&v"(t_) : "v"(voff)); } while (0)

constexpr float LOG2E = 1.4426950408889634f;

#define SB() __builtin_amdgcn_sched_barrier(0)
constexpr int L3_PART = 0;
constexpr int L3_PL = 16384;
constexpr int L3_PT = 20480;
constexpr int L3_NM = 36864;
constexpr int L3_CNT = 38048;
constexpr int L3_OF = 41472, L3_OFL = 43520;
constexpr int L3_QB = 45568;
constexpr int CS = 3584, QMO = 0, Q2O = 1024;
constexpr int L3_MOD = 74240;
constexpr int L3_XR = 78336;
constexpr int L3_W8 = 87552;
constexpr int L3_SIZE = 159872;
constexpr int E_STR = 2576;

typedef __attribute__((ext_vector_type(8))) unsigned u32x8;
typedef __attribute__((ext_vector_type(16))) unsigned u32x16;
typedef __attribute__((ext_vector_type(32))) unsigned u32x32;
typedef __amdgpu_buffer_rsrc_t rsrc_t;
DEV rsrc_t mkrs(const void* p, unsigned bytes) { return __builtin_amdgcn_make_buffer_rsrc((void*)p, 0, bytes, 0x00020000); }
DEV u32x4 bld(rsrc_t r, unsigned voff, unsigned soff) { return __builtin_amdgcn_raw_buffer_load_b128(r, voff, soff, 16); }
DEV void bst(u32x4 v, rsrc_t r, unsigned voff, unsigned soff) { __builtin_amdgcn_raw_buffer_store_b128(v, r, voff, soff, 16); }
#define OPAQUE(x) asm volatile("" : "+v"(x))
DEV unsigned sopq(unsigned x) { asm volatile("" : "+s"(x)); return x; }
template <typename T> DEV T lds_ld(unsigned a) { return *(__attribute__((address_space(3))) const T*)(a); }
template <typename T> DEV void lds_st(unsigned a, T v) { *(__attribute__((address_space(3))) T*)(a) = v; }
template <int I, typename T> DEV u32x4 sub4(const T& v) { return (u32x4){v[I], v[I + 1], v[I + 2], v[I + 3]}; }
template <int CTRL> DEV unsigned dppu(unsigned v) { return __builtin_amdgcn_update_dpp(0u, v, CTRL, 0xf, 0xf, true); }
DEV float dot2(unsigned a, float c) { const bf2_t v = __builtin_bit_cast(bf2_t, a); return __builtin_amdgcn_fdot2_f32_bf16(v, v, c, false); }
DEV unsigned umax(unsigned a, unsigned b) { return a > b ? a : b; }

#define K3_STO

DEV const void* mkp(unsigned lo, unsigned hi) {
  return (const void*)((unsigned)__builtin_amdgcn_readfirstlane(lo) | ((unsigned long long)(unsigned)__builtin_amdgcn_readfirstlane(hi) << 32));
}
#define RFL(x) __builtin_amdgcn_readfirstlane(x)

#define DSC(n) const void* p##n, unsigned z##n, unsigned f##n
__global__ __launch_bounds__(512, 2) __attribute__((aligned(4096))) void k3_attn(DSC(0), DSC(1), DSC(2), DSC(3), DSC(4), DSC(5), DSC(6), DSC(7), const u16* wg_, const u16* wu_, const u16* wd_, const void* out_) {
  u32x16 ka; u32x4 kc; u32x2 kd;
  unsigned tid = threadIdx.x, bi = blockIdx.x, tag, flag, w, s_qb, s_tm, s_t1, l16;
  u32x8 pc, ad0; u32x4 ad1, q0, q1, q2; u32x2 fv;
  asm volatile(
    "s_load_dwordx16 s[36:51], %[ka], 0\n"
    "s_load_dwordx16 s[52:67], %[ka], 0x40\n"
    "s_load_dwordx4 s[68:71], %[ka], 0x80\n"
    "s_load_dwordx2 s[72:73], %[ka], 0x90\n"
    "v_readfirstlane_b32 s75, v0\n"
    "s_and_b32 s77, %[bi], 0xf8\n"
    "v_lshrrev_b32 v1, 4, v0\n"
    "s_lshl_b32 s77, s77, 9\n"
    "s_getpc_b64 s[80:81]\n"
    "LK3B:\n"
    "v_lshl_or_b32 v1, v1, 7, s77\n"
    "s_xor_b32 s74, %[bi], 128\n"
    "v_min_u32 v1, LCODE_END-LK3B-16, v1\n"
    "s_lshr_b32 s75, s75, 6\n"
    "s_cmp_lt_u32 s74, 36\n"
    "global_load_dword v100, v1, s[80:81]\n"
    "s_cbranch_scc0 G1R%=\n"
    "s_cmp_lt_u32 s75, 4\n"
    "s_cbranch_scc0 G1Z%=\n"
    "s_setprio 3\n"
    "s_and_b32 s77, s74, 7\n"
    "s_bfe_u32 s78, s74, 0x10003\n"
    "s_lshl1_add_u32 s89, s77, s78\n"
    "s_and_b32 s77, s74, 16\n"
    "s_or_b32 s89, s89, s77\n"
    "s_cmp_lt_u32 s74, 32\n"
    "s_cselect_b32 s89, s89, s74\n"
    "s_lshl_b32 s90, s89, 6\n"
    "v_bfe_u32 v9, v0, 2, 6\n"
    "v_and_b32 v8, 3, v0\n"
    "v_mul_u32_u24 v7, 6912, v9\n"
    "v_lshlrev_b32 v8, 4, v8\n"
    "v_lshl_add_u32 v6, v7, 3, v8\n"
    "v_add_u32 v7, v7, v8\n"
    "v_and_b32 v8, 255, v0\n"
    "v_lshlrev_b32 v9, 1, v9\n"
    "v_lshlrev_b32 v8, 2, v8\n"
    "s_waitcnt lgkmcnt(0)\n"
    "buffer_load_dword v1, off, s[36:39], 0 sc1\n"
    "buffer_load_dword v208, v8, s[64:67], 0 offen\n"
    "buffer_load_ushort v209, v9, s[64:67], 0 offen offset:1024\n"
    "buffer_load_dwordx4 v[172:175], v6, s[48:51], s90 offen\n"
    "s_add_u32 s77, s90, 6912\n"
    "buffer_load_dwordx4 v[176:179], v6, s[48:51], s77 offen\n"
    "s_add_u32 s77, s90, 13824\n"
    "buffer_load_dwordx4 v[180:183], v6, s[48:51], s77 offen\n"
    "s_branch G1R%=\n"
    "LTB:\n"
    "s_branch LPP1\n"
    "s_branch LPRM\n"
    "s_branch LPP3\n"
    "s_branch LPRV\n"
    "s_branch LPP4\n"
    "s_branch LPLO\n"
    "s_branch LPP6\n"
    "s_branch LPP5\n"
    "LTR:\n"
    "s_setpc_b64 vcc\n"
    "G1Z%=:\n"
    "s_or_b32 s74, s74, 256\n"
    "s_waitcnt lgkmcnt(0)\n"
    "buffer_load_dword v1, off, s[36:39], 0 sc1\n"
    "G1R%=:\n"
    "s_and_b32 s97, %[bi], 7\n"
    "s_bfe_u32 s85, %[bi], 0x20003\n"
    "v_bfe_u32 v3, v0, 3, 3\n"
    "s_lshl2_add_u32 s96, s97, s85\n"
    "v_and_b32 v69, 7, v0\n"
    "s_mul_i32 s79, s96, 73728\n"
    "v_lshrrev_b32 v78, 2, v0\n"
    "s_lshl_b32 s82, s75, 7\n"
    "v_mul_u32_u24 v4, 1152, v3\n"
    "s_add_u32 s82, s82, s79\n"
    "v_bitop3_b32 v78, v78, v69, 4 bitop3:0x6c\n"
    "s_lshr_b32 s76, %[bi], 5\n"
    "v_lshl_or_b32 v4, v78, 4, v4\n"
    "s_lshl_b32 s85, s85, 1\n"
    "s_mul_i32 s83, s75, 9216\n"
    "s_lshl_b32 s78, s76, 7\n"
    "s_lshl_b32 s84, s75, 10\n"
    "s_add_u32 s83, s83, s78\n"
    "s_add_u32 s84, s84, 66560\n"
    "s_and_b32 s77, s74, 255\n"
    "s_cmp_lt_u32 s77, 36\n"
    "s_cbranch_scc1 G1L%=\n"
    "s_xor_b32 s77, s76, 0\n"
    "s_mul_i32 s77, s77, 9216\n"
    "s_add_u32 s77, s77, s82\n"
    "s_waitcnt lgkmcnt(0)\n"
    "buffer_load_dword v1, off, s[36:39], 0 sc1\n"
    "buffer_load_dwordx4 v[94:97], v4, s[40:43], s77 offen\n"
    "s_cmp_lt_u32 s75, 4\n"
    "s_cbranch_scc1 PN%=\n"
    "s_lshr_b32 s78, %[bi], 3\n"
    "s_mov_b32 s23, s39\n"
    "s_add_u32 s78, s78, 11\n"
    "s_and_b32 s78, s78, 31\n"
    "v_and_b32 v6, 63, v0\n"
    "s_cmp_gt_u32 s75, 5\n"
    "s_cbranch_scc1 PM1%=\n"
    "s_cmp_eq_u32 s75, 5\n"
    "s_cbranch_scc1 PM0%=\n"
    "s_mul_i32 s77, s78, 43\n"
    "s_lshr_b32 s77, s77, 8\n"
    "s_mul_i32 s91, s77, 6\n"
    "s_sub_u32 s91, s78, s91\n"
    "s_xor_b32 s78, s97, 0\n"
    "s_lshl3_add_u32 s77, s77, s78\n"
    "s_cmp_lt_u32 s77, 20\n"
    "s_cbranch_scc0 PN%=\n"
    "s_lshr_b32 s78, s77, 1\n"
    "s_add_u32 s78, s78, 8\n"
    "s_min_u32 s77, s77, s78\n"
    "s_lshl_b32 s77, s77, 7\n"
    "s_mov_b64 s[20:21], s[48:49]\n"
    "s_mov_b32 s22, s50\n"
    "s_cmp_lt_u32 s91, 5\n"
    "s_cselect_b32 s91, s91, 509\n"
    "s_cselect_b32 s78, 8, 1\n"
    "s_mul_i32 s91, s91, 6912\n"
    "s_mul_i32 s78, s78, 6912\n"
    "s_add_u32 s91, s91, s77\n"
    "s_add_u32 s91, s91, 20736\n"
    "v_mul_u32_u24 v6, s78, v6\n"
    "s_branch PL%=\n"
    "PM0%=:\n"
    "s_cmp_lt_u32 s78, 26\n"
    "s_cbranch_scc0 PN%=\n"
    "s_mul_i32 s77, s78, 73728\n"
    "s_add_u32 s91, s77, 4294304256\n"
    "s_addk_i32 s77, 1024\n"
    "s_cmp_lt_u32 s78, 9\n"
    "s_cselect_b32 s91, s77, s91\n"
    "s_lshl_b32 s77, s78, 7\n"
    "s_add_u32 s77, s77, 587520\n"
    "s_cmp_lt_u32 s78, 18\n"
    "s_cselect_b32 s91, s91, s77\n"
    "s_mov_b64 s[20:21], s[44:45]\n"
    "s_mov_b32 s22, s46\n"
    "v_mul_u32_u24 v6, 1152, v6\n"
    "s_branch PL%=\n"
    "PM1%=:\n"
    "s_cmp_eq_u32 s75, 6\n"
    "s_cselect_b32 s77, s76, 4\n"
    "s_cselect_b32 s78, 7, s76\n"
    "s_cmp_lt_u32 s78, 6\n"
    "s_cbranch_scc1 PN%=\n"
    "s_mul_i32 s91, s77, 9216\n"
    "s_add_u32 s91, s91, s79\n"
    "s_cmp_eq_u32 s78, 6\n"
    "s_cselect_b32 s77, 0, 1024\n"
    "s_cselect_b32 s78, 128, 0\n"
    "s_add_u32 s91, s91, s77\n"
    "s_mov_b64 s[20:21], s[40:41]\n"
    "s_mov_b32 s22, s42\n"
    "v_mad_u32_u24 v6, v69, s78, v4\n"
    "PL%=:\n"
    "buffer_load_dword v172, v6, s[20:23], s91 offen\n"
    "PN%=:\n"
    "s_add_u32 m0, s84, 0\n"
    "s_xor_b32 s77, s85, 0\n"
    "s_mul_i32 s77, s77, 73728\n"
    "s_add_u32 s77, s77, s83\n"
    "buffer_load_dwordx4 v4, s[44:47], s77 offen lds\n"
    "s_add_u32 m0, s84, 8192\n"
    "s_xor_b32 s77, s85, 1\n"
    "s_mul_i32 s77, s77, 73728\n"
    "s_add_u32 s77, s77, s83\n"
    "buffer_load_dwordx4 v4, s[44:47], s77 offen lds\n"
    "G1L%=:\n"
    "s_bitcmp1_b32 s76, 0\n"
    "s_cselect_b64 s[52:53], s[56:57], s[52:53]\n"
    "s_lshl_b32 s78, s76, 1\n"
    "s_and_b32 s78, s78, 4\n"
    "s_lshl_b32 s87, s76, 5\n"
    "s_addk_i32 s87, 1024\n"
    "s_cmp_lt_u32 s76, 4\n"
    "s_cselect_b64 s[52:53], s[44:45], s[52:53]\n"
    "s_cselect_b32 s54, s46, s54\n"
    "s_cselect_b32 s86, 1152, 36\n"
    "s_cselect_b32 s87, s87, s78\n"
    "s_lshl_b32 s78, s85, 1\n"
    "s_and_b32 s78, s78, 4\n"
    "s_xor_b32 s25, s75, s78\n"
    "v_bfe_u32 v58, v0, 1, 2\n"
    "v_bfe_u32 v59, v0, 5, 1\n"
    "v_and_or_b32 v58, v0, 24, v58\n"
    "v_and_b32 v5, 1, v0\n"
    "v_lshl_or_b32 v58, v59, 2, v58\n"
    "v_lshlrev_b32 v5, 4, v5\n"
    "s_mul_i32 s78, s25, s86\n"
    "v_mad_u32_u24 v5, v58, s86, v5\n"
    "s_lshl_b32 s78, s78, 5\n"
    "s_add_u32 s87, s87, s78\n"
    "s_lshl_b32 s88, s85, 6\n"
    "s_and_b32 s88, s88, 256\n"
    "v_and_b32 v2, 63, v0\n"
    "v_mov_b64 v[58:59], 0\n"
    "s_mov_b32 s78, 158720\n"
    "v_lshlrev_b32 v88, 4, v2\n"
    "v_lshl_add_u32 v15, v3, 3, s78\n"
    "v_lshlrev_b32 v12, 3, v0\n"
    "s_lshl_b32 s77, s75, 12\n"
    "ds_write_b64 v15, v[58:59] offset:512\n"
    "v_lshl_add_u32 v13, v2, 3, s77\n"
    "s_xor_b32 s77, s75, s85\n"
    "s_mul_i32 s77, s77, 8320\n"
    "v_add_u32 v16, s77, v88\n"
    "v_lshl_add_u32 v17, v78, 5, s77\n"
    "v_lshl_add_u32 v18, v3, 4, s77\n"
    "v_mul_u32_u24 v78, 1040, v78\n"
    "s_sub_u32 s95, 66560, s77\n"
    "v_add_u32 v18, v18, v78\n"
    "s_lshl_b32 s77, s75, 6\n"
    "s_lshl1_add_u32 s95, s77, s95\n"
    "v_bfe_u32 v78, v0, 5, 1\n"
    "v_bfe_u32 v79, v0, 4, 2\n"
    "s_mul_i32 s94, s96, 147456\n"
    "v_add_u32 v93, s77, v15\n"
    "v_add_u32 v91, s95, v18\n"
    "s_sub_u32 s29, s84, 65536\n"
    "s_cmp_lt_u32 s75, 4\n"
    "s_cbranch_scc0 HF%=\n"
    "v_and_b32 v19, 44, v0\n"
    "v_and_b32 v58, 11, v0\n"
    "v_lshlrev_b32 v19, 5, v19\n"
    "v_and_b32 v59, 16, v0\n"
    "v_lshl_or_b32 v19, v58, 3, v19\n"
    "s_and_b32 s77, s75, 2\n"
    "s_lshl_b32 s77, s77, 5\n"
    "v_lshl_or_b32 v19, v59, 1, v19\n"
    "v_xor_b32 v19, s77, v19\n"
    "v_add_u32 v19, 66560, v19\n"
    "s_and_b32 s79, s75, 1\n"
    "v_mul_u32_u24 v20, 528, v78\n"
    "s_lshl_b32 s77, s79, 9\n"
    "v_and_b32 v11, 3, v3\n"
    "v_add3_u32 v20, v20, v88, s77\n"
    "s_lshl_b32 s77, s79, 2\n"
    "s_xor_b32 s77, s77, s76\n"
    "v_xor_b32 v11, s77, v11\n"
    "s_lshr_b32 s77, s75, 1\n"
    "v_mul_u32_u24 v11, 18432, v11\n"
    "s_lshl1_add_u32 s77, s76, s77\n"
    "v_lshl_add_u32 v11, v69, 4, v11\n"
    "s_lshl_b32 s77, s77, 10\n"
    "v_lshl_add_u32 v11, v78, 7, v11\n"
    "s_add_u32 s94, s94, s77\n"
    "s_add_u32 s94, s94, 745472\n"
    "v_add_u32 v89, 32768, v19\n"
    "s_lshr_b32 s77, %[bi], 3\n"
    "s_lshl_b32 s78, s75, 5\n"
    "s_add_u32 s77, s77, s78\n"
    "s_mul_i32 s78, s77, 57\n"
    "s_lshr_b32 s78, s78, 9\n"
    "s_mul_i32 s79, s78, 9\n"
    "s_sub_u32 s77, s77, s79\n"
    "s_mul_i32 s77, s77, 442368\n"
    "s_lshl3_add_u32 s78, s78, s97\n"
    "v_mul_u32_u24 v92, 6912, v2\n"
    "s_lshl_b32 s79, s78, 7\n"
    "s_add_u32 s77, s77, s79\n"
    "s_addk_i32 s77, 2304\n"
    "s_cmp_lt_u32 s78, 36\n"
    "s_cselect_b32 s28, s77, 0x40000000\n"
    "s_branch HE%=\n"
    "G1X%=:\n"
    "s_xor_b32 s77, s76, 0\n"
    "s_mul_i32 s77, s77, 9216\n"
    "s_add_u32 s77, s77, s82\n"
    "buffer_load_dwordx4 v[94:97], v4, s[40:43], s77 offen\n"
    "s_add_u32 m0, s84, 0\n"
    "s_xor_b32 s77, s85, 0\n"
    "s_mul_i32 s77, s77, 73728\n"
    "s_add_u32 s77, s77, s83\n"
    "buffer_load_dwordx4 v4, s[44:47], s77 offen lds\n"
    "s_add_u32 m0, s84, 8192\n"
    "s_xor_b32 s77, s85, 1\n"
    "s_mul_i32 s77, s77, 73728\n"
    "s_add_u32 s77, s77, s83\n"
    "buffer_load_dwordx4 v4, s[44:47], s77 offen lds\n"
    "s_branch G1Y%=\n"
    "HF%=:\n"
    "s_mov_b32 s77, 140288\n"
    "v_lshl_add_u32 v19, v2, 3, s77\n"
    "s_and_b32 s79, s75, 3\n"
    "v_mul_u32_u24 v20, 784, v79\n"
    "s_lshl_b32 s77, s79, 8\n"
    "v_add3_u32 v20, v20, v88, s77\n"
    "s_cmp_lt_u32 s76, 4\n"
    "s_cbranch_scc0 HK%=\n"
    "s_and_b32 s78, %[bi], 0x70\n"
    "s_bfe_u32 s77, %[bi], 0x10003\n"
    "s_lshl1_add_u32 s89, s97, s78\n"
    "s_or_b32 s89, s89, s77\n"
    "s_lshl_b32 s95, s75, 13\n"
    "s_sub_u32 s95, 66560, s95\n"
    "v_and_b32 v11, 1, v3\n"
    "s_lshl_b32 s77, s79, 1\n"
    "s_xor_b32 s77, s77, s76\n"
    "v_xor_b32 v11, s77, v11\n"
    "s_lshl_b32 s77, s76, 9\n"
    "v_mul_u32_u24 v11, 18432, v11\n"
    "s_add_u32 s94, s94, s77\n"
    "v_lshl_add_u32 v11, v69, 4, v11\n"
    "s_add_u32 s94, s94, 761856\n"
    "v_lshl_add_u32 v11, v79, 7, v11\n"
    "s_cmp_lt_u32 s89, 72\n"
    "s_cselect_b32 s62, 0, s62\n"
    "s_cbranch_scc1 HE%=\n"
    "s_sub_u32 s77, s89, 72\n"
    "s_mul_i32 s91, s77, 171\n"
    "s_lshr_b32 s91, s91, 9\n"
    "s_mul_i32 s92, s91, 3\n"
    "s_sub_u32 s92, s77, s92\n"
    "s_lshr_b32 s77, s79, 1\n"
    "s_lshl1_add_u32 s77, s91, s77\n"
    "s_mul_i32 s77, s77, 18\n"
    "s_and_b32 s78, s79, 1\n"
    "s_lshl2_add_u32 s77, s78, s77\n"
    "s_lshl2_add_u32 s78, s91, s79\n"
    "s_mul_i32 s78, s78, 72\n"
    "s_sub_u32 s78, s78, 4592\n"
    "s_cmp_lt_u32 s91, 16\n"
    "s_cselect_b32 s77, s77, s78\n"
    "s_cselect_b32 s78, 4, 32\n"
    "s_cselect_b32 s93, 0, 16\n"
    "v_mad_u32_u24 v6, v78, s78, v3\n"
    "v_and_b32 v8, 1, v79\n"
    "s_mul_i32 s77, s77, 1152\n"
    "v_mad_u32_u24 v6, v8, s93, v6\n"
    "s_mul_i32 s78, s92, 384\n"
    "v_mul_u32_u24 v6, 1152, v6\n"
    "s_add_u32 s90, s77, s78\n"
    "v_lshl_add_u32 v6, v69, 4, v6\n"
    "s_branch HE%=\n"
    "HK%=:\n"
    "s_mov_b32 s62, 0\n"
    "s_lshr_b32 s77, s76, 1\n"
    "s_xor_b32 s79, s79, s77\n"
    "s_bfe_u32 s77, %[bi], 0x40003\n"
    "s_lshl4_add_u32 s28, s97, s77\n"
    "s_lshl_b32 s28, s28, 7\n"
    "s_add_u32 s28, s28, 32768\n"
    "s_lshl2_add_u32 s79, s96, s79\n"
    "v_and_b32 v11, 15, v0\n"
    "v_and_b32 v79, 1, v79\n"
    "s_bitcmp1_b32 s76, 0\n"
    "s_cbranch_scc1 HV%=\n"
    "s_lshl_b32 s94, s79, 9\n"
    "s_and_b32 s77, s79, 1\n"
    "s_lshl_b32 s77, s77, 8\n"
    "s_sub_u32 s94, s94, s77\n"
    "s_add_u32 s94, s94, 5464064\n"
    "s_lshl_b32 s77, s79, 7\n"
    "s_add_u32 s77, s77, 5529080\n"
    "s_cmp_lt_u32 s76, 6\n"
    "s_cselect_b32 s94, s94, s77\n"
    "s_cselect_b32 s77, 4, 3\n"
    "v_lshlrev_b32 v11, s77, v11\n"
    "v_lshl_add_u32 v11, v78, 9, v11\n"
    "v_lshl_add_u32 v11, v79, 3, v11\n"
    "s_cselect_b32 s30, -1, 0\n"
    "s_cselect_b32 s31, -1, 0xffff0000\n"
    "s_branch HE%=\n"
    "HV%=:\n"
    "s_mul_i32 s94, s79, 640\n"
    "s_add_u32 s94, s94, 5545984\n"
    "s_cmp_lt_u32 s76, 6\n"
    "s_cselect_b32 s79, 0, 320\n"
    "s_cselect_b32 s77, 8, 6\n"
    "s_cselect_b32 s30, -1, 0xf000f000\n"
    "s_cselect_b32 s90, -1, 0xcfffcfff\n"
    "s_cselect_b32 s78, 0, 2\n"
    "s_add_u32 s94, s94, s79\n"
    "v_xor_b32 v78, 1, v78\n"
    "v_xor_b32 v11, s78, v11\n"
    "v_lshlrev_b32 v11, 4, v11\n"
    "v_lshl_add_u32 v11, v79, s77, v11\n"
    "v_lshl_add_u32 v11, v78, 3, v11\n"
    "s_mov_b32 s31, s30\n"
    "HE%=:\n"
    "v_add_u32 v90, 66560, v20\n"
    "s_cmp_lt_u32 s74, 36\n"
    "s_cbranch_scc1 GC1%=\n"
    "s_cmp_gt_u32 s74, 255\n"
    "s_waitcnt lgkmcnt(0)\n"
    "s_barrier\n"
    "s_cbranch_scc1 G1X%=\n"
    "G1Y%=:\n"
    "s_xor_b32 s77, s76, 1\n"
    "s_mul_i32 s77, s77, 9216\n"
    "s_add_u32 s77, s77, s82\n"
    "buffer_load_dwordx4 v[112:115], v4, s[40:43], s77 offen\n"
    "s_xor_b32 s77, s76, 2\n"
    "s_mul_i32 s77, s77, 9216\n"
    "s_add_u32 s77, s77, s82\n"
    "buffer_load_dwordx4 v[120:123], v4, s[40:43], s77 offen\n"
    "s_xor_b32 s77, s76, 3\n"
    "s_mul_i32 s77, s77, 9216\n"
    "s_add_u32 s77, s77, s82\n"
    "buffer_load_dwordx4 v[128:131], v4, s[40:43], s77 offen\n"
    "s_xor_b32 s77, s76, 4\n"
    "s_mul_i32 s77, s77, 9216\n"
    "s_add_u32 s77, s77, s82\n"
    "buffer_load_dwordx4 v[136:139], v4, s[40:43], s77 offen\n"
    "s_xor_b32 s77, s76, 5\n"
    "s_mul_i32 s77, s77, 9216\n"
    "s_add_u32 s77, s77, s82\n"
    "buffer_load_dwordx4 v[144:147], v4, s[40:43], s77 offen\n"
    "s_xor_b32 s77, s76, 6\n"
    "s_mul_i32 s77, s77, 9216\n"
    "s_add_u32 s77, s77, s82\n"
    "buffer_load_dwordx4 v[152:155], v4, s[40:43], s77 offen\n"
    "s_xor_b32 s77, s76, 7\n"
    "s_mul_i32 s77, s77, 9216\n"
    "s_add_u32 s77, s77, s82\n"
    "buffer_load_dwordx4 v[160:163], v4, s[40:43], s77 offen\n"
    "s_xor_b32 s77, s75, s76\n"
    "s_lshl3_add_u32 s77, s96, s77\n"
    "s_mul_i32 s77, s77, 9216\n"
    "buffer_load_dwordx4 v[82:85], v4, s[40:43], s77 offen offset:1024\n"
    "s_cmp_lg_u32 s75, 3\n"
    "s_cbranch_scc1 TW%=\n"
    "s_lshl3_add_u32 s78, s76, 0x80\n"
    "s_lshr_b32 s77, %[bi], 3\n"
    "s_cmp_lt_u32 s76, 4\n"
    "s_cselect_b32 s78, s78, 0\n"
    "s_load_dwordx2 s[20:21], %[ka], s78\n"
    "s_cselect_b32 s91, 21, 22\n"
    "s_cselect_b32 s92, 5308412, -1\n"
    "s_cselect_b32 s78, 3, 15\n"
    "s_and_b32 s77, s77, s78\n"
    "s_cmp_eq_u32 s76, 3\n"
    "s_cselect_b32 s91, 20, s91\n"
    "s_cselect_b32 s92, 2359292, s92\n"
    "s_lshl_b32 s77, s77, s91\n"
    "s_min_u32 s77, s77, s92\n"
    "s_waitcnt lgkmcnt(0)\n"
    "s_load_dword s20, s[20:21], s77\n"
    "TW%=:\n"
    "s_waitcnt vmcnt(11)\n"
    "v_readfirstlane_b32 s74, v1\n"
    "s_mul_i32 s74, s74, 0x0019660d\n"
    "s_add_u32 s74, s74, 0x3c6ef35f\n"
    "G1S%=:\n"
    "s_mov_b32 s91, 0x3e003e00\n"
    "s_waitcnt vmcnt(10)\n"
    "v_dot2_f32_bf16 v24, v94, s91, 0\n"
    "v_dot2_f32_bf16 v25, v94, v94, 0\n"
    "v_dot2_f32_bf16 v24, v95, s91, v24\n"
    "v_dot2_f32_bf16 v25, v95, v95, v25\n"
    "v_dot2_f32_bf16 v24, v96, s91, v24\n"
    "v_dot2_f32_bf16 v25, v96, v96, v25\n"
    "v_dot2_f32_bf16 v24, v97, s91, v24\n"
    "v_dot2_f32_bf16 v25, v97, v97, v25\n"
    "v_lshlrev_b32 v100, 16, v94\n"
    "v_and_b32 v101, 0xffff0000, v94\n"
    "v_lshlrev_b32 v102, 16, v95\n"
    "v_mul_f32 v58, -8.0, v24\n"
    "v_fmac_f32 v25, v58, v24\n"
    "ds_write_b64 v12, v[24:25] offset:0\n"
    "v_and_b32 v103, 0xffff0000, v95\n"
    "v_lshlrev_b32 v104, 16, v96\n"
    "v_and_b32 v105, 0xffff0000, v96\n"
    "v_lshlrev_b32 v106, 16, v97\n"
    "v_and_b32 v107, 0xffff0000, v97\n"
    "s_waitcnt vmcnt(7)\n"
    "v_dot2_f32_bf16 v26, v112, s91, 0\n"
    "v_dot2_f32_bf16 v27, v112, v112, 0\n"
    "v_dot2_f32_bf16 v26, v113, s91, v26\n"
    "v_dot2_f32_bf16 v27, v113, v113, v27\n"
    "v_dot2_f32_bf16 v26, v114, s91, v26\n"
    "v_dot2_f32_bf16 v27, v114, v114, v27\n"
    "v_dot2_f32_bf16 v26, v115, s91, v26\n"
    "v_dot2_f32_bf16 v27, v115, v115, v27\n"
    "v_lshlrev_b32 v108, 16, v112\n"
    "v_and_b32 v109, 0xffff0000, v112\n"
    "v_lshlrev_b32 v110, 16, v113\n"
    "v_mul_f32 v58, -8.0, v26\n"
    "v_fmac_f32 v27, v58, v26\n"
    "ds_write_b64 v12, v[26:27] offset:4096\n"
    "v_and_b32 v111, 0xffff0000, v113\n"
    "v_lshlrev_b32 v112, 16, v114\n"
    "v_and_b32 v113, 0xffff0000, v114\n"
    "v_lshlrev_b32 v114, 16, v115\n"
    "v_and_b32 v115, 0xffff0000, v115\n"
    "s_waitcnt vmcnt(6)\n"
    "v_dot2_f32_bf16 v24, v120, s91, 0\n"
    "v_dot2_f32_bf16 v25, v120, v120, 0\n"
    "v_dot2_f32_bf16 v24, v121, s91, v24\n"
    "v_dot2_f32_bf16 v25, v121, v121, v25\n"
    "v_dot2_f32_bf16 v24, v122, s91, v24\n"
    "v_dot2_f32_bf16 v25, v122, v122, v25\n"
    "v_dot2_f32_bf16 v24, v123, s91, v24\n"
    "v_dot2_f32_bf16 v25, v123, v123, v25\n"
    "v_lshlrev_b32 v116, 16, v120\n"
    "v_and_b32 v117, 0xffff0000, v120\n"
    "v_lshlrev_b32 v118, 16, v121\n"
    "v_mul_f32 v58, -8.0, v24\n"
    "v_fmac_f32 v25, v58, v24\n"
    "ds_write_b64 v12, v[24:25] offset:8192\n"
    "v_and_b32 v119, 0xffff0000, v121\n"
    "v_lshlrev_b32 v120, 16, v122\n"
    "v_and_b32 v121, 0xffff0000, v122\n"
    "v_lshlrev_b32 v122, 16, v123\n"
    "v_and_b32 v123, 0xffff0000, v123\n"
    "s_waitcnt vmcnt(5)\n"
    "v_dot2_f32_bf16 v26, v128, s91, 0\n"
    "v_dot2_f32_bf16 v27, v128, v128, 0\n"
    "v_dot2_f32_bf16 v26, v129, s91, v26\n"
    "v_dot2_f32_bf16 v27, v129, v129, v27\n"
    "v_dot2_f32_bf16 v26, v130, s91, v26\n"
    "v_dot2_f32_bf16 v27, v130, v130, v27\n"
    "v_dot2_f32_bf16 v26, v131, s91, v26\n"
    "v_dot2_f32_bf16 v27, v131, v131, v27\n"
    "v_lshlrev_b32 v124, 16, v128\n"
    "v_and_b32 v125, 0xffff0000, v128\n"
    "v_lshlrev_b32 v126, 16, v129\n"
    "v_mul_f32 v58, -8.0, v26\n"
    "v_fmac_f32 v27, v58, v26\n"
    "ds_write_b64 v12, v[26:27] offset:12288\n"
    "v_and_b32 v127, 0xffff0000, v129\n"
    "v_lshlrev_b32 v128, 16, v130\n"
    "v_and_b32 v129, 0xffff0000, v130\n"
    "v_lshlrev_b32 v130, 16, v131\n"
    "v_and_b32 v131, 0xffff0000, v131\n"
    "s_waitcnt vmcnt(4)\n"
    "v_dot2_f32_bf16 v24, v136, s91, 0\n"
    "v_dot2_f32_bf16 v25, v136, v136, 0\n"
    "v_dot2_f32_bf16 v24, v137, s91, v24\n"
    "v_dot2_f32_bf16 v25, v137, v137, v25\n"
    "v_dot2_f32_bf16 v24, v138, s91, v24\n"
    "v_dot2_f32_bf16 v25, v138, v138, v25\n"
    "v_dot2_f32_bf16 v24, v139, s91, v24\n"
    "v_dot2_f32_bf16 v25, v139, v139, v25\n"
    "v_lshlrev_b32 v132, 16, v136\n"
    "v_and_b32 v133, 0xffff0000, v136\n"
    "v_lshlrev_b32 v134, 16, v137\n"
    "v_mul_f32 v58, -8.0, v24\n"
    "v_fmac_f32 v25, v58, v24\n"
    "ds_write_b64 v12, v[24:25] offset:16384\n"
    "v_and_b32 v135, 0xffff0000, v137\n"
    "v_lshlrev_b32 v136, 16, v138\n"
    "v_and_b32 v137, 0xffff0000, v138\n"
    "v_lshlrev_b32 v138, 16, v139\n"
    "v_and_b32 v139, 0xffff0000, v139\n"
    "s_waitcnt vmcnt(3)\n"
    "v_dot2_f32_bf16 v26, v144, s91, 0\n"
    "v_dot2_f32_bf16 v27, v144, v144, 0\n"
    "v_dot2_f32_bf16 v26, v145, s91, v26\n"
    "v_dot2_f32_bf16 v27, v145, v145, v27\n"
    "v_dot2_f32_bf16 v26, v146, s91, v26\n"
    "v_dot2_f32_bf16 v27, v146, v146, v27\n"
    "v_dot2_f32_bf16 v26, v147, s91, v26\n"
    "v_dot2_f32_bf16 v27, v147, v147, v27\n"
    "v_lshlrev_b32 v140, 16, v144\n"
    "v_and_b32 v141, 0xffff0000, v144\n"
    "v_lshlrev_b32 v142, 16, v145\n"
    "v_mul_f32 v58, -8.0, v26\n"
    "v_fmac_f32 v27, v58, v26\n"
    "ds_write_b64 v12, v[26:27] offset:20480\n"
    "v_and_b32 v143, 0xffff0000, v145\n"
    "v_lshlrev_b32 v144, 16, v146\n"
    "v_and_b32 v145, 0xffff0000, v146\n"
    "v_lshlrev_b32 v146, 16, v147\n"
    "v_and_b32 v147, 0xffff0000, v147\n"
    "s_waitcnt vmcnt(2)\n"
    "v_dot2_f32_bf16 v24, v152, s91, 0\n"
    "v_dot2_f32_bf16 v25, v152, v152, 0\n"
    "v_dot2_f32_bf16 v24, v153, s91, v24\n"
    "v_dot2_f32_bf16 v25, v153, v153, v25\n"
    "v_dot2_f32_bf16 v24, v154, s91, v24\n"
    "v_dot2_f32_bf16 v25, v154, v154, v25\n"
    "v_dot2_f32_bf16 v24, v155, s91, v24\n"
    "v_dot2_f32_bf16 v25, v155, v155, v25\n"
    "v_lshlrev_b32 v148, 16, v152\n"
    "v_and_b32 v149, 0xffff0000, v152\n"
    "v_lshlrev_b32 v150, 16, v153\n"
    "v_mul_f32 v58, -8.0, v24\n"
    "v_fmac_f32 v25, v58, v24\n"
    "ds_write_b64 v12, v[24:25] offset:24576\n"
    "v_and_b32 v151, 0xffff0000, v153\n"
    "v_lshlrev_b32 v152, 16, v154\n"
    "v_and_b32 v153, 0xffff0000, v154\n"
    "v_lshlrev_b32 v154, 16, v155\n"
    "v_and_b32 v155, 0xffff0000, v155\n"
    "s_waitcnt vmcnt(1)\n"
    "v_dot2_f32_bf16 v26, v160, s91, 0\n"
    "v_dot2_f32_bf16 v27, v160, v160, 0\n"
    "v_dot2_f32_bf16 v26, v161, s91, v26\n"
    "v_dot2_f32_bf16 v27, v161, v161, v27\n"
    "v_dot2_f32_bf16 v26, v162, s91, v26\n"
    "v_dot2_f32_bf16 v27, v162, v162, v27\n"
    "v_dot2_f32_bf16 v26, v163, s91, v26\n"
    "v_dot2_f32_bf16 v27, v163, v163, v27\n"
    "v_lshlrev_b32 v156, 16, v160\n"
    "v_and_b32 v157, 0xffff0000, v160\n"
    "v_lshlrev_b32 v158, 16, v161\n"
    "v_mul_f32 v58, -8.0, v26\n"
    "v_fmac_f32 v27, v58, v26\n"
    "ds_write_b64 v12, v[26:27] offset:28672\n"
    "v_and_b32 v159, 0xffff0000, v161\n"
    "v_lshlrev_b32 v160, 16, v162\n"
    "v_and_b32 v161, 0xffff0000, v162\n"
    "v_lshlrev_b32 v162, 16, v163\n"
    "v_and_b32 v163, 0xffff0000, v163\n"
    "s_waitcnt vmcnt(0)\n"
    "v_dot2_f32_bf16 v40, v82, s91, 0\n"
    "v_dot2_f32_bf16 v41, v82, v82, 0\n"
    "v_dot2_f32_bf16 v40, v83, s91, v40\n"
    "v_dot2_f32_bf16 v41, v83, v83, v41\n"
    "v_dot2_f32_bf16 v40, v84, s91, v40\n"
    "v_dot2_f32_bf16 v41, v84, v84, v41\n"
    "v_dot2_f32_bf16 v40, v85, s91, v40\n"
    "v_dot2_f32_bf16 v41, v85, v85, v41\n"
    "v_lshlrev_b32 v164, 16, v82\n"
    "v_and_b32 v165, 0xffff0000, v82\n"
    "v_lshlrev_b32 v166, 16, v83\n"
    "v_mul_f32 v58, -8.0, v40\n"
    "v_fmac_f32 v41, v58, v40\n"
    "v_and_b32 v167, 0xffff0000, v83\n"
    "v_lshlrev_b32 v168, 16, v84\n"
    "v_and_b32 v169, 0xffff0000, v84\n"
    "v_lshlrev_b32 v170, 16, v85\n"
    "v_and_b32 v171, 0xffff0000, v85\n"
    "s_add_u32 m0, s84, 73728\n"
    "s_xor_b32 s77, s88, 0\n"
    "s_mul_i32 s77, s77, s86\n"
    "s_add_u32 s77, s77, s87\n"
    "buffer_load_dwordx4 v5, s[52:55], s77 offen lds\n"
    "s_cmp_gt_u32 s25, 1\n"
    "s_cbranch_scc1 FB28_%=\n"
    "s_lshl_b32 s77, s25, 10\n"
    "s_add_u32 m0, s77, 156672\n"
    "s_lshl_b32 s77, s86, 9\n"
    "s_add_u32 s77, s77, s87\n"
    "buffer_load_dwordx4 v5, s[52:55], s77 offen lds\n"
    "FB28_%=:\n"
    "s_add_u32 m0, s84, 81920\n"
    "s_xor_b32 s77, s88, 256\n"
    "s_mul_i32 s77, s77, s86\n"
    "s_add_u32 s77, s77, s87\n"
    "buffer_load_dwordx4 v5, s[52:55], s77 offen lds\n"
    "s_add_u32 m0, s84, 16384\n"
    "s_xor_b32 s77, s85, 2\n"
    "s_mul_i32 s77, s77, 73728\n"
    "s_add_u32 s77, s77, s83\n"
    "buffer_load_dwordx4 v4, s[44:47], s77 offen lds\n"
    "s_add_u32 m0, s84, 24576\n"
    "s_xor_b32 s77, s85, 3\n"
    "s_mul_i32 s77, s77, 73728\n"
    "s_add_u32 s77, s77, s83\n"
    "buffer_load_dwordx4 v4, s[44:47], s77 offen lds\n"
    "s_add_u32 m0, s84, 32768\n"
    "s_xor_b32 s77, s85, 4\n"
    "s_mul_i32 s77, s77, 73728\n"
    "s_add_u32 s77, s77, s83\n"
    "buffer_load_dwordx4 v4, s[44:47], s77 offen lds\n"
    "s_add_u32 m0, s84, 40960\n"
    "s_xor_b32 s77, s85, 5\n"
    "s_mul_i32 s77, s77, 73728\n"
    "s_add_u32 s77, s77, s83\n"
    "buffer_load_dwordx4 v4, s[44:47], s77 offen lds\n"
    "s_add_u32 m0, s84, 49152\n"
    "s_xor_b32 s77, s85, 6\n"
    "s_mul_i32 s77, s77, 73728\n"
    "s_add_u32 s77, s77, s83\n"
    "buffer_load_dwordx4 v4, s[44:47], s77 offen lds\n"
    "s_add_u32 m0, s84, 57344\n"
    "s_xor_b32 s77, s85, 7\n"
    "s_mul_i32 s77, s77, 73728\n"
    "s_add_u32 s77, s77, s83\n"
    "buffer_load_dwordx4 v4, s[44:47], s77 offen lds\n"
    "s_add_u32 m0, s84, 65536\n"
    "s_add_u32 s77, s83, 589824\n"
    "buffer_load_dwordx4 v4, s[44:47], s77 offen lds\n"
    "s_cmp_gt_u32 s75, 3\n"
    "s_cbranch_scc1 H0%=\n"
    "s_and_b32 s77, s75, 3\n"
    "s_lshl2_add_u32 s77, s77, LTB-LK3B+0\n"
    "s_add_u32 s22, s80, s77\n"
    "s_addc_u32 s23, s81, 0\n"
    "s_mov_b64 exec, 0\n"
    "s_swappc_b64 vcc, s[22:23]\n"
    "s_mov_b64 exec, -1\n"
    "H0%=:\n"
    "s_waitcnt lgkmcnt(0)\n"
    "s_barrier\n"
    "ds_read2st64_b64 v[42:45], v13 offset0:0 offset1:1\n"
    "ds_read2st64_b64 v[46:49], v13 offset0:2 offset1:3\n"
    "ds_read2st64_b64 v[50:53], v13 offset0:4 offset1:5\n"
    "ds_read2st64_b64 v[54:57], v13 offset0:6 offset1:7\n"
    "s_waitcnt lgkmcnt(3)\n"
    "v_pk_add_f32 v[24:25], v[42:43], v[44:45]\n"
    "s_waitcnt lgkmcnt(2)\n"
    "v_pk_add_f32 v[26:27], v[46:47], v[48:49]\n"
    "s_waitcnt lgkmcnt(1)\n"
    "v_pk_add_f32 v[28:29], v[50:51], v[52:53]\n"
    "s_waitcnt lgkmcnt(0)\n"
    "v_pk_add_f32 v[30:31], v[54:55], v[56:57]\n"
    "v_pk_add_f32 v[24:25], v[24:25], v[26:27]\n"
    "v_pk_add_f32 v[28:29], v[28:29], v[30:31]\n"
    "v_pk_add_f32 v[24:25], v[24:25], v[40:41]\n"
    "v_pk_add_f32 v[24:25], v[24:25], v[28:29]\n"
    "s_nop 1\n"
    "v_add_f32_dpp v24, v24, v24 quad_perm:[1,0,3,2] row_mask:0xf bank_mask:0xf\n"
    "s_nop 1\n"
    "v_add_f32_dpp v24, v24, v24 quad_perm:[2,3,0,1] row_mask:0xf bank_mask:0xf\n"
    "s_nop 1\n"
    "v_add_f32_dpp v24, v24, v24 row_half_mirror row_mask:0xf bank_mask:0xf\n"
    "v_mul_f32 v24, 0xbc638e39, v24\n"
    "v_add_f32 v42, v42, v24\n"
    "v_add_f32 v44, v44, v24\n"
    "v_add_f32 v46, v46, v24\n"
    "v_add_f32 v48, v48, v24\n"
    "v_add_f32 v50, v50, v24\n"
    "v_add_f32 v52, v52, v24\n"
    "v_add_f32 v54, v54, v24\n"
    "LPP1:\n"
    "s_cbranch_execz LPP2\n"
    "v_add_f32 v56, v56, v24\n"
    "v_add_f32 v40, v40, v24\n"
    "v_mul_f32 v58, v42, v42\n"
    "v_mul_f32 v59, v44, v44\n"
    "v_fmac_f32 v58, v46, v46\n"
    "v_fmac_f32 v59, v48, v48\n"
    "v_fmac_f32 v58, v50, v50\n"
    "v_fmac_f32 v59, v52, v52\n"
    "v_fmac_f32 v58, v54, v54\n"
    "v_fmac_f32 v59, v56, v56\n"
    "v_fmac_f32 v58, v40, v40\n"
    "v_fmac_f32 v25, 0x41000000, v59\n"
    "v_fmac_f32 v25, 0x41000000, v58\n"
    "s_nop 1\n"
    "v_add_f32_dpp v25, v25, v25 quad_perm:[1,0,3,2] row_mask:0xf bank_mask:0xf\n"
    "s_nop 1\n"
    "v_add_f32_dpp v25, v25, v25 quad_perm:[2,3,0,1] row_mask:0xf bank_mask:0xf\n"
    "s_nop 1\n"
    "v_add_f32_dpp v25, v25, v25 row_half_mirror row_mask:0xf bank_mask:0xf\n"
    "v_mul_f32 v61, 0x3ae38e39, v25\n"
    "v_cvt_pk_bf16_f32 v61, 0, v61\n"
    "v_add_f32 v61, 0x3727c5ac, v61\n"
    "v_cvt_pk_bf16_f32 v61, 0, v61\n"
    "v_sqrt_f32 v61, v61\n"
    "v_cvt_pk_bf16_f32 v24, 0, v24\n"
    "v_cvt_pk_bf16_f32 v61, 0, v61\n"
    "v_rcp_f32 v61, v61\n"
    "s_nop 0\n"
    "v_mul_f32 v60, v24, v61\n"
    "ds_write_b64 v93, v[60:61]\n"
    "s_cmp_gt_u32 s75, 3\n"
    "s_cbranch_scc1 AE%=\n"
    "ds_read_b64_tr_b16 v[172:173], v19 offset:0\n"
    "ds_read_b64_tr_b16 v[174:175], v19 offset:512\n"
    "ds_read_b64_tr_b16 v[176:177], v19 offset:2048\n"
    "ds_read_b64_tr_b16 v[178:179], v19 offset:2560\n"
    "ds_read_b64_tr_b16 v[180:181], v19 offset:4096\n"
    "ds_read_b64_tr_b16 v[182:183], v19 offset:4608\n"
    "ds_read_b64_tr_b16 v[184:185], v19 offset:6144\n"
    "ds_read_b64_tr_b16 v[186:187], v19 offset:6656\n"
    "ds_read_b64_tr_b16 v[188:189], v19 offset:8192\n"
    "ds_read_b64_tr_b16 v[190:191], v19 offset:8704\n"
    "ds_read_b64_tr_b16 v[192:193], v19 offset:10240\n"
    "ds_read_b64_tr_b16 v[194:195], v19 offset:10752\n"
    "ds_read_b64_tr_b16 v[196:197], v19 offset:12288\n"
    "ds_read_b64_tr_b16 v[198:199], v19 offset:12800\n"
    "ds_read_b64_tr_b16 v[200:201], v19 offset:14336\n"
    "ds_read_b64_tr_b16 v[202:203], v19 offset:14848\n"
    "AE%=:\n"
    "s_cmp_lt_u32 s75, 4\n"
    "s_waitcnt lgkmcnt(0)\n"
    "s_barrier\n"
    "ds_read2_b64 v[42:45], v15 offset0:0 offset1:8\n"
    "ds_read2_b64 v[46:49], v15 offset0:16 offset1:24\n"
    "ds_read2_b64 v[50:53], v15 offset0:32 offset1:40\n"
    "ds_read2_b64 v[54:57], v15 offset0:48 offset1:56\n"
    "s_cbranch_scc1 RM%=\n"
    "RF%=:\n"
    "buffer_load_dwordx4 v[64:67], v88, s[36:39], s29 offen sc1\n"
    "v_pk_fma_f32 v[164:165], v[164:165], v[60:61], v[60:61] op_sel:[0,1,0] op_sel_hi:[1,1,0]\n"
    "v_pk_fma_f32 v[166:167], v[166:167], v[60:61], v[60:61] op_sel:[0,1,0] op_sel_hi:[1,1,0]\n"
    "v_pk_fma_f32 v[168:169], v[168:169], v[60:61], v[60:61] op_sel:[0,1,0] op_sel_hi:[1,1,0]\n"
    "v_pk_fma_f32 v[170:171], v[170:171], v[60:61], v[60:61] op_sel:[0,1,0] op_sel_hi:[1,1,0]\n"
    "s_waitcnt lgkmcnt(3)\n"
    "v_pk_fma_f32 v[100:101], v[100:101], v[42:43], v[42:43] op_sel:[0,1,0] op_sel_hi:[1,1,0]\n"
    "v_pk_fma_f32 v[102:103], v[102:103], v[42:43], v[42:43] op_sel:[0,1,0] op_sel_hi:[1,1,0]\n"
    "v_pk_fma_f32 v[104:105], v[104:105], v[42:43], v[42:43] op_sel:[0,1,0] op_sel_hi:[1,1,0]\n"
    "v_pk_fma_f32 v[106:107], v[106:107], v[42:43], v[42:43] op_sel:[0,1,0] op_sel_hi:[1,1,0]\n"
    "v_pk_fma_f32 v[108:109], v[108:109], v[44:45], v[44:45] op_sel:[0,1,0] op_sel_hi:[1,1,0]\n"
    "v_pk_fma_f32 v[110:111], v[110:111], v[44:45], v[44:45] op_sel:[0,1,0] op_sel_hi:[1,1,0]\n"
    "v_pk_fma_f32 v[112:113], v[112:113], v[44:45], v[44:45] op_sel:[0,1,0] op_sel_hi:[1,1,0]\n"
    "v_pk_fma_f32 v[114:115], v[114:115], v[44:45], v[44:45] op_sel:[0,1,0] op_sel_hi:[1,1,0]\n"
    "s_waitcnt lgkmcnt(2)\n"
    "v_pk_fma_f32 v[116:117], v[116:117], v[46:47], v[46:47] op_sel:[0,1,0] op_sel_hi:[1,1,0]\n"
    "v_pk_fma_f32 v[118:119], v[118:119], v[46:47], v[46:47] op_sel:[0,1,0] op_sel_hi:[1,1,0]\n"
    "v_pk_fma_f32 v[120:121], v[120:121], v[46:47], v[46:47] op_sel:[0,1,0] op_sel_hi:[1,1,0]\n"
    "v_pk_fma_f32 v[122:123], v[122:123], v[46:47], v[46:47] op_sel:[0,1,0] op_sel_hi:[1,1,0]\n"
    "v_pk_fma_f32 v[124:125], v[124:125], v[48:49], v[48:49] op_sel:[0,1,0] op_sel_hi:[1,1,0]\n"
    "v_pk_fma_f32 v[126:127], v[126:127], v[48:49], v[48:49] op_sel:[0,1,0] op_sel_hi:[1,1,0]\n"
    "v_pk_fma_f32 v[128:129], v[128:129], v[48:49], v[48:49] op_sel:[0,1,0] op_sel_hi:[1,1,0]\n"
    "v_pk_fma_f32 v[130:131], v[130:131], v[48:49], v[48:49] op_sel:[0,1,0] op_sel_hi:[1,1,0]\n"
    "s_waitcnt lgkmcnt(1)\n"
    "v_pk_fma_f32 v[132:133], v[132:133], v[50:51], v[50:51] op_sel:[0,1,0] op_sel_hi:[1,1,0]\n"
    "v_pk_fma_f32 v[134:135], v[134:135], v[50:51], v[50:51] op_sel:[0,1,0] op_sel_hi:[1,1,0]\n"
    "v_pk_fma_f32 v[136:137], v[136:137], v[50:51], v[50:51] op_sel:[0,1,0] op_sel_hi:[1,1,0]\n"
    "v_pk_fma_f32 v[138:139], v[138:139], v[50:51], v[50:51] op_sel:[0,1,0] op_sel_hi:[1,1,0]\n"
    "v_pk_fma_f32 v[140:141], v[140:141], v[52:53], v[52:53] op_sel:[0,1,0] op_sel_hi:[1,1,0]\n"
    "v_pk_fma_f32 v[142:143], v[142:143], v[52:53], v[52:53] op_sel:[0,1,0] op_sel_hi:[1,1,0]\n"
    "v_pk_fma_f32 v[144:145], v[144:145], v[52:53], v[52:53] op_sel:[0,1,0] op_sel_hi:[1,1,0]\n"
    "v_pk_fma_f32 v[146:147], v[146:147], v[52:53], v[52:53] op_sel:[0,1,0] op_sel_hi:[1,1,0]\n"
    "s_waitcnt lgkmcnt(0)\n"
    "v_pk_fma_f32 v[148:149], v[148:149], v[54:55], v[54:55] op_sel:[0,1,0] op_sel_hi:[1,1,0]\n"
    "v_pk_fma_f32 v[150:151], v[150:151], v[54:55], v[54:55] op_sel:[0,1,0] op_sel_hi:[1,1,0]\n"
    "v_pk_fma_f32 v[152:153], v[152:153], v[54:55], v[54:55] op_sel:[0,1,0] op_sel_hi:[1,1,0]\n"
    "v_pk_fma_f32 v[154:155], v[154:155], v[54:55], v[54:55] op_sel:[0,1,0] op_sel_hi:[1,1,0]\n"
    "v_pk_fma_f32 v[156:157], v[156:157], v[56:57], v[56:57] op_sel:[0,1,0] op_sel_hi:[1,1,0]\n"
    "v_pk_fma_f32 v[158:159], v[158:159], v[56:57], v[56:57] op_sel:[0,1,0] op_sel_hi:[1,1,0]\n"
    "v_pk_fma_f32 v[160:161], v[160:161], v[56:57], v[56:57] op_sel:[0,1,0] op_sel_hi:[1,1,0]\n"
    "v_pk_fma_f32 v[162:163], v[162:163], v[56:57], v[56:57] op_sel:[0,1,0] op_sel_hi:[1,1,0]\n"
    "s_movk_i32 s77, 0x4000\n"
    "PAF%=:\n"
    "s_waitcnt vmcnt(0)\n"
    "v_bitop3_b32 v58, v65, v67, s74 bitop3:0x7e\n"
    "v_cmp_ne_u32 vcc, 0, v58\n"
    "s_cbranch_vccz PBF%=\n"
    "s_sub_u32 s77, s77, 1\n"
    "s_cbranch_scc1 PBF%=\n"
    "s_sleep 2\n"
    "s_mov_b64 exec, vcc\n"
    "buffer_load_dwordx4 v[64:67], v88, s[36:39], s29 offen sc1\n"
    "s_mov_b64 exec, -1\n"
    "s_waitcnt vmcnt(0)\n"
    "s_branch PAF%=\n"
    "PBF%=:\n"
    "v_and_b32 v65, 0xffff0000, v64\n"
    "v_and_b32 v67, 0xffff0000, v66\n"
    "v_lshlrev_b32 v64, 16, v64\n"
    "v_lshlrev_b32 v66, 16, v66\n"
    "ds_write_b128 v16, v[64:67]\n"
    "ds_read_b128 v[32:35], v17 offset:0\n"
    "ds_read_b128 v[40:43], v17 offset:256\n"
    "ds_read_b128 v[36:39], v17 offset:16\n"
    "ds_read_b128 v[44:47], v17 offset:272\n"
    "ds_read_b128 v[48:51], v17 offset:512\n"
    "ds_read_b128 v[56:59], v17 offset:768\n"
    "ds_read_b128 v[52:55], v17 offset:528\n"
    "ds_read_b128 v[60:63], v17 offset:784\n"
    "s_lshl3_add_u32 s12, s96, s76\n"
    "s_lshl_b32 s77, s75, 11\n"
    "s_add_u32 s77, s77, 32768\n"
    "s_lshl3_add_u32 s12, s12, s75\n"
    "s_mul_i32 s12, s12, 2304\n"
    "v_lshl_add_u32 v80, v88, 3, s77\n"
    "s_add_u32 s12, s12, 745472\n"
    "s_cmp_eq_u32 s62, 0\n"
    "s_cbranch_scc1 JL%=\n"
    "buffer_load_dwordx4 v[172:175], v6, s[60:63], s90 offen offset:0\n"
    "buffer_load_dwordx4 v[176:179], v6, s[60:63], s90 offen offset:128\n"
    "buffer_load_dwordx4 v[180:183], v6, s[60:63], s90 offen offset:256\n"
    "JL%=:\n"
    "s_waitcnt lgkmcnt(6)\n"
    "v_pk_fma_f32 v[100:101], v[100:101], v[32:33], v[40:41]\n"
    "v_pk_fma_f32 v[102:103], v[102:103], v[34:35], v[42:43]\n"
    "v_cvt_pk_bf16_f32 v100, v100, v101\n"
    "s_waitcnt lgkmcnt(4)\n"
    "v_pk_fma_f32 v[104:105], v[104:105], v[36:37], v[44:45]\n"
    "v_cvt_pk_bf16_f32 v101, v102, v103\n"
    "v_pk_fma_f32 v[106:107], v[106:107], v[38:39], v[46:47]\n"
    "v_cvt_pk_bf16_f32 v102, v104, v105\n"
    "v_cvt_pk_bf16_f32 v103, v106, v107\n"
    "ds_write_b128 v18, v[100:103] offset:0\n"
    "v_pk_fma_f32 v[108:109], v[108:109], v[32:33], v[40:41]\n"
    "v_pk_fma_f32 v[110:111], v[110:111], v[34:35], v[42:43]\n"
    "v_cvt_pk_bf16_f32 v108, v108, v109\n"
    "v_pk_fma_f32 v[112:113], v[112:113], v[36:37], v[44:45]\n"
    "v_cvt_pk_bf16_f32 v109, v110, v111\n"
    "v_pk_fma_f32 v[114:115], v[114:115], v[38:39], v[46:47]\n"
    "v_cvt_pk_bf16_f32 v110, v112, v113\n"
    "v_cvt_pk_bf16_f32 v111, v114, v115\n"
    "ds_write_b128 v18, v[108:111] offset:128\n"
    "v_pk_fma_f32 v[116:117], v[116:117], v[32:33], v[40:41]\n"
    "v_pk_fma_f32 v[118:119], v[118:119], v[34:35], v[42:43]\n"
    "v_cvt_pk_bf16_f32 v116, v116, v117\n"
    "v_pk_fma_f32 v[120:121], v[120:121], v[36:37], v[44:45]\n"
    "v_cvt_pk_bf16_f32 v117, v118, v119\n"
    "v_pk_fma_f32 v[122:123], v[122:123], v[38:39], v[46:47]\n"
    "v_cvt_pk_bf16_f32 v118, v120, v121\n"
    "v_cvt_pk_bf16_f32 v119, v122, v123\n"
    "ds_write_b128 v18, v[116:119] offset:256\n"
    "v_pk_fma_f32 v[124:125], v[124:125], v[32:33], v[40:41]\n"
    "v_pk_fma_f32 v[126:127], v[126:127], v[34:35], v[42:43]\n"
    "v_cvt_pk_bf16_f32 v124, v124, v125\n"
    "v_pk_fma_f32 v[128:129], v[128:129], v[36:37], v[44:45]\n"
    "v_cvt_pk_bf16_f32 v125, v126, v127\n"
    "v_pk_fma_f32 v[130:131], v[130:131], v[38:39], v[46:47]\n"
    "v_cvt_pk_bf16_f32 v126, v128, v129\n"
    "v_cvt_pk_bf16_f32 v127, v130, v131\n"
    "ds_write_b128 v18, v[124:127] offset:384\n"
    "v_pk_fma_f32 v[132:133], v[132:133], v[32:33], v[40:41]\n"
    "v_pk_fma_f32 v[134:135], v[134:135], v[34:35], v[42:43]\n"
    "v_cvt_pk_bf16_f32 v132, v132, v133\n"
    "v_pk_fma_f32 v[136:137], v[136:137], v[36:37], v[44:45]\n"
    "v_cvt_pk_bf16_f32 v133, v134, v135\n"
    "v_pk_fma_f32 v[138:139], v[138:139], v[38:39], v[46:47]\n"
    "v_cvt_pk_bf16_f32 v134, v136, v137\n"
    "v_cvt_pk_bf16_f32 v135, v138, v139\n"
    "ds_write_b128 v18, v[132:135] offset:512\n"
    "v_pk_fma_f32 v[140:141], v[140:141], v[32:33], v[40:41]\n"
    "v_pk_fma_f32 v[142:143], v[142:143], v[34:35], v[42:43]\n"
    "v_cvt_pk_bf16_f32 v140, v140, v141\n"
    "v_pk_fma_f32 v[144:145], v[144:145], v[36:37], v[44:45]\n"
    "v_cvt_pk_bf16_f32 v141, v142, v143\n"
    "v_pk_fma_f32 v[146:147], v[146:147], v[38:39], v[46:47]\n"
    "v_cvt_pk_bf16_f32 v142, v144, v145\n"
    "v_cvt_pk_bf16_f32 v143, v146, v147\n"
    "ds_write_b128 v18, v[140:143] offset:640\n"
    "v_pk_fma_f32 v[148:149], v[148:149], v[32:33], v[40:41]\n"
    "v_pk_fma_f32 v[150:151], v[150:151], v[34:35], v[42:43]\n"
    "v_cvt_pk_bf16_f32 v148, v148, v149\n"
    "v_pk_fma_f32 v[152:153], v[152:153], v[36:37], v[44:45]\n"
    "v_cvt_pk_bf16_f32 v149, v150, v151\n"
    "v_pk_fma_f32 v[154:155], v[154:155], v[38:39], v[46:47]\n"
    "v_cvt_pk_bf16_f32 v150, v152, v153\n"
    "v_cvt_pk_bf16_f32 v151, v154, v155\n"
    "ds_write_b128 v18, v[148:151] offset:768\n"
    "v_pk_fma_f32 v[156:157], v[156:157], v[32:33], v[40:41]\n"
    "v_pk_fma_f32 v[158:159], v[158:159], v[34:35], v[42:43]\n"
    "v_cvt_pk_bf16_f32 v156, v156, v157\n"
    "v_pk_fma_f32 v[160:161], v[160:161], v[36:37], v[44:45]\n"
    "v_cvt_pk_bf16_f32 v157, v158, v159\n"
    "v_pk_fma_f32 v[162:163], v[162:163], v[38:39], v[46:47]\n"
    "v_cvt_pk_bf16_f32 v158, v160, v161\n"
    "v_cvt_pk_bf16_f32 v159, v162, v163\n"
    "ds_write_b128 v18, v[156:159] offset:896\n"
    "s_waitcnt lgkmcnt(10)\n"
    "v_pk_fma_f32 v[164:165], v[164:165], v[48:49], v[56:57]\n"
    "v_pk_fma_f32 v[166:167], v[166:167], v[50:51], v[58:59]\n"
    "v_cvt_pk_bf16_f32 v164, v164, v165\n"
    "s_waitcnt lgkmcnt(8)\n"
    "v_pk_fma_f32 v[168:169], v[168:169], v[52:53], v[60:61]\n"
    "v_cvt_pk_bf16_f32 v165, v166, v167\n"
    "v_pk_fma_f32 v[170:171], v[170:171], v[54:55], v[62:63]\n"
    "v_cvt_pk_bf16_f32 v166, v168, v169\n"
    "v_cvt_pk_bf16_f32 v167, v170, v171\n"
    "ds_write_b128 v91, v[164:167] offset:0\n"
    "s_setprio 2\n"
    "s_cmp_lt_u32 s76, 4\n"
    "s_cbranch_scc1 RQ%=\n"
    "s_bitcmp1_b32 s76, 0\n"
    "s_cbranch_scc1 RV%=\n"
    "RK%=:\n"
    "s_waitcnt lgkmcnt(0)\n"
    "s_barrier\n"
    "s_memtime s[14:15]\n"
    "ds_read_b128 v[100:103], v20 offset:0\n"
    "ds_read_b64_tr_b16 v[184:185], v19 offset:0\n"
    "ds_read_b64_tr_b16 v[186:187], v19 offset:512\n"
    "ds_read_b128 v[104:107], v20 offset:4160\n"
    "ds_read_b64_tr_b16 v[188:189], v19 offset:1024\n"
    "ds_read_b64_tr_b16 v[190:191], v19 offset:1536\n"
    "ds_read_b128 v[108:111], v20 offset:8320\n"
    "ds_read_b64_tr_b16 v[192:193], v19 offset:2048\n"
    "ds_read_b64_tr_b16 v[194:195], v19 offset:2560\n"
    "ds_read_b128 v[112:115], v20 offset:12480\n"
    "ds_read_b64_tr_b16 v[196:197], v19 offset:3072\n"
    "ds_read_b64_tr_b16 v[198:199], v19 offset:3584\n"
    "ds_read_b128 v[116:119], v20 offset:16640\n"
    "ds_read_b64_tr_b16 v[200:201], v19 offset:4096\n"
    "ds_read_b64_tr_b16 v[202:203], v19 offset:4608\n"
    "s_waitcnt lgkmcnt(12)\n"
    "v_mfma_f32_16x16x32_bf16 v[32:35], v[184:187], v[100:103], 0\n"
    "ds_read_b128 v[120:123], v20 offset:20800\n"
    "ds_read_b64_tr_b16 v[204:205], v19 offset:5120\n"
    "ds_read_b64_tr_b16 v[206:207], v19 offset:5632\n"
    "s_waitcnt lgkmcnt(12)\n"
    "v_mfma_f32_16x16x32_bf16 v[32:35], v[188:191], v[104:107], v[32:35]\n"
    "ds_read_b128 v[124:127], v20 offset:24960\n"
    "ds_read_b64_tr_b16 v[208:209], v19 offset:6144\n"
    "ds_read_b64_tr_b16 v[210:211], v19 offset:6656\n"
    "s_waitcnt lgkmcnt(12)\n"
    "v_mfma_f32_16x16x32_bf16 v[32:35], v[192:195], v[108:111], v[32:35]\n"
    "ds_read_b128 v[128:131], v20 offset:29120\n"
    "ds_read_b64_tr_b16 v[212:213], v19 offset:7168\n"
    "ds_read_b64_tr_b16 v[214:215], v19 offset:7680\n"
    "s_waitcnt lgkmcnt(12)\n"
    "v_mfma_f32_16x16x32_bf16 v[32:35], v[196:199], v[112:115], v[32:35]\n"
    "ds_read_b128 v[132:135], v20 offset:33280\n"
    "ds_read_b64_tr_b16 v[216:217], v19 offset:8192\n"
    "ds_read_b64_tr_b16 v[218:219], v19 offset:8704\n"
    "s_waitcnt lgkmcnt(12)\n"
    "v_mfma_f32_16x16x32_bf16 v[32:35], v[200:203], v[116:119], v[32:35]\n"
    "ds_read_b128 v[136:139], v20 offset:37440\n"
    "ds_read_b64_tr_b16 v[220:221], v19 offset:9216\n"
    "ds_read_b64_tr_b16 v[222:223], v19 offset:9728\n"
    "s_waitcnt lgkmcnt(12)\n"
    "v_mfma_f32_16x16x32_bf16 v[32:35], v[204:207], v[120:123], v[32:35]\n"
    "ds_read_b128 v[140:143], v20 offset:41600\n"
    "ds_read_b64_tr_b16 v[224:225], v19 offset:10240\n"
    "ds_read_b64_tr_b16 v[226:227], v19 offset:10752\n"
    "s_waitcnt lgkmcnt(12)\n"
    "v_mfma_f32_16x16x32_bf16 v[32:35], v[208:211], v[124:127], v[32:35]\n"
    "ds_read_b128 v[144:147], v20 offset:45760\n"
    "ds_read_b64_tr_b16 v[228:229], v19 offset:11264\n"
    "ds_read_b64_tr_b16 v[230:231], v19 offset:11776\n"
    "s_waitcnt lgkmcnt(12)\n"
    "v_mfma_f32_16x16x32_bf16 v[32:35], v[212:215], v[128:131], v[32:35]\n"
    "ds_read_b128 v[148:151], v20 offset:49920\n"
    "ds_read_b64_tr_b16 v[232:233], v19 offset:12288\n"
    "ds_read_b64_tr_b16 v[234:235], v19 offset:12800\n"
    "s_waitcnt lgkmcnt(12)\n"
    "v_mfma_f32_16x16x32_bf16 v[32:35], v[216:219], v[132:135], v[32:35]\n"
    "ds_read_b128 v[152:155], v20 offset:54080\n"
    "ds_read_b64_tr_b16 v[236:237], v19 offset:13312\n"
    "ds_read_b64_tr_b16 v[238:239], v19 offset:13824\n"
    "s_waitcnt lgkmcnt(12)\n"
    "v_mfma_f32_16x16x32_bf16 v[32:35], v[220:223], v[136:139], v[32:35]\n"
    "ds_read_b128 v[156:159], v20 offset:58240\n"
    "ds_read_b64_tr_b16 v[240:241], v19 offset:14336\n"
    "ds_read_b64_tr_b16 v[242:243], v19 offset:14848\n"
    "s_waitcnt lgkmcnt(12)\n"
    "v_mfma_f32_16x16x32_bf16 v[32:35], v[224:227], v[140:143], v[32:35]\n"
    "ds_read_b128 v[160:163], v20 offset:62400\n"
    "ds_read_b64_tr_b16 v[244:245], v19 offset:15360\n"
    "ds_read_b64_tr_b16 v[246:247], v19 offset:15872\n"
    "s_waitcnt lgkmcnt(12)\n"
    "v_mfma_f32_16x16x32_bf16 v[32:35], v[228:231], v[144:147], v[32:35]\n"
    "ds_read_b128 v[164:167], v90 offset:0\n"
    "ds_read_b64_tr_b16 v[248:249], v19 offset:16384\n"
    "ds_read_b64_tr_b16 v[250:251], v19 offset:16896\n"
    "s_waitcnt lgkmcnt(12)\n"
    "v_mfma_f32_16x16x32_bf16 v[32:35], v[232:235], v[148:151], v[32:35]\n"
    "ds_read_b128 v[168:171], v90 offset:4160\n"
    "ds_read_b64_tr_b16 v[252:253], v19 offset:17408\n"
    "ds_read_b64_tr_b16 v[254:255], v19 offset:17920\n"
    "s_waitcnt lgkmcnt(12)\n"
    "v_mfma_f32_16x16x32_bf16 v[32:35], v[236:239], v[152:155], v[32:35]\n"
    "s_waitcnt lgkmcnt(9)\n"
    "v_mfma_f32_16x16x32_bf16 v[32:35], v[240:243], v[156:159], v[32:35]\n"
    "s_waitcnt lgkmcnt(6)\n"
    "v_mfma_f32_16x16x32_bf16 v[32:35], v[244:247], v[160:163], v[32:35]\n"
    "s_waitcnt lgkmcnt(3)\n"
    "v_mfma_f32_16x16x32_bf16 v[32:35], v[248:251], v[164:167], v[32:35]\n"
    "s_waitcnt lgkmcnt(0)\n"
    "v_mfma_f32_16x16x32_bf16 v[32:35], v[252:255], v[168:171], v[32:35]\n"
    "v_mov_b32 v36, 0\n"
    "s_nop 6\n"
    "v_cvt_pk_bf16_f32 v32, 0, v32\n"
    "v_cvt_pk_bf16_f32 v33, 0, v33\n"
    "v_cvt_pk_bf16_f32 v34, 0, v34\n"
    "v_cvt_pk_bf16_f32 v35, 0, v35\n"
    "v_mul_f32 v32, 0x3eae1a93, v32\n"
    "v_mul_f32 v33, 0x3eae1a93, v33\n"
    "v_mul_f32 v34, 0x3eae1a93, v34\n"
    "v_mul_f32 v35, 0x3eae1a93, v35\n"
    "v_cvt_pk_bf16_f32 v32, v32, v33\n"
    "v_cvt_pk_bf16_f32 v33, v34, v35\n"
    "s_mov_b64 exec, s[30:31]\n"
    "buffer_store_dwordx2 v[32:33], v11, s[36:39], s94 offen sc1\n"
    "v_dot2c_f32_bf16 v36, v33, v33\n"
    "s_mov_b32 exec_hi, s30\n"
    "v_dot2c_f32_bf16 v36, v32, v32\n"
    "s_mov_b64 exec, -1\n"
    "v_mov_b32 v38, 159232\n"
    "s_nop 0\n"
    "v_mov_b32 v37, v36\n"
    "s_nop 1\n"
    "v_permlane32_swap_b32 v36, v37\n"
    "v_add_f32 v36, v36, v37\n"
    "v_mov_b32 v37, v36\n"
    "s_nop 1\n"
    "v_permlane16_swap_b32 v36, v37\n"
    "v_add_f32 v36, v36, v37\n"
    "s_mov_b64 exec, 0xffff\n"
    "ds_max_u32 v38, v36\n"
    "s_mov_b64 exec, -1\n"
    "v_mov_b32 v59, s74\n"
    "v_mov_b32 v21, 159232\n"
    "v_mov_b32 v22, 1\n"
    "s_mov_b64 exec, 1\n"
    "s_waitcnt vmcnt(0)\n"
    "ds_add_rtn_u32 v22, v21, v22 offset:8\n"
    "s_mov_b64 exec, -1\n"
    "ds_read_b32 v58, v21\n"
    "s_waitcnt lgkmcnt(1)\n"
    "v_readfirstlane_b32 s77, v22\n"
    "s_cmp_lg_u32 s77, 3\n"
    "s_cbranch_scc1 KLK%=\n"
    "s_mov_b64 exec, 1\n"
    "s_waitcnt lgkmcnt(0)\n"
    "buffer_store_dwordx2 v[58:59], off, s[36:39], s28 sc1\n"
    "s_mov_b64 exec, -1\n"
    "KLK%=:\n"
    "s_setprio 0\n"
    "s_and_b32 s77, s75, 3\n"
    "s_lshl2_add_u32 s77, s77, LTB-LK3B+16\n"
    "s_add_u32 s22, s80, s77\n"
    "s_addc_u32 s23, s81, 0\n"
    "s_mov_b64 exec, 0\n"
    "s_swappc_b64 vcc, s[22:23]\n"
    "s_mov_b64 exec, -1\n"
    "s_waitcnt lgkmcnt(0)\n"
    "s_barrier\n"
    "s_branch PO%=\n"
    "GC1%=:\n"
    "v_mov_b64 v[70:71], 0\n"
    "v_mov_b64 v[72:73], 0\n"
    "v_mov_b64 v[74:75], 0\n"
    "v_mov_b64 v[76:77], 0\n"
    "v_and_b32 v58, 15, v0\n"
    "v_lshlrev_b32 v58, 3, v58\n"
    "v_add_u32 v59, 159360, v58\n"
    "v_bfe_u32 v60, v0, 4, 2\n"
    "v_lshl_add_u32 v58, v60, 10, v58\n"
    "v_and_b32 v62, 3, v0\n"
    "v_lshl_add_u32 v62, v62, 2, v60\n"
    "s_and_b32 s77, s75, 3\n"
    "s_lshl2_add_u32 s78, s77, 159264\n"
    "v_mov_b32 v21, s78\n"
    "s_lshl_b32 s77, s77, 7\n"
    "s_add_u32 s77, s77, 159360\n"
    "v_lshl_add_u32 v62, v62, 3, s77\n"
    "v_mov_b32 v22, 159264\n"
    "v_mov_b32 v23, 1\n"
    "s_sub_u32 s78, s89, 18\n"
    "s_min_u32 s79, s89, s78\n"
    "s_cselect_b32 s91, 256, 0\n"
    "s_cselect_b32 s93, 0, 1.0\n"
    "s_addk_i32 s91, 1024\n"
    "s_and_b32 s78, s79, 1\n"
    "s_lshl_b32 s78, s78, 7\n"
    "s_add_u32 s91, s91, s78\n"
    "s_lshr_b32 s78, s79, 1\n"
    "s_lshl_b32 s78, s78, 10\n"
    "s_cmp_lt_u32 s79, 16\n"
    "s_cselect_b32 s78, s78, 512\n"
    "s_cselect_b32 s92, 0, 4096\n"
    "s_add_u32 s91, s91, s78\n"
    "s_add_u32 s77, s90, 20736\n"
    "buffer_load_dwordx4 v[184:187], v6, s[48:51], s77 offen\n"
    "s_add_u32 s77, s90, 27648\n"
    "buffer_load_dwordx4 v[188:191], v6, s[48:51], s77 offen\n"
    "s_add_u32 s77, s90, 34560\n"
    "buffer_load_dwordx4 v[192:195], v6, s[48:51], s77 offen\n"
    "s_add_u32 s77, s90, 41472\n"
    "buffer_load_dwordx4 v[196:199], v6, s[48:51], s77 offen\n"
    "s_add_u32 s77, s90, 48384\n"
    "buffer_load_dwordx4 v[200:203], v6, s[48:51], s77 offen\n"
    "s_add_u32 s77, s90, 3538944\n"
    "buffer_load_dwordx4 v[204:207], v7, s[48:51], s77 offen\n"
    "s_waitcnt lgkmcnt(0)\n"
    "s_barrier\n"
    "s_waitcnt vmcnt(10)\n"
    "v_lshlrev_b32 v210, 16, v208\n"
    "v_and_b32 v212, 0xffff0000, v208\n"
    "s_waitcnt vmcnt(9)\n"
    "v_lshlrev_b32 v214, 16, v209\n"
    "v_mul_f32 v211, 0xbfb8aa3b, v210\n"
    "v_mul_f32 v213, 0xbfb8aa3b, v212\n"
    "v_mul_f32 v215, 0xbfb8aa3b, v214\n"
    "v_exp_f32 v211, v211\n"
    "v_exp_f32 v213, v213\n"
    "v_exp_f32 v215, v215\n"
    "v_add_f32 v211, 1.0, v211\n"
    "v_add_f32 v213, 1.0, v213\n"
    "v_add_f32 v215, 1.0, v215\n"
    "v_rcp_f32 v211, v211\n"
    "v_rcp_f32 v213, v213\n"
    "v_rcp_f32 v215, v215\n"
    "v_mul_f32 v210, v210, v211\n"
    "v_mul_f32 v212, v212, v213\n"
    "v_mul_f32 v214, v214, v215\n"
    "v_cvt_pk_bf16_f32 v210, v210, 0\n"
    "v_cvt_pk_bf16_f32 v212, v212, 0\n"
    "v_cvt_pk_bf16_f32 v214, v214, 0\n"
    "v_lshlrev_b32 v211, 16, v210\n"
    "v_lshlrev_b32 v213, 16, v212\n"
    "v_lshlrev_b32 v215, 16, v214\n"
    "v_readfirstlane_b32 s74, v1\n"
    "s_mul_i32 s74, s74, 0x0019660d\n"
    "s_add_u32 s74, s74, 0x3c6ef35f\n"
    "v_mov_b32 v61, s74\n"
    "s_waitcnt vmcnt(8)\n"
    "v_dot2c_f32_bf16_dpp v70, v210, v172 quad_perm:[0,0,0,0] row_mask:0xf bank_mask:0xf\n"
    "v_dot2c_f32_bf16_dpp v71, v211, v172 quad_perm:[0,0,0,0] row_mask:0xf bank_mask:0xf\n"
    "v_dot2c_f32_bf16_dpp v72, v210, v173 quad_perm:[0,0,0,0] row_mask:0xf bank_mask:0xf\n"
    "v_dot2c_f32_bf16_dpp v73, v211, v173 quad_perm:[0,0,0,0] row_mask:0xf bank_mask:0xf\n"
    "v_dot2c_f32_bf16_dpp v74, v210, v174 quad_perm:[0,0,0,0] row_mask:0xf bank_mask:0xf\n"
    "v_dot2c_f32_bf16_dpp v75, v211, v174 quad_perm:[0,0,0,0] row_mask:0xf bank_mask:0xf\n"
    "v_dot2c_f32_bf16_dpp v76, v210, v175 quad_perm:[0,0,0,0] row_mask:0xf bank_mask:0xf\n"
    "v_dot2c_f32_bf16_dpp v77, v211, v175 quad_perm:[0,0,0,0] row_mask:0xf bank_mask:0xf\n"
    "s_xor_b32 s77, s76, 0\n"
    "s_mul_i32 s77, s77, 9216\n"
    "s_add_u32 s77, s77, s82\n"
    "buffer_load_dwordx4 v[94:97], v4, s[40:43], s77 offen\n"
    "s_waitcnt vmcnt(8)\n"
    "v_dot2c_f32_bf16_dpp v70, v212, v176 quad_perm:[0,0,0,0] row_mask:0xf bank_mask:0xf\n"
    "v_dot2c_f32_bf16_dpp v71, v213, v176 quad_perm:[0,0,0,0] row_mask:0xf bank_mask:0xf\n"
    "v_dot2c_f32_bf16_dpp v72, v212, v177 quad_perm:[0,0,0,0] row_mask:0xf bank_mask:0xf\n"
    "v_dot2c_f32_bf16_dpp v73, v213, v177 quad_perm:[0,0,0,0] row_mask:0xf bank_mask:0xf\n"
    "v_dot2c_f32_bf16_dpp v74, v212, v178 quad_perm:[0,0,0,0] row_mask:0xf bank_mask:0xf\n"
    "v_dot2c_f32_bf16_dpp v75, v213, v178 quad_perm:[0,0,0,0] row_mask:0xf bank_mask:0xf\n"
    "v_dot2c_f32_bf16_dpp v76, v212, v179 quad_perm:[0,0,0,0] row_mask:0xf bank_mask:0xf\n"
    "v_dot2c_f32_bf16_dpp v77, v213, v179 quad_perm:[0,0,0,0] row_mask:0xf bank_mask:0xf\n"
    "s_xor_b32 s77, s76, 1\n"
    "s_mul_i32 s77, s77, 9216\n"
    "s_add_u32 s77, s77, s82\n"
    "buffer_load_dwordx4 v[112:115], v4, s[40:43], s77 offen\n"
    "s_waitcnt vmcnt(8)\n"
    "v_dot2c_f32_bf16_dpp v70, v210, v180 quad_perm:[1,1,1,1] row_mask:0xf bank_mask:0xf\n"
    "v_dot2c_f32_bf16_dpp v71, v211, v180 quad_perm:[1,1,1,1] row_mask:0xf bank_mask:0xf\n"
    "v_dot2c_f32_bf16_dpp v72, v210, v181 quad_perm:[1,1,1,1] row_mask:0xf bank_mask:0xf\n"
    "v_dot2c_f32_bf16_dpp v73, v211, v181 quad_perm:[1,1,1,1] row_mask:0xf bank_mask:0xf\n"
    "v_dot2c_f32_bf16_dpp v74, v210, v182 quad_perm:[1,1,1,1] row_mask:0xf bank_mask:0xf\n"
    "v_dot2c_f32_bf16_dpp v75, v211, v182 quad_perm:[1,1,1,1] row_mask:0xf bank_mask:0xf\n"
    "v_dot2c_f32_bf16_dpp v76, v210, v183 quad_perm:[1,1,1,1] row_mask:0xf bank_mask:0xf\n"
    "v_dot2c_f32_bf16_dpp v77, v211, v183 quad_perm:[1,1,1,1] row_mask:0xf bank_mask:0xf\n"
    "s_xor_b32 s77, s76, 2\n"
    "s_mul_i32 s77, s77, 9216\n"
    "s_add_u32 s77, s77, s82\n"
    "buffer_load_dwordx4 v[120:123], v4, s[40:43], s77 offen\n"
    "s_waitcnt vmcnt(8)\n"
    "v_dot2c_f32_bf16_dpp v70, v212, v184 quad_perm:[1,1,1,1] row_mask:0xf bank_mask:0xf\n"
    "v_dot2c_f32_bf16_dpp v71, v213, v184 quad_perm:[1,1,1,1] row_mask:0xf bank_mask:0xf\n"
    "v_dot2c_f32_bf16_dpp v72, v212, v185 quad_perm:[1,1,1,1] row_mask:0xf bank_mask:0xf\n"
    "v_dot2c_f32_bf16_dpp v73, v213, v185 quad_perm:[1,1,1,1] row_mask:0xf bank_mask:0xf\n"
    "v_dot2c_f32_bf16_dpp v74, v212, v186 quad_perm:[1,1,1,1] row_mask:0xf bank_mask:0xf\n"
    "v_dot2c_f32_bf16_dpp v75, v213, v186 quad_perm:[1,1,1,1] row_mask:0xf bank_mask:0xf\n"
    "v_dot2c_f32_bf16_dpp v76, v212, v187 quad_perm:[1,1,1,1] row_mask:0xf bank_mask:0xf\n"
    "v_dot2c_f32_bf16_dpp v77, v213, v187 quad_perm:[1,1,1,1] row_mask:0xf bank_mask:0xf\n"
    "s_xor_b32 s77, s76, 3\n"
    "s_mul_i32 s77, s77, 9216\n"
    "s_add_u32 s77, s77, s82\n"
    "buffer_load_dwordx4 v[128:131], v4, s[40:43], s77 offen\n"
    "s_waitcnt vmcnt(8)\n"
    "v_dot2c_f32_bf16_dpp v70, v210, v188 quad_perm:[2,2,2,2] row_mask:0xf bank_mask:0xf\n"
    "v_dot2c_f32_bf16_dpp v71, v211, v188 quad_perm:[2,2,2,2] row_mask:0xf bank_mask:0xf\n"
    "v_dot2c_f32_bf16_dpp v72, v210, v189 quad_perm:[2,2,2,2] row_mask:0xf bank_mask:0xf\n"
    "v_dot2c_f32_bf16_dpp v73, v211, v189 quad_perm:[2,2,2,2] row_mask:0xf bank_mask:0xf\n"
    "v_dot2c_f32_bf16_dpp v74, v210, v190 quad_perm:[2,2,2,2] row_mask:0xf bank_mask:0xf\n"
    "v_dot2c_f32_bf16_dpp v75, v211, v190 quad_perm:[2,2,2,2] row_mask:0xf bank_mask:0xf\n"
    "v_dot2c_f32_bf16_dpp v76, v210, v191 quad_perm:[2,2,2,2] row_mask:0xf bank_mask:0xf\n"
    "v_dot2c_f32_bf16_dpp v77, v211, v191 quad_perm:[2,2,2,2] row_mask:0xf bank_mask:0xf\n"
    "s_xor_b32 s77, s76, 4\n"
    "s_mul_i32 s77, s77, 9216\n"
    "s_add_u32 s77, s77, s82\n"
    "buffer_load_dwordx4 v[136:139], v4, s[40:43], s77 offen\n"
    "s_waitcnt vmcnt(8)\n"
    "v_dot2c_f32_bf16_dpp v70, v212, v192 quad_perm:[2,2,2,2] row_mask:0xf bank_mask:0xf\n"
    "v_dot2c_f32_bf16_dpp v71, v213, v192 quad_perm:[2,2,2,2] row_mask:0xf bank_mask:0xf\n"
    "v_dot2c_f32_bf16_dpp v72, v212, v193 quad_perm:[2,2,2,2] row_mask:0xf bank_mask:0xf\n"
    "v_dot2c_f32_bf16_dpp v73, v213, v193 quad_perm:[2,2,2,2] row_mask:0xf bank_mask:0xf\n"
    "v_dot2c_f32_bf16_dpp v74, v212, v194 quad_perm:[2,2,2,2] row_mask:0xf bank_mask:0xf\n"
    "v_dot2c_f32_bf16_dpp v75, v213, v194 quad_perm:[2,2,2,2] row_mask:0xf bank_mask:0xf\n"
    "v_dot2c_f32_bf16_dpp v76, v212, v195 quad_perm:[2,2,2,2] row_mask:0xf bank_mask:0xf\n"
    "v_dot2c_f32_bf16_dpp v77, v213, v195 quad_perm:[2,2,2,2] row_mask:0xf bank_mask:0xf\n"
    "s_xor_b32 s77, s76, 5\n"
    "s_mul_i32 s77, s77, 9216\n"
    "s_add_u32 s77, s77, s82\n"
    "buffer_load_dwordx4 v[144:147], v4, s[40:43], s77 offen\n"
    "s_waitcnt vmcnt(8)\n"
    "v_dot2c_f32_bf16_dpp v70, v210, v196 quad_perm:[3,3,3,3] row_mask:0xf bank_mask:0xf\n"
    "v_dot2c_f32_bf16_dpp v71, v211, v196 quad_perm:[3,3,3,3] row_mask:0xf bank_mask:0xf\n"
    "v_dot2c_f32_bf16_dpp v72, v210, v197 quad_perm:[3,3,3,3] row_mask:0xf bank_mask:0xf\n"
    "v_dot2c_f32_bf16_dpp v73, v211, v197 quad_perm:[3,3,3,3] row_mask:0xf bank_mask:0xf\n"
    "v_dot2c_f32_bf16_dpp v74, v210, v198 quad_perm:[3,3,3,3] row_mask:0xf bank_mask:0xf\n"
    "v_dot2c_f32_bf16_dpp v75, v211, v198 quad_perm:[3,3,3,3] row_mask:0xf bank_mask:0xf\n"
    "v_dot2c_f32_bf16_dpp v76, v210, v199 quad_perm:[3,3,3,3] row_mask:0xf bank_mask:0xf\n"
    "v_dot2c_f32_bf16_dpp v77, v211, v199 quad_perm:[3,3,3,3] row_mask:0xf bank_mask:0xf\n"
    "s_xor_b32 s77, s76, 6\n"
    "s_mul_i32 s77, s77, 9216\n"
    "s_add_u32 s77, s77, s82\n"
    "buffer_load_dwordx4 v[152:155], v4, s[40:43], s77 offen\n"
    "s_waitcnt vmcnt(8)\n"
    "v_dot2c_f32_bf16_dpp v70, v212, v200 quad_perm:[3,3,3,3] row_mask:0xf bank_mask:0xf\n"
    "v_dot2c_f32_bf16_dpp v71, v213, v200 quad_perm:[3,3,3,3] row_mask:0xf bank_mask:0xf\n"
    "v_dot2c_f32_bf16_dpp v72, v212, v201 quad_perm:[3,3,3,3] row_mask:0xf bank_mask:0xf\n"
    "v_dot2c_f32_bf16_dpp v73, v213, v201 quad_perm:[3,3,3,3] row_mask:0xf bank_mask:0xf\n"
    "v_dot2c_f32_bf16_dpp v74, v212, v202 quad_perm:[3,3,3,3] row_mask:0xf bank_mask:0xf\n"
    "v_dot2c_f32_bf16_dpp v75, v213, v202 quad_perm:[3,3,3,3] row_mask:0xf bank_mask:0xf\n"
    "v_dot2c_f32_bf16_dpp v76, v212, v203 quad_perm:[3,3,3,3] row_mask:0xf bank_mask:0xf\n"
    "v_dot2c_f32_bf16_dpp v77, v213, v203 quad_perm:[3,3,3,3] row_mask:0xf bank_mask:0xf\n"
    "s_xor_b32 s77, s76, 7\n"
    "s_mul_i32 s77, s77, 9216\n"
    "s_add_u32 s77, s77, s82\n"
    "buffer_load_dwordx4 v[160:163], v4, s[40:43], s77 offen\n"
    "s_waitcnt vmcnt(8)\n"
    "v_dot2c_f32_bf16 v70, v214, v204\n"
    "v_dot2c_f32_bf16 v71, v215, v204\n"
    "v_dot2c_f32_bf16 v72, v214, v205\n"
    "v_dot2c_f32_bf16 v73, v215, v205\n"
    "v_dot2c_f32_bf16 v74, v214, v206\n"
    "v_dot2c_f32_bf16 v75, v215, v206\n"
    "v_dot2c_f32_bf16 v76, v214, v207\n"
    "v_dot2c_f32_bf16 v77, v215, v207\n"
    "v_permlane32_swap_b32 v70, v74\n"
    "v_permlane32_swap_b32 v71, v75\n"
    "v_permlane32_swap_b32 v72, v76\n"
    "v_permlane32_swap_b32 v73, v77\n"
    "v_add_f32 v70, v70, v74\n"
    "v_add_f32 v71, v71, v75\n"
    "v_add_f32 v72, v72, v76\n"
    "v_add_f32 v73, v73, v77\n"
    "s_nop 0\n"
    "v_permlane16_swap_b32 v70, v72\n"
    "v_permlane16_swap_b32 v71, v73\n"
    "v_add_f32 v70, v70, v72\n"
    "v_add_f32 v71, v71, v73\n"
    "s_nop 0\n"
    "v_add_f32_dpp v70, v70, v70 row_ror:8 row_mask:0xf bank_mask:0xf\n"
    "v_add_f32_dpp v71, v71, v71 row_ror:8 row_mask:0xf bank_mask:0xf\n"
    "s_nop 0\n"
    "v_add_f32_dpp v70, v70, v70 row_ror:4 row_mask:0xf bank_mask:0xf\n"
    "v_add_f32_dpp v71, v71, v71 row_ror:4 row_mask:0xf bank_mask:0xf\n"
    "ds_write_b64 v62, v[70:71]\n"
    "ds_write_b32 v21, v23\n"
    "ds_read_b128 v[74:77], v22\n"
    "ds_read2_b64 v[172:175], v59 offset0:0 offset1:16\n"
    "ds_read2_b64 v[176:179], v59 offset0:32 offset1:48\n"
    "s_waitcnt lgkmcnt(2)\n"
    "v_bitop3_b32 v74, v74, v75, v76 bitop3:0x80\n"
    "v_and_b32 v74, v74, v77\n"
    "v_cmp_eq_u32 vcc, 0, v74\n"
    "s_cbranch_vccnz GL1%=\n"
    "s_waitcnt lgkmcnt(1)\n"
    "v_pk_add_f32 v[172:173], v[172:173], v[174:175]\n"
    "s_waitcnt lgkmcnt(0)\n"
    "v_pk_add_f32 v[176:177], v[176:177], v[178:179]\n"
    "v_pk_add_f32 v[172:173], v[172:173], v[176:177]\n"
    "v_cvt_pk_bf16_f32 v172, v172, v173\n"
    "s_cmp_eq_u32 s92, 0\n"
    "v_and_b32 v173, 0xffff0000, v172\n"
    "v_lshlrev_b32 v172, 16, v172\n"
    "v_add_f32 v173, s93, v173\n"
    "v_add_f32 v172, s93, v172\n"
    "v_cvt_pk_bf16_f32 v60, v172, v173\n"
    "s_cselect_b64 exec, 0xffff, -1\n"
    "buffer_store_dwordx2 v[60:61], v58, s[36:39], s91 offen sc1\n"
    "s_cbranch_scc1 GR%=\n"
    "s_add_u32 s91, s91, s92\n"
    "buffer_store_dwordx2 v[60:61], v58, s[36:39], s91 offen sc1\n"
    "GR%=:\n"
    "s_mov_b64 exec, -1\n"
    "GL1%=:\n"
    "s_waitcnt lgkmcnt(0)\n"
    "s_setprio 0\n"
    "s_add_u32 m0, s84, 0\n"
    "s_xor_b32 s77, s85, 0\n"
    "s_mul_i32 s77, s77, 73728\n"
    "s_add_u32 s77, s77, s83\n"
    "buffer_load_dwordx4 v4, s[44:47], s77 offen lds\n"
    "s_add_u32 m0, s84, 8192\n"
    "s_xor_b32 s77, s85, 1\n"
    "s_mul_i32 s77, s77, 73728\n"
    "s_add_u32 s77, s77, s83\n"
    "buffer_load_dwordx4 v4, s[44:47], s77 offen lds\n"
    "LPP2:\n"
    "s_cbranch_execz LTR\n"
    "s_xor_b32 s77, s75, s76\n"
    "s_lshl3_add_u32 s77, s96, s77\n"
    "s_mul_i32 s77, s77, 9216\n"
    "buffer_load_dwordx4 v[82:85], v4, s[40:43], s77 offen offset:1024\n"
    "s_branch G1S%=\n"
    "GC2%=:\n"
    "LPGC2:\n"
    "s_cbranch_execz LTR\n"
    "s_lshl_b32 s90, s89, 6\n"
    "s_addk_i32 s90, 2304\n"
    "v_bfe_u32 v9, v0, 2, 6\n"
    "v_and_b32 v8, 3, v0\n"
    "v_mul_u32_u24 v7, 6912, v9\n"
    "v_lshlrev_b32 v8, 4, v8\n"
    "v_lshl_add_u32 v6, v7, 3, v8\n"
    "v_add_u32 v7, v7, v8\n"
    "v_and_b32 v8, 255, v0\n"
    "v_lshlrev_b32 v9, 1, v9\n"
    "v_lshlrev_b32 v8, 2, v8\n"
    "s_setprio 0\n"
    "s_barrier\n"
    "buffer_load_dword v208, v8, s[64:67], 0 offen\n"
    "buffer_load_ushort v209, v9, s[64:67], 0 offen offset:1024\n"
    "buffer_load_dwordx4 v[172:175], v6, s[48:51], s90 offen\n"
    "s_add_u32 s77, s90, 6912\n"
    "buffer_load_dwordx4 v[176:179], v6, s[48:51], s77 offen\n"
    "s_add_u32 s77, s90, 13824\n"
    "buffer_load_dwordx4 v[180:183], v6, s[48:51], s77 offen\n"
    "s_add_u32 s77, s90, 20736\n"
    "buffer_load_dwordx4 v[184:187], v6, s[48:51], s77 offen\n"
    "s_add_u32 s77, s90, 27648\n"
    "buffer_load_dwordx4 v[188:191], v6, s[48:51], s77 offen\n"
    "s_add_u32 s77, s90, 34560\n"
    "buffer_load_dwordx4 v[192:195], v6, s[48:51], s77 offen\n"
    "s_add_u32 s77, s90, 41472\n"
    "buffer_load_dwordx4 v[196:199], v6, s[48:51], s77 offen\n"
    "s_add_u32 s77, s90, 48384\n"
    "buffer_load_dwordx4 v[200:203], v6, s[48:51], s77 offen\n"
    "s_add_u32 s77, s90, 3538944\n"
    "buffer_load_dwordx4 v[204:207], v7, s[48:51], s77 offen\n"
    "v_mov_b64 v[70:71], 0\n"
    "v_mov_b64 v[72:73], 0\n"
    "v_mov_b64 v[74:75], 0\n"
    "v_mov_b64 v[76:77], 0\n"
    "v_and_b32 v58, 15, v0\n"
    "v_lshlrev_b32 v58, 3, v58\n"
    "v_add_u32 v59, 159360, v58\n"
    "v_bfe_u32 v60, v0, 4, 2\n"
    "v_lshl_add_u32 v58, v60, 10, v58\n"
    "v_and_b32 v62, 3, v0\n"
    "v_lshl_add_u32 v62, v62, 2, v60\n"
    "s_and_b32 s77, s75, 3\n"
    "s_lshl2_add_u32 s78, s77, 159264\n"
    "v_mov_b32 v21, s78\n"
    "s_lshl_b32 s77, s77, 7\n"
    "s_add_u32 s77, s77, 159360\n"
    "v_lshl_add_u32 v62, v62, 3, s77\n"
    "v_mov_b32 v22, 159264\n"
    "v_mov_b32 v23, 1\n"
    "s_sub_u32 s78, s89, 36\n"
    "s_cmp_lt_u32 s78, 18\n"
    "s_cselect_b32 s93, 1.0, 0\n"
    "s_lshl_b32 s91, s89, 7\n"
    "s_add_u32 s91, s91, 9216\n"
    "s_waitcnt vmcnt(10)\n"
    "v_lshlrev_b32 v210, 16, v208\n"
    "v_and_b32 v212, 0xffff0000, v208\n"
    "s_waitcnt vmcnt(9)\n"
    "v_lshlrev_b32 v214, 16, v209\n"
    "v_mul_f32 v211, 0xbfb8aa3b, v210\n"
    "v_mul_f32 v213, 0xbfb8aa3b, v212\n"
    "v_mul_f32 v215, 0xbfb8aa3b, v214\n"
    "v_exp_f32 v211, v211\n"
    "v_exp_f32 v213, v213\n"
    "v_exp_f32 v215, v215\n"
    "v_add_f32 v211, 1.0, v211\n"
    "v_add_f32 v213, 1.0, v213\n"
    "v_add_f32 v215, 1.0, v215\n"
    "v_rcp_f32 v211, v211\n"
    "v_rcp_f32 v213, v213\n"
    "v_rcp_f32 v215, v215\n"
    "v_mul_f32 v210, v210, v211\n"
    "v_mul_f32 v212, v212, v213\n"
    "v_mul_f32 v214, v214, v215\n"
    "v_cvt_pk_bf16_f32 v210, v210, 0\n"
    "v_cvt_pk_bf16_f32 v212, v212, 0\n"
    "v_cvt_pk_bf16_f32 v214, v214, 0\n"
    "v_lshlrev_b32 v211, 16, v210\n"
    "v_lshlrev_b32 v213, 16, v212\n"
    "v_lshlrev_b32 v215, 16, v214\n"
    "v_mov_b32 v61, s74\n"
    "s_waitcnt vmcnt(8)\n"
    "v_dot2c_f32_bf16_dpp v70, v210, v172 quad_perm:[0,0,0,0] row_mask:0xf bank_mask:0xf\n"
    "v_dot2c_f32_bf16_dpp v71, v211, v172 quad_perm:[0,0,0,0] row_mask:0xf bank_mask:0xf\n"
    "v_dot2c_f32_bf16_dpp v72, v210, v173 quad_perm:[0,0,0,0] row_mask:0xf bank_mask:0xf\n"
    "v_dot2c_f32_bf16_dpp v73, v211, v173 quad_perm:[0,0,0,0] row_mask:0xf bank_mask:0xf\n"
    "v_dot2c_f32_bf16_dpp v74, v210, v174 quad_perm:[0,0,0,0] row_mask:0xf bank_mask:0xf\n"
    "v_dot2c_f32_bf16_dpp v75, v211, v174 quad_perm:[0,0,0,0] row_mask:0xf bank_mask:0xf\n"
    "v_dot2c_f32_bf16_dpp v76, v210, v175 quad_perm:[0,0,0,0] row_mask:0xf bank_mask:0xf\n"
    "v_dot2c_f32_bf16_dpp v77, v211, v175 quad_perm:[0,0,0,0] row_mask:0xf bank_mask:0xf\n"
    "s_waitcnt vmcnt(7)\n"
    "v_dot2c_f32_bf16_dpp v70, v212, v176 quad_perm:[0,0,0,0] row_mask:0xf bank_mask:0xf\n"
    "v_dot2c_f32_bf16_dpp v71, v213, v176 quad_perm:[0,0,0,0] row_mask:0xf bank_mask:0xf\n"
    "v_dot2c_f32_bf16_dpp v72, v212, v177 quad_perm:[0,0,0,0] row_mask:0xf bank_mask:0xf\n"
    "v_dot2c_f32_bf16_dpp v73, v213, v177 quad_perm:[0,0,0,0] row_mask:0xf bank_mask:0xf\n"
    "v_dot2c_f32_bf16_dpp v74, v212, v178 quad_perm:[0,0,0,0] row_mask:0xf bank_mask:0xf\n"
    "v_dot2c_f32_bf16_dpp v75, v213, v178 quad_perm:[0,0,0,0] row_mask:0xf bank_mask:0xf\n"
    "v_dot2c_f32_bf16_dpp v76, v212, v179 quad_perm:[0,0,0,0] row_mask:0xf bank_mask:0xf\n"
    "v_dot2c_f32_bf16_dpp v77, v213, v179 quad_perm:[0,0,0,0] row_mask:0xf bank_mask:0xf\n"
    "s_waitcnt vmcnt(6)\n"
    "v_dot2c_f32_bf16_dpp v70, v210, v180 quad_perm:[1,1,1,1] row_mask:0xf bank_mask:0xf\n"
    "v_dot2c_f32_bf16_dpp v71, v211, v180 quad_perm:[1,1,1,1] row_mask:0xf bank_mask:0xf\n"
    "v_dot2c_f32_bf16_dpp v72, v210, v181 quad_perm:[1,1,1,1] row_mask:0xf bank_mask:0xf\n"
    "v_dot2c_f32_bf16_dpp v73, v211, v181 quad_perm:[1,1,1,1] row_mask:0xf bank_mask:0xf\n"
    "v_dot2c_f32_bf16_dpp v74, v210, v182 quad_perm:[1,1,1,1] row_mask:0xf bank_mask:0xf\n"
    "v_dot2c_f32_bf16_dpp v75, v211, v182 quad_perm:[1,1,1,1] row_mask:0xf bank_mask:0xf\n"
    "v_dot2c_f32_bf16_dpp v76, v210, v183 quad_perm:[1,1,1,1] row_mask:0xf bank_mask:0xf\n"
    "v_dot2c_f32_bf16_dpp v77, v211, v183 quad_perm:[1,1,1,1] row_mask:0xf bank_mask:0xf\n"
    "s_waitcnt vmcnt(5)\n"
    "v_dot2c_f32_bf16_dpp v70, v212, v184 quad_perm:[1,1,1,1] row_mask:0xf bank_mask:0xf\n"
    "v_dot2c_f32_bf16_dpp v71, v213, v184 quad_perm:[1,1,1,1] row_mask:0xf bank_mask:0xf\n"
    "v_dot2c_f32_bf16_dpp v72, v212, v185 quad_perm:[1,1,1,1] row_mask:0xf bank_mask:0xf\n"
    "v_dot2c_f32_bf16_dpp v73, v213, v185 quad_perm:[1,1,1,1] row_mask:0xf bank_mask:0xf\n"
    "v_dot2c_f32_bf16_dpp v74, v212, v186 quad_perm:[1,1,1,1] row_mask:0xf bank_mask:0xf\n"
    "v_dot2c_f32_bf16_dpp v75, v213, v186 quad_perm:[1,1,1,1] row_mask:0xf bank_mask:0xf\n"
    "v_dot2c_f32_bf16_dpp v76, v212, v187 quad_perm:[1,1,1,1] row_mask:0xf bank_mask:0xf\n"
    "v_dot2c_f32_bf16_dpp v77, v213, v187 quad_perm:[1,1,1,1] row_mask:0xf bank_mask:0xf\n"
    "s_waitcnt vmcnt(4)\n"
    "v_dot2c_f32_bf16_dpp v70, v210, v188 quad_perm:[2,2,2,2] row_mask:0xf bank_mask:0xf\n"
    "v_dot2c_f32_bf16_dpp v71, v211, v188 quad_perm:[2,2,2,2] row_mask:0xf bank_mask:0xf\n"
    "v_dot2c_f32_bf16_dpp v72, v210, v189 quad_perm:[2,2,2,2] row_mask:0xf bank_mask:0xf\n"
    "v_dot2c_f32_bf16_dpp v73, v211, v189 quad_perm:[2,2,2,2] row_mask:0xf bank_mask:0xf\n"
    "v_dot2c_f32_bf16_dpp v74, v210, v190 quad_perm:[2,2,2,2] row_mask:0xf bank_mask:0xf\n"
    "v_dot2c_f32_bf16_dpp v75, v211, v190 quad_perm:[2,2,2,2] row_mask:0xf bank_mask:0xf\n"
    "v_dot2c_f32_bf16_dpp v76, v210, v191 quad_perm:[2,2,2,2] row_mask:0xf bank_mask:0xf\n"
    "v_dot2c_f32_bf16_dpp v77, v211, v191 quad_perm:[2,2,2,2] row_mask:0xf bank_mask:0xf\n"
    "s_waitcnt vmcnt(3)\n"
    "v_dot2c_f32_bf16_dpp v70, v212, v192 quad_perm:[2,2,2,2] row_mask:0xf bank_mask:0xf\n"
    "v_dot2c_f32_bf16_dpp v71, v213, v192 quad_perm:[2,2,2,2] row_mask:0xf bank_mask:0xf\n"
    "v_dot2c_f32_bf16_dpp v72, v212, v193 quad_perm:[2,2,2,2] row_mask:0xf bank_mask:0xf\n"
    "v_dot2c_f32_bf16_dpp v73, v213, v193 quad_perm:[2,2,2,2] row_mask:0xf bank_mask:0xf\n"
    "v_dot2c_f32_bf16_dpp v74, v212, v194 quad_perm:[2,2,2,2] row_mask:0xf bank_mask:0xf\n"
    "v_dot2c_f32_bf16_dpp v75, v213, v194 quad_perm:[2,2,2,2] row_mask:0xf bank_mask:0xf\n"
    "v_dot2c_f32_bf16_dpp v76, v212, v195 quad_perm:[2,2,2,2] row_mask:0xf bank_mask:0xf\n"
    "v_dot2c_f32_bf16_dpp v77, v213, v195 quad_perm:[2,2,2,2] row_mask:0xf bank_mask:0xf\n"
    "s_waitcnt vmcnt(2)\n"
    "v_dot2c_f32_bf16_dpp v70, v210, v196 quad_perm:[3,3,3,3] row_mask:0xf bank_mask:0xf\n"
    "v_dot2c_f32_bf16_dpp v71, v211, v196 quad_perm:[3,3,3,3] row_mask:0xf bank_mask:0xf\n"
    "v_dot2c_f32_bf16_dpp v72, v210, v197 quad_perm:[3,3,3,3] row_mask:0xf bank_mask:0xf\n"
    "v_dot2c_f32_bf16_dpp v73, v211, v197 quad_perm:[3,3,3,3] row_mask:0xf bank_mask:0xf\n"
    "v_dot2c_f32_bf16_dpp v74, v210, v198 quad_perm:[3,3,3,3] row_mask:0xf bank_mask:0xf\n"
    "v_dot2c_f32_bf16_dpp v75, v211, v198 quad_perm:[3,3,3,3] row_mask:0xf bank_mask:0xf\n"
    "v_dot2c_f32_bf16_dpp v76, v210, v199 quad_perm:[3,3,3,3] row_mask:0xf bank_mask:0xf\n"
    "v_dot2c_f32_bf16_dpp v77, v211, v199 quad_perm:[3,3,3,3] row_mask:0xf bank_mask:0xf\n"
    "s_waitcnt vmcnt(1)\n"
    "v_dot2c_f32_bf16_dpp v70, v212, v200 quad_perm:[3,3,3,3] row_mask:0xf bank_mask:0xf\n"
    "v_dot2c_f32_bf16_dpp v71, v213, v200 quad_perm:[3,3,3,3] row_mask:0xf bank_mask:0xf\n"
    "v_dot2c_f32_bf16_dpp v72, v212, v201 quad_perm:[3,3,3,3] row_mask:0xf bank_mask:0xf\n"
    "v_dot2c_f32_bf16_dpp v73, v213, v201 quad_perm:[3,3,3,3] row_mask:0xf bank_mask:0xf\n"
    "v_dot2c_f32_bf16_dpp v74, v212, v202 quad_perm:[3,3,3,3] row_mask:0xf bank_mask:0xf\n"
    "v_dot2c_f32_bf16_dpp v75, v213, v202 quad_perm:[3,3,3,3] row_mask:0xf bank_mask:0xf\n"
    "v_dot2c_f32_bf16_dpp v76, v212, v203 quad_perm:[3,3,3,3] row_mask:0xf bank_mask:0xf\n"
    "v_dot2c_f32_bf16_dpp v77, v213, v203 quad_perm:[3,3,3,3] row_mask:0xf bank_mask:0xf\n"
    "s_waitcnt vmcnt(0)\n"
    "v_dot2c_f32_bf16 v70, v214, v204\n"
    "v_dot2c_f32_bf16 v71, v215, v204\n"
    "v_dot2c_f32_bf16 v72, v214, v205\n"
    "v_dot2c_f32_bf16 v73, v215, v205\n"
    "v_dot2c_f32_bf16 v74, v214, v206\n"
    "v_dot2c_f32_bf16 v75, v215, v206\n"
    "v_dot2c_f32_bf16 v76, v214, v207\n"
    "v_dot2c_f32_bf16 v77, v215, v207\n"
    "v_permlane32_swap_b32 v70, v74\n"
    "v_permlane32_swap_b32 v71, v75\n"
    "v_permlane32_swap_b32 v72, v76\n"
    "v_permlane32_swap_b32 v73, v77\n"
    "v_add_f32 v70, v70, v74\n"
    "v_add_f32 v71, v71, v75\n"
    "v_add_f32 v72, v72, v76\n"
    "v_add_f32 v73, v73, v77\n"
    "s_nop 0\n"
    "v_permlane16_swap_b32 v70, v72\n"
    "v_permlane16_swap_b32 v71, v73\n"
    "v_add_f32 v70, v70, v72\n"
    "v_add_f32 v71, v71, v73\n"
    "s_nop 0\n"
    "v_add_f32_dpp v70, v70, v70 row_ror:8 row_mask:0xf bank_mask:0xf\n"
    "v_add_f32_dpp v71, v71, v71 row_ror:8 row_mask:0xf bank_mask:0xf\n"
    "s_nop 0\n"
    "v_add_f32_dpp v70, v70, v70 row_ror:4 row_mask:0xf bank_mask:0xf\n"
    "v_add_f32_dpp v71, v71, v71 row_ror:4 row_mask:0xf bank_mask:0xf\n"
    "ds_write_b64 v62, v[70:71]\n"
    "ds_write_b32 v21, v23\n"
    "ds_read_b128 v[74:77], v22\n"
    "ds_read2_b64 v[172:175], v59 offset0:0 offset1:16\n"
    "ds_read2_b64 v[176:179], v59 offset0:32 offset1:48\n"
    "s_waitcnt lgkmcnt(2)\n"
    "v_bitop3_b32 v74, v74, v75, v76 bitop3:0x80\n"
    "v_and_b32 v74, v74, v77\n"
    "v_cmp_eq_u32 vcc, 0, v74\n"
    "s_cbranch_vccnz GL2%=\n"
    "s_waitcnt lgkmcnt(1)\n"
    "v_pk_add_f32 v[172:173], v[172:173], v[174:175]\n"
    "s_waitcnt lgkmcnt(0)\n"
    "v_pk_add_f32 v[176:177], v[176:177], v[178:179]\n"
    "v_pk_add_f32 v[172:173], v[172:173], v[176:177]\n"
    "v_cvt_pk_bf16_f32 v172, v172, v173\n"
    "v_and_b32 v173, 0xffff0000, v172\n"
    "v_lshlrev_b32 v172, 16, v172\n"
    "v_add_f32 v173, s93, v173\n"
    "v_add_f32 v172, s93, v172\n"
    "v_cvt_pk_bf16_f32 v60, v172, v173\n"
    "s_mov_b64 exec, 0xffff\n"
    "buffer_store_dwordx2 v[60:61], v58, s[36:39], s91 offen sc1\n"
    "s_mov_b64 exec, -1\n"
    "GL2%=:\n"
    "s_waitcnt lgkmcnt(0)\n"
    "s_setprio 0\n"
    "s_branch PO%=\n"
    "RV%=:\n"
    "LPRV:\n"
    "s_cbranch_execz LTR\n"
    "s_waitcnt lgkmcnt(0)\n"
    "s_barrier\n"
    "s_memtime s[14:15]\n"
    "ds_read_b128 v[100:103], v20 offset:0\n"
    "ds_read_b64_tr_b16 v[184:185], v19 offset:0\n"
    "ds_read_b64_tr_b16 v[186:187], v19 offset:512\n"
    "ds_read_b128 v[104:107], v20 offset:4160\n"
    "ds_read_b64_tr_b16 v[188:189], v19 offset:1024\n"
    "ds_read_b64_tr_b16 v[190:191], v19 offset:1536\n"
    "ds_read_b128 v[108:111], v20 offset:8320\n"
    "ds_read_b64_tr_b16 v[192:193], v19 offset:2048\n"
    "ds_read_b64_tr_b16 v[194:195], v19 offset:2560\n"
    "ds_read_b128 v[112:115], v20 offset:12480\n"
    "ds_read_b64_tr_b16 v[196:197], v19 offset:3072\n"
    "ds_read_b64_tr_b16 v[198:199], v19 offset:3584\n"
    "ds_read_b128 v[116:119], v20 offset:16640\n"
    "ds_read_b64_tr_b16 v[200:201], v19 offset:4096\n"
    "ds_read_b64_tr_b16 v[202:203], v19 offset:4608\n"
    "s_waitcnt lgkmcnt(12)\n"
    "v_mfma_f32_16x16x32_bf16 v[32:35], v[100:103], v[184:187], 0\n"
    "ds_read_b128 v[120:123], v20 offset:20800\n"
    "ds_read_b64_tr_b16 v[204:205], v19 offset:5120\n"
    "ds_read_b64_tr_b16 v[206:207], v19 offset:5632\n"
    "v_mov_b32 v48, 0x3f803f80\n"
    "s_waitcnt lgkmcnt(12)\n"
    "v_mfma_f32_16x16x32_bf16 v[32:35], v[104:107], v[188:191], v[32:35]\n"
    "ds_read_b128 v[124:127], v20 offset:24960\n"
    "ds_read_b64_tr_b16 v[208:209], v19 offset:6144\n"
    "ds_read_b64_tr_b16 v[210:211], v19 offset:6656\n"
    "v_mov_b32 v49, 0x3f803f80\n"
    "s_waitcnt lgkmcnt(12)\n"
    "v_mfma_f32_16x16x32_bf16 v[32:35], v[108:111], v[192:195], v[32:35]\n"
    "ds_read_b128 v[128:131], v20 offset:29120\n"
    "ds_read_b64_tr_b16 v[212:213], v19 offset:7168\n"
    "ds_read_b64_tr_b16 v[214:215], v19 offset:7680\n"
    "s_waitcnt lgkmcnt(12)\n"
    "v_mfma_f32_16x16x32_bf16 v[32:35], v[112:115], v[196:199], v[32:35]\n"
    "ds_read_b128 v[132:135], v20 offset:33280\n"
    "ds_read_b64_tr_b16 v[216:217], v19 offset:8192\n"
    "ds_read_b64_tr_b16 v[218:219], v19 offset:8704\n"
    "s_waitcnt lgkmcnt(12)\n"
    "v_mfma_f32_16x16x32_bf16 v[32:35], v[116:119], v[200:203], v[32:35]\n"
    "ds_read_b128 v[136:139], v20 offset:37440\n"
    "ds_read_b64_tr_b16 v[220:221], v19 offset:9216\n"
    "ds_read_b64_tr_b16 v[222:223], v19 offset:9728\n"
    "s_waitcnt lgkmcnt(12)\n"
    "v_mfma_f32_16x16x32_bf16 v[32:35], v[120:123], v[204:207], v[32:35]\n"
    "ds_read_b128 v[140:143], v20 offset:41600\n"
    "ds_read_b64_tr_b16 v[224:225], v19 offset:10240\n"
    "ds_read_b64_tr_b16 v[226:227], v19 offset:10752\n"
    "s_waitcnt lgkmcnt(12)\n"
    "v_mfma_f32_16x16x32_bf16 v[32:35], v[124:127], v[208:211], v[32:35]\n"
    "ds_read_b128 v[144:147], v20 offset:45760\n"
    "ds_read_b64_tr_b16 v[228:229], v19 offset:11264\n"
    "ds_read_b64_tr_b16 v[230:231], v19 offset:11776\n"
    "s_waitcnt lgkmcnt(12)\n"
    "v_mfma_f32_16x16x32_bf16 v[32:35], v[128:131], v[212:215], v[32:35]\n"
    "ds_read_b128 v[148:151], v20 offset:49920\n"
    "ds_read_b64_tr_b16 v[232:233], v19 offset:12288\n"
    "ds_read_b64_tr_b16 v[234:235], v19 offset:12800\n"
    "s_waitcnt lgkmcnt(12)\n"
    "v_mfma_f32_16x16x32_bf16 v[32:35], v[132:135], v[216:219], v[32:35]\n"
    "ds_read_b128 v[152:155], v20 offset:54080\n"
    "ds_read_b64_tr_b16 v[236:237], v19 offset:13312\n"
    "ds_read_b64_tr_b16 v[238:239], v19 offset:13824\n"
    "s_waitcnt lgkmcnt(12)\n"
    "v_mfma_f32_16x16x32_bf16 v[32:35], v[136:139], v[220:223], v[32:35]\n"
    "ds_read_b128 v[156:159], v20 offset:58240\n"
    "ds_read_b64_tr_b16 v[240:241], v19 offset:14336\n"
    "ds_read_b64_tr_b16 v[242:243], v19 offset:14848\n"
    "s_waitcnt lgkmcnt(12)\n"
    "v_mfma_f32_16x16x32_bf16 v[32:35], v[140:143], v[224:227], v[32:35]\n"
    "ds_read_b128 v[160:163], v20 offset:62400\n"
    "ds_read_b64_tr_b16 v[244:245], v19 offset:15360\n"
    "ds_read_b64_tr_b16 v[246:247], v19 offset:15872\n"
    "s_waitcnt lgkmcnt(12)\n"
    "v_mfma_f32_16x16x32_bf16 v[32:35], v[144:147], v[228:231], v[32:35]\n"
    "ds_read_b128 v[164:167], v90 offset:0\n"
    "ds_read_b64_tr_b16 v[248:249], v19 offset:16384\n"
    "ds_read_b64_tr_b16 v[250:251], v19 offset:16896\n"
    "s_waitcnt lgkmcnt(12)\n"
    "v_mfma_f32_16x16x32_bf16 v[32:35], v[148:151], v[232:235], v[32:35]\n"
    "ds_read_b128 v[168:171], v90 offset:4160\n"
    "ds_read_b64_tr_b16 v[252:253], v19 offset:17408\n"
    "ds_read_b64_tr_b16 v[254:255], v19 offset:17920\n"
    "s_waitcnt lgkmcnt(12)\n"
    "v_mfma_f32_16x16x32_bf16 v[32:35], v[152:155], v[236:239], v[32:35]\n"
    "s_waitcnt lgkmcnt(9)\n"
    "v_mfma_f32_16x16x32_bf16 v[32:35], v[156:159], v[240:243], v[32:35]\n"
    "s_waitcnt lgkmcnt(6)\n"
    "v_mfma_f32_16x16x32_bf16 v[32:35], v[160:163], v[244:247], v[32:35]\n"
    "s_waitcnt lgkmcnt(3)\n"
    "v_mfma_f32_16x16x32_bf16 v[32:35], v[164:167], v[248:251], v[32:35]\n"
    "s_waitcnt lgkmcnt(0)\n"
    "v_mfma_f32_16x16x32_bf16 v[32:35], v[168:171], v[252:255], v[32:35]\n"
    "s_mov_b32 exec_lo, s90\n"
    "s_mov_b32 exec_hi, s90\n"
    "s_nop 5\n"
    "v_cvt_pk_bf16_f32 v48, v32, v33\n"
    "v_cvt_pk_bf16_f32 v49, v34, v35\n"
    "s_mov_b64 exec, s[30:31]\n"
    "buffer_store_dwordx2 v[48:49], v11, s[36:39], s94 offen sc1\n"
    "s_mov_b64 exec, -1\n"
    "v_mov_b32 v59, s74\n"
    "v_mov_b32 v21, 159232\n"
    "v_mov_b32 v22, 1\n"
    "s_mov_b64 exec, 1\n"
    "s_waitcnt vmcnt(0)\n"
    "ds_add_rtn_u32 v22, v21, v22 offset:8\n"
    "s_mov_b64 exec, -1\n"
    "ds_read_b32 v58, v21\n"
    "s_waitcnt lgkmcnt(1)\n"
    "v_readfirstlane_b32 s77, v22\n"
    "s_cmp_lg_u32 s77, 3\n"
    "s_cbranch_scc1 KLV%=\n"
    "s_mov_b64 exec, 1\n"
    "s_waitcnt lgkmcnt(0)\n"
    "buffer_store_dwordx2 v[58:59], off, s[36:39], s28 sc1\n"
    "s_mov_b64 exec, -1\n"
    "KLV%=:\n"
    "s_setprio 0\n"
    "s_and_b32 s77, s75, 3\n"
    "s_lshl2_add_u32 s77, s77, LTB-LK3B+16\n"
    "s_add_u32 s22, s80, s77\n"
    "s_addc_u32 s23, s81, 0\n"
    "s_mov_b64 exec, 0\n"
    "s_swappc_b64 vcc, s[22:23]\n"
    "s_mov_b64 exec, -1\n"
    "s_waitcnt lgkmcnt(0)\n"
    "s_barrier\n"
    "s_branch PO%=\n"
    "RQ%=:\n"
    "s_setprio 0\n"
    "s_waitcnt lgkmcnt(0)\n"
    "s_barrier\n"
    "s_memtime s[14:15]\n"
    "ds_read_b128 v[100:103], v20 offset:0\n"
    "ds_read_b64_tr_b16 v[184:185], v19 offset:0\n"
    "ds_read_b64_tr_b16 v[186:187], v19 offset:512\n"
    "ds_read_b128 v[104:107], v20 offset:4160\n"
    "ds_read_b64_tr_b16 v[188:189], v19 offset:1024\n"
    "ds_read_b64_tr_b16 v[190:191], v19 offset:1536\n"
    "ds_read_b128 v[108:111], v20 offset:8320\n"
    "ds_read_b64_tr_b16 v[192:193], v19 offset:2048\n"
    "ds_read_b64_tr_b16 v[194:195], v19 offset:2560\n"
    "ds_read_b128 v[112:115], v20 offset:12480\n"
    "ds_read_b64_tr_b16 v[196:197], v19 offset:3072\n"
    "ds_read_b64_tr_b16 v[198:199], v19 offset:3584\n"
    "ds_read_b128 v[116:119], v20 offset:16640\n"
    "ds_read_b64_tr_b16 v[200:201], v19 offset:4096\n"
    "ds_read_b64_tr_b16 v[202:203], v19 offset:4608\n"
    "s_waitcnt lgkmcnt(12)\n"
    "v_mfma_f32_16x16x32_bf16 v[32:35], v[184:187], v[100:103], 0\n"
    "ds_read_b128 v[120:123], v20 offset:20800\n"
    "ds_read_b64_tr_b16 v[204:205], v19 offset:5120\n"
    "ds_read_b64_tr_b16 v[206:207], v19 offset:5632\n"
    "v_mov_b32 v49, s74\n"
    "s_waitcnt lgkmcnt(12)\n"
    "v_mfma_f32_16x16x32_bf16 v[32:35], v[188:191], v[104:107], v[32:35]\n"
    "ds_read_b128 v[124:127], v20 offset:24960\n"
    "ds_read_b64_tr_b16 v[208:209], v19 offset:6144\n"
    "ds_read_b64_tr_b16 v[210:211], v19 offset:6656\n"
    "v_mov_b32 v51, s74\n"
    "s_waitcnt lgkmcnt(12)\n"
    "v_mfma_f32_16x16x32_bf16 v[32:35], v[192:195], v[108:111], v[32:35]\n"
    "ds_read_b128 v[128:131], v20 offset:29120\n"
    "ds_read_b64_tr_b16 v[212:213], v19 offset:7168\n"
    "ds_read_b64_tr_b16 v[214:215], v19 offset:7680\n"
    "s_waitcnt lgkmcnt(12)\n"
    "v_mfma_f32_16x16x32_bf16 v[32:35], v[196:199], v[112:115], v[32:35]\n"
    "ds_read_b128 v[132:135], v20 offset:33280\n"
    "ds_read_b64_tr_b16 v[216:217], v19 offset:8192\n"
    "ds_read_b64_tr_b16 v[218:219], v19 offset:8704\n"
    "s_waitcnt lgkmcnt(12)\n"
    "v_mfma_f32_16x16x32_bf16 v[32:35], v[200:203], v[116:119], v[32:35]\n"
    "ds_read_b128 v[136:139], v20 offset:37440\n"
    "ds_read_b64_tr_b16 v[220:221], v19 offset:9216\n"
    "ds_read_b64_tr_b16 v[222:223], v19 offset:9728\n"
    "s_waitcnt lgkmcnt(12)\n"
    "v_mfma_f32_16x16x32_bf16 v[32:35], v[204:207], v[120:123], v[32:35]\n"
    "ds_read_b128 v[140:143], v20 offset:41600\n"
    "ds_read_b64_tr_b16 v[224:225], v19 offset:10240\n"
    "ds_read_b64_tr_b16 v[226:227], v19 offset:10752\n"
    "s_waitcnt lgkmcnt(12)\n"
    "v_mfma_f32_16x16x32_bf16 v[32:35], v[208:211], v[124:127], v[32:35]\n"
    "ds_read_b128 v[144:147], v20 offset:45760\n"
    "ds_read_b64_tr_b16 v[228:229], v19 offset:11264\n"
    "ds_read_b64_tr_b16 v[230:231], v19 offset:11776\n"
    "s_waitcnt lgkmcnt(12)\n"
    "v_mfma_f32_16x16x32_bf16 v[32:35], v[212:215], v[128:131], v[32:35]\n"
    "ds_read_b128 v[148:151], v20 offset:49920\n"
    "ds_read_b64_tr_b16 v[232:233], v19 offset:12288\n"
    "ds_read_b64_tr_b16 v[234:235], v19 offset:12800\n"
    "s_waitcnt lgkmcnt(12)\n"
    "v_mfma_f32_16x16x32_bf16 v[32:35], v[216:219], v[132:135], v[32:35]\n"
    "ds_read_b128 v[152:155], v20 offset:54080\n"
    "ds_read_b64_tr_b16 v[236:237], v19 offset:13312\n"
    "ds_read_b64_tr_b16 v[238:239], v19 offset:13824\n"
    "s_waitcnt lgkmcnt(12)\n"
    "v_mfma_f32_16x16x32_bf16 v[32:35], v[220:223], v[136:139], v[32:35]\n"
    "ds_read_b128 v[156:159], v20 offset:58240\n"
    "ds_read_b64_tr_b16 v[240:241], v19 offset:14336\n"
    "ds_read_b64_tr_b16 v[242:243], v19 offset:14848\n"
    "s_waitcnt lgkmcnt(12)\n"
    "v_mfma_f32_16x16x32_bf16 v[32:35], v[224:227], v[140:143], v[32:35]\n"
    "ds_read_b128 v[160:163], v20 offset:62400\n"
    "ds_read_b64_tr_b16 v[244:245], v19 offset:15360\n"
    "ds_read_b64_tr_b16 v[246:247], v19 offset:15872\n"
    "s_waitcnt lgkmcnt(12)\n"
    "v_mfma_f32_16x16x32_bf16 v[32:35], v[228:231], v[144:147], v[32:35]\n"
    "ds_read_b128 v[164:167], v90 offset:0\n"
    "ds_read_b64_tr_b16 v[248:249], v19 offset:16384\n"
    "ds_read_b64_tr_b16 v[250:251], v19 offset:16896\n"
    "s_waitcnt lgkmcnt(12)\n"
    "v_mfma_f32_16x16x32_bf16 v[32:35], v[232:235], v[148:151], v[32:35]\n"
    "ds_read_b128 v[168:171], v90 offset:4160\n"
    "ds_read_b64_tr_b16 v[252:253], v19 offset:17408\n"
    "ds_read_b64_tr_b16 v[254:255], v19 offset:17920\n"
    "s_waitcnt lgkmcnt(12)\n"
    "v_mfma_f32_16x16x32_bf16 v[32:35], v[236:239], v[152:155], v[32:35]\n"
    "s_waitcnt lgkmcnt(9)\n"
    "v_mfma_f32_16x16x32_bf16 v[32:35], v[240:243], v[156:159], v[32:35]\n"
    "s_waitcnt lgkmcnt(6)\n"
    "v_mfma_f32_16x16x32_bf16 v[32:35], v[244:247], v[160:163], v[32:35]\n"
    "s_waitcnt lgkmcnt(3)\n"
    "v_mfma_f32_16x16x32_bf16 v[32:35], v[248:251], v[164:167], v[32:35]\n"
    "s_waitcnt lgkmcnt(0)\n"
    "v_mfma_f32_16x16x32_bf16 v[32:35], v[252:255], v[168:171], v[32:35]\n"
    "s_nop 7\n"
    "v_cvt_pk_bf16_f32 v48, v32, v33\n"
    "v_cvt_pk_bf16_f32 v50, v34, v35\n"
    "buffer_store_dwordx4 v[48:51], v11, s[36:39], s94 offen sc1\n"
    "s_waitcnt vmcnt(1)\n"
    "s_cmp_lt_u32 s89, 72\n"
    "s_cbranch_scc1 GC2%=\n"
    "v_add_u32 v58, s95, v88\n"
    "v_and_b32 v61, 3, v0\n"
    "v_and_b32 v60, 12, v0\n"
    "v_lshl_add_u32 v61, v79, 2, v61\n"
    "v_mul_u32_u24 v59, 8960, v79\n"
    "v_lshl_add_u32 v61, v60, 2, v61\n"
    "v_add_u32 v59, v59, v88\n"
    "v_lshl_add_u32 v61, v61, 3, s95\n"
    "s_sub_u32 s77, s89, 72\n"
    "s_mul_i32 s91, s77, 171\n"
    "s_lshr_b32 s91, s91, 9\n"
    "s_mul_i32 s92, s91, 3\n"
    "s_sub_u32 s92, s77, s92\n"
    "s_and_b32 s79, s75, 3\n"
    "s_lshr_b32 s77, s91, 1\n"
    "s_mul_i32 s93, s77, 73728\n"
    "s_lshl_b32 s77, s79, 8\n"
    "s_add_u32 s93, s93, s77\n"
    "s_add_u32 s93, s93, 81920\n"
    "s_and_b32 s91, s91, 1\n"
    "s_mul_i32 s92, s92, 3\n"
    "s_and_b32 s77, s92, 1\n"
    "s_mul_i32 s77, s77, 36\n"
    "s_lshr_b32 s78, s92, 1\n"
    "s_add_u32 s77, s77, s78\n"
    "s_cmp_lt_u32 s92, 8\n"
    "s_cselect_b32 s78, 5, 36\n"
    "s_mul_i32 s78, s78, s91\n"
    "s_add_u32 s77, s77, s78\n"
    "s_lshl_b32 s77, s77, 10\n"
    "s_add_u32 s30, s77, s93\n"
    "s_add_u32 s92, s92, 1\n"
    "s_and_b32 s77, s92, 1\n"
    "s_mul_i32 s77, s77, 36\n"
    "s_lshr_b32 s78, s92, 1\n"
    "s_add_u32 s77, s77, s78\n"
    "s_cmp_lt_u32 s92, 8\n"
    "s_cselect_b32 s78, 5, 36\n"
    "s_mul_i32 s78, s78, s91\n"
    "s_add_u32 s77, s77, s78\n"
    "s_lshl_b32 s77, s77, 10\n"
    "s_add_u32 s31, s77, s93\n"
    "s_add_u32 s92, s92, 1\n"
    "s_and_b32 s77, s92, 1\n"
    "s_mul_i32 s77, s77, 36\n"
    "s_lshr_b32 s78, s92, 1\n"
    "s_add_u32 s77, s77, s78\n"
    "s_cmp_lt_u32 s92, 8\n"
    "s_cselect_b32 s78, 5, 36\n"
    "s_mul_i32 s78, s78, s91\n"
    "s_add_u32 s77, s77, s78\n"
    "s_lshl_b32 s77, s77, 10\n"
    "s_add_u32 s28, s77, s93\n"
    "s_add_u32 s92, s92, 1\n"
    "s_setprio 0\n"
    "s_cmp_gt_u32 s89, 125\n"
    "s_barrier\n"
    "s_cbranch_scc1 PO%=\n"
    "ds_write_b128 v58, v[172:175] offset:0\n"
    "ds_write_b128 v58, v[176:179] offset:1024\n"
    "ds_write_b128 v58, v[180:183] offset:2048\n"
    "ds_read_b64_tr_b16 v[172:173], v61 offset:0\n"
    "ds_read_b64_tr_b16 v[174:175], v61 offset:512\n"
    "ds_read_b64_tr_b16 v[176:177], v61 offset:1024\n"
    "ds_read_b64_tr_b16 v[178:179], v61 offset:1536\n"
    "ds_read_b64_tr_b16 v[180:181], v61 offset:2048\n"
    "ds_read_b64_tr_b16 v[182:183], v61 offset:2560\n"
    "s_waitcnt lgkmcnt(4)\n"
    "buffer_store_dwordx4 v[172:175], v59, s[36:39], s30 offen sc1\n"
    "s_waitcnt lgkmcnt(2)\n"
    "buffer_store_dwordx4 v[176:179], v59, s[36:39], s31 offen sc1\n"
    "s_waitcnt lgkmcnt(0)\n"
    "buffer_store_dwordx4 v[180:183], v59, s[36:39], s28 offen sc1\n"
    "s_branch PO%=\n"
    "RM%=:\n"
    "LPRM:\n"
    "s_cbranch_execz LPGC2\n"
    "buffer_load_dwordx4 v[64:67], v88, s[36:39], s29 offen sc1\n"
    "v_pk_fma_f32 v[164:165], v[164:165], v[60:61], v[60:61] op_sel:[0,1,0] op_sel_hi:[1,1,0]\n"
    "v_pk_fma_f32 v[166:167], v[166:167], v[60:61], v[60:61] op_sel:[0,1,0] op_sel_hi:[1,1,0]\n"
    "v_pk_fma_f32 v[168:169], v[168:169], v[60:61], v[60:61] op_sel:[0,1,0] op_sel_hi:[1,1,0]\n"
    "v_pk_fma_f32 v[170:171], v[170:171], v[60:61], v[60:61] op_sel:[0,1,0] op_sel_hi:[1,1,0]\n"
    "s_waitcnt lgkmcnt(3)\n"
    "v_pk_fma_f32 v[100:101], v[100:101], v[42:43], v[42:43] op_sel:[0,1,0] op_sel_hi:[1,1,0]\n"
    "v_pk_fma_f32 v[102:103], v[102:103], v[42:43], v[42:43] op_sel:[0,1,0] op_sel_hi:[1,1,0]\n"
    "v_pk_fma_f32 v[104:105], v[104:105], v[42:43], v[42:43] op_sel:[0,1,0] op_sel_hi:[1,1,0]\n"
    "v_pk_fma_f32 v[106:107], v[106:107], v[42:43], v[42:43] op_sel:[0,1,0] op_sel_hi:[1,1,0]\n"
    "v_pk_fma_f32 v[108:109], v[108:109], v[44:45], v[44:45] op_sel:[0,1,0] op_sel_hi:[1,1,0]\n"
    "v_pk_fma_f32 v[110:111], v[110:111], v[44:45], v[44:45] op_sel:[0,1,0] op_sel_hi:[1,1,0]\n"
    "v_pk_fma_f32 v[112:113], v[112:113], v[44:45], v[44:45] op_sel:[0,1,0] op_sel_hi:[1,1,0]\n"
    "v_pk_fma_f32 v[114:115], v[114:115], v[44:45], v[44:45] op_sel:[0,1,0] op_sel_hi:[1,1,0]\n"
    "s_waitcnt lgkmcnt(2)\n"
    "v_pk_fma_f32 v[116:117], v[116:117], v[46:47], v[46:47] op_sel:[0,1,0] op_sel_hi:[1,1,0]\n"
    "v_pk_fma_f32 v[118:119], v[118:119], v[46:47], v[46:47] op_sel:[0,1,0] op_sel_hi:[1,1,0]\n"
    "v_pk_fma_f32 v[120:121], v[120:121], v[46:47], v[46:47] op_sel:[0,1,0] op_sel_hi:[1,1,0]\n"
    "v_pk_fma_f32 v[122:123], v[122:123], v[46:47], v[46:47] op_sel:[0,1,0] op_sel_hi:[1,1,0]\n"
    "v_pk_fma_f32 v[124:125], v[124:125], v[48:49], v[48:49] op_sel:[0,1,0] op_sel_hi:[1,1,0]\n"
    "v_pk_fma_f32 v[126:127], v[126:127], v[48:49], v[48:49] op_sel:[0,1,0] op_sel_hi:[1,1,0]\n"
    "v_pk_fma_f32 v[128:129], v[128:129], v[48:49], v[48:49] op_sel:[0,1,0] op_sel_hi:[1,1,0]\n"
    "v_pk_fma_f32 v[130:131], v[130:131], v[48:49], v[48:49] op_sel:[0,1,0] op_sel_hi:[1,1,0]\n"
    "s_waitcnt lgkmcnt(1)\n"
    "v_pk_fma_f32 v[132:133], v[132:133], v[50:51], v[50:51] op_sel:[0,1,0] op_sel_hi:[1,1,0]\n"
    "v_pk_fma_f32 v[134:135], v[134:135], v[50:51], v[50:51] op_sel:[0,1,0] op_sel_hi:[1,1,0]\n"
    "v_pk_fma_f32 v[136:137], v[136:137], v[50:51], v[50:51] op_sel:[0,1,0] op_sel_hi:[1,1,0]\n"
    "v_pk_fma_f32 v[138:139], v[138:139], v[50:51], v[50:51] op_sel:[0,1,0] op_sel_hi:[1,1,0]\n"
    "v_pk_fma_f32 v[140:141], v[140:141], v[52:53], v[52:53] op_sel:[0,1,0] op_sel_hi:[1,1,0]\n"
    "v_pk_fma_f32 v[142:143], v[142:143], v[52:53], v[52:53] op_sel:[0,1,0] op_sel_hi:[1,1,0]\n"
    "v_pk_fma_f32 v[144:145], v[144:145], v[52:53], v[52:53] op_sel:[0,1,0] op_sel_hi:[1,1,0]\n"
    "v_pk_fma_f32 v[146:147], v[146:147], v[52:53], v[52:53] op_sel:[0,1,0] op_sel_hi:[1,1,0]\n"
    "s_waitcnt lgkmcnt(0)\n"
    "v_pk_fma_f32 v[148:149], v[148:149], v[54:55], v[54:55] op_sel:[0,1,0] op_sel_hi:[1,1,0]\n"
    "v_pk_fma_f32 v[150:151], v[150:151], v[54:55], v[54:55] op_sel:[0,1,0] op_sel_hi:[1,1,0]\n"
    "v_pk_fma_f32 v[152:153], v[152:153], v[54:55], v[54:55] op_sel:[0,1,0] op_sel_hi:[1,1,0]\n"
    "v_pk_fma_f32 v[154:155], v[154:155], v[54:55], v[54:55] op_sel:[0,1,0] op_sel_hi:[1,1,0]\n"
    "v_pk_fma_f32 v[156:157], v[156:157], v[56:57], v[56:57] op_sel:[0,1,0] op_sel_hi:[1,1,0]\n"
    "v_pk_fma_f32 v[158:159], v[158:159], v[56:57], v[56:57] op_sel:[0,1,0] op_sel_hi:[1,1,0]\n"
    "v_pk_fma_f32 v[160:161], v[160:161], v[56:57], v[56:57] op_sel:[0,1,0] op_sel_hi:[1,1,0]\n"
    "v_pk_fma_f32 v[162:163], v[162:163], v[56:57], v[56:57] op_sel:[0,1,0] op_sel_hi:[1,1,0]\n"
    "s_movk_i32 s77, 0x4000\n"
    "PAM%=:\n"
    "s_waitcnt vmcnt(0)\n"
    "v_bitop3_b32 v58, v65, v67, s74 bitop3:0x7e\n"
    "v_cmp_ne_u32 vcc, 0, v58\n"
    "s_cbranch_vccz PBM%=\n"
    "s_sub_u32 s77, s77, 1\n"
    "s_cbranch_scc1 PBM%=\n"
    "s_sleep 2\n"
    "s_mov_b64 exec, vcc\n"
    "buffer_load_dwordx4 v[64:67], v88, s[36:39], s29 offen sc1\n"
    "s_mov_b64 exec, -1\n"
    "s_waitcnt vmcnt(0)\n"
    "s_branch PAM%=\n"
    "PBM%=:\n"
    "v_and_b32 v65, 0xffff0000, v64\n"
    "v_and_b32 v67, 0xffff0000, v66\n"
    "v_lshlrev_b32 v64, 16, v64\n"
    "v_lshlrev_b32 v66, 16, v66\n"
    "ds_write_b128 v16, v[64:67]\n"
    "ds_read_b128 v[32:35], v17 offset:0\n"
    "ds_read_b128 v[40:43], v17 offset:256\n"
    "ds_read_b128 v[36:39], v17 offset:16\n"
    "ds_read_b128 v[44:47], v17 offset:272\n"
    "ds_read_b128 v[48:51], v17 offset:512\n"
    "ds_read_b128 v[56:59], v17 offset:768\n"
    "ds_read_b128 v[52:55], v17 offset:528\n"
    "ds_read_b128 v[60:63], v17 offset:784\n"
    "s_lshl3_add_u32 s12, s96, s76\n"
    "s_lshl_b32 s77, s75, 11\n"
    "s_add_u32 s77, s77, 32768\n"
    "s_lshl3_add_u32 s12, s12, s75\n"
    "s_mul_i32 s12, s12, 2304\n"
    "v_lshl_add_u32 v80, v88, 3, s77\n"
    "s_add_u32 s12, s12, 745472\n"
    "buffer_load_dword v92, v92, s[48:51], s28 offen\n"
    "s_waitcnt lgkmcnt(6)\n"
    "v_pk_fma_f32 v[100:101], v[100:101], v[32:33], v[40:41]\n"
    "v_pk_fma_f32 v[102:103], v[102:103], v[34:35], v[42:43]\n"
    "v_cvt_pk_bf16_f32 v100, v100, v101\n"
    "s_waitcnt lgkmcnt(4)\n"
    "v_pk_fma_f32 v[104:105], v[104:105], v[36:37], v[44:45]\n"
    "v_cvt_pk_bf16_f32 v101, v102, v103\n"
    "v_pk_fma_f32 v[106:107], v[106:107], v[38:39], v[46:47]\n"
    "v_cvt_pk_bf16_f32 v102, v104, v105\n"
    "v_cvt_pk_bf16_f32 v103, v106, v107\n"
    "ds_write_b128 v18, v[100:103] offset:0\n"
    "v_pk_fma_f32 v[108:109], v[108:109], v[32:33], v[40:41]\n"
    "v_pk_fma_f32 v[110:111], v[110:111], v[34:35], v[42:43]\n"
    "v_cvt_pk_bf16_f32 v108, v108, v109\n"
    "v_pk_fma_f32 v[112:113], v[112:113], v[36:37], v[44:45]\n"
    "v_cvt_pk_bf16_f32 v109, v110, v111\n"
    "v_pk_fma_f32 v[114:115], v[114:115], v[38:39], v[46:47]\n"
    "v_cvt_pk_bf16_f32 v110, v112, v113\n"
    "v_cvt_pk_bf16_f32 v111, v114, v115\n"
    "ds_write_b128 v18, v[108:111] offset:128\n"
    "v_pk_fma_f32 v[116:117], v[116:117], v[32:33], v[40:41]\n"
    "v_pk_fma_f32 v[118:119], v[118:119], v[34:35], v[42:43]\n"
    "v_cvt_pk_bf16_f32 v116, v116, v117\n"
    "v_pk_fma_f32 v[120:121], v[120:121], v[36:37], v[44:45]\n"
    "v_cvt_pk_bf16_f32 v117, v118, v119\n"
    "v_pk_fma_f32 v[122:123], v[122:123], v[38:39], v[46:47]\n"
    "v_cvt_pk_bf16_f32 v118, v120, v121\n"
    "v_cvt_pk_bf16_f32 v119, v122, v123\n"
    "ds_write_b128 v18, v[116:119] offset:256\n"
    "v_pk_fma_f32 v[124:125], v[124:125], v[32:33], v[40:41]\n"
    "v_pk_fma_f32 v[126:127], v[126:127], v[34:35], v[42:43]\n"
    "v_cvt_pk_bf16_f32 v124, v124, v125\n"
    "v_pk_fma_f32 v[128:129], v[128:129], v[36:37], v[44:45]\n"
    "LPP3:\n"
    "s_cbranch_execz LTR\n"
    "v_cvt_pk_bf16_f32 v125, v126, v127\n"
    "v_pk_fma_f32 v[130:131], v[130:131], v[38:39], v[46:47]\n"
    "v_cvt_pk_bf16_f32 v126, v128, v129\n"
    "v_cvt_pk_bf16_f32 v127, v130, v131\n"
    "ds_write_b128 v18, v[124:127] offset:384\n"
    "v_pk_fma_f32 v[132:133], v[132:133], v[32:33], v[40:41]\n"
    "v_pk_fma_f32 v[134:135], v[134:135], v[34:35], v[42:43]\n"
    "v_cvt_pk_bf16_f32 v132, v132, v133\n"
    "v_pk_fma_f32 v[136:137], v[136:137], v[36:37], v[44:45]\n"
    "v_cvt_pk_bf16_f32 v133, v134, v135\n"
    "v_pk_fma_f32 v[138:139], v[138:139], v[38:39], v[46:47]\n"
    "v_cvt_pk_bf16_f32 v134, v136, v137\n"
    "v_cvt_pk_bf16_f32 v135, v138, v139\n"
    "ds_write_b128 v18, v[132:135] offset:512\n"
    "v_pk_fma_f32 v[140:141], v[140:141], v[32:33], v[40:41]\n"
    "v_pk_fma_f32 v[142:143], v[142:143], v[34:35], v[42:43]\n"
    "v_cvt_pk_bf16_f32 v140, v140, v141\n"
    "v_pk_fma_f32 v[144:145], v[144:145], v[36:37], v[44:45]\n"
    "v_cvt_pk_bf16_f32 v141, v142, v143\n"
    "v_pk_fma_f32 v[146:147], v[146:147], v[38:39], v[46:47]\n"
    "v_cvt_pk_bf16_f32 v142, v144, v145\n"
    "v_cvt_pk_bf16_f32 v143, v146, v147\n"
    "ds_write_b128 v18, v[140:143] offset:640\n"
    "v_pk_fma_f32 v[148:149], v[148:149], v[32:33], v[40:41]\n"
    "v_pk_fma_f32 v[150:151], v[150:151], v[34:35], v[42:43]\n"
    "v_cvt_pk_bf16_f32 v148, v148, v149\n"
    "v_pk_fma_f32 v[152:153], v[152:153], v[36:37], v[44:45]\n"
    "v_cvt_pk_bf16_f32 v149, v150, v151\n"
    "v_pk_fma_f32 v[154:155], v[154:155], v[38:39], v[46:47]\n"
    "v_cvt_pk_bf16_f32 v150, v152, v153\n"
    "v_cvt_pk_bf16_f32 v151, v154, v155\n"
    "ds_write_b128 v18, v[148:151] offset:768\n"
    "v_pk_fma_f32 v[156:157], v[156:157], v[32:33], v[40:41]\n"
    "v_pk_fma_f32 v[158:159], v[158:159], v[34:35], v[42:43]\n"
    "v_cvt_pk_bf16_f32 v156, v156, v157\n"
    "v_pk_fma_f32 v[160:161], v[160:161], v[36:37], v[44:45]\n"
    "v_cvt_pk_bf16_f32 v157, v158, v159\n"
    "v_pk_fma_f32 v[162:163], v[162:163], v[38:39], v[46:47]\n"
    "v_cvt_pk_bf16_f32 v158, v160, v161\n"
    "v_cvt_pk_bf16_f32 v159, v162, v163\n"
    "ds_write_b128 v18, v[156:159] offset:896\n"
    "s_waitcnt lgkmcnt(10)\n"
    "v_pk_fma_f32 v[164:165], v[164:165], v[48:49], v[56:57]\n"
    "v_pk_fma_f32 v[166:167], v[166:167], v[50:51], v[58:59]\n"
    "v_cvt_pk_bf16_f32 v164, v164, v165\n"
    "s_waitcnt lgkmcnt(8)\n"
    "v_pk_fma_f32 v[168:169], v[168:169], v[52:53], v[60:61]\n"
    "v_cvt_pk_bf16_f32 v165, v166, v167\n"
    "v_pk_fma_f32 v[170:171], v[170:171], v[54:55], v[62:63]\n"
    "v_cvt_pk_bf16_f32 v166, v168, v169\n"
    "v_cvt_pk_bf16_f32 v167, v170, v171\n"
    "ds_write_b128 v91, v[164:167] offset:0\n"
    "s_waitcnt lgkmcnt(0)\n"
    "s_barrier\n"
    "s_memtime s[14:15]\n"
    "ds_read_b128 v[100:103], v20 offset:0\n"
    "ds_read_b128 v[104:107], v20 offset:2080\n"
    "ds_read_b128 v[108:111], v20 offset:4160\n"
    "ds_read_b128 v[112:115], v20 offset:6240\n"
    "ds_read_b128 v[116:119], v20 offset:8320\n"
    "ds_read_b128 v[120:123], v20 offset:10400\n"
    "ds_read_b128 v[124:127], v20 offset:12480\n"
    "ds_read_b128 v[128:131], v20 offset:14560\n"
    "s_waitcnt lgkmcnt(7)\n"
    "v_mfma_f32_32x32x16_bf16 v[32:47], v[172:175], v[100:103], 0\n"
    "ds_read_b128 v[132:135], v20 offset:16640\n"
    "ds_read_b64_tr_b16 v[204:205], v19 offset:16384\n"
    "ds_read_b64_tr_b16 v[206:207], v19 offset:16896\n"
    "v_mov_b32 v49, s74\n"
    "s_waitcnt lgkmcnt(9)\n"
    "v_mfma_f32_32x32x16_bf16 v[32:47], v[176:179], v[104:107], v[32:47]\n"
    "ds_read_b128 v[136:139], v20 offset:18720\n"
    "ds_read_b64_tr_b16 v[208:209], v19 offset:18432\n"
    "ds_read_b64_tr_b16 v[210:211], v19 offset:18944\n"
    "v_mov_b32 v51, s74\n"
    "s_waitcnt lgkmcnt(11)\n"
    "v_mfma_f32_32x32x16_bf16 v[32:47], v[180:183], v[108:111], v[32:47]\n"
    "ds_read_b128 v[140:143], v20 offset:20800\n"
    "ds_read_b64_tr_b16 v[212:213], v19 offset:20480\n"
    "ds_read_b64_tr_b16 v[214:215], v19 offset:20992\n"
    "v_mov_b32 v53, s74\n"
    "s_waitcnt lgkmcnt(13)\n"
    "v_mfma_f32_32x32x16_bf16 v[32:47], v[184:187], v[112:115], v[32:47]\n"
    "ds_read_b128 v[144:147], v20 offset:22880\n"
    "ds_read_b64_tr_b16 v[216:217], v19 offset:22528\n"
    "ds_read_b64_tr_b16 v[218:219], v19 offset:23040\n"
    "v_mov_b32 v55, s74\n"
    "s_waitcnt lgkmcnt(15)\n"
    "v_mfma_f32_32x32x16_bf16 v[32:47], v[188:191], v[116:119], v[32:47]\n"
    "ds_read_b128 v[148:151], v20 offset:24960\n"
    "ds_read_b64_tr_b16 v[220:221], v19 offset:24576\n"
    "ds_read_b64_tr_b16 v[222:223], v19 offset:25088\n"
    "v_mov_b32 v57, s74\n"
    "s_waitcnt lgkmcnt(15)\n"
    "v_mfma_f32_32x32x16_bf16 v[32:47], v[192:195], v[120:123], v[32:47]\n"
    "ds_read_b128 v[152:155], v20 offset:27040\n"
    "ds_read_b64_tr_b16 v[224:225], v19 offset:26624\n"
    "ds_read_b64_tr_b16 v[226:227], v19 offset:27136\n"
    "v_mov_b32 v59, s74\n"
    "v_mfma_f32_32x32x16_bf16 v[32:47], v[196:199], v[124:127], v[32:47]\n"
    "ds_read_b128 v[156:159], v20 offset:29120\n"
    "ds_read_b64_tr_b16 v[228:229], v19 offset:28672\n"
    "ds_read_b64_tr_b16 v[230:231], v19 offset:29184\n"
    "v_mov_b32 v61, s74\n"
    "v_mfma_f32_32x32x16_bf16 v[32:47], v[200:203], v[128:131], v[32:47]\n"
    "ds_read_b128 v[160:163], v20 offset:31200\n"
    "ds_read_b64_tr_b16 v[232:233], v19 offset:30720\n"
    "ds_read_b64_tr_b16 v[234:235], v19 offset:31232\n"
    "v_mov_b32 v63, s74\n"
    "s_waitcnt lgkmcnt(15)\n"
    "v_mfma_f32_32x32x16_bf16 v[32:47], v[204:207], v[132:135], v[32:47]\n"
    "ds_read_b128 v[164:167], v20 offset:33280\n"
    "ds_read_b64_tr_b16 v[236:237], v89 offset:0\n"
    "ds_read_b64_tr_b16 v[238:239], v89 offset:512\n"
    "v_mfma_f32_32x32x16_bf16 v[32:47], v[208:211], v[136:139], v[32:47]\n"
    "ds_read_b128 v[168:171], v20 offset:35360\n"
    "ds_read_b64_tr_b16 v[240:241], v89 offset:2048\n"
    "ds_read_b64_tr_b16 v[242:243], v89 offset:2560\n"
    "v_mfma_f32_32x32x16_bf16 v[32:47], v[212:215], v[140:143], v[32:47]\n"
    "ds_read_b128 v[100:103], v20 offset:37440\n"
    "ds_read_b64_tr_b16 v[244:245], v89 offset:4096\n"
    "ds_read_b64_tr_b16 v[246:247], v89 offset:4608\n"
    "s_waitcnt lgkmcnt(15)\n"
    "v_mfma_f32_32x32x16_bf16 v[32:47], v[216:219], v[144:147], v[32:47]\n"
    "ds_read_b128 v[104:107], v20 offset:39520\n"
    "ds_read_b64_tr_b16 v[248:249], v89 offset:6144\n"
    "ds_read_b64_tr_b16 v[250:251], v89 offset:6656\n"
    "v_mfma_f32_32x32x16_bf16 v[32:47], v[220:223], v[148:151], v[32:47]\n"
    "ds_read_b128 v[108:111], v20 offset:41600\n"
    "ds_read_b64_tr_b16 v[252:253], v89 offset:8192\n"
    "ds_read_b64_tr_b16 v[254:255], v89 offset:8704\n"
    "v_mfma_f32_32x32x16_bf16 v[32:47], v[224:227], v[152:155], v[32:47]\n"
    "ds_read_b128 v[112:115], v20 offset:43680\n"
    "ds_read_b64_tr_b16 v[172:173], v89 offset:10240\n"
    "ds_read_b64_tr_b16 v[174:175], v89 offset:10752\n"
    "s_waitcnt lgkmcnt(15)\n"
    "v_mfma_f32_32x32x16_bf16 v[32:47], v[228:231], v[156:159], v[32:47]\n"
    "ds_read_b128 v[116:119], v20 offset:45760\n"
    "ds_read_b64_tr_b16 v[176:177], v89 offset:12288\n"
    "ds_read_b64_tr_b16 v[178:179], v89 offset:12800\n"
    "v_mfma_f32_32x32x16_bf16 v[32:47], v[232:235], v[160:163], v[32:47]\n"
    "ds_read_b128 v[120:123], v20 offset:47840\n"
    "ds_read_b64_tr_b16 v[180:181], v89 offset:14336\n"
    "ds_read_b64_tr_b16 v[182:183], v89 offset:14848\n"
    "v_mfma_f32_32x32x16_bf16 v[32:47], v[236:239], v[164:167], v[32:47]\n"
    "ds_read_b128 v[124:127], v20 offset:49920\n"
    "ds_read_b64_tr_b16 v[184:185], v89 offset:16384\n"
    "ds_read_b64_tr_b16 v[186:187], v89 offset:16896\n"
    "s_waitcnt lgkmcnt(15)\n"
    "v_mfma_f32_32x32x16_bf16 v[32:47], v[240:243], v[168:171], v[32:47]\n"
    "ds_read_b128 v[128:131], v20 offset:52000\n"
    "ds_read_b64_tr_b16 v[188:189], v89 offset:18432\n"
    "ds_read_b64_tr_b16 v[190:191], v89 offset:18944\n"
    "v_mfma_f32_32x32x16_bf16 v[32:47], v[244:247], v[100:103], v[32:47]\n"
    "ds_read_b128 v[132:135], v20 offset:54080\n"
    "ds_read_b64_tr_b16 v[192:193], v89 offset:20480\n"
    "ds_read_b64_tr_b16 v[194:195], v89 offset:20992\n"
    "v_mfma_f32_32x32x16_bf16 v[32:47], v[248:251], v[104:107], v[32:47]\n"
    "ds_read_b128 v[136:139], v20 offset:56160\n"
    "ds_read_b64_tr_b16 v[196:197], v89 offset:22528\n"
    "ds_read_b64_tr_b16 v[198:199], v89 offset:23040\n"
    "s_waitcnt lgkmcnt(15)\n"
    "v_mfma_f32_32x32x16_bf16 v[32:47], v[252:255], v[108:111], v[32:47]\n"
    "ds_read_b128 v[140:143], v20 offset:58240\n"
    "ds_read_b64_tr_b16 v[200:201], v89 offset:24576\n"
    "ds_read_b64_tr_b16 v[202:203], v89 offset:25088\n"
    "v_mfma_f32_32x32x16_bf16 v[32:47], v[172:175], v[112:115], v[32:47]\n"
    "ds_read_b128 v[144:147], v20 offset:60320\n"
    "ds_read_b64_tr_b16 v[204:205], v89 offset:26624\n"
    "ds_read_b64_tr_b16 v[206:207], v89 offset:27136\n"
    "v_mfma_f32_32x32x16_bf16 v[32:47], v[176:179], v[116:119], v[32:47]\n"
    "ds_read_b128 v[148:151], v20 offset:62400\n"
    "ds_read_b64_tr_b16 v[208:209], v89 offset:28672\n"
    "ds_read_b64_tr_b16 v[210:211], v89 offset:29184\n"
    "s_waitcnt lgkmcnt(15)\n"
    "v_mfma_f32_32x32x16_bf16 v[32:47], v[180:183], v[120:123], v[32:47]\n"
    "ds_read_b128 v[152:155], v20 offset:64480\n"
    "ds_read_b64_tr_b16 v[212:213], v89 offset:30720\n"
    "ds_read_b64_tr_b16 v[214:215], v89 offset:31232\n"
    "v_mfma_f32_32x32x16_bf16 v[32:47], v[184:187], v[124:127], v[32:47]\n"
    "ds_read_b128 v[156:159], v90 offset:0\n"
    "ds_read_b64_tr_b16 v[216:217], v89 offset:32768\n"
    "ds_read_b64_tr_b16 v[218:219], v89 offset:33280\n"
    "v_mfma_f32_32x32x16_bf16 v[32:47], v[188:191], v[128:131], v[32:47]\n"
    "ds_read_b128 v[160:163], v90 offset:2080\n"
    "ds_read_b64_tr_b16 v[220:221], v89 offset:34816\n"
    "ds_read_b64_tr_b16 v[222:223], v89 offset:35328\n"
    "s_waitcnt lgkmcnt(15)\n"
    "v_mfma_f32_32x32x16_bf16 v[32:47], v[192:195], v[132:135], v[32:47]\n"
    "ds_read_b128 v[164:167], v90 offset:4160\n"
    "ds_read_b64_tr_b16 v[224:225], v89 offset:36864\n"
    "ds_read_b64_tr_b16 v[226:227], v89 offset:37376\n"
    "v_mfma_f32_32x32x16_bf16 v[32:47], v[196:199], v[136:139], v[32:47]\n"
    "ds_read_b128 v[168:171], v90 offset:6240\n"
    "ds_read_b64_tr_b16 v[228:229], v89 offset:38912\n"
    "ds_read_b64_tr_b16 v[230:231], v89 offset:39424\n"
    "v_mfma_f32_32x32x16_bf16 v[32:47], v[200:203], v[140:143], v[32:47]\n"
    "s_waitcnt lgkmcnt(15)\n"
    "v_mfma_f32_32x32x16_bf16 v[32:47], v[204:207], v[144:147], v[32:47]\n"
    "v_mfma_f32_32x32x16_bf16 v[32:47], v[208:211], v[148:151], v[32:47]\n"
    "s_waitcnt lgkmcnt(12)\n"
    "v_mfma_f32_32x32x16_bf16 v[32:47], v[212:215], v[152:155], v[32:47]\n"
    "s_waitcnt lgkmcnt(9)\n"
    "v_mfma_f32_32x32x16_bf16 v[32:47], v[216:219], v[156:159], v[32:47]\n"
    "s_waitcnt lgkmcnt(6)\n"
    "v_mfma_f32_32x32x16_bf16 v[32:47], v[220:223], v[160:163], v[32:47]\n"
    "s_waitcnt lgkmcnt(3)\n"
    "v_mfma_f32_32x32x16_bf16 v[32:47], v[224:227], v[164:167], v[32:47]\n"
    "s_waitcnt lgkmcnt(0)\n"
    "v_mfma_f32_32x32x16_bf16 v[32:47], v[228:231], v[168:171], v[32:47]\n"
    "s_nop 11\n"
    "v_cvt_pk_bf16_f32 v48, v32, v33\n"
    "v_cvt_pk_bf16_f32 v50, v34, v35\n"
    "buffer_store_dwordx4 v[48:51], v11, s[36:39], s94 offen offset:0 sc1\n"
    "v_cvt_pk_bf16_f32 v52, v36, v37\n"
    "v_cvt_pk_bf16_f32 v54, v38, v39\n"
    "buffer_store_dwordx4 v[52:55], v11, s[36:39], s94 offen offset:256 sc1\n"
    "v_cvt_pk_bf16_f32 v56, v40, v41\n"
    "v_cvt_pk_bf16_f32 v58, v42, v43\n"
    "buffer_store_dwordx4 v[56:59], v11, s[36:39], s94 offen offset:512 sc1\n"
    "v_cvt_pk_bf16_f32 v60, v44, v45\n"
    "v_cvt_pk_bf16_f32 v62, v46, v47\n"
    "buffer_store_dwordx4 v[60:63], v11, s[36:39], s94 offen offset:768 sc1\n"
    "s_cmp_gt_u32 s76, 3\n"
    "s_cbranch_scc1 H1%=\n"
    "s_and_b32 s77, s75, 3\n"
    "s_lshl2_add_u32 s77, s77, LTB-LK3B+16\n"
    "s_add_u32 s22, s80, s77\n"
    "s_addc_u32 s23, s81, 0\n"
    "s_mov_b64 exec, 0\n"
    "s_swappc_b64 vcc, s[22:23]\n"
    "s_mov_b64 exec, -1\n"
    "H1%=:\n"
    "s_waitcnt vmcnt(4)\n"
    "s_barrier\n"
    "PO%=:\n"
    "s_lshl_b32 s77, s75, 7\n"
    "s_add_u32 s77, s77, 78336\n"
    "v_add_u32 v4, s77, v4\n"
    "s_cmp_lg_u32 s75, 0\n"
    "ds_write_b128 v4, v[94:97]\n"
    "s_cselect_b64 exec, 0, -1\n"
    "ds_write_b128 v4, v[82:85] offset:1024\n"
    "s_mov_b64 exec, -1\n"
    "s_waitcnt lgkmcnt(0)\n"
      : "=&{s[36:51]}"(ka), "=&{s[68:71]}"(kc), "=&{s[72:73]}"(kd), "=&{s74}"(tag), "=&{s75}"(w), "=&{s12}"(s_qb), "+{v0}"(tid), "={v[80:87]}"(pc), "={v88}"(l16),
        "=&{s14}"(s_tm), [bi] "+s"(bi)
      : [ka] "s"(__builtin_amdgcn_kernarg_segment_ptr())
      : "v1", "v2", "v3", "v4", "v5", "v6", "v7", "v8", "v9", "v10", "v11", "v12", "v13", "v14", "v15", "v16", "v17", "v18", "v19", "v20", "v21", "v22", "v23", "v24", "v25", "v26", "v27", "v28", "v29", "v30", "v31", "v32", "v33", "v34", "v35", "v36", "v37", "v38", "v39", "v40", "v41", "v42", "v43", "v44", "v45", "v46", "v47", "v48", "v49", "v50", "v51", "v52", "v53", "v54", "v55", "v56", "v57", "v58", "v59", "v60", "v61", "v62", "v63", "v64", "v65", "v66", "v67", "v68", "v69", "v70", "v71", "v72", "v73", "v74", "v75", "v76", "v77", "v78", "v79", "v89", "v90", "v91", "v92", "v93", "v94", "v95", "v96", "v97", "v100", "v101", "v102", "v103", "v104", "v105", "v106", "v107", "v108", "v109", "v110", "v111", "v112", "v113", "v114", "v115", "v116", "v117", "v118", "v119", "v120", "v121", "v122", "v123", "v124", "v125", "v126", "v127", "v128", "v129", "v130", "v131", "v132", "v133", "v134", "v135", "v136", "v137", "v138", "v139", "v140", "v141", "v142", "v143", "v144", "v145", "v146", "v147", "v148", "v149", "v150", "v151", "v152", "v153", "v154", "v155", "v156", "v157", "v158", "v159", "v160", "v161", "v162", "v163", "v164", "v165", "v166", "v167", "v168", "v169", "v170", "v171", "v172", "v173", "v174", "v175", "v176", "v177", "v178", "v179", "v180", "v181", "v182", "v183", "v184", "v185", "v186", "v187", "v188", "v189", "v190", "v191", "v192", "v193", "v194", "v195", "v196", "v197", "v198", "v199", "v200", "v201", "v202", "v203", "v204", "v205", "v206", "v207", "v208", "v209", "v210", "v211", "v212", "v213", "v214", "v215", "v216", "v217", "v218", "v219", "v220", "v221", "v222", "v223", "v224", "v225", "v226", "v227", "v228", "v229", "v230", "v231", "v232", "v233", "v234", "v235", "v236", "v237", "v238", "v239", "v240", "v241", "v242", "v243", "v244", "v245", "v246", "v247", "v248", "v249", "v250", "v251", "v252", "v253", "v254", "v255", "s15", "s20", "s21", "s22", "s23", "s25", "s28", "s29", "s30", "s31", "s52", "s53", "s54", "s55", "s56", "s57", "s58", "s59", "s60", "s61", "s62", "s63", "s64", "s65", "s66", "s67", "s76", "s77", "s78", "s79", "s80", "s81", "s82", "s83", "s84", "s85", "s86", "s87", "s88", "s89", "s90", "s91", "s92", "s93", "s94", "s95", "s96", "s97", "vcc", "scc", "memory");
  u32x4 k2; u32x32 kA, vA, vB;
  u32x16 ac0; u32x4 ac1;
  unsigned s_w8b = L3_W8 + w * 7168, vo;
  {
    const int c = tid & 31, h = (tid >> 5) & 1, gq = (c >> 3) * 9 + h * 4;
    pc[1] = (gq >> 1) * 128 + (gq & 1) * 8 + (c & 7) * 16 + s_w8b; pc[2] = pc[1] + ((gq & 1) ? 120 : 8); pc[3] = pc[1] + 512 - h * 256;
    pc[4] = h * 1024 + c * 8 + 4;
    pc[5] = L3_QB + w * CS + Q2O + 512 + h * 1028 + c * 16;
    vo = c < 16 ? h * 256 + c * 16 : 512 + h * 64 + (c & 4) * 8 + (c & 1) * 16 + ((c & 10) << 27);
  }
  const unsigned s_kb = (unsigned)WS_KM + w * 8192, s_vb = (unsigned)WS_VI + w * 10240, s_k2b = (unsigned)WS_K2 + w * 2048;
  unsigned s_pa, s_pb, s_pi, s_pd, s_pdi, s_pm = L3_PT + w * 2048, s_ld, s_ds, s_h4;
  int gw = bi * 8 + w;
  if (gw >= 1944) gw -= 1944;
  const int cls = gw / 648;
  {
    const int i = gw - cls * 648;
    const bool u = cls < 2;
    const int rb = u ? i / 72 : i / 9, cb = i - rb * (u ? 72 : 9);
    s_ld = u ? F_ * 2 : D_ * 2; s_ds = u ? 36864 : 147456; s_h4 = u ? 0 : 4;
    s_pa = rb * 64 * s_ld + cb * 128; s_pi = 16 * s_ld; s_pb = s_pa + (u ? 8 : 4) * s_ld;
    s_pd = (u ? (unsigned)WS_W1T + rb * 4096 + cls * 256 : (unsigned)WS_WDT + rb * 2048) + cb * 4 * s_ds; s_pdi = u ? 512 : 256;
  }
  s_pa = RFL(s_pa); s_pb = RFL(s_pb); s_pd = RFL(s_pd); s_pi = RFL(s_pi); s_pdi = RFL(s_pdi); s_ld = RFL(s_ld); s_ds = RFL(s_ds); s_h4 = RFL(s_h4);
  unsigned rp0 = RFL(cls == 0 ? kc[0] : (cls == 1 ? kc[2] : kd[0])), rp1 = RFL(cls == 0 ? kc[1] : (cls == 1 ? kc[3] : kd[1]));
  const rsrc_t rws = mkrs(mkp(ka[0], ka[1]), 0x10000000u), rpt = mkrs(mkp(rp0, rp1), D_ * F_ * 2);
  if ((bi >> 1) == 60) __builtin_amdgcn_raw_buffer_store_b128((u32x4){0u, 0u, 0u, 0u}, rws, l16, (unsigned)WS_FLAG + (bi & 1) * 8192 + w * 1024, 0);
  asm volatile(
    "s_movk_i32 %[t1], 200\n"
    "v_mov_b32 v13, %[tag]\n"
    "v_mov_b32 v15, %[tag]\n"
    "PW%=:\n"
    "s_memtime s[82:83]\n"
    "s_sub_u32 %[t1], %[t1], 1\n"
    "s_waitcnt lgkmcnt(0)\n"
    "s_cbranch_scc1 PX%=\n"
    "s_sub_u32 s82, s82, %[tg]\n"
    "s_cmp_lt_i32 s82, 0\n"
    "s_cbranch_scc1 PW%=\n"
    "PX%=:\n"
    "s_mov_b64 exec, 0xffff\n"
    "buffer_load_dwordx2 v[2:3], v80, %[rws], 0 offen sc1\n"
    "buffer_load_dwordx4 v[12:15], v88, %[rws], %[qb] offen offset:2048 sc1\n"
    "s_mov_b64 exec, -1\n"
    "buffer_load_dwordx4 v[4:7], v88, %[rws], %[qb] offen offset:0 sc1\n"
    "buffer_load_dwordx4 v[8:11], v88, %[rws], %[qb] offen offset:1024 sc1\n"
      : "+{v0}"(tid), "+{v[80:87]}"(pc), "+{v88}"(l16), "+{v68}"(vo), "={v[2:3]}"(fv), "={v[4:7]}"(q0), "={v[8:11]}"(q1), "={v[12:15]}"(q2),
        "+s"(s_pa), "+s"(s_pb), "+s"(s_pd), "+s"(s_pi), "+s"(s_pdi), "+s"(s_ld), "+s"(s_ds), "+s"(s_h4), "+s"(bi),  [t1] "=&s"(s_t1)
      : [rws] "s"(rws), "s"(rpt), [tag] "s"(tag), [qb] "s"(s_qb), [tg] "s"(RFL(s_tm + ((bi ^ 128u) < 36 ? 4096u : 4864u)))
      : "s82", "s83", "vcc", "scc", "memory");
  const int ab = (bi & 7) * 32 + (bi & 24) + (bi >> 5), t0 = ab * 8;
  const unsigned wj = (bi & 0xf0) + (bi & 7) * 2 + ((bi >> 3) & 1) - 72;
  {
    const int lane = tid & 63, t16 = lane & 15, g = lane >> 4, c = lane & 31, h = lane >> 5, r8 = lane >> 3;
    ad1[1] = (r8 + s_h4 * h) * s_ld + (lane & 7) * 16;
    ad1[2] = g * s_ds + t16 * 16;
    const int rc = lane >> 1, rhalf = lane & 1, rhf = (w & 1) ^ rhalf, rch = (w >> 1) & 1, rh = w >> 2;
    ad0[0] = l16;
    ad0[1] = l16 + L3_QB;
    ad0[2] = l16 + L3_OF + (w >> 2) * 1024;
    ad0[3] = L3_PL + w * 512 + lane * 8;
    ad0[4] = L3_PL + rhalf * 2048 + rc * 8;
    ad0[5] = L3_OF + ((rc >> 4) * 4 + 2 * ((rc >> 3) & 1) + rh) * 256 + (rhalf * 8 + (rc & 7)) * 16 + rch * 8 + rhf * 4;
    ad0[6] = l16 + L3_PART + w * 2048;
    ad0[7] = L3_PART + rhalf * 8192 + rch * 1024 + (rh * 32 + rc) * 16 + rhf * 8;
    ad1[0] = L3_PT + w * 2048 + (t16 >> 2) * 128 + g * 32 + (lane & 3) * 8;
    ad1[3] = L3_NM + c * 4;
  }
  unsigned s_wo = (unsigned)WS_WO + w * 9216, s_wo4 = s_wo + 4096, s_wo8 = s_wo + 8192, s_w8g = s_wo + 64 * 9216, s_kb4 = s_kb + 4096, s_vb3 = s_vb + 3840, s_vb6 = s_vb + 7680;
  unsigned s_lw = 7 - w, s_lo = L3_OFL + w * 256, s_w64 = w * 8, s_wf = (unsigned)WS_WF, s_mr = (unsigned)WS_MG + w * 1024;
  unsigned s_wm = (unsigned)WS_WO + (w & 3) * 9216 + 4096 + 7 * 73728, s_wl = L3_W8 + (w & 3) * 7168 + 4096, s_m1 = L3_MOD + w * 32, s_m2 = L3_MOD + 1024 + (w & 3) * 32;
  unsigned s_x4 = (unsigned)WS_X1 + (t0 >> 4) * 18432 + (t0 & 15) * 16, s_xo = RFL(s_x4 + w * 512 + (bi == 0 && w == 7 ? 0x80000000u : 0u));
  s_x4 += 16384 + (w & 3) * 512;
  unsigned long long s_me = 1ull << w, s_mk = 0x00ff00ffull << ((w & 4) * 8);
  asm volatile("" : "+s"(s_wo), "+s"(s_wo4), "+s"(s_wo8), "+s"(s_w8g), "+s"(s_kb4), "+s"(s_vb3), "+s"(s_vb6), "+s"(s_lw), "+s"(s_lo), "+s"(s_w64), "+s"(s_wf), "+s"(s_mr), "+s"(s_me),
               "+s"(s_wm), "+s"(s_wl), "+s"(s_m1), "+s"(s_m2), "+s"(s_x4), "+s"(s_xo), "+s"(s_mk));
  asm volatile(
    "v_add_u32 v16, %[qw], v88\n"
    "v_mov_b64 v[20:21], 0\n"
    "v_mov_b64 v[22:23], 0\n"
    "v_and_b32 v0, 7, v0\n"
    "v_lshlrev_b32 v0, 2, v0\n"
    "v_add_u32 v0, 38048, v0\n"
    "ds_write_b128 v16, v[20:23] offset:1024\n"
    "ds_write_b128 v16, v[20:23] offset:2048\n"
    "s_mov_b32 exec_hi, 0\n"
    "ds_write_b128 v16, v[20:23] offset:3072\n"
    "ds_write_b32 v0, v20\n"
    "s_mov_b32 exec_hi, -1\n"
    "v_add_u32 v17, %[w8b], v88\n"
    "v_mov_b32 v19, 159232\n"
    "v_mov_b64 v[100:101], 0\n"
    "v_mov_b64 v[102:103], 0\n"
    "v_mov_b64 v[104:105], 0\n"
    "v_mov_b64 v[106:107], 0\n"
    "v_mov_b64 v[108:109], 0\n"
    "v_mov_b64 v[110:111], 0\n"
    "v_mov_b64 v[112:113], 0\n"
    "v_mov_b64 v[114:115], 0\n"
    "v_mov_b64 v[116:117], 0\n"
    "v_mov_b64 v[118:119], 0\n"
    "s_movk_i32 %[t0], 0x4000\n"
    "s_mov_b64 exec, 0xffff\n"
    "s_waitcnt vmcnt(3)\n"
    "KA%=:\n"
    "v_cmpx_ne_u32 vcc, %[tag], v3\n"
    "s_cbranch_execz KB%=\n"
    "s_sub_u32 %[t0], %[t0], 1\n"
    "s_cbranch_scc1 KB%=\n"
    "s_sleep 4\n"
    "buffer_load_dwordx2 v[2:3], v80, %[rws], 0 offen sc1\n"
    "s_waitcnt vmcnt(0)\n"
    "s_branch KA%=\n"
    "KB%=:\n"
    "s_mov_b64 exec, -1\n"
    "buffer_load_dwordx4 v[164:167], v88, %[rws], %[kb] offen offset:0 sc1\n"
    "buffer_load_dword v156, v84, %[rws], %[k2b] offen offset:0 sc1\n"
    "buffer_load_dword v157, v84, %[rws], %[k2b] offen offset:512 sc1\n"
    "s_waitcnt vmcnt(3)\n"
    "v_bitop3_b32 v20, v5, v7, %[tag] bitop3:0x7e\n"
    "v_bitop3_b32 v21, v9, v11, %[tag] bitop3:0x7e\n"
    "v_bitop3_b32 v22, v13, v15, %[tag] bitop3:0x7e\n"
    "v_cmp_ne_u32 s[82:83], 0, v20\n"
    "v_cmp_ne_u32 s[84:85], 0, v21\n"
    "v_cmp_ne_u32 s[86:87], 0, v22\n"
    "s_movk_i32 %[t0], 0x4000\n"
    "QA%=:\n"
    "s_or_b64 vcc, s[82:83], s[84:85]\n"
    "s_or_b64 vcc, vcc, s[86:87]\n"
    "s_cbranch_scc0 QB%=\n"
    "s_sub_u32 %[t0], %[t0], 1\n"
    "s_cbranch_scc1 QB%=\n"
    "s_mov_b64 exec, s[82:83]\n"
    "buffer_load_dwordx4 v[4:7], v88, %[rws], %[qb] offen offset:0 sc1\n"
    "s_mov_b64 exec, s[84:85]\n"
    "buffer_load_dwordx4 v[8:11], v88, %[rws], %[qb] offen offset:1024 sc1\n"
    "s_mov_b64 exec, s[86:87]\n"
    "buffer_load_dwordx4 v[12:15], v88, %[rws], %[qb] offen offset:2048 sc1\n"
    "s_mov_b64 exec, -1\n"
    "s_waitcnt vmcnt(0)\n"
    "v_bitop3_b32 v20, v5, v7, %[tag] bitop3:0x7e\n"
    "v_bitop3_b32 v21, v9, v11, %[tag] bitop3:0x7e\n"
    "v_bitop3_b32 v22, v13, v15, %[tag] bitop3:0x7e\n"
    "v_cmp_ne_u32 s[82:83], 0, v20\n"
    "v_cmp_ne_u32 s[84:85], 0, v21\n"
    "v_cmp_ne_u32 s[86:87], 0, v22\n"
    "s_branch QA%=\n"
    "QB%=:\n"
    "ds_write_b128 v17, v[4:7] offset:0\n"
    "ds_write_b128 v17, v[8:11] offset:1024\n"
    "ds_write_b128 v17, v[12:15] offset:2048\n"
    "ds_read2_b32 v[4:5], v81 offset1:32\n"
    "ds_read2_b32 v[6:7], v82 offset1:32\n"
    "ds_read_b32 v12, v83\n"
    "v_add_f32_dpp v2, v2, v2 row_ror:8 row_mask:0xf bank_mask:0xf\n"
    "s_mov_b64 exec, 15\n"
    "ds_max_u32 v19, v2 offset:20\n"
    "s_mov_b64 exec, -1\n"
    "s_waitcnt lgkmcnt(3)\n"
    "v_dot2_f32_bf16 v18, v4, v4, 0\n"
    "v_dot2_f32_bf16 v18, v5, v5, v18\n"
    "s_waitcnt lgkmcnt(2)\n"
    "v_dot2_f32_bf16 v18, v6, v6, v18\n"
    "v_dot2_f32_bf16 v18, v7, v7, v18\n"
    "v_swap_b32 v5, v6\n"
    "s_waitcnt lgkmcnt(1)\n"
    "ds_write_b32 v85, v12\n"
    "ds_write_b128 v16, v[4:7]\n"
    "v_mov_b32 v8, v18\n"
    "s_nop 1\n"
    "v_permlane32_swap_b32 v18, v8\n"
    "v_add_f32 v18, v18, v8\n"
    "v_dot2c_f32_bf16 v18, v12, v12\n"
    "s_nop 2\n"
    "v_max_u32_dpp v18, v18, v18 quad_perm:[1,0,3,2] row_mask:0xf bank_mask:0xf\n"
    "s_nop 1\n"
    "v_max_u32_dpp v18, v18, v18 quad_perm:[2,3,0,1] row_mask:0xf bank_mask:0xf\n"
    "s_mov_b64 exec, 0x11111111\n"
    "ds_max_u32 v19, v18 offset:16\n"
    "s_mov_b64 exec, -1\n"
    "buffer_load_dwordx4 v[168:171], v88, %[rws], %[kb] offen offset:1024 sc1\n"
    "buffer_load_dword v158, v84, %[rws], %[k2b] offen offset:256 sc1\n"
    "buffer_load_dword v159, v84, %[rws], %[k2b] offen offset:768 sc1\n"
    "s_waitcnt lgkmcnt(0)\n"
    "s_barrier\n"
    "ds_read_b64 v[4:5], v19 offset:16\n"
    "buffer_load_dwordx4 v[192:195], v68, %[rws], %[vb] offen offset:0 sc1\n"
    "buffer_load_dwordx4 v[172:175], v88, %[rws], %[kb] offen offset:2048 sc1\n"
    "buffer_load_dwordx4 v[196:199], v68, %[rws], %[vb] offen offset:640 sc1\n"
    "buffer_load_dwordx4 v[200:203], v68, %[rws], %[vb] offen offset:1280 sc1\n"
    "s_cmp_eq_u32 %[wfa], 0\n"
    "s_cbranch_scc1 WL%=\n"
    "s_mov_b64 exec, 1\n"
    "buffer_store_dword v3, off, %[rws], %[wfa] sc1\n"
    "s_mov_b64 exec, -1\n"
    "WL%=:\n"
    "s_waitcnt lgkmcnt(0)\n"
    "v_mul_f32 v4, v4, v5\n"
    "v_cmp_nge_f32 vcc, 0x45610000, v4\n"
    "s_mov_b32 %[t0], vcc_lo\n"
      : "+{v0}"(tid), "+{v[80:87]}"(pc), "+{v[88:95]}"(ad0), "+{v[96:99]}"(ad1), "+{v68}"(vo), "+{v[2:3]}"(fv), "+{v[4:7]}"(q0), "+{v[8:11]}"(q1), "+{v[12:15]}"(q2),
        "={v[156:159]}"(k2), "={v[160:191]}"(kA), "={v[192:223]}"(vA), "={v[224:255]}"(vB), "={v[100:115]}"(ac0), "={v[116:119]}"(ac1), [t0] "=&s"(flag)
      : [rws] "s"(rws), [tag] "s"(tag), [qb] "s"(s_qb), [qw] "s"(L3_QB + w * CS), [w8b] "s"(s_w8b),
        [wfa] "s"(w == 4 && wj < 54u ? (unsigned)WS_WF + wj * 16 : 0u), [kb] "s"(s_kb), [vb] "s"(s_vb), [k2b] "s"(s_k2b)
      : "v1", "v16", "v17", "v18", "v19", "v20", "v21", "v22", "v23", "s82", "s83", "s84", "s85", "s86", "s87", "vcc", "scc", "memory");
  STAMP(1); STAMP_R(58);
  if (__builtin_expect(flag != 0, 0)) {
    const unsigned l16 = ad0[0], lane = l16 >> 4, tid = w * 64 + lane;
    u32x4 km[8];
#pragma unroll
    for (int t = 0; t < 8; ++t) km[t] = bld(rws, l16, (unsigned)WS_KM + w * 8192 + t * 1024);
    u32x4 kl;
#pragma unroll
    for (int i = 0; i < 4; ++i) kl[i] = __builtin_amdgcn_raw_buffer_load_b32(rws, pc[4], (unsigned)WS_K2 + w * 2048 + ((i >> 1) + 2 * (i & 1)) * 256, 16);
#pragma unroll 1
    for (int ct = 0; ct < 8; ++ct) {
      const unsigned b = L3_QB + ct * CS + l16;
      const u32x4 qm = lds_ld<u32x4>(b + QMO);
      float m = -__builtin_inff();
#pragma unroll
      for (int t = 0; t < 8; ++t) {
        const int j = t >> 1;
        f32x16 s = {};
        s = mfma32(asb(km[t]), asb(qm), s);
        s = mfma32(asb((u32x4){kl[2 * (t & 1)], kl[2 * (t & 1) + 1], 0u, 0u}), asb(lds_ld<u32x4>(b + Q2O + (j & 1) * 1024 + (j < 2 ? 512 : 0))), s);
#pragma unroll
        for (int i = 0; i < 16; ++i) m = fmaxf(m, s[i]);
      }
      m = fmaxf(m, __shfl_xor(m, 32));
      if (lane < 32) lds_st<float>(L3_PART + w * 1024 + ct * 128 + lane * 4, m);
    }
    RAW_BARRIER();
    if (tid < 256) {
      float m = lds_ld<float>(L3_PART + tid * 4);
#pragma unroll
      for (int k = 1; k < 8; ++k) m = fmaxf(m, lds_ld<float>(L3_PART + k * 1024 + tid * 4));
      lds_st<float>(L3_NM + tid * 4, -m);
    }
    RAW_BARRIER();
  }
  {
    unsigned s_t0, s_t2, s_t3;
    asm volatile(
        "s_cmp_lg_u32 %[flag], 0\n"
        "s_cbranch_scc1 LS%=\n"
    "s_cmp_gt_u32 %[lw], 3\n"
    "LPP4:\n"
    "s_cbranch_execz LPP7\n"
    "s_cbranch_scc1 LO%=\n"
    "ds_read_b128 v[60:63], v89 offset:0\n"
    "ds_read_b128 v[64:67], v89 offset:1536\n"
    "s_setprio 3\n"
    "s_waitcnt vmcnt(9) lgkmcnt(1)\n"
    "v_mfma_f32_32x32x16_bf16 v[4:19], v[164:167], v[60:63], 0\n"
    "s_waitcnt vmcnt(7) lgkmcnt(0)\n"
    "v_mfma_f32_32x32x8_bf16 v[4:19], v[156:157], v[64:65], v[4:19]\n"
    "s_mov_b32 %[cnt], 7\n"
    "s_mov_b32 %[nd], 0\n"
    "buffer_load_dwordx4 v[176:179], v88, %[rws], %[kb] offen offset:3072 sc1\n"
    "buffer_load_dwordx4 v[204:207], v68, %[rws], %[vb] offen offset:1920 sc1\n"
    "buffer_load_dwordx4 v[208:211], v68, %[rws], %[vb] offen offset:2560 sc1\n"
    "s_nop 6\n"
    "s_waitcnt vmcnt(9)\n"
    "v_mfma_f32_32x32x16_bf16 v[20:35], v[168:171], v[60:63], 0\n"
    "v_exp_f32 v4, v4\n"
    "v_exp_f32 v5, v5\n"
    "v_exp_f32 v6, v6\n"
    "v_exp_f32 v7, v7\n"
    "v_exp_f32 v8, v8\n"
    "v_cvt_pk_bf16_f32 v52, v4, v5\n"
    "v_exp_f32 v9, v9\n"
    "v_cvt_pk_bf16_f32 v53, v6, v7\n"
    "v_exp_f32 v10, v10\n"
    "v_exp_f32 v11, v11\n"
    "s_waitcnt vmcnt(7)\n"
    "v_mfma_f32_32x32x8_bf16 v[20:35], v[158:159], v[64:65], v[20:35]\n"
    "ds_read_b128 v[64:67], v89 offset:2560\n"
    "v_exp_f32 v12, v12\n"
    "v_cvt_pk_bf16_f32 v54, v8, v9\n"
    "v_exp_f32 v13, v13\n"
    "v_cvt_pk_bf16_f32 v55, v10, v11\n"
    "v_exp_f32 v14, v14\n"
    "v_exp_f32 v15, v15\n"
    "s_waitcnt vmcnt(6)\n"
    "v_mfma_f32_32x32x16_bf16 v[36:51], v[192:195], v[52:55], 0\n"
    "v_exp_f32 v58, v16\n"
    "v_cvt_pk_bf16_f32 v56, v12, v13\n"
    "v_exp_f32 v52, v17\n"
    "v_cvt_pk_bf16_f32 v57, v14, v15\n"
    "v_exp_f32 v59, v18\n"
    "v_exp_f32 v53, v19\n"
    "s_waitcnt vmcnt(5)\n"
    "v_mfma_f32_32x32x16_bf16 v[4:19], v[172:175], v[60:63], 0\n"
    "v_exp_f32 v20, v20\n"
    "v_cvt_pk_bf16_f32 v58, v58, v52\n"
    "buffer_load_dwordx4 v[180:183], v88, %[rws], %[kb4] offen offset:0 sc1\n"
    "v_exp_f32 v21, v21\n"
    "v_cvt_pk_bf16_f32 v59, v59, v53\n"
    "v_exp_f32 v22, v22\n"
    "v_exp_f32 v23, v23\n"
    "s_waitcnt vmcnt(5)\n"
    "v_mfma_f32_32x32x16_bf16 v[36:51], v[196:199], v[56:59], v[36:51]\n"
    "v_exp_f32 v24, v24\n"
    "v_cvt_pk_bf16_f32 v52, v20, v21\n"
    "buffer_load_dwordx4 v[76:79], v88, %[rws], %[mr] offen sc1\n"
    "v_exp_f32 v25, v25\n"
    "v_cvt_pk_bf16_f32 v53, v22, v23\n"
    "v_exp_f32 v26, v26\n"
    "v_exp_f32 v27, v27\n"
    "s_waitcnt lgkmcnt(0)\n"
    "v_mfma_f32_32x32x8_bf16 v[4:19], v[156:157], v[64:65], v[4:19]\n"
    "v_exp_f32 v28, v28\n"
    "v_cvt_pk_bf16_f32 v54, v24, v25\n"
    "buffer_load_dwordx4 v[212:215], v68, %[rws], %[vb] offen offset:3200 sc1\n"
    "buffer_load_dwordx4 v[216:219], v68, %[rws], %[vb3] offen offset:0 sc1\n"
    "v_exp_f32 v29, v29\n"
    "v_cvt_pk_bf16_f32 v55, v26, v27\n"
    "v_exp_f32 v30, v30\n"
    "v_exp_f32 v31, v31\n"
    "s_waitcnt vmcnt(7)\n"
    "v_mfma_f32_32x32x16_bf16 v[36:51], v[200:203], v[52:55], v[36:51]\n"
    "v_exp_f32 v58, v32\n"
    "v_cvt_pk_bf16_f32 v56, v28, v29\n"
    "v_exp_f32 v52, v33\n"
    "v_cvt_pk_bf16_f32 v57, v30, v31\n"
    "v_exp_f32 v59, v34\n"
    "v_exp_f32 v53, v35\n"
    "s_waitcnt vmcnt(6)\n"
    "v_mfma_f32_32x32x16_bf16 v[20:35], v[176:179], v[60:63], 0\n"
    "v_exp_f32 v4, v4\n"
    "v_cvt_pk_bf16_f32 v58, v58, v52\n"
    "buffer_load_dwordx4 v[184:187], v88, %[rws], %[kb4] offen offset:1024 sc1\n"
    "v_exp_f32 v5, v5\n"
    "v_cvt_pk_bf16_f32 v59, v59, v53\n"
    "v_exp_f32 v6, v6\n"
    "v_exp_f32 v7, v7\n"
    "s_waitcnt vmcnt(6)\n"
    "v_mfma_f32_32x32x16_bf16 v[36:51], v[204:207], v[56:59], v[36:51]\n"
    "v_exp_f32 v8, v8\n"
    "v_cvt_pk_bf16_f32 v52, v4, v5\n"
    "v_exp_f32 v9, v9\n"
    "v_cvt_pk_bf16_f32 v53, v6, v7\n"
    "v_exp_f32 v10, v10\n"
    "v_exp_f32 v11, v11\n"
    "s_setprio 3\n"
    "v_mfma_f32_32x32x8_bf16 v[20:35], v[158:159], v[64:65], v[20:35]\n"
    "ds_read_b128 v[64:67], v89 offset:1024\n"
    "v_exp_f32 v12, v12\n"
    "v_cvt_pk_bf16_f32 v54, v8, v9\n"
    "buffer_load_dwordx4 v[220:223], v68, %[rws], %[vb3] offen offset:640 sc1\n"
    "buffer_load_dwordx4 v[224:227], v68, %[rws], %[vb3] offen offset:1280 sc1\n"
    "v_exp_f32 v13, v13\n"
    "v_cvt_pk_bf16_f32 v55, v10, v11\n"
    "v_exp_f32 v14, v14\n"
    "v_exp_f32 v15, v15\n"
    "s_waitcnt vmcnt(7)\n"
    "v_mfma_f32_32x32x16_bf16 v[36:51], v[208:211], v[52:55], v[36:51]\n"
    "v_exp_f32 v58, v16\n"
    "v_cvt_pk_bf16_f32 v56, v12, v13\n"
    "v_exp_f32 v52, v17\n"
    "v_cvt_pk_bf16_f32 v57, v14, v15\n"
    "v_exp_f32 v59, v18\n"
    "v_exp_f32 v53, v19\n"
    "s_waitcnt vmcnt(6)\n"
    "v_mfma_f32_32x32x16_bf16 v[4:19], v[180:183], v[60:63], 0\n"
    "v_exp_f32 v20, v20\n"
    "v_cvt_pk_bf16_f32 v58, v58, v52\n"
    "buffer_load_dwordx4 v[188:191], v88, %[rws], %[kb4] offen offset:2048 sc1\n"
    "v_exp_f32 v21, v21\n"
    "v_cvt_pk_bf16_f32 v59, v59, v53\n"
    "v_exp_f32 v22, v22\n"
    "v_exp_f32 v23, v23\n"
    "s_waitcnt vmcnt(5)\n"
    "v_mfma_f32_32x32x16_bf16 v[36:51], v[212:215], v[56:59], v[36:51]\n"
    "v_exp_f32 v24, v24\n"
    "v_cvt_pk_bf16_f32 v52, v20, v21\n"
    "v_exp_f32 v25, v25\n"
    "v_cvt_pk_bf16_f32 v53, v22, v23\n"
    "v_exp_f32 v26, v26\n"
    "v_exp_f32 v27, v27\n"
    "s_waitcnt lgkmcnt(0)\n"
    "v_mfma_f32_32x32x8_bf16 v[4:19], v[156:157], v[64:65], v[4:19]\n"
    "v_exp_f32 v28, v28\n"
    "v_cvt_pk_bf16_f32 v54, v24, v25\n"
    "buffer_load_dwordx4 v[228:231], v68, %[rws], %[vb3] offen offset:1920 sc1\n"
    "buffer_load_dwordx4 v[232:235], v68, %[rws], %[vb3] offen offset:2560 sc1\n"
    "v_exp_f32 v29, v29\n"
    "v_cvt_pk_bf16_f32 v55, v26, v27\n"
    "v_exp_f32 v30, v30\n"
    "v_exp_f32 v31, v31\n"
    "s_waitcnt vmcnt(6)\n"
    "v_mfma_f32_32x32x16_bf16 v[36:51], v[216:219], v[52:55], v[36:51]\n"
    "v_exp_f32 v58, v32\n"
    "v_cvt_pk_bf16_f32 v56, v28, v29\n"
    "v_exp_f32 v52, v33\n"
    "v_cvt_pk_bf16_f32 v57, v30, v31\n"
    "v_exp_f32 v59, v34\n"
    "v_exp_f32 v53, v35\n"
    "s_waitcnt vmcnt(5)\n"
    "v_mfma_f32_32x32x16_bf16 v[20:35], v[184:187], v[60:63], 0\n"
    "v_exp_f32 v4, v4\n"
    "v_cvt_pk_bf16_f32 v58, v58, v52\n"
    "buffer_load_dwordx4 v[160:163], v88, %[rws], %[kb4] offen offset:3072 sc1\n"
    "v_exp_f32 v5, v5\n"
    "v_cvt_pk_bf16_f32 v59, v59, v53\n"
    "v_exp_f32 v6, v6\n"
    "v_exp_f32 v7, v7\n"
    "s_waitcnt vmcnt(5)\n"
    "v_mfma_f32_32x32x16_bf16 v[36:51], v[220:223], v[56:59], v[36:51]\n"
    "v_exp_f32 v8, v8\n"
    "v_cvt_pk_bf16_f32 v52, v4, v5\n"
    "buffer_load_dword v1, v88, %[rws], %[wf] offen sc1\n"
    "v_exp_f32 v9, v9\n"
    "v_cvt_pk_bf16_f32 v53, v6, v7\n"
    "v_exp_f32 v10, v10\n"
    "v_exp_f32 v11, v11\n"
    "s_setprio 1\n"
    "v_mfma_f32_32x32x8_bf16 v[20:35], v[158:159], v[64:65], v[20:35]\n"
    "ds_read_b128 v[64:67], v89 offset:2048\n"
    "v_exp_f32 v12, v12\n"
    "v_cvt_pk_bf16_f32 v54, v8, v9\n"
    "buffer_load_dwordx4 v[236:239], v68, %[rws], %[vb3] offen offset:3200 sc1\n"
    "buffer_load_dwordx4 v[240:243], v68, %[rws], %[vb6] offen offset:0 sc1\n"
    "v_exp_f32 v13, v13\n"
    "v_cvt_pk_bf16_f32 v55, v10, v11\n"
    "v_exp_f32 v14, v14\n"
    "v_exp_f32 v15, v15\n"
    "s_waitcnt vmcnt(7)\n"
    "v_mfma_f32_32x32x16_bf16 v[36:51], v[224:227], v[52:55], v[36:51]\n"
    "v_exp_f32 v58, v16\n"
    "v_cvt_pk_bf16_f32 v56, v12, v13\n"
    "v_exp_f32 v52, v17\n"
    "v_cvt_pk_bf16_f32 v57, v14, v15\n"
    "v_exp_f32 v59, v18\n"
    "v_exp_f32 v53, v19\n"
    "s_waitcnt vmcnt(6)\n"
    "v_mfma_f32_32x32x16_bf16 v[4:19], v[188:191], v[60:63], 0\n"
    "v_exp_f32 v20, v20\n"
    "v_cvt_pk_bf16_f32 v58, v58, v52\n"
    "v_exp_f32 v21, v21\n"
    "v_cvt_pk_bf16_f32 v59, v59, v53\n"
    "v_exp_f32 v22, v22\n"
    "v_exp_f32 v23, v23\n"
    "s_waitcnt vmcnt(5)\n"
    "v_mfma_f32_32x32x16_bf16 v[36:51], v[228:231], v[56:59], v[36:51]\n"
    "v_exp_f32 v24, v24\n"
    "v_cvt_pk_bf16_f32 v52, v20, v21\n"
    "v_bitop3_b32 v79, v77, v79, %[tag] bitop3:0x7e\n"
    "v_mov_b32 v77, v78\n"
    "v_cmp_ne_u32 vcc, 0, v79\n"
    "s_cbranch_vccnz LPFY1_%=\n"
    "LRFY1_%=:\n"
    "ds_write_b64 v91, v[76:77] offset:57856\n"
    "v_exp_f32 v25, v25\n"
    "v_cvt_pk_bf16_f32 v53, v22, v23\n"
    "v_exp_f32 v26, v26\n"
    "v_exp_f32 v27, v27\n"
    "s_waitcnt lgkmcnt(1)\n"
    "v_mfma_f32_32x32x8_bf16 v[4:19], v[156:157], v[64:65], v[4:19]\n"
    "v_exp_f32 v28, v28\n"
    "v_cvt_pk_bf16_f32 v54, v24, v25\n"
    "buffer_load_dwordx4 v[244:247], v68, %[rws], %[vb6] offen offset:640 sc1\n"
    "buffer_load_dwordx4 v[248:251], v68, %[rws], %[vb6] offen offset:1280 sc1\n"
    "v_exp_f32 v29, v29\n"
    "v_cvt_pk_bf16_f32 v55, v26, v27\n"
    "v_exp_f32 v30, v30\n"
    "v_exp_f32 v31, v31\n"
    "s_waitcnt vmcnt(6)\n"
    "v_mfma_f32_32x32x16_bf16 v[36:51], v[232:235], v[52:55], v[36:51]\n"
    "v_exp_f32 v58, v32\n"
    "v_cvt_pk_bf16_f32 v56, v28, v29\n"
    "v_exp_f32 v52, v33\n"
    "v_cvt_pk_bf16_f32 v57, v30, v31\n"
    "v_exp_f32 v59, v34\n"
    "v_exp_f32 v53, v35\n"
    "s_setprio 3\n"
    "s_waitcnt vmcnt(5)\n"
    "v_mfma_f32_32x32x16_bf16 v[20:35], v[160:163], v[60:63], 0\n"
    "ds_read_b128 v[60:63], v89 offset:3584\n"
    "v_exp_f32 v4, v4\n"
    "v_cvt_pk_bf16_f32 v58, v58, v52\n"
    "v_exp_f32 v5, v5\n"
    "v_cvt_pk_bf16_f32 v59, v59, v53\n"
    "v_exp_f32 v6, v6\n"
    "v_exp_f32 v7, v7\n"
    "s_waitcnt vmcnt(3)\n"
    "v_mfma_f32_32x32x16_bf16 v[36:51], v[236:239], v[56:59], v[36:51]\n"
    "v_exp_f32 v8, v8\n"
    "v_cvt_pk_bf16_f32 v52, v4, v5\n"
    "v_exp_f32 v9, v9\n"
    "v_cvt_pk_bf16_f32 v53, v6, v7\n"
    "v_exp_f32 v10, v10\n"
    "v_exp_f32 v11, v11\n"
    "v_mfma_f32_32x32x8_bf16 v[20:35], v[158:159], v[64:65], v[20:35]\n"
    "ds_read_b128 v[64:67], v89 offset:5120\n"
    "v_exp_f32 v12, v12\n"
    "v_cvt_pk_bf16_f32 v54, v8, v9\n"
    "buffer_load_dwordx4 v[252:255], v68, %[rws], %[vb6] offen offset:1920 sc1\n"
    "v_exp_f32 v13, v13\n"
    "v_cvt_pk_bf16_f32 v55, v10, v11\n"
    "v_exp_f32 v14, v14\n"
    "v_exp_f32 v15, v15\n"
    "s_waitcnt vmcnt(3)\n"
    "v_mfma_f32_32x32x16_bf16 v[36:51], v[240:243], v[52:55], v[36:51]\n"
    "v_exp_f32 v58, v16\n"
    "v_cvt_pk_bf16_f32 v56, v12, v13\n"
    "s_mov_b32 exec_hi, 0x3fffff\n"
    "v_cmp_ne_u32 vcc, %[tag], v1\n"
    "s_mov_b32 exec_hi, -1\n"
    "s_cbranch_vccnz LPFY0_%=\n"
    "LRFY0_%=:\n"
    "buffer_load_dwordx4 v[80:83], v88, %[rws], %[w8g] offen sc1\n"
    "s_add_u32 %[w8g], %[w8g], 1024\n"
    "v_exp_f32 v52, v17\n"
    "v_cvt_pk_bf16_f32 v57, v14, v15\n"
    "v_exp_f32 v59, v18\n"
    "v_exp_f32 v53, v19\n"
    "s_waitcnt lgkmcnt(1)\n"
    "v_mfma_f32_32x32x16_bf16 v[4:19], v[164:167], v[60:63], 0\n"
    "v_exp_f32 v20, v20\n"
    "v_cvt_pk_bf16_f32 v58, v58, v52\n"
    "buffer_load_dwordx4 v[136:139], v88, %[rws], %[wo4] offen offset:0 sc1\n"
    "v_exp_f32 v21, v21\n"
    "v_cvt_pk_bf16_f32 v59, v59, v53\n"
    "v_exp_f32 v22, v22\n"
    "v_exp_f32 v23, v23\n"
    "s_waitcnt vmcnt(4)\n"
    "v_mfma_f32_32x32x16_bf16 v[36:51], v[244:247], v[56:59], v[36:51]\n"
    "v_exp_f32 v24, v24\n"
    "v_cvt_pk_bf16_f32 v52, v20, v21\n"
    "v_exp_f32 v25, v25\n"
    "v_cvt_pk_bf16_f32 v53, v22, v23\n"
    "s_mov_b32 m0, %[pm]\n"
    "s_add_u32 %[t0], %[pa], %[pi]\n"
    "buffer_load_dwordx4 v97, %[rpt], %[pa] offen lds\n"
    "s_mov_b32 %[pa], %[pb]\n"
    "s_mov_b32 %[pb], %[t0]\n"
    "s_xor_b32 %[pm], %[pm], 1024\n"
    "v_exp_f32 v26, v26\n"
    "v_exp_f32 v27, v27\n"
    "s_waitcnt lgkmcnt(0)\n"
    "v_mfma_f32_32x32x8_bf16 v[4:19], v[156:157], v[64:65], v[4:19]\n"
    "v_exp_f32 v28, v28\n"
    "v_cvt_pk_bf16_f32 v54, v24, v25\n"
    "v_exp_f32 v29, v29\n"
    "v_cvt_pk_bf16_f32 v55, v26, v27\n"
    "v_exp_f32 v30, v30\n"
    "v_exp_f32 v31, v31\n"
    "s_waitcnt vmcnt(4)\n"
    "v_mfma_f32_32x32x16_bf16 v[36:51], v[248:251], v[52:55], v[36:51]\n"
    "v_exp_f32 v58, v32\n"
    "v_cvt_pk_bf16_f32 v56, v28, v29\n"
    "buffer_load_dwordx4 v[120:123], v88, %[rws], %[wo] offen offset:0 sc1\n"
    "v_exp_f32 v52, v33\n"
    "v_cvt_pk_bf16_f32 v57, v30, v31\n"
    "v_exp_f32 v59, v34\n"
    "v_exp_f32 v53, v35\n"
    "v_mfma_f32_32x32x16_bf16 v[20:35], v[168:171], v[60:63], 0\n"
    "v_exp_f32 v4, v4\n"
    "v_cvt_pk_bf16_f32 v58, v58, v52\n"
    "v_exp_f32 v5, v5\n"
    "v_cvt_pk_bf16_f32 v59, v59, v53\n"
    "s_mov_b32 m0, %[pm]\n"
    "s_add_u32 %[t0], %[pa], %[pi]\n"
    "buffer_load_dwordx4 v97, %[rpt], %[pa] offen lds\n"
    "s_mov_b32 %[pa], %[pb]\n"
    "s_mov_b32 %[pb], %[t0]\n"
    "s_xor_b32 %[pm], %[pm], 1024\n"
    "v_exp_f32 v6, v6\n"
    "v_exp_f32 v7, v7\n"
    "s_waitcnt vmcnt(5)\n"
    "v_mfma_f32_32x32x16_bf16 v[36:51], v[252:255], v[56:59], v[36:51]\n"
    "v_exp_f32 v8, v8\n"
    "v_cvt_pk_bf16_f32 v52, v4, v5\n"
    "v_exp_f32 v9, v9\n"
    "v_cvt_pk_bf16_f32 v53, v6, v7\n"
    "v_exp_f32 v10, v10\n"
    "v_exp_f32 v11, v11\n"
    "s_setprio 1\n"
    "v_mfma_f32_32x32x8_bf16 v[20:35], v[158:159], v[64:65], v[20:35]\n"
    "ds_read_b128 v[64:67], v89 offset:6144\n"
    "v_exp_f32 v12, v12\n"
    "v_cvt_pk_bf16_f32 v54, v8, v9\n"
    "buffer_load_dwordx4 v[124:127], v88, %[rws], %[wo] offen offset:1024 sc1\n"
    "v_exp_f32 v13, v13\n"
    "v_cvt_pk_bf16_f32 v55, v10, v11\n"
    "v_exp_f32 v14, v14\n"
    "v_exp_f32 v15, v15\n"
    "ds_write_b128 v94, v[36:39]\n"
    "ds_write_b128 v94, v[40:43] offset:1024\n"
    "ds_write_b64 v91, v[44:45]\n"
    "v_mfma_f32_32x32x16_bf16 v[36:51], v[192:195], v[52:55], 0\n"
    "v_exp_f32 v58, v16\n"
    "v_cvt_pk_bf16_f32 v56, v12, v13\n"
    "v_add_u32 v89, 3584, v89\n"
    "v_exp_f32 v52, v17\n"
    "v_cvt_pk_bf16_f32 v57, v14, v15\n"
    "v_exp_f32 v59, v18\n"
    "v_exp_f32 v53, v19\n"
    "LBFY_%=:\n"
    "v_mfma_f32_32x32x16_bf16 v[4:19], v[172:175], v[60:63], 0\n"
    "v_exp_f32 v20, v20\n"
    "v_cvt_pk_bf16_f32 v58, v58, v52\n"
    "v_exp_f32 v21, v21\n"
    "v_cvt_pk_bf16_f32 v59, v59, v53\n"
    "v_exp_f32 v22, v22\n"
    "v_exp_f32 v23, v23\n"
    "v_mfma_f32_32x32x16_bf16 v[36:51], v[196:199], v[56:59], v[36:51]\n"
    "s_waitcnt lgkmcnt(0)\n"
    "s_mov_b64 exec, %[me]\n"
    "ds_inc_u32 v0, v0\n"
    "s_mov_b64 exec, -1\n"
    "s_add_u32 %[nd], %[nd], 1\n"
    "v_exp_f32 v24, v24\n"
    "v_cvt_pk_bf16_f32 v52, v20, v21\n"
    "buffer_load_dwordx4 v[128:131], v88, %[rws], %[wo] offen offset:2048 sc1\n"
    "v_exp_f32 v25, v25\n"
    "v_cvt_pk_bf16_f32 v53, v22, v23\n"
    "v_exp_f32 v26, v26\n"
    "v_exp_f32 v27, v27\n"
    "v_mfma_f32_32x32x8_bf16 v[4:19], v[156:157], v[64:65], v[4:19]\n"
    "v_exp_f32 v28, v28\n"
    "v_cvt_pk_bf16_f32 v54, v24, v25\n"
    "v_exp_f32 v29, v29\n"
    "v_cvt_pk_bf16_f32 v55, v26, v27\n"
    "v_exp_f32 v30, v30\n"
    "v_exp_f32 v31, v31\n"
    "v_mfma_f32_32x32x16_bf16 v[36:51], v[200:203], v[52:55], v[36:51]\n"
    "v_exp_f32 v58, v32\n"
    "v_cvt_pk_bf16_f32 v56, v28, v29\n"
    "v_exp_f32 v52, v33\n"
    "v_cvt_pk_bf16_f32 v57, v30, v31\n"
    "v_add_u32 v1, %[w8b], v88\n"
    "s_add_u32 %[w8b], %[w8b], 1024\n"
    "s_waitcnt vmcnt(6)\n"
    "ds_write_b128 v1, v[80:83]\n"
    "v_exp_f32 v59, v34\n"
    "v_exp_f32 v53, v35\n"
    "v_mfma_f32_32x32x16_bf16 v[20:35], v[176:179], v[60:63], 0\n"
    "v_exp_f32 v4, v4\n"
    "v_cvt_pk_bf16_f32 v58, v58, v52\n"
    "buffer_load_dwordx4 v[132:135], v88, %[rws], %[wo] offen offset:3072 sc1\n"
    "v_exp_f32 v5, v5\n"
    "v_cvt_pk_bf16_f32 v59, v59, v53\n"
    "v_exp_f32 v6, v6\n"
    "v_exp_f32 v7, v7\n"
    "v_mfma_f32_32x32x16_bf16 v[36:51], v[204:207], v[56:59], v[36:51]\n"
    "v_exp_f32 v8, v8\n"
    "v_cvt_pk_bf16_f32 v52, v4, v5\n"
    "v_exp_f32 v9, v9\n"
    "v_cvt_pk_bf16_f32 v53, v6, v7\n"
    "v_exp_f32 v10, v10\n"
    "v_exp_f32 v11, v11\n"
    "s_setprio 3\n"
    "v_mfma_f32_32x32x8_bf16 v[20:35], v[158:159], v[64:65], v[20:35]\n"
    "ds_read_b128 v[64:67], v89 offset:1024\n"
    "v_exp_f32 v12, v12\n"
    "v_cvt_pk_bf16_f32 v54, v8, v9\n"
    "ds_read_b32 v1, v0\n"
    "v_exp_f32 v13, v13\n"
    "v_cvt_pk_bf16_f32 v55, v10, v11\n"
    "buffer_load_dwordx4 v[140:143], v88, %[rws], %[wo4] offen offset:1024 sc1\n"
    "v_exp_f32 v14, v14\n"
    "v_exp_f32 v15, v15\n"
    "v_mfma_f32_32x32x16_bf16 v[36:51], v[208:211], v[52:55], v[36:51]\n"
    "v_exp_f32 v58, v16\n"
    "v_cvt_pk_bf16_f32 v56, v12, v13\n"
    "s_waitcnt lgkmcnt(0)\n"
    "v_cmp_gt_u32 vcc, %[nd], v1\n"
    "s_cbranch_vccnz LPFY2_%=\n"
    "LRFY2_%=:\n"
    "ds_read2st64_b32 v[84:85], v92 offset0:1 offset1:3\n"
    "ds_read2st64_b32 v[86:87], v92 offset0:5 offset1:7\n"
    "ds_read2st64_b64 v[76:79], v95 offset1:4\n"
    "ds_read2st64_b64 v[80:83], v95 offset0:8 offset1:12\n"
    "ds_read2st64_b64 v[68:71], v92 offset1:1\n"
    "ds_read2st64_b64 v[72:75], v92 offset0:2 offset1:3\n"
    "v_exp_f32 v52, v17\n"
    "v_cvt_pk_bf16_f32 v57, v14, v15\n"
    "v_exp_f32 v59, v18\n"
    "v_exp_f32 v53, v19\n"
    "v_mfma_f32_32x32x16_bf16 v[4:19], v[180:183], v[60:63], 0\n"
    "v_exp_f32 v20, v20\n"
    "v_cvt_pk_bf16_f32 v58, v58, v52\n"
    "v_exp_f32 v21, v21\n"
    "v_cvt_pk_bf16_f32 v59, v59, v53\n"
    "v_exp_f32 v22, v22\n"
    "v_exp_f32 v23, v23\n"
    "v_mfma_f32_32x32x16_bf16 v[36:51], v[212:215], v[56:59], v[36:51]\n"
    "s_waitcnt lgkmcnt(5)\n"
    "v_add_f32 v84, v84, v85\n"
    "s_waitcnt lgkmcnt(4)\n"
    "v_add_f32 v86, v86, v87\n"
    "v_exp_f32 v24, v24\n"
    "v_cvt_pk_bf16_f32 v52, v20, v21\n"
    "buffer_load_dwordx4 v[144:147], v88, %[rws], %[wo4] offen offset:2048 sc1\n"
    "v_exp_f32 v25, v25\n"
    "v_cvt_pk_bf16_f32 v53, v22, v23\n"
    "v_add_f32 v84, v84, v86\n"
    "v_exp_f32 v26, v26\n"
    "s_waitcnt lgkmcnt(3)\n"
    "v_add_f32 v76, v76, v78\n"
    "v_exp_f32 v27, v27\n"
    "v_mfma_f32_32x32x8_bf16 v[4:19], v[156:157], v[64:65], v[4:19]\n"
    "v_add_f32 v77, v77, v79\n"
    "v_exp_f32 v28, v28\n"
    "v_cvt_pk_bf16_f32 v54, v24, v25\n"
    "v_exp_f32 v29, v29\n"
    "v_cvt_pk_bf16_f32 v55, v26, v27\n"
    "v_add_f32_dpp v84, v84, v84 quad_perm:[1,0,3,2] row_mask:0xf bank_mask:0xf\n"
    "v_exp_f32 v30, v30\n"
    "s_waitcnt lgkmcnt(2)\n"
    "v_add_f32 v80, v80, v82\n"
    "v_exp_f32 v31, v31\n"
    "v_mfma_f32_32x32x16_bf16 v[36:51], v[216:219], v[52:55], v[36:51]\n"
    "v_rcp_f32 v84, v84\n"
    "v_exp_f32 v58, v32\n"
    "v_cvt_pk_bf16_f32 v56, v28, v29\n"
    "v_exp_f32 v52, v33\n"
    "v_cvt_pk_bf16_f32 v57, v30, v31\n"
    "v_add_f32 v81, v81, v83\n"
    "v_exp_f32 v59, v34\n"
    "v_add_f32 v76, v76, v80\n"
    "v_exp_f32 v53, v35\n"
    "v_mfma_f32_32x32x16_bf16 v[20:35], v[184:187], v[60:63], 0\n"
    "v_add_f32 v77, v77, v81\n"
    "v_exp_f32 v4, v4\n"
    "v_cvt_pk_bf16_f32 v58, v58, v52\n"
    "buffer_load_dwordx4 v[148:151], v88, %[rws], %[wo4] offen offset:3072 sc1\n"
    "v_exp_f32 v5, v5\n"
    "v_cvt_pk_bf16_f32 v59, v59, v53\n"
    "v_mul_f32 v76, v76, v84\n"
    "v_exp_f32 v6, v6\n"
    "v_mul_f32 v77, v77, v84\n"
    "v_exp_f32 v7, v7\n"
    "v_mfma_f32_32x32x16_bf16 v[36:51], v[220:223], v[56:59], v[36:51]\n"
    "v_cvt_pk_bf16_f32 v76, v76, v77\n"
    "v_exp_f32 v8, v8\n"
    "v_cvt_pk_bf16_f32 v52, v4, v5\n"
    "ds_write_b32 v93, v76\n"
    "v_exp_f32 v9, v9\n"
    "v_cvt_pk_bf16_f32 v53, v6, v7\n"
    "s_cmp_lg_u32 %[lw], %[cnt]\n"
    "s_cbranch_scc1 LLFY1_%=\n"
    "s_waitcnt lgkmcnt(2)\n"
    "v_add_f32 v68, v68, v70\n"
    "v_add_f32 v69, v69, v71\n"
    "s_waitcnt lgkmcnt(1)\n"
    "v_add_f32 v72, v72, v74\n"
    "v_add_f32 v73, v73, v75\n"
    "v_add_f32 v68, v68, v72\n"
    "v_add_f32 v69, v69, v73\n"
    "v_and_b32 v70, 0xf0, v93\n"
    "v_lshrrev_b32 v71, 8, v88\n"
    "v_mul_f32 v68, v68, v84\n"
    "v_mul_f32 v69, v69, v84\n"
    "v_lshl_add_u32 v70, v71, 2, v70\n"
    "v_cvt_pk_bf16_f32 v68, v68, v69\n"
    "v_add_u32 v70, %[lo], v70\n"
    "ds_write_b32 v70, v68\n"
    "LLFY1_%=:\n"
    "v_exp_f32 v10, v10\n"
    "v_exp_f32 v11, v11\n"
    "s_setprio 1\n"
    "v_mfma_f32_32x32x8_bf16 v[20:35], v[158:159], v[64:65], v[20:35]\n"
    "ds_read_b128 v[64:67], v89 offset:2048\n"
    "v_exp_f32 v12, v12\n"
    "v_cvt_pk_bf16_f32 v54, v8, v9\n"
    "v_exp_f32 v13, v13\n"
    "v_cvt_pk_bf16_f32 v55, v10, v11\n"
    "buffer_load_dwordx4 v[152:155], v88, %[rws], %[wo8] offen offset:0 sc1\n"
    "v_exp_f32 v14, v14\n"
    "v_exp_f32 v15, v15\n"
    "v_mfma_f32_32x32x16_bf16 v[36:51], v[224:227], v[52:55], v[36:51]\n"
    "s_waitcnt lgkmcnt(1)\n"
    "s_mov_b64 exec, %[me]\n"
    "ds_inc_u32 v0, v0\n"
    "s_mov_b64 exec, -1\n"
    "s_add_u32 %[nd], %[nd], 1\n"
    "v_exp_f32 v58, v16\n"
    "v_cvt_pk_bf16_f32 v56, v12, v13\n"
    "v_exp_f32 v52, v17\n"
    "v_cvt_pk_bf16_f32 v57, v14, v15\n"
    "v_exp_f32 v59, v18\n"
    "v_exp_f32 v53, v19\n"
    "v_mfma_f32_32x32x16_bf16 v[4:19], v[188:191], v[60:63], 0\n"
    "s_waitcnt vmcnt(9)\n"
    "ds_read_b64_tr_b16 v[76:77], v96\n"
    "ds_read_b64_tr_b16 v[78:79], v96 offset:512\n"
    "v_exp_f32 v20, v20\n"
    "v_cvt_pk_bf16_f32 v58, v58, v52\n"
    "v_exp_f32 v21, v21\n"
    "v_cvt_pk_bf16_f32 v59, v59, v53\n"
    "v_exp_f32 v22, v22\n"
    "v_exp_f32 v23, v23\n"
    "v_mfma_f32_32x32x16_bf16 v[36:51], v[228:231], v[56:59], v[36:51]\n"
    "v_exp_f32 v24, v24\n"
    "v_cvt_pk_bf16_f32 v52, v20, v21\n"
    "buffer_load_dwordx4 v[80:83], v88, %[rws], %[w8g] offen sc1\n"
    "s_add_u32 %[w8g], %[w8g], 1024\n"
    "v_exp_f32 v25, v25\n"
    "v_cvt_pk_bf16_f32 v53, v22, v23\n"
    "v_exp_f32 v26, v26\n"
    "v_exp_f32 v27, v27\n"
    "s_waitcnt lgkmcnt(3)\n"
    "v_mfma_f32_32x32x8_bf16 v[4:19], v[156:157], v[64:65], v[4:19]\n"
    "s_waitcnt lgkmcnt(0)\n"
    "buffer_store_dwordx4 v[76:79], v98, %[rws], %[pd] offen sc1\n"
    "s_add_u32 %[pd], %[pd], %[pdi]\n"
    "v_exp_f32 v28, v28\n"
    "v_cvt_pk_bf16_f32 v54, v24, v25\n"
    "v_exp_f32 v29, v29\n"
    "v_cvt_pk_bf16_f32 v55, v26, v27\n"
    "s_mov_b32 m0, %[pm]\n"
    "s_add_u32 %[t0], %[pa], %[pi]\n"
    "buffer_load_dwordx4 v97, %[rpt], %[pa] offen lds\n"
    "s_mov_b32 %[pa], %[pb]\n"
    "s_mov_b32 %[pb], %[t0]\n"
    "s_xor_b32 %[pm], %[pm], 1024\n"
    "v_exp_f32 v30, v30\n"
    "v_exp_f32 v31, v31\n"
    "v_mfma_f32_32x32x16_bf16 v[36:51], v[232:235], v[52:55], v[36:51]\n"
    "v_xor_b32 v96, 1024, v96\n"
    "v_exp_f32 v58, v32\n"
    "v_cvt_pk_bf16_f32 v56, v28, v29\n"
    "v_exp_f32 v52, v33\n"
    "v_cvt_pk_bf16_f32 v57, v30, v31\n"
    "v_exp_f32 v59, v34\n"
    "v_exp_f32 v53, v35\n"
    "s_setprio 3\n"
    "v_mfma_f32_32x32x16_bf16 v[20:35], v[160:163], v[60:63], 0\n"
    "ds_read_b128 v[60:63], v89 offset:3584\n"
    "v_exp_f32 v4, v4\n"
    "v_cvt_pk_bf16_f32 v58, v58, v52\n"
    "ds_read_b32 v1, v0\n"
    "v_exp_f32 v5, v5\n"
    "v_cvt_pk_bf16_f32 v59, v59, v53\n"
    "v_exp_f32 v6, v6\n"
    "v_exp_f32 v7, v7\n"
    "v_mfma_f32_32x32x16_bf16 v[36:51], v[236:239], v[56:59], v[36:51]\n"
    "v_exp_f32 v8, v8\n"
    "v_cvt_pk_bf16_f32 v52, v4, v5\n"
    "s_waitcnt lgkmcnt(0)\n"
    "v_cmp_gt_u32 vcc, %[nd], v1\n"
    "s_cbranch_vccnz LPFY3_%=\n"
    "LRFY3_%=:\n"
    "ds_read_b128 v[72:75], v90 offset:0\n"
    "ds_read_b128 v[68:71], v88 offset:41472\n"
    "v_exp_f32 v9, v9\n"
    "v_cvt_pk_bf16_f32 v53, v6, v7\n"
    "v_exp_f32 v10, v10\n"
    "v_exp_f32 v11, v11\n"
    "v_mfma_f32_32x32x8_bf16 v[20:35], v[158:159], v[64:65], v[20:35]\n"
    "ds_read_b128 v[64:67], v89 offset:5120\n"
    "v_exp_f32 v12, v12\n"
    "v_cvt_pk_bf16_f32 v54, v8, v9\n"
    "s_add_u32 %[wo], %[wo], 73728\n"
    "s_add_u32 %[wo4], %[wo4], 73728\n"
    "s_add_u32 %[wo8], %[wo8], 73728\n"
    "v_exp_f32 v13, v13\n"
    "v_cvt_pk_bf16_f32 v55, v10, v11\n"
    "v_exp_f32 v14, v14\n"
    "v_exp_f32 v15, v15\n"
    "v_mfma_f32_32x32x16_bf16 v[36:51], v[240:243], v[52:55], v[36:51]\n"
    "v_exp_f32 v58, v16\n"
    "v_cvt_pk_bf16_f32 v56, v12, v13\n"
    "v_exp_f32 v52, v17\n"
    "v_cvt_pk_bf16_f32 v57, v14, v15\n"
    "s_waitcnt vmcnt(11) lgkmcnt(2)\n"
    "v_mfma_f32_16x16x32_bf16 v[116:119], v[136:139], v[72:75], v[116:119]\n"
    "v_exp_f32 v59, v18\n"
    "v_exp_f32 v53, v19\n"
    "v_mfma_f32_32x32x16_bf16 v[4:19], v[164:167], v[60:63], 0\n"
    "ds_read_b128 v[72:75], v88 offset:42496\n"
    "v_exp_f32 v20, v20\n"
    "v_cvt_pk_bf16_f32 v58, v58, v52\n"
    "buffer_load_dwordx4 v[136:139], v88, %[rws], %[wo4] offen offset:0 sc1\n"
    "v_exp_f32 v21, v21\n"
    "v_cvt_pk_bf16_f32 v59, v59, v53\n"
    "s_waitcnt vmcnt(11) lgkmcnt(2)\n"
    "v_mfma_f32_16x16x32_bf16 v[100:103], v[120:123], v[68:71], v[100:103]\n"
    "v_exp_f32 v22, v22\n"
    "v_exp_f32 v23, v23\n"
    "v_mfma_f32_32x32x16_bf16 v[36:51], v[244:247], v[56:59], v[36:51]\n"
    "v_exp_f32 v24, v24\n"
    "v_cvt_pk_bf16_f32 v52, v20, v21\n"
    "v_exp_f32 v25, v25\n"
    "v_cvt_pk_bf16_f32 v53, v22, v23\n"
    "s_waitcnt vmcnt(10)\n"
    "v_mfma_f32_16x16x32_bf16 v[104:107], v[124:127], v[68:71], v[104:107]\n"
    "v_exp_f32 v26, v26\n"
    "v_exp_f32 v27, v27\n"
    "s_waitcnt lgkmcnt(1)\n"
    "v_mfma_f32_32x32x8_bf16 v[4:19], v[156:157], v[64:65], v[4:19]\n"
    "v_exp_f32 v28, v28\n"
    "v_cvt_pk_bf16_f32 v54, v24, v25\n"
    "v_exp_f32 v29, v29\n"
    "v_cvt_pk_bf16_f32 v55, v26, v27\n"
    "s_waitcnt vmcnt(9)\n"
    "v_mfma_f32_16x16x32_bf16 v[108:111], v[128:131], v[68:71], v[108:111]\n"
    "v_exp_f32 v30, v30\n"
    "v_exp_f32 v31, v31\n"
    "v_mfma_f32_32x32x16_bf16 v[36:51], v[248:251], v[52:55], v[36:51]\n"
    "v_exp_f32 v58, v32\n"
    "v_cvt_pk_bf16_f32 v56, v28, v29\n"
    "buffer_load_dwordx4 v[120:123], v88, %[rws], %[wo] offen offset:0 sc1\n"
    "v_exp_f32 v52, v33\n"
    "v_cvt_pk_bf16_f32 v57, v30, v31\n"
    "s_waitcnt vmcnt(9)\n"
    "v_mfma_f32_16x16x32_bf16 v[112:115], v[132:135], v[68:71], v[112:115]\n"
    "v_exp_f32 v59, v34\n"
    "v_exp_f32 v53, v35\n"
    "v_mfma_f32_32x32x16_bf16 v[20:35], v[168:171], v[60:63], 0\n"
    "v_exp_f32 v4, v4\n"
    "v_cvt_pk_bf16_f32 v58, v58, v52\n"
    "v_exp_f32 v5, v5\n"
    "v_cvt_pk_bf16_f32 v59, v59, v53\n"
    "s_waitcnt vmcnt(8) lgkmcnt(0)\n"
    "v_mfma_f32_16x16x32_bf16 v[100:103], v[140:143], v[72:75], v[100:103]\n"
    "v_exp_f32 v6, v6\n"
    "v_exp_f32 v7, v7\n"
    "v_mfma_f32_32x32x16_bf16 v[36:51], v[252:255], v[56:59], v[36:51]\n"
    "v_exp_f32 v8, v8\n"
    "v_cvt_pk_bf16_f32 v52, v4, v5\n"
    "v_exp_f32 v9, v9\n"
    "v_cvt_pk_bf16_f32 v53, v6, v7\n"
    "s_waitcnt vmcnt(7)\n"
    "v_mfma_f32_16x16x32_bf16 v[104:107], v[144:147], v[72:75], v[104:107]\n"
    "v_exp_f32 v10, v10\n"
    "v_exp_f32 v11, v11\n"
    "s_setprio 1\n"
    "v_mfma_f32_32x32x8_bf16 v[20:35], v[158:159], v[64:65], v[20:35]\n"
    "ds_read_b128 v[64:67], v89 offset:6144\n"
    "v_exp_f32 v12, v12\n"
    "v_cvt_pk_bf16_f32 v54, v8, v9\n"
    "buffer_load_dwordx4 v[124:127], v88, %[rws], %[wo] offen offset:1024 sc1\n"
    "v_exp_f32 v13, v13\n"
    "v_cvt_pk_bf16_f32 v55, v10, v11\n"
    "s_waitcnt vmcnt(7)\n"
    "v_mfma_f32_16x16x32_bf16 v[108:111], v[148:151], v[72:75], v[108:111]\n"
    "v_exp_f32 v14, v14\n"
    "v_exp_f32 v15, v15\n"
    "ds_write_b128 v94, v[36:39]\n"
    "ds_write_b128 v94, v[40:43] offset:1024\n"
    "ds_write_b64 v91, v[44:45]\n"
    "v_mfma_f32_32x32x16_bf16 v[36:51], v[192:195], v[52:55], 0\n"
    "v_exp_f32 v58, v16\n"
    "v_cvt_pk_bf16_f32 v56, v12, v13\n"
    "v_add_u32 v89, 3584, v89\n"
    "v_exp_f32 v52, v17\n"
    "v_cvt_pk_bf16_f32 v57, v14, v15\n"
    "s_waitcnt vmcnt(6)\n"
    "v_mfma_f32_16x16x32_bf16 v[112:115], v[152:155], v[72:75], v[112:115]\n"
    "v_exp_f32 v59, v18\n"
    "v_exp_f32 v53, v19\n"
    "s_sub_u32 %[cnt], %[cnt], 1\n"
    "s_cmp_lg_u32 %[cnt], 1\n"
    "s_cbranch_scc1 LBFY_%=\n"
    "s_add_u32 %[pa], %[wo], 73728\n"
    "s_add_u32 %[pb], %[wo4], 73728\n"
    "s_add_u32 %[pm], %[wo8], 73728\n"
    "v_mfma_f32_32x32x16_bf16 v[4:19], v[172:175], v[60:63], 0\n"
    "s_bitcmp1_b32 %[xo], 31\n"
    "s_cbranch_scc1 LPFY4_%=\n"
    "LRFY4_%=:\n"
    "v_exp_f32 v20, v20\n"
    "LPP5:\n"
    "s_cbranch_execz LPK1\n"
    "v_cvt_pk_bf16_f32 v58, v58, v52\n"
    "buffer_load_dwordx4 v[168:171], v88, %[rws], %[pa] offen offset:1024 sc1\n"
    "v_exp_f32 v21, v21\n"
    "v_cvt_pk_bf16_f32 v59, v59, v53\n"
    "v_exp_f32 v22, v22\n"
    "v_exp_f32 v23, v23\n"
    "v_mfma_f32_32x32x16_bf16 v[36:51], v[196:199], v[56:59], v[36:51]\n"
    "s_waitcnt lgkmcnt(0)\n"
    "s_mov_b64 exec, %[me]\n"
    "ds_inc_u32 v0, v0\n"
    "s_mov_b64 exec, -1\n"
    "s_add_u32 %[nd], %[nd], 1\n"
    "v_exp_f32 v24, v24\n"
    "v_cvt_pk_bf16_f32 v52, v20, v21\n"
    "buffer_load_dwordx4 v[128:131], v88, %[rws], %[wo] offen offset:2048 sc1\n"
    "v_exp_f32 v25, v25\n"
    "v_cvt_pk_bf16_f32 v53, v22, v23\n"
    "v_exp_f32 v26, v26\n"
    "v_exp_f32 v27, v27\n"
    "v_mfma_f32_32x32x8_bf16 v[4:19], v[156:157], v[64:65], v[4:19]\n"
    "s_waitcnt vmcnt(17)\n"
    "ds_read_b64_tr_b16 v[76:77], v96\n"
    "ds_read_b64_tr_b16 v[78:79], v96 offset:512\n"
    "v_exp_f32 v28, v28\n"
    "v_cvt_pk_bf16_f32 v54, v24, v25\n"
    "v_exp_f32 v29, v29\n"
    "v_cvt_pk_bf16_f32 v55, v26, v27\n"
    "v_exp_f32 v30, v30\n"
    "v_exp_f32 v31, v31\n"
    "v_mfma_f32_32x32x16_bf16 v[36:51], v[200:203], v[52:55], v[36:51]\n"
    "v_exp_f32 v58, v32\n"
    "v_cvt_pk_bf16_f32 v56, v28, v29\n"
    "buffer_load_dwordx4 v[192:195], v88, %[rws], %[pa] offen offset:2048 sc1\n"
    "v_exp_f32 v52, v33\n"
    "v_cvt_pk_bf16_f32 v57, v30, v31\n"
    "v_add_u32 v1, %[w8b], v88\n"
    "s_add_u32 %[w8b], %[w8b], 1024\n"
    "s_waitcnt vmcnt(8)\n"
    "ds_write_b128 v1, v[80:83]\n"
    "v_exp_f32 v59, v34\n"
    "v_exp_f32 v53, v35\n"
    "v_mfma_f32_32x32x16_bf16 v[20:35], v[176:179], v[60:63], 0\n"
    "s_waitcnt lgkmcnt(1)\n"
    "buffer_store_dwordx4 v[76:79], v98, %[rws], %[pd] offen sc1\n"
    "s_add_u32 %[pd], %[pd], %[pdi]\n"
    "v_xor_b32 v96, 1024, v96\n"
    "v_exp_f32 v4, v4\n"
    "v_cvt_pk_bf16_f32 v58, v58, v52\n"
    "buffer_load_dwordx4 v[132:135], v88, %[rws], %[wo] offen offset:3072 sc1\n"
    "v_exp_f32 v5, v5\n"
    "v_cvt_pk_bf16_f32 v59, v59, v53\n"
    "v_exp_f32 v6, v6\n"
    "v_exp_f32 v7, v7\n"
    "v_mfma_f32_32x32x16_bf16 v[36:51], v[204:207], v[56:59], v[36:51]\n"
    "v_exp_f32 v8, v8\n"
    "v_cvt_pk_bf16_f32 v52, v4, v5\n"
    "v_exp_f32 v9, v9\n"
    "v_cvt_pk_bf16_f32 v53, v6, v7\n"
    "v_exp_f32 v10, v10\n"
    "v_exp_f32 v11, v11\n"
    "s_setprio 3\n"
    "v_mfma_f32_32x32x8_bf16 v[20:35], v[158:159], v[64:65], v[20:35]\n"
    "ds_read_b128 v[64:67], v89 offset:1024\n"
    "v_exp_f32 v12, v12\n"
    "v_cvt_pk_bf16_f32 v54, v8, v9\n"
    "ds_read_b32 v1, v0\n"
    "v_exp_f32 v13, v13\n"
    "v_cvt_pk_bf16_f32 v55, v10, v11\n"
    "buffer_load_dwordx4 v[140:143], v88, %[rws], %[wo4] offen offset:1024 sc1\n"
    "v_exp_f32 v14, v14\n"
    "v_exp_f32 v15, v15\n"
    "v_mfma_f32_32x32x16_bf16 v[36:51], v[208:211], v[52:55], v[36:51]\n"
    "v_exp_f32 v58, v16\n"
    "v_cvt_pk_bf16_f32 v56, v12, v13\n"
    "s_waitcnt lgkmcnt(0)\n"
    "v_cmp_gt_u32 vcc, %[nd], v1\n"
    "s_cbranch_vccnz LPFY6_%=\n"
    "LRFY6_%=:\n"
    "ds_read2st64_b32 v[84:85], v92 offset0:1 offset1:3\n"
    "ds_read2st64_b32 v[86:87], v92 offset0:5 offset1:7\n"
    "ds_read2st64_b64 v[76:79], v95 offset1:4\n"
    "ds_read2st64_b64 v[80:83], v95 offset0:8 offset1:12\n"
    "ds_read2st64_b64 v[68:71], v92 offset1:1\n"
    "ds_read2st64_b64 v[72:75], v92 offset0:2 offset1:3\n"
    "v_exp_f32 v52, v17\n"
    "v_cvt_pk_bf16_f32 v57, v14, v15\n"
    "v_exp_f32 v59, v18\n"
    "v_exp_f32 v53, v19\n"
    "v_mfma_f32_32x32x16_bf16 v[4:19], v[180:183], v[60:63], 0\n"
    "buffer_load_dwordx4 v[172:175], v88, %[rws], %[pa] offen offset:3072 sc1\n"
    "v_exp_f32 v20, v20\n"
    "v_cvt_pk_bf16_f32 v58, v58, v52\n"
    "v_exp_f32 v21, v21\n"
    "v_cvt_pk_bf16_f32 v59, v59, v53\n"
    "v_exp_f32 v22, v22\n"
    "v_exp_f32 v23, v23\n"
    "v_mfma_f32_32x32x16_bf16 v[36:51], v[212:215], v[56:59], v[36:51]\n"
    "s_waitcnt lgkmcnt(5)\n"
    "v_add_f32 v84, v84, v85\n"
    "s_waitcnt lgkmcnt(4)\n"
    "v_add_f32 v86, v86, v87\n"
    "v_exp_f32 v24, v24\n"
    "v_cvt_pk_bf16_f32 v52, v20, v21\n"
    "buffer_load_dwordx4 v[144:147], v88, %[rws], %[wo4] offen offset:2048 sc1\n"
    "v_exp_f32 v25, v25\n"
    "v_cvt_pk_bf16_f32 v53, v22, v23\n"
    "v_add_f32 v84, v84, v86\n"
    "v_exp_f32 v26, v26\n"
    "s_waitcnt lgkmcnt(3)\n"
    "v_add_f32 v76, v76, v78\n"
    "v_exp_f32 v27, v27\n"
    "v_mfma_f32_32x32x8_bf16 v[4:19], v[156:157], v[64:65], v[4:19]\n"
    "s_waitcnt vmcnt(6)\n"
    "s_bitcmp1_b32 %[xo], 31\n"
    "s_cbranch_scc1 LPFY5_%=\n"
    "LRFY5_%=:\n"
    "v_add_f32 v77, v77, v79\n"
    "v_exp_f32 v28, v28\n"
    "v_cvt_pk_bf16_f32 v54, v24, v25\n"
    "v_exp_f32 v29, v29\n"
    "v_cvt_pk_bf16_f32 v55, v26, v27\n"
    "v_add_f32_dpp v84, v84, v84 quad_perm:[1,0,3,2] row_mask:0xf bank_mask:0xf\n"
    "buffer_load_dwordx4 v[164:167], v88, %[rws], %[pa] offen offset:0 sc1\n"
    "v_exp_f32 v30, v30\n"
    "s_waitcnt lgkmcnt(2)\n"
    "v_add_f32 v80, v80, v82\n"
    "v_exp_f32 v31, v31\n"
    "v_mfma_f32_32x32x16_bf16 v[36:51], v[216:219], v[52:55], v[36:51]\n"
    "v_rcp_f32 v84, v84\n"
    "v_exp_f32 v58, v32\n"
    "v_cvt_pk_bf16_f32 v56, v28, v29\n"
    "v_exp_f32 v52, v33\n"
    "v_cvt_pk_bf16_f32 v57, v30, v31\n"
    "v_add_f32 v81, v81, v83\n"
    "v_exp_f32 v59, v34\n"
    "v_add_f32 v76, v76, v80\n"
    "v_exp_f32 v53, v35\n"
    "v_mfma_f32_32x32x16_bf16 v[20:35], v[184:187], v[60:63], 0\n"
    "v_add_f32 v77, v77, v81\n"
    "v_exp_f32 v4, v4\n"
    "v_cvt_pk_bf16_f32 v58, v58, v52\n"
    "buffer_load_dwordx4 v[148:151], v88, %[rws], %[wo4] offen offset:3072 sc1\n"
    "v_exp_f32 v5, v5\n"
    "v_cvt_pk_bf16_f32 v59, v59, v53\n"
    "v_mul_f32 v76, v76, v84\n"
    "v_exp_f32 v6, v6\n"
    "v_mul_f32 v77, v77, v84\n"
    "v_exp_f32 v7, v7\n"
    "v_mfma_f32_32x32x16_bf16 v[36:51], v[220:223], v[56:59], v[36:51]\n"
    "v_cvt_pk_bf16_f32 v76, v76, v77\n"
    "v_exp_f32 v8, v8\n"
    "v_cvt_pk_bf16_f32 v52, v4, v5\n"
    "ds_write_b32 v93, v76\n"
    "v_exp_f32 v9, v9\n"
    "v_cvt_pk_bf16_f32 v53, v6, v7\n"
    "s_cmp_lg_u32 %[lw], %[cnt]\n"
    "s_cbranch_scc1 LLFY2_%=\n"
    "s_waitcnt lgkmcnt(2)\n"
    "v_add_f32 v68, v68, v70\n"
    "v_add_f32 v69, v69, v71\n"
    "s_waitcnt lgkmcnt(1)\n"
    "v_add_f32 v72, v72, v74\n"
    "v_add_f32 v73, v73, v75\n"
    "v_add_f32 v68, v68, v72\n"
    "v_add_f32 v69, v69, v73\n"
    "v_and_b32 v70, 0xf0, v93\n"
    "v_lshrrev_b32 v71, 8, v88\n"
    "v_mul_f32 v68, v68, v84\n"
    "v_mul_f32 v69, v69, v84\n"
    "v_lshl_add_u32 v70, v71, 2, v70\n"
    "v_cvt_pk_bf16_f32 v68, v68, v69\n"
    "v_add_u32 v70, %[lo], v70\n"
    "ds_write_b32 v70, v68\n"
    "LLFY2_%=:\n"
    "v_exp_f32 v10, v10\n"
    "buffer_load_dwordx4 v[196:199], v88, %[rws], %[wm] offen sc1\n"
    "s_add_u32 %[wm], %[wm], 36864\n"
    "v_exp_f32 v11, v11\n"
    "s_setprio 1\n"
    "v_mfma_f32_32x32x8_bf16 v[20:35], v[158:159], v[64:65], v[20:35]\n"
    "ds_read_b128 v[64:67], v89 offset:2048\n"
    "v_exp_f32 v12, v12\n"
    "v_cvt_pk_bf16_f32 v54, v8, v9\n"
    "v_exp_f32 v13, v13\n"
    "v_cvt_pk_bf16_f32 v55, v10, v11\n"
    "buffer_load_dwordx4 v[152:155], v88, %[rws], %[wo8] offen offset:0 sc1\n"
    "v_exp_f32 v14, v14\n"
    "v_exp_f32 v15, v15\n"
    "v_mfma_f32_32x32x16_bf16 v[36:51], v[224:227], v[52:55], v[36:51]\n"
    "s_waitcnt lgkmcnt(1)\n"
    "s_mov_b64 exec, %[me]\n"
    "ds_inc_u32 v0, v0\n"
    "s_mov_b64 exec, -1\n"
    "s_add_u32 %[nd], %[nd], 1\n"
    "v_exp_f32 v58, v16\n"
    "v_cvt_pk_bf16_f32 v56, v12, v13\n"
    "v_exp_f32 v52, v17\n"
    "v_cvt_pk_bf16_f32 v57, v14, v15\n"
    "v_exp_f32 v59, v18\n"
    "v_exp_f32 v53, v19\n"
    "v_mfma_f32_32x32x16_bf16 v[4:19], v[188:191], v[60:63], 0\n"
    "ds_read_b64_tr_b16 v[76:77], v96\n"
    "ds_read_b64_tr_b16 v[78:79], v96 offset:512\n"
    "v_exp_f32 v20, v20\n"
    "v_cvt_pk_bf16_f32 v58, v58, v52\n"
    "buffer_load_dwordx4 v[200:203], v88, %[rws], %[pb] offen offset:1024 sc1\n"
    "v_exp_f32 v21, v21\n"
    "v_cvt_pk_bf16_f32 v59, v59, v53\n"
    "v_add_u32 v89, %[wl], v88\n"
    "v_exp_f32 v22, v22\n"
    "v_exp_f32 v23, v23\n"
    "v_mfma_f32_32x32x16_bf16 v[36:51], v[228:231], v[56:59], v[36:51]\n"
    "v_exp_f32 v24, v24\n"
    "v_cvt_pk_bf16_f32 v52, v20, v21\n"
    "v_exp_f32 v25, v25\n"
    "v_cvt_pk_bf16_f32 v53, v22, v23\n"
    "v_exp_f32 v26, v26\n"
    "v_exp_f32 v27, v27\n"
    "s_waitcnt lgkmcnt(3)\n"
    "v_mfma_f32_32x32x8_bf16 v[4:19], v[156:157], v[64:65], v[4:19]\n"
    "s_waitcnt lgkmcnt(0)\n"
    "buffer_store_dwordx4 v[76:79], v98, %[rws], %[pd] offen sc1\n"
    "s_add_u32 %[pd], %[pd], %[pdi]\n"
    "v_exp_f32 v28, v28\n"
    "v_cvt_pk_bf16_f32 v54, v24, v25\n"
    "v_exp_f32 v29, v29\n"
    "v_cvt_pk_bf16_f32 v55, v26, v27\n"
    "v_lshrrev_b32 v84, 5, v88\n"
    "v_exp_f32 v30, v30\n"
    "v_and_or_b32 v80, v84, 24, %[m1]\n"
    "v_exp_f32 v31, v31\n"
    "v_mfma_f32_32x32x16_bf16 v[36:51], v[232:235], v[52:55], v[36:51]\n"
    "v_exp_f32 v58, v32\n"
    "v_cvt_pk_bf16_f32 v56, v28, v29\n"
    "buffer_load_dwordx4 v[188:191], v88, %[rws], %[wm] offen sc1\n"
    "s_movk_i32 %[wm], 1152\n"
    "s_sub_u32 %[w8b], %[w8b], 7168\n"
    "v_add_u32 v96, %[w8b], v88\n"
    "v_exp_f32 v52, v33\n"
    "v_cvt_pk_bf16_f32 v57, v30, v31\n"
    "v_and_or_b32 v81, v84, 24, %[m2]\n"
    "v_exp_f32 v59, v34\n"
    "v_and_b32 v84, 4, v84\n"
    "v_exp_f32 v53, v35\n"
    "s_setprio 3\n"
    "v_mfma_f32_32x32x16_bf16 v[20:35], v[160:163], v[60:63], 0\n"
    "v_exp_f32 v4, v4\n"
    "v_cvt_pk_bf16_f32 v58, v58, v52\n"
    "ds_read_b32 v1, v0\n"
    "v_exp_f32 v5, v5\n"
    "v_cvt_pk_bf16_f32 v59, v59, v53\n"
    "v_bfe_u32 v83, v88, 4, 3\n"
    "buffer_load_dwordx4 v[204:207], v88, %[rws], %[pb] offen offset:3072 sc1\n"
    "v_exp_f32 v6, v6\n"
    "v_lshl_add_u32 v80, v84, 7, v80\n"
    "ds_read_b128 v[216:219], v96 offset:0\n"
    "v_exp_f32 v7, v7\n"
    "v_mfma_f32_32x32x16_bf16 v[36:51], v[236:239], v[56:59], v[36:51]\n"
    "v_exp_f32 v8, v8\n"
    "v_cvt_pk_bf16_f32 v52, v4, v5\n"
    "s_waitcnt lgkmcnt(1)\n"
    "v_cmp_gt_u32 vcc, %[nd], v1\n"
    "s_cbranch_vccnz LPFY7_%=\n"
    "LRFY7_%=:\n"
    "ds_read_b128 v[72:75], v90 offset:0\n"
    "ds_read_b128 v[68:71], v88 offset:41472\n"
    "v_exp_f32 v9, v9\n"
    "v_cvt_pk_bf16_f32 v53, v6, v7\n"
    "v_mad_u32_u24 v82, v83, %[wm], v80\n"
    "v_exp_f32 v10, v10\n"
    "v_mad_u32_u24 v83, v83, %[wm], v81\n"
    "ds_read_b128 v[184:187], v96 offset:1024\n"
    "v_exp_f32 v11, v11\n"
    "v_mfma_f32_32x32x8_bf16 v[20:35], v[158:159], v[64:65], v[20:35]\n"
    "v_exp_f32 v12, v12\n"
    "v_cvt_pk_bf16_f32 v54, v8, v9\n"
    "buffer_load_dwordx4 v[180:183], v88, %[rws], %[w8g] offen offset:0 sc1\n"
    "v_exp_f32 v13, v13\n"
    "v_cvt_pk_bf16_f32 v55, v10, v11\n"
    "v_exp_f32 v14, v14\n"
    "ds_read_b128 v[220:223], v96 offset:2048\n"
    "v_exp_f32 v15, v15\n"
    "v_mfma_f32_32x32x16_bf16 v[36:51], v[240:243], v[52:55], v[36:51]\n"
    "v_exp_f32 v58, v16\n"
    "v_cvt_pk_bf16_f32 v56, v12, v13\n"
    "buffer_load_dwordx4 v[176:179], v88, %[rws], %[pb] offen offset:2048 sc1\n"
    "v_exp_f32 v52, v17\n"
    "v_cvt_pk_bf16_f32 v57, v14, v15\n"
    "s_waitcnt lgkmcnt(3)\n"
    "v_mfma_f32_16x16x32_bf16 v[116:119], v[136:139], v[72:75], v[116:119]\n"
    "v_exp_f32 v59, v18\n"
    "ds_read_b128 v[224:227], v96 offset:3072\n"
    "v_exp_f32 v53, v19\n"
    "s_setprio 1\n"
    "ds_read_b128 v[72:75], v88 offset:42496\n"
    "v_exp_f32 v20, v20\n"
    "ds_read2_b64 v[4:7], v80 offset1:32\n"
    "v_cvt_pk_bf16_f32 v58, v58, v52\n"
    "ds_read2_b64 v[16:19], v81 offset1:144\n"
    "buffer_load_dwordx4 v[208:211], v88, %[rws], %[pm] offen offset:0 sc1\n"
    "v_exp_f32 v21, v21\n"
    "ds_read_b64 v[60:61], v82 offset:4096\n"
    "v_cvt_pk_bf16_f32 v59, v59, v53\n"
    "s_waitcnt lgkmcnt(7)\n"
    "v_mfma_f32_16x16x32_bf16 v[100:103], v[120:123], v[68:71], v[100:103]\n"
    "v_exp_f32 v22, v22\n"
    "v_exp_f32 v23, v23\n"
    "v_mfma_f32_32x32x16_bf16 v[36:51], v[244:247], v[56:59], v[36:51]\n"
    "v_exp_f32 v24, v24\n"
    "ds_read_b64 v[62:63], v82 offset:4352\n"
    "v_cvt_pk_bf16_f32 v52, v20, v21\n"
    "ds_read_b64 v[66:67], v83 offset:4096\n"
    "v_exp_f32 v25, v25\n"
    "ds_read2_b64 v[8:11], v80 offset0:144 offset1:176\n"
    "v_cvt_pk_bf16_f32 v53, v22, v23\n"
    "buffer_load_dwordx4 v[212:215], v88, %[rws], %[w8g] offen offset:1024 sc1\n"
    "v_mfma_f32_16x16x32_bf16 v[104:107], v[124:127], v[68:71], v[104:107]\n"
    "v_exp_f32 v26, v26\n"
    "ds_read_b128 v[228:231], v96 offset:5120\n"
    "v_exp_f32 v27, v27\n"
    "v_exp_f32 v28, v28\n"
    "ds_read_b64 v[12:13], v80 offset:2304\n"
    "v_cvt_pk_bf16_f32 v54, v24, v25\n"
    "ds_read_b64 v[14:15], v80 offset:2560\n"
    "v_exp_f32 v29, v29\n"
    "ds_read_b64 v[64:65], v81 offset:2304\n"
    "v_cvt_pk_bf16_f32 v55, v26, v27\n"
    "v_mfma_f32_16x16x32_bf16 v[108:111], v[128:131], v[68:71], v[108:111]\n"
    "v_exp_f32 v30, v30\n"
    "ds_read_b128 v[232:235], v96 offset:6144\n"
    "v_exp_f32 v31, v31\n"
    "v_mfma_f32_32x32x16_bf16 v[36:51], v[248:251], v[52:55], v[36:51]\n"
    "v_exp_f32 v58, v32\n"
    "ds_read_b128 v[156:159], v89\n"
    "v_cvt_pk_bf16_f32 v56, v28, v29\n"
    "ds_read_b128 v[160:163], v89 offset:28672\n"
    "v_exp_f32 v52, v33\n"
    "ds_write_b128 v94, v[116:119] offset:20480\n"
    "v_cvt_pk_bf16_f32 v57, v30, v31\n"
    "s_waitcnt vmcnt(15)\n"
    "v_mfma_f32_16x16x32_bf16 v[112:115], v[132:135], v[68:71], v[112:115]\n"
    "v_exp_f32 v59, v34\n"
    "v_exp_f32 v53, v35\n"
    "v_cvt_pk_bf16_f32 v58, v58, v52\n"
    "v_cvt_pk_bf16_f32 v59, v59, v53\n"
    "ds_read_b128 v[20:23], v88 offset:43520\n"
    "s_setprio 0\n"
    "v_mfma_f32_32x32x16_bf16 v[36:51], v[252:255], v[56:59], v[36:51]\n"
    "s_waitcnt vmcnt(14) lgkmcnt(15)\n"
    "v_mfma_f32_16x16x32_bf16 v[100:103], v[140:143], v[72:75], v[100:103]\n"
    "v_and_b32 v32, 0x70, v88\n"
    "v_xor_b32 v35, 8192, v94\n"
    "v_lshrrev_b32 v33, 9, v88\n"
    "s_waitcnt vmcnt(12)\n"
    "v_mfma_f32_16x16x32_bf16 v[104:107], v[144:147], v[72:75], v[104:107]\n"
    "v_mul_u32_u24 v34, 5, v32\n"
    "v_lshl_or_b32 v32, v33, 8, v32\n"
    "v_lshl_add_u32 v33, v33, 2, v34\n"
    "s_waitcnt vmcnt(10)\n"
    "v_mfma_f32_16x16x32_bf16 v[108:111], v[148:151], v[72:75], v[108:111]\n"
    "v_bfe_u32 v1, v88, 8, 1\n"
    "v_add_u32 v33, %[w64], v33\n"
    "v_lshl_or_b32 v32, v1, 3, v32\n"
    "s_waitcnt vmcnt(8)\n"
    "v_mfma_f32_16x16x32_bf16 v[112:115], v[152:155], v[72:75], v[112:115]\n"
    "v_and_b32 v1, 128, v88\n"
    "v_lshl_or_b32 v32, v1, 6, v32\n"
    "ds_write_b128 v94, v[36:39]\n"
    "ds_write_b128 v94, v[40:43] offset:1024\n"
    "ds_write_b64 v91, v[44:45]\n"
    "s_waitcnt lgkmcnt(3)\n"
    "v_mfma_f32_16x16x32_bf16 v[100:103], v[216:219], v[20:23], v[100:103]\n"
    "v_mfma_f32_16x16x32_bf16 v[104:107], v[184:187], v[20:23], v[104:107]\n"
    "v_mfma_f32_16x16x32_bf16 v[108:111], v[220:223], v[20:23], v[108:111]\n"
    "v_mfma_f32_16x16x32_bf16 v[112:115], v[224:227], v[20:23], v[112:115]\n"
    "s_mov_b64 %[me], 0x3fb8aa3b\n"
    "s_bitset0_b32 %[xo], 31\n"
    "s_cmp_eq_u32 %[lw], 0\n"
    "s_waitcnt lgkmcnt(0)\n"
    "s_barrier\n"
    "s_cbranch_scc1 LTFY_%=\n"
    "ds_read2st64_b32 v[84:85], v92 offset0:1 offset1:3\n"
    "ds_read2st64_b32 v[86:87], v92 offset0:5 offset1:7\n"
    "ds_read2st64_b64 v[76:79], v95 offset1:4\n"
    "ds_read2st64_b64 v[80:83], v95 offset0:8 offset1:12\n"
    "v_lshlrev_b32 v144, 16, v8\n"
    "v_and_b32 v145, 0xffff0000, v8\n"
    "v_lshlrev_b32 v146, 16, v9\n"
    "v_and_b32 v147, 0xffff0000, v9\n"
    "v_lshlrev_b32 v148, 16, v10\n"
    "v_and_b32 v149, 0xffff0000, v10\n"
    "v_lshlrev_b32 v150, 16, v11\n"
    "v_and_b32 v151, 0xffff0000, v11\n"
    "v_lshlrev_b32 v244, 16, v18\n"
    "v_and_b32 v245, 0xffff0000, v18\n"
    "v_lshlrev_b32 v246, 16, v19\n"
    "v_and_b32 v247, 0xffff0000, v19\n"
    "v_pk_mul_f32 v[144:145], v[144:145], %[me] op_sel_hi:[1,0]\n"
    "v_pk_mul_f32 v[146:147], v[146:147], %[me] op_sel_hi:[1,0]\n"
    "v_pk_mul_f32 v[148:149], v[148:149], %[me] op_sel_hi:[1,0]\n"
    "v_pk_mul_f32 v[150:151], v[150:151], %[me] op_sel_hi:[1,0]\n"
    "s_waitcnt lgkmcnt(3)\n"
    "v_add_f32 v84, v84, v85\n"
    "s_waitcnt lgkmcnt(2)\n"
    "v_add_f32 v86, v86, v87\n"
    "v_add_f32 v84, v84, v86\n"
    "s_waitcnt lgkmcnt(1)\n"
    "v_add_f32 v76, v76, v78\n"
    "ds_read_b128 v[28:31], v35 offset:20480\n"
    "v_add_f32 v77, v77, v79\n"
    "v_add_f32_dpp v84, v84, v84 quad_perm:[1,0,3,2] row_mask:0xf bank_mask:0xf\n"
    "s_waitcnt lgkmcnt(1)\n"
    "v_add_f32 v80, v80, v82\n"
    "v_rcp_f32 v84, v84\n"
    "v_add_f32 v81, v81, v83\n"
    "v_add_f32 v76, v76, v80\n"
    "v_add_f32 v77, v77, v81\n"
    "v_mul_f32 v76, v76, v84\n"
    "v_mul_f32 v77, v77, v84\n"
    "v_cvt_pk_bf16_f32 v76, v76, v77\n"
    "ds_write_b32 v93, v76\n"
    "s_waitcnt lgkmcnt(1)\n"
    "v_pk_add_f32 v[116:117], v[116:117], v[28:29]\n"
    "v_pk_add_f32 v[118:119], v[118:119], v[30:31]\n"
    "v_pk_mul_f32 v[244:245], v[244:245], %[me] op_sel_hi:[1,0]\n"
    "v_pk_mul_f32 v[246:247], v[246:247], %[me] op_sel_hi:[1,0]\n"
    "v_lshlrev_b32 v120, 16, v4\n"
    "v_and_b32 v121, 0xffff0000, v4\n"
    "v_lshlrev_b32 v122, 16, v5\n"
    "v_and_b32 v123, 0xffff0000, v5\n"
    "v_lshlrev_b32 v124, 16, v6\n"
    "v_and_b32 v125, 0xffff0000, v6\n"
    "v_lshlrev_b32 v126, 16, v7\n"
    "v_and_b32 v127, 0xffff0000, v7\n"
    "LTFY_%=j:\n"
    "v_mfma_f32_16x16x32_bf16 v[116:119], v[156:159], v[20:23], v[116:119]\n"
    "s_waitcnt lgkmcnt(0)\n"
    "s_barrier\n"
    "ds_read_b128 v[68:71], v88 offset:41472\n"
    "ds_read_b128 v[72:75], v88 offset:42496\n"
    "ds_read_b128 v[24:27], v88 offset:44544\n"
    "v_lshlrev_b32 v152, 16, v16\n"
    "v_and_b32 v153, 0xffff0000, v16\n"
    "v_lshlrev_b32 v154, 16, v17\n"
    "v_and_b32 v155, 0xffff0000, v17\n"
    "v_lshlrev_b32 v136, 16, v12\n"
    "v_and_b32 v137, 0xffff0000, v12\n"
    "v_lshlrev_b32 v138, 16, v13\n"
    "v_and_b32 v139, 0xffff0000, v13\n"
    "v_lshlrev_b32 v140, 16, v14\n"
    "v_and_b32 v141, 0xffff0000, v14\n"
    "s_waitcnt lgkmcnt(2)\n"
    "v_mfma_f32_16x16x32_bf16 v[100:103], v[164:167], v[68:71], v[100:103]\n"
    "v_lshlrev_b32 v142, 16, v15\n"
    "v_and_b32 v143, 0xffff0000, v15\n"
    "v_mfma_f32_16x16x32_bf16 v[108:111], v[192:195], v[68:71], v[108:111]\n"
    "v_lshlrev_b32 v240, 16, v64\n"
    "v_and_b32 v241, 0xffff0000, v64\n"
    "v_mfma_f32_16x16x32_bf16 v[116:119], v[196:199], v[68:71], v[116:119]\n"
    "v_lshlrev_b32 v242, 16, v65\n"
    "v_and_b32 v243, 0xffff0000, v65\n"
    "s_waitcnt vmcnt(7) lgkmcnt(1)\n"
    "v_mfma_f32_16x16x32_bf16 v[100:103], v[200:203], v[72:75], v[100:103]\n"
    "v_lshlrev_b32 v128, 16, v60\n"
    "v_and_b32 v129, 0xffff0000, v60\n"
    "s_waitcnt vmcnt(4)\n"
    "v_mfma_f32_16x16x32_bf16 v[108:111], v[204:207], v[72:75], v[108:111]\n"
    "v_lshlrev_b32 v130, 16, v61\n"
    "v_and_b32 v131, 0xffff0000, v61\n"
    "v_mfma_f32_16x16x32_bf16 v[116:119], v[188:191], v[72:75], v[116:119]\n"
    "v_lshlrev_b32 v132, 16, v62\n"
    "v_and_b32 v133, 0xffff0000, v62\n"
    "s_waitcnt lgkmcnt(0)\n"
    "v_mfma_f32_16x16x32_bf16 v[100:103], v[228:231], v[24:27], v[100:103]\n"
    "v_lshlrev_b32 v134, 16, v63\n"
    "v_and_b32 v135, 0xffff0000, v63\n"
    "s_waitcnt vmcnt(3)\n"
    "v_mfma_f32_16x16x32_bf16 v[108:111], v[180:183], v[24:27], v[108:111]\n"
    "v_lshlrev_b32 v236, 16, v66\n"
    "v_and_b32 v237, 0xffff0000, v66\n"
    "v_mfma_f32_16x16x32_bf16 v[116:119], v[160:163], v[24:27], v[116:119]\n"
    "v_lshlrev_b32 v238, 16, v67\n"
    "v_and_b32 v239, 0xffff0000, v67\n"
    "v_mfma_f32_16x16x32_bf16 v[104:107], v[168:171], v[68:71], v[104:107]\n"
    "v_mfma_f32_16x16x32_bf16 v[112:115], v[172:175], v[68:71], v[112:115]\n"
    "s_waitcnt vmcnt(2)\n"
    "v_mfma_f32_16x16x32_bf16 v[104:107], v[176:179], v[72:75], v[104:107]\n"
    "v_add_f32_dpp v36, v100, v100 row_ror:8 row_mask:0xf bank_mask:0x3\n"
    "v_add_f32_dpp v37, v101, v101 row_ror:8 row_mask:0xf bank_mask:0x3\n"
    "v_add_f32_dpp v38, v102, v102 row_ror:8 row_mask:0xf bank_mask:0x3\n"
    "s_waitcnt vmcnt(1)\n"
    "v_mfma_f32_16x16x32_bf16 v[112:115], v[208:211], v[72:75], v[112:115]\n"
    "v_add_f32_dpp v39, v103, v103 row_ror:8 row_mask:0xf bank_mask:0x3\n"
    "v_add_f32_dpp v36, v108, v108 row_ror:8 row_mask:0xf bank_mask:0xc\n"
    "v_add_f32_dpp v37, v109, v109 row_ror:8 row_mask:0xf bank_mask:0xc\n"
    "v_mfma_f32_16x16x32_bf16 v[104:107], v[232:235], v[24:27], v[104:107]\n"
    "v_add_f32_dpp v38, v110, v110 row_ror:8 row_mask:0xf bank_mask:0xc\n"
    "v_add_f32_dpp v39, v111, v111 row_ror:8 row_mask:0xf bank_mask:0xc\n"
    "v_add_f32_dpp v116, v116, v116 row_ror:8 row_mask:0xf bank_mask:0xf\n"
    "s_waitcnt vmcnt(0)\n"
    "v_mfma_f32_16x16x32_bf16 v[112:115], v[212:215], v[24:27], v[112:115]\n"
    "v_add_f32_dpp v117, v117, v117 row_ror:8 row_mask:0xf bank_mask:0xf\n"
    "v_add_f32_dpp v118, v118, v118 row_ror:8 row_mask:0xf bank_mask:0xf\n"
    "v_add_f32_dpp v119, v119, v119 row_ror:8 row_mask:0xf bank_mask:0xf\n"
    "v_pk_fma_f32 v[36:37], v[36:37], v[120:121], v[128:129]\n"
    "v_pk_fma_f32 v[38:39], v[38:39], v[122:123], v[130:131]\n"
    "v_pk_fma_f32 v[116:117], v[116:117], v[152:153], v[236:237]\n"
    "v_pk_fma_f32 v[118:119], v[118:119], v[154:155], v[238:239]\n"
    "v_pk_add_f32 v[52:53], v[36:37], v[38:39]\n"
    "v_pk_mul_f32 v[54:55], v[36:37], v[36:37]\n"
    "v_pk_fma_f32 v[54:55], v[38:39], v[38:39], v[54:55]\n"
    "s_mov_b64 exec, %[mk]\n"
    "v_pk_add_f32 v[52:53], v[52:53], v[116:117]\n"
    "v_pk_fma_f32 v[54:55], v[116:117], v[116:117], v[54:55]\n"
    "v_pk_add_f32 v[52:53], v[52:53], v[118:119]\n"
    "v_pk_fma_f32 v[54:55], v[118:119], v[118:119], v[54:55]\n"
    "s_mov_b64 exec, -1\n"
    "v_add_f32_dpp v40, v104, v104 row_ror:8 row_mask:0xf bank_mask:0x3\n"
    "v_add_f32_dpp v41, v105, v105 row_ror:8 row_mask:0xf bank_mask:0x3\n"
    "v_add_f32_dpp v42, v106, v106 row_ror:8 row_mask:0xf bank_mask:0x3\n"
    "v_add_f32_dpp v43, v107, v107 row_ror:8 row_mask:0xf bank_mask:0x3\n"
    "v_add_f32_dpp v40, v112, v112 row_ror:8 row_mask:0xf bank_mask:0xc\n"
    "v_add_f32_dpp v41, v113, v113 row_ror:8 row_mask:0xf bank_mask:0xc\n"
    "v_add_f32_dpp v42, v114, v114 row_ror:8 row_mask:0xf bank_mask:0xc\n"
    "v_add_f32_dpp v43, v115, v115 row_ror:8 row_mask:0xf bank_mask:0xc\n"
    "v_pk_fma_f32 v[40:41], v[40:41], v[124:125], v[132:133]\n"
    "v_pk_fma_f32 v[42:43], v[42:43], v[126:127], v[134:135]\n"
    "v_pk_add_f32 v[52:53], v[52:53], v[40:41]\n"
    "v_pk_fma_f32 v[54:55], v[40:41], v[40:41], v[54:55]\n"
    "v_pk_add_f32 v[52:53], v[52:53], v[42:43]\n"
    "v_pk_fma_f32 v[54:55], v[42:43], v[42:43], v[54:55]\n"
    "v_add_f32 v52, v52, v53\n"
    "v_cvt_pk_bf16_f32 v44, v36, v37\n"
    "v_add_f32 v54, v54, v55\n"
    "v_cvt_pk_bf16_f32 v45, v38, v39\n"
    "s_nop 0\n"
    "v_permlane32_swap_b32 v52, v54\n"
    "v_add_f32 v52, v52, v54\n"
    "v_mov_b32 v53, v52\n"
    "buffer_store_dwordx2 v[44:45], v32, %[rws], %[xo] offen sc1\n"
    "s_add_u32 %[t0], %[xo], 4096\n"
    "v_permlane16_swap_b32 v52, v53\n"
    "v_add_f32 v52, v52, v53\n"
    "v_cvt_pk_bf16_f32 v46, v40, v41\n"
    "v_cvt_pk_bf16_f32 v47, v42, v43\n"
    "v_add_f32_dpp v52, v52, v52 row_ror:8 row_mask:0xf bank_mask:0xf\n"
    "ds_write_b32 v33, v52 offset:36864\n"
    "buffer_store_dwordx2 v[46:47], v32, %[rws], %[t0] offen sc1\n"
    "s_mov_b64 exec, %[mk]\n"
    "v_cvt_pk_bf16_f32 v48, v116, v117\n"
    "v_cvt_pk_bf16_f32 v49, v118, v119\n"
    "buffer_store_dwordx2 v[48:49], v32, %[rws], %[x4] offen sc1\n"
    "s_mov_b64 exec, -1\n"
    "v_mov_b32 v54, 0x36a1369a\n"
    "s_sub_u32 %[xo], %[xo], 2359296\n"
    "s_sub_u32 %[t0], %[t0], 2359296\n"
    "s_sub_u32 %[x4], %[x4], 2359296\n"
    "s_waitcnt lgkmcnt(0)\n"
    "s_barrier\n"
    "ds_read_b128 v[192:195], v34 offset:36864\n"
    "ds_read_b128 v[196:199], v34 offset:36880\n"
    "ds_read_b128 v[200:203], v34 offset:36896\n"
    "ds_read_b128 v[204:207], v34 offset:36912\n"
    "s_waitcnt lgkmcnt(3)\n"
    "v_pk_add_f32 v[192:193], v[192:193], v[194:195]\n"
    "s_waitcnt lgkmcnt(2)\n"
    "v_pk_add_f32 v[196:197], v[196:197], v[198:199]\n"
    "s_waitcnt lgkmcnt(1)\n"
    "v_pk_add_f32 v[200:201], v[200:201], v[202:203]\n"
    "s_waitcnt lgkmcnt(0)\n"
    "v_pk_add_f32 v[204:205], v[204:205], v[206:207]\n"
    "v_pk_add_f32 v[192:193], v[192:193], v[196:197]\n"
    "v_pk_add_f32 v[200:201], v[200:201], v[204:205]\n"
    "v_pk_add_f32 v[192:193], v[192:193], v[200:201]\n"
    "v_mul_f32 v52, 0xbae38e39, v192\n"
    "v_mul_f32 v55, 0x3a5aa8d5, v192\n"
    "v_fmac_f32 v54, 0x3a5aa8d5, v193\n"
    "v_fmac_f32 v54, v55, v52\n"
    "v_max_f32 v54, 0x36a1369a, v54\n"
    "v_rsq_f32 v53, v54\n"
    "s_nop 0\n"
    "v_mul_f32 v52, v52, v53\n"
    "v_pk_fma_f32 v[36:37], v[36:37], v[52:53], v[52:53] op_sel:[0,1,0] op_sel_hi:[1,1,0]\n"
    "v_pk_fma_f32 v[38:39], v[38:39], v[52:53], v[52:53] op_sel:[0,1,0] op_sel_hi:[1,1,0]\n"
    "v_pk_fma_f32 v[36:37], v[36:37], v[136:137], v[144:145]\n"
    "v_pk_fma_f32 v[38:39], v[38:39], v[138:139], v[146:147]\n"
    "v_cvt_pk_bf16_f32 v50, v36, v37\n"
    "v_cvt_pk_bf16_f32 v51, v38, v39\n"
    "buffer_store_dwordx2 v[50:51], v32, %[rws], %[xo] offen sc1\n"
    "v_pk_fma_f32 v[40:41], v[40:41], v[52:53], v[52:53] op_sel:[0,1,0] op_sel_hi:[1,1,0]\n"
    "v_pk_fma_f32 v[42:43], v[42:43], v[52:53], v[52:53] op_sel:[0,1,0] op_sel_hi:[1,1,0]\n"
    "v_pk_fma_f32 v[40:41], v[40:41], v[140:141], v[148:149]\n"
    "v_pk_fma_f32 v[42:43], v[42:43], v[142:143], v[150:151]\n"
    "v_cvt_pk_bf16_f32 v44, v40, v41\n"
    "v_cvt_pk_bf16_f32 v45, v42, v43\n"
    "buffer_store_dwordx2 v[44:45], v32, %[rws], %[t0] offen sc1\n"
    "s_mov_b64 exec, %[mk]\n"
    "v_pk_fma_f32 v[116:117], v[116:117], v[52:53], v[52:53] op_sel:[0,1,0] op_sel_hi:[1,1,0]\n"
    "v_pk_fma_f32 v[118:119], v[118:119], v[52:53], v[52:53] op_sel:[0,1,0] op_sel_hi:[1,1,0]\n"
    "v_pk_fma_f32 v[116:117], v[116:117], v[240:241], v[244:245]\n"
    "v_pk_fma_f32 v[118:119], v[118:119], v[242:243], v[246:247]\n"
    "v_cvt_pk_bf16_f32 v46, v116, v117\n"
    "v_cvt_pk_bf16_f32 v47, v118, v119\n"
    "buffer_store_dwordx2 v[46:47], v32, %[rws], %[x4] offen sc1\n"
    "s_mov_b64 exec, -1\n"
    "s_endpgm\n"
    "LO%=:\n"
    "LPLO:\n"
    "s_cbranch_execz LPLTFY_\n"
    "ds_read_b128 v[60:63], v89 offset:0\n"
    "ds_read_b128 v[64:67], v89 offset:1536\n"
    "s_setprio 2\n"
    "s_waitcnt vmcnt(9) lgkmcnt(1)\n"
    "v_mfma_f32_32x32x16_bf16 v[4:19], v[164:167], v[60:63], 0\n"
    "s_waitcnt vmcnt(7) lgkmcnt(0)\n"
    "v_mfma_f32_32x32x8_bf16 v[4:19], v[156:157], v[64:65], v[4:19]\n"
    "s_mov_b32 %[cnt], 7\n"
    "s_mov_b32 %[nd], 0\n"
    "buffer_load_dwordx4 v[176:179], v88, %[rws], %[kb] offen offset:3072 sc1\n"
    "buffer_load_dwordx4 v[204:207], v68, %[rws], %[vb] offen offset:1920 sc1\n"
    "buffer_load_dwordx4 v[208:211], v68, %[rws], %[vb] offen offset:2560 sc1\n"
    "s_nop 6\n"
    "s_waitcnt vmcnt(9)\n"
    "v_mfma_f32_32x32x16_bf16 v[20:35], v[168:171], v[60:63], 0\n"
    "v_exp_f32 v4, v4\n"
    "v_exp_f32 v5, v5\n"
    "v_exp_f32 v6, v6\n"
    "v_exp_f32 v7, v7\n"
    "v_exp_f32 v8, v8\n"
    "v_cvt_pk_bf16_f32 v52, v4, v5\n"
    "v_exp_f32 v9, v9\n"
    "v_cvt_pk_bf16_f32 v53, v6, v7\n"
    "v_exp_f32 v10, v10\n"
    "v_exp_f32 v11, v11\n"
    "s_waitcnt vmcnt(7)\n"
    "v_mfma_f32_32x32x8_bf16 v[20:35], v[158:159], v[64:65], v[20:35]\n"
    "ds_read_b128 v[64:67], v89 offset:2560\n"
    "v_exp_f32 v12, v12\n"
    "v_cvt_pk_bf16_f32 v54, v8, v9\n"
    "v_exp_f32 v13, v13\n"
    "v_cvt_pk_bf16_f32 v55, v10, v11\n"
    "v_exp_f32 v14, v14\n"
    "v_exp_f32 v15, v15\n"
    "s_waitcnt vmcnt(6)\n"
    "v_mfma_f32_32x32x16_bf16 v[36:51], v[192:195], v[52:55], 0\n"
    "v_exp_f32 v58, v16\n"
    "v_cvt_pk_bf16_f32 v56, v12, v13\n"
    "v_exp_f32 v52, v17\n"
    "v_cvt_pk_bf16_f32 v57, v14, v15\n"
    "v_exp_f32 v59, v18\n"
    "v_exp_f32 v53, v19\n"
    "s_waitcnt vmcnt(5)\n"
    "v_mfma_f32_32x32x16_bf16 v[4:19], v[172:175], v[60:63], 0\n"
    "v_exp_f32 v20, v20\n"
    "v_cvt_pk_bf16_f32 v58, v58, v52\n"
    "buffer_load_dwordx4 v[180:183], v88, %[rws], %[kb4] offen offset:0 sc1\n"
    "v_exp_f32 v21, v21\n"
    "v_cvt_pk_bf16_f32 v59, v59, v53\n"
    "v_exp_f32 v22, v22\n"
    "v_exp_f32 v23, v23\n"
    "s_waitcnt vmcnt(5)\n"
    "v_mfma_f32_32x32x16_bf16 v[36:51], v[196:199], v[56:59], v[36:51]\n"
    "v_exp_f32 v24, v24\n"
    "v_cvt_pk_bf16_f32 v52, v20, v21\n"
    "buffer_load_dwordx4 v[76:79], v88, %[rws], %[mr] offen sc1\n"
    "v_exp_f32 v25, v25\n"
    "v_cvt_pk_bf16_f32 v53, v22, v23\n"
    "v_exp_f32 v26, v26\n"
    "v_exp_f32 v27, v27\n"
    "s_setprio 0\n"
    "s_waitcnt lgkmcnt(0)\n"
    "v_mfma_f32_32x32x8_bf16 v[4:19], v[156:157], v[64:65], v[4:19]\n"
    "LPP6:\n"
    "s_cbranch_execz LPK0\n"
    "v_exp_f32 v28, v28\n"
    "v_cvt_pk_bf16_f32 v54, v24, v25\n"
    "buffer_load_dwordx4 v[212:215], v68, %[rws], %[vb] offen offset:3200 sc1\n"
    "buffer_load_dwordx4 v[216:219], v68, %[rws], %[vb3] offen offset:0 sc1\n"
    "v_exp_f32 v29, v29\n"
    "v_cvt_pk_bf16_f32 v55, v26, v27\n"
    "v_exp_f32 v30, v30\n"
    "v_exp_f32 v31, v31\n"
    "s_waitcnt vmcnt(7)\n"
    "v_mfma_f32_32x32x16_bf16 v[36:51], v[200:203], v[52:55], v[36:51]\n"
    "v_exp_f32 v58, v32\n"
    "v_cvt_pk_bf16_f32 v56, v28, v29\n"
    "v_exp_f32 v52, v33\n"
    "v_cvt_pk_bf16_f32 v57, v30, v31\n"
    "v_exp_f32 v59, v34\n"
    "v_exp_f32 v53, v35\n"
    "s_waitcnt vmcnt(6)\n"
    "v_mfma_f32_32x32x16_bf16 v[20:35], v[176:179], v[60:63], 0\n"
    "v_exp_f32 v4, v4\n"
    "v_cvt_pk_bf16_f32 v58, v58, v52\n"
    "buffer_load_dwordx4 v[184:187], v88, %[rws], %[kb4] offen offset:1024 sc1\n"
    "v_exp_f32 v5, v5\n"
    "v_cvt_pk_bf16_f32 v59, v59, v53\n"
    "v_exp_f32 v6, v6\n"
    "v_exp_f32 v7, v7\n"
    "s_waitcnt vmcnt(6)\n"
    "v_mfma_f32_32x32x16_bf16 v[36:51], v[204:207], v[56:59], v[36:51]\n"
    "v_exp_f32 v8, v8\n"
    "v_cvt_pk_bf16_f32 v52, v4, v5\n"
    "v_exp_f32 v9, v9\n"
    "v_cvt_pk_bf16_f32 v53, v6, v7\n"
    "v_exp_f32 v10, v10\n"
    "v_exp_f32 v11, v11\n"
    "s_setprio 2\n"
    "v_mfma_f32_32x32x8_bf16 v[20:35], v[158:159], v[64:65], v[20:35]\n"
    "ds_read_b128 v[64:67], v89 offset:1024\n"
    "v_exp_f32 v12, v12\n"
    "v_cvt_pk_bf16_f32 v54, v8, v9\n"
    "buffer_load_dwordx4 v[220:223], v68, %[rws], %[vb3] offen offset:640 sc1\n"
    "buffer_load_dwordx4 v[224:227], v68, %[rws], %[vb3] offen offset:1280 sc1\n"
    "v_exp_f32 v13, v13\n"
    "v_cvt_pk_bf16_f32 v55, v10, v11\n"
    "v_exp_f32 v14, v14\n"
    "v_exp_f32 v15, v15\n"
    "s_waitcnt vmcnt(7)\n"
    "v_mfma_f32_32x32x16_bf16 v[36:51], v[208:211], v[52:55], v[36:51]\n"
    "v_exp_f32 v58, v16\n"
    "v_cvt_pk_bf16_f32 v56, v12, v13\n"
    "v_exp_f32 v52, v17\n"
    "v_cvt_pk_bf16_f32 v57, v14, v15\n"
    "v_exp_f32 v59, v18\n"
    "v_exp_f32 v53, v19\n"
    "s_waitcnt vmcnt(6)\n"
    "v_mfma_f32_32x32x16_bf16 v[4:19], v[180:183], v[60:63], 0\n"
    "v_exp_f32 v20, v20\n"
    "v_cvt_pk_bf16_f32 v58, v58, v52\n"
    "buffer_load_dwordx4 v[188:191], v88, %[rws], %[kb4] offen offset:2048 sc1\n"
    "v_exp_f32 v21, v21\n"
    "v_cvt_pk_bf16_f32 v59, v59, v53\n"
    "v_exp_f32 v22, v22\n"
    "v_exp_f32 v23, v23\n"
    "s_waitcnt vmcnt(5)\n"
    "v_mfma_f32_32x32x16_bf16 v[36:51], v[212:215], v[56:59], v[36:51]\n"
    "v_exp_f32 v24, v24\n"
    "v_cvt_pk_bf16_f32 v52, v20, v21\n"
    "v_exp_f32 v25, v25\n"
    "v_cvt_pk_bf16_f32 v53, v22, v23\n"
    "v_exp_f32 v26, v26\n"
    "v_exp_f32 v27, v27\n"
    "s_waitcnt lgkmcnt(0)\n"
    "v_mfma_f32_32x32x8_bf16 v[4:19], v[156:157], v[64:65], v[4:19]\n"
    "v_exp_f32 v28, v28\n"
    "v_cvt_pk_bf16_f32 v54, v24, v25\n"
    "buffer_load_dwordx4 v[228:231], v68, %[rws], %[vb3] offen offset:1920 sc1\n"
    "buffer_load_dwordx4 v[232:235], v68, %[rws], %[vb3] offen offset:2560 sc1\n"
    "v_exp_f32 v29, v29\n"
    "v_cvt_pk_bf16_f32 v55, v26, v27\n"
    "v_exp_f32 v30, v30\n"
    "v_exp_f32 v31, v31\n"
    "s_waitcnt vmcnt(6)\n"
    "v_mfma_f32_32x32x16_bf16 v[36:51], v[216:219], v[52:55], v[36:51]\n"
    "v_exp_f32 v58, v32\n"
    "v_cvt_pk_bf16_f32 v56, v28, v29\n"
    "v_exp_f32 v52, v33\n"
    "v_cvt_pk_bf16_f32 v57, v30, v31\n"
    "v_exp_f32 v59, v34\n"
    "v_exp_f32 v53, v35\n"
    "s_waitcnt vmcnt(5)\n"
    "v_mfma_f32_32x32x16_bf16 v[20:35], v[184:187], v[60:63], 0\n"
    "v_exp_f32 v4, v4\n"
    "v_cvt_pk_bf16_f32 v58, v58, v52\n"
    "buffer_load_dwordx4 v[160:163], v88, %[rws], %[kb4] offen offset:3072 sc1\n"
    "v_exp_f32 v5, v5\n"
    "v_cvt_pk_bf16_f32 v59, v59, v53\n"
    "v_exp_f32 v6, v6\n"
    "v_exp_f32 v7, v7\n"
    "s_waitcnt vmcnt(5)\n"
    "v_mfma_f32_32x32x16_bf16 v[36:51], v[220:223], v[56:59], v[36:51]\n"
    "v_exp_f32 v8, v8\n"
    "v_cvt_pk_bf16_f32 v52, v4, v5\n"
    "buffer_load_dword v1, v88, %[rws], %[wf] offen sc1\n"
    "v_exp_f32 v9, v9\n"
    "v_cvt_pk_bf16_f32 v53, v6, v7\n"
    "v_exp_f32 v10, v10\n"
    "v_exp_f32 v11, v11\n"
    "v_mfma_f32_32x32x8_bf16 v[20:35], v[158:159], v[64:65], v[20:35]\n"
    "ds_read_b128 v[64:67], v89 offset:2048\n"
    "v_exp_f32 v12, v12\n"
    "v_cvt_pk_bf16_f32 v54, v8, v9\n"
    "buffer_load_dwordx4 v[236:239], v68, %[rws], %[vb3] offen offset:3200 sc1\n"
    "buffer_load_dwordx4 v[240:243], v68, %[rws], %[vb6] offen offset:0 sc1\n"
    "v_exp_f32 v13, v13\n"
    "v_cvt_pk_bf16_f32 v55, v10, v11\n"
    "v_exp_f32 v14, v14\n"
    "v_exp_f32 v15, v15\n"
    "s_waitcnt vmcnt(7)\n"
    "v_mfma_f32_32x32x16_bf16 v[36:51], v[224:227], v[52:55], v[36:51]\n"
    "v_exp_f32 v58, v16\n"
    "v_cvt_pk_bf16_f32 v56, v12, v13\n"
    "v_exp_f32 v52, v17\n"
    "v_cvt_pk_bf16_f32 v57, v14, v15\n"
    "v_exp_f32 v59, v18\n"
    "v_exp_f32 v53, v19\n"
    "s_setprio 0\n"
    "s_waitcnt vmcnt(6)\n"
    "v_mfma_f32_32x32x16_bf16 v[4:19], v[188:191], v[60:63], 0\n"
    "v_exp_f32 v20, v20\n"
    "v_cvt_pk_bf16_f32 v58, v58, v52\n"
    "v_exp_f32 v21, v21\n"
    "v_cvt_pk_bf16_f32 v59, v59, v53\n"
    "v_exp_f32 v22, v22\n"
    "v_exp_f32 v23, v23\n"
    "s_waitcnt vmcnt(5)\n"
    "v_mfma_f32_32x32x16_bf16 v[36:51], v[228:231], v[56:59], v[36:51]\n"
    "v_exp_f32 v24, v24\n"
    "v_cvt_pk_bf16_f32 v52, v20, v21\n"
    "v_bitop3_b32 v79, v77, v79, %[tag] bitop3:0x7e\n"
    "v_mov_b32 v77, v78\n"
    "v_cmp_ne_u32 vcc, 0, v79\n"
    "s_cbranch_vccnz LPFO1_%=\n"
    "LRFO1_%=:\n"
    "ds_write_b64 v91, v[76:77] offset:57856\n"
    "v_exp_f32 v25, v25\n"
    "v_cvt_pk_bf16_f32 v53, v22, v23\n"
    "v_exp_f32 v26, v26\n"
    "v_exp_f32 v27, v27\n"
    "s_waitcnt lgkmcnt(1)\n"
    "v_mfma_f32_32x32x8_bf16 v[4:19], v[156:157], v[64:65], v[4:19]\n"
    "v_exp_f32 v28, v28\n"
    "v_cvt_pk_bf16_f32 v54, v24, v25\n"
    "buffer_load_dwordx4 v[244:247], v68, %[rws], %[vb6] offen offset:640 sc1\n"
    "buffer_load_dwordx4 v[248:251], v68, %[rws], %[vb6] offen offset:1280 sc1\n"
    "v_exp_f32 v29, v29\n"
    "v_cvt_pk_bf16_f32 v55, v26, v27\n"
    "v_exp_f32 v30, v30\n"
    "v_exp_f32 v31, v31\n"
    "s_waitcnt vmcnt(6)\n"
    "v_mfma_f32_32x32x16_bf16 v[36:51], v[232:235], v[52:55], v[36:51]\n"
    "v_exp_f32 v58, v32\n"
    "v_cvt_pk_bf16_f32 v56, v28, v29\n"
    "v_exp_f32 v52, v33\n"
    "v_cvt_pk_bf16_f32 v57, v30, v31\n"
    "v_exp_f32 v59, v34\n"
    "v_exp_f32 v53, v35\n"
    "s_setprio 2\n"
    "s_waitcnt vmcnt(5)\n"
    "v_mfma_f32_32x32x16_bf16 v[20:35], v[160:163], v[60:63], 0\n"
    "ds_read_b128 v[60:63], v89 offset:3584\n"
    "v_exp_f32 v4, v4\n"
    "v_cvt_pk_bf16_f32 v58, v58, v52\n"
    "v_exp_f32 v5, v5\n"
    "v_cvt_pk_bf16_f32 v59, v59, v53\n"
    "v_exp_f32 v6, v6\n"
    "v_exp_f32 v7, v7\n"
    "s_waitcnt vmcnt(3)\n"
    "v_mfma_f32_32x32x16_bf16 v[36:51], v[236:239], v[56:59], v[36:51]\n"
    "v_exp_f32 v8, v8\n"
    "v_cvt_pk_bf16_f32 v52, v4, v5\n"
    "v_exp_f32 v9, v9\n"
    "v_cvt_pk_bf16_f32 v53, v6, v7\n"
    "v_exp_f32 v10, v10\n"
    "v_exp_f32 v11, v11\n"
    "v_mfma_f32_32x32x8_bf16 v[20:35], v[158:159], v[64:65], v[20:35]\n"
    "ds_read_b128 v[64:67], v89 offset:5120\n"
    "v_exp_f32 v12, v12\n"
    "v_cvt_pk_bf16_f32 v54, v8, v9\n"
    "buffer_load_dwordx4 v[252:255], v68, %[rws], %[vb6] offen offset:1920 sc1\n"
    "v_exp_f32 v13, v13\n"
    "v_cvt_pk_bf16_f32 v55, v10, v11\n"
    "v_exp_f32 v14, v14\n"
    "v_exp_f32 v15, v15\n"
    "s_waitcnt vmcnt(3)\n"
    "v_mfma_f32_32x32x16_bf16 v[36:51], v[240:243], v[52:55], v[36:51]\n"
    "v_exp_f32 v58, v16\n"
    "v_cvt_pk_bf16_f32 v56, v12, v13\n"
    "s_mov_b32 exec_hi, 0x3fffff\n"
    "v_cmp_ne_u32 vcc, %[tag], v1\n"
    "s_mov_b32 exec_hi, -1\n"
    "s_cbranch_vccnz LPFO0_%=\n"
    "LRFO0_%=:\n"
    "buffer_load_dwordx4 v[80:83], v88, %[rws], %[w8g] offen sc1\n"
    "s_add_u32 %[w8g], %[w8g], 1024\n"
    "v_exp_f32 v52, v17\n"
    "v_cvt_pk_bf16_f32 v57, v14, v15\n"
    "v_exp_f32 v59, v18\n"
    "v_exp_f32 v53, v19\n"
    "s_waitcnt lgkmcnt(1)\n"
    "v_mfma_f32_32x32x16_bf16 v[4:19], v[164:167], v[60:63], 0\n"
    "v_exp_f32 v20, v20\n"
    "v_cvt_pk_bf16_f32 v58, v58, v52\n"
    "buffer_load_dwordx4 v[136:139], v88, %[rws], %[wo4] offen offset:0 sc1\n"
    "v_exp_f32 v21, v21\n"
    "v_cvt_pk_bf16_f32 v59, v59, v53\n"
    "v_exp_f32 v22, v22\n"
    "v_exp_f32 v23, v23\n"
    "s_waitcnt vmcnt(4)\n"
    "v_mfma_f32_32x32x16_bf16 v[36:51], v[244:247], v[56:59], v[36:51]\n"
    "v_exp_f32 v24, v24\n"
    "v_cvt_pk_bf16_f32 v52, v20, v21\n"
    "v_exp_f32 v25, v25\n"
    "v_cvt_pk_bf16_f32 v53, v22, v23\n"
    "s_mov_b32 m0, %[pm]\n"
    "s_add_u32 %[t0], %[pa], %[pi]\n"
    "buffer_load_dwordx4 v97, %[rpt], %[pa] offen lds\n"
    "s_mov_b32 %[pa], %[pb]\n"
    "s_mov_b32 %[pb], %[t0]\n"
    "s_xor_b32 %[pm], %[pm], 1024\n"
    "v_exp_f32 v26, v26\n"
    "v_exp_f32 v27, v27\n"
    "s_waitcnt lgkmcnt(0)\n"
    "v_mfma_f32_32x32x8_bf16 v[4:19], v[156:157], v[64:65], v[4:19]\n"
    "v_exp_f32 v28, v28\n"
    "v_cvt_pk_bf16_f32 v54, v24, v25\n"
    "v_exp_f32 v29, v29\n"
    "v_cvt_pk_bf16_f32 v55, v26, v27\n"
    "v_exp_f32 v30, v30\n"
    "v_exp_f32 v31, v31\n"
    "s_waitcnt vmcnt(4)\n"
    "v_mfma_f32_32x32x16_bf16 v[36:51], v[248:251], v[52:55], v[36:51]\n"
    "v_exp_f32 v58, v32\n"
    "v_cvt_pk_bf16_f32 v56, v28, v29\n"
    "buffer_load_dwordx4 v[120:123], v88, %[rws], %[wo] offen offset:0 sc1\n"
    "v_exp_f32 v52, v33\n"
    "v_cvt_pk_bf16_f32 v57, v30, v31\n"
    "v_exp_f32 v59, v34\n"
    "v_exp_f32 v53, v35\n"
    "v_mfma_f32_32x32x16_bf16 v[20:35], v[168:171], v[60:63], 0\n"
    "v_exp_f32 v4, v4\n"
    "v_cvt_pk_bf16_f32 v58, v58, v52\n"
    "v_exp_f32 v5, v5\n"
    "v_cvt_pk_bf16_f32 v59, v59, v53\n"
    "s_mov_b32 m0, %[pm]\n"
    "s_add_u32 %[t0], %[pa], %[pi]\n"
    "buffer_load_dwordx4 v97, %[rpt], %[pa] offen lds\n"
    "s_mov_b32 %[pa], %[pb]\n"
    "s_mov_b32 %[pb], %[t0]\n"
    "s_xor_b32 %[pm], %[pm], 1024\n"
    "v_exp_f32 v6, v6\n"
    "v_exp_f32 v7, v7\n"
    "s_waitcnt vmcnt(5)\n"
    "v_mfma_f32_32x32x16_bf16 v[36:51], v[252:255], v[56:59], v[36:51]\n"
    "v_exp_f32 v8, v8\n"
    "v_cvt_pk_bf16_f32 v52, v4, v5\n"
    "v_exp_f32 v9, v9\n"
    "v_cvt_pk_bf16_f32 v53, v6, v7\n"
    "v_exp_f32 v10, v10\n"
    "v_exp_f32 v11, v11\n"
    "v_mfma_f32_32x32x8_bf16 v[20:35], v[158:159], v[64:65], v[20:35]\n"
    "ds_read_b128 v[64:67], v89 offset:6144\n"
    "v_exp_f32 v12, v12\n"
    "v_cvt_pk_bf16_f32 v54, v8, v9\n"
    "buffer_load_dwordx4 v[124:127], v88, %[rws], %[wo] offen offset:1024 sc1\n"
    "v_exp_f32 v13, v13\n"
    "v_cvt_pk_bf16_f32 v55, v10, v11\n"
    "v_exp_f32 v14, v14\n"
    "v_exp_f32 v15, v15\n"
    "ds_write_b128 v94, v[36:39]\n"
    "ds_write_b128 v94, v[40:43] offset:1024\n"
    "ds_write_b64 v91, v[44:45]\n"
    "v_mfma_f32_32x32x16_bf16 v[36:51], v[192:195], v[52:55], 0\n"
    "v_exp_f32 v58, v16\n"
    "v_cvt_pk_bf16_f32 v56, v12, v13\n"
    "v_add_u32 v89, 3584, v89\n"
    "v_exp_f32 v52, v17\n"
    "v_cvt_pk_bf16_f32 v57, v14, v15\n"
    "v_exp_f32 v59, v18\n"
    "v_exp_f32 v53, v19\n"
    "LBFO_%=:\n"
    "v_mfma_f32_32x32x16_bf16 v[4:19], v[172:175], v[60:63], 0\n"
    "v_exp_f32 v20, v20\n"
    "v_cvt_pk_bf16_f32 v58, v58, v52\n"
    "v_exp_f32 v21, v21\n"
    "v_cvt_pk_bf16_f32 v59, v59, v53\n"
    "v_exp_f32 v22, v22\n"
    "v_exp_f32 v23, v23\n"
    "v_mfma_f32_32x32x16_bf16 v[36:51], v[196:199], v[56:59], v[36:51]\n"
    "s_waitcnt lgkmcnt(0)\n"
    "s_mov_b64 exec, %[me]\n"
    "ds_inc_u32 v0, v0\n"
    "s_mov_b64 exec, -1\n"
    "s_add_u32 %[nd], %[nd], 1\n"
    "v_exp_f32 v24, v24\n"
    "v_cvt_pk_bf16_f32 v52, v20, v21\n"
    "buffer_load_dwordx4 v[128:131], v88, %[rws], %[wo] offen offset:2048 sc1\n"
    "v_exp_f32 v25, v25\n"
    "v_cvt_pk_bf16_f32 v53, v22, v23\n"
    "v_exp_f32 v26, v26\n"
    "v_exp_f32 v27, v27\n"
    "s_setprio 0\n"
    "v_mfma_f32_32x32x8_bf16 v[4:19], v[156:157], v[64:65], v[4:19]\n"
    "v_exp_f32 v28, v28\n"
    "v_cvt_pk_bf16_f32 v54, v24, v25\n"
    "v_exp_f32 v29, v29\n"
    "v_cvt_pk_bf16_f32 v55, v26, v27\n"
    "v_exp_f32 v30, v30\n"
    "v_exp_f32 v31, v31\n"
    "v_mfma_f32_32x32x16_bf16 v[36:51], v[200:203], v[52:55], v[36:51]\n"
    "v_exp_f32 v58, v32\n"
    "v_cvt_pk_bf16_f32 v56, v28, v29\n"
    "v_exp_f32 v52, v33\n"
    "v_cvt_pk_bf16_f32 v57, v30, v31\n"
    "v_add_u32 v1, %[w8b], v88\n"
    "s_add_u32 %[w8b], %[w8b], 1024\n"
    "s_waitcnt vmcnt(6)\n"
    "ds_write_b128 v1, v[80:83]\n"
    "v_exp_f32 v59, v34\n"
    "v_exp_f32 v53, v35\n"
    "v_mfma_f32_32x32x16_bf16 v[20:35], v[176:179], v[60:63], 0\n"
    "v_exp_f32 v4, v4\n"
    "v_cvt_pk_bf16_f32 v58, v58, v52\n"
    "buffer_load_dwordx4 v[132:135], v88, %[rws], %[wo] offen offset:3072 sc1\n"
    "v_exp_f32 v5, v5\n"
    "v_cvt_pk_bf16_f32 v59, v59, v53\n"
    "v_exp_f32 v6, v6\n"
    "v_exp_f32 v7, v7\n"
    "v_mfma_f32_32x32x16_bf16 v[36:51], v[204:207], v[56:59], v[36:51]\n"
    "v_exp_f32 v8, v8\n"
    "v_cvt_pk_bf16_f32 v52, v4, v5\n"
    "v_exp_f32 v9, v9\n"
    "v_cvt_pk_bf16_f32 v53, v6, v7\n"
    "v_exp_f32 v10, v10\n"
    "v_exp_f32 v11, v11\n"
    "s_setprio 2\n"
    "v_mfma_f32_32x32x8_bf16 v[20:35], v[158:159], v[64:65], v[20:35]\n"
    "ds_read_b128 v[64:67], v89 offset:1024\n"
    "v_exp_f32 v12, v12\n"
    "v_cvt_pk_bf16_f32 v54, v8, v9\n"
    "ds_read_b32 v1, v0\n"
    "v_exp_f32 v13, v13\n"
    "v_cvt_pk_bf16_f32 v55, v10, v11\n"
    "buffer_load_dwordx4 v[140:143], v88, %[rws], %[wo4] offen offset:1024 sc1\n"
    "v_exp_f32 v14, v14\n"
    "v_exp_f32 v15, v15\n"
    "v_mfma_f32_32x32x16_bf16 v[36:51], v[208:211], v[52:55], v[36:51]\n"
    "v_exp_f32 v58, v16\n"
    "v_cvt_pk_bf16_f32 v56, v12, v13\n"
    "s_waitcnt lgkmcnt(0)\n"
    "v_cmp_gt_u32 vcc, %[nd], v1\n"
    "s_cbranch_vccnz LPFO2_%=\n"
    "LRFO2_%=:\n"
    "ds_read2st64_b32 v[84:85], v92 offset0:1 offset1:3\n"
    "ds_read2st64_b32 v[86:87], v92 offset0:5 offset1:7\n"
    "ds_read2st64_b64 v[76:79], v95 offset1:4\n"
    "ds_read2st64_b64 v[80:83], v95 offset0:8 offset1:12\n"
    "ds_read2st64_b64 v[68:71], v92 offset1:1\n"
    "ds_read2st64_b64 v[72:75], v92 offset0:2 offset1:3\n"
    "v_exp_f32 v52, v17\n"
    "v_cvt_pk_bf16_f32 v57, v14, v15\n"
    "v_exp_f32 v59, v18\n"
    "v_exp_f32 v53, v19\n"
    "v_mfma_f32_32x32x16_bf16 v[4:19], v[180:183], v[60:63], 0\n"
    "v_exp_f32 v20, v20\n"
    "v_cvt_pk_bf16_f32 v58, v58, v52\n"
    "v_exp_f32 v21, v21\n"
    "v_cvt_pk_bf16_f32 v59, v59, v53\n"
    "v_exp_f32 v22, v22\n"
    "v_exp_f32 v23, v23\n"
    "v_mfma_f32_32x32x16_bf16 v[36:51], v[212:215], v[56:59], v[36:51]\n"
    "s_waitcnt lgkmcnt(5)\n"
    "v_add_f32 v84, v84, v85\n"
    "s_waitcnt lgkmcnt(4)\n"
    "v_add_f32 v86, v86, v87\n"
    "v_exp_f32 v24, v24\n"
    "v_cvt_pk_bf16_f32 v52, v20, v21\n"
    "buffer_load_dwordx4 v[144:147], v88, %[rws], %[wo4] offen offset:2048 sc1\n"
    "v_exp_f32 v25, v25\n"
    "v_cvt_pk_bf16_f32 v53, v22, v23\n"
    "v_add_f32 v84, v84, v86\n"
    "v_exp_f32 v26, v26\n"
    "s_waitcnt lgkmcnt(3)\n"
    "v_add_f32 v76, v76, v78\n"
    "v_exp_f32 v27, v27\n"
    "v_mfma_f32_32x32x8_bf16 v[4:19], v[156:157], v[64:65], v[4:19]\n"
    "v_add_f32 v77, v77, v79\n"
    "v_exp_f32 v28, v28\n"
    "v_cvt_pk_bf16_f32 v54, v24, v25\n"
    "v_exp_f32 v29, v29\n"
    "v_cvt_pk_bf16_f32 v55, v26, v27\n"
    "v_add_f32_dpp v84, v84, v84 quad_perm:[1,0,3,2] row_mask:0xf bank_mask:0xf\n"
    "v_exp_f32 v30, v30\n"
    "s_waitcnt lgkmcnt(2)\n"
    "v_add_f32 v80, v80, v82\n"
    "v_exp_f32 v31, v31\n"
    "v_mfma_f32_32x32x16_bf16 v[36:51], v[216:219], v[52:55], v[36:51]\n"
    "v_rcp_f32 v84, v84\n"
    "v_exp_f32 v58, v32\n"
    "v_cvt_pk_bf16_f32 v56, v28, v29\n"
    "v_exp_f32 v52, v33\n"
    "v_cvt_pk_bf16_f32 v57, v30, v31\n"
    "v_add_f32 v81, v81, v83\n"
    "v_exp_f32 v59, v34\n"
    "v_add_f32 v76, v76, v80\n"
    "v_exp_f32 v53, v35\n"
    "v_mfma_f32_32x32x16_bf16 v[20:35], v[184:187], v[60:63], 0\n"
    "v_add_f32 v77, v77, v81\n"
    "v_exp_f32 v4, v4\n"
    "v_cvt_pk_bf16_f32 v58, v58, v52\n"
    "buffer_load_dwordx4 v[148:151], v88, %[rws], %[wo4] offen offset:3072 sc1\n"
    "v_exp_f32 v5, v5\n"
    "v_cvt_pk_bf16_f32 v59, v59, v53\n"
    "v_mul_f32 v76, v76, v84\n"
    "v_exp_f32 v6, v6\n"
    "v_mul_f32 v77, v77, v84\n"
    "v_exp_f32 v7, v7\n"
    "v_mfma_f32_32x32x16_bf16 v[36:51], v[220:223], v[56:59], v[36:51]\n"
    "v_cvt_pk_bf16_f32 v76, v76, v77\n"
    "v_exp_f32 v8, v8\n"
    "v_cvt_pk_bf16_f32 v52, v4, v5\n"
    "ds_write_b32 v93, v76\n"
    "v_exp_f32 v9, v9\n"
    "v_cvt_pk_bf16_f32 v53, v6, v7\n"
    "s_cmp_lg_u32 %[lw], %[cnt]\n"
    "s_cbranch_scc1 LLFO1_%=\n"
    "s_waitcnt lgkmcnt(2)\n"
    "v_add_f32 v68, v68, v70\n"
    "v_add_f32 v69, v69, v71\n"
    "s_waitcnt lgkmcnt(1)\n"
    "v_add_f32 v72, v72, v74\n"
    "v_add_f32 v73, v73, v75\n"
    "v_add_f32 v68, v68, v72\n"
    "v_add_f32 v69, v69, v73\n"
    "v_and_b32 v70, 0xf0, v93\n"
    "v_lshrrev_b32 v71, 8, v88\n"
    "v_mul_f32 v68, v68, v84\n"
    "v_mul_f32 v69, v69, v84\n"
    "v_lshl_add_u32 v70, v71, 2, v70\n"
    "v_cvt_pk_bf16_f32 v68, v68, v69\n"
    "v_add_u32 v70, %[lo], v70\n"
    "ds_write_b32 v70, v68\n"
    "LLFO1_%=:\n"
    "v_exp_f32 v10, v10\n"
    "v_exp_f32 v11, v11\n"
    "v_mfma_f32_32x32x8_bf16 v[20:35], v[158:159], v[64:65], v[20:35]\n"
    "ds_read_b128 v[64:67], v89 offset:2048\n"
    "v_exp_f32 v12, v12\n"
    "v_cvt_pk_bf16_f32 v54, v8, v9\n"
    "v_exp_f32 v13, v13\n"
    "v_cvt_pk_bf16_f32 v55, v10, v11\n"
    "buffer_load_dwordx4 v[152:155], v88, %[rws], %[wo8] offen offset:0 sc1\n"
    "v_exp_f32 v14, v14\n"
    "v_exp_f32 v15, v15\n"
    "v_mfma_f32_32x32x16_bf16 v[36:51], v[224:227], v[52:55], v[36:51]\n"
    "s_waitcnt lgkmcnt(1)\n"
    "s_mov_b64 exec, %[me]\n"
    "ds_inc_u32 v0, v0\n"
    "s_mov_b64 exec, -1\n"
    "s_add_u32 %[nd], %[nd], 1\n"
    "v_exp_f32 v58, v16\n"
    "v_cvt_pk_bf16_f32 v56, v12, v13\n"
    "v_exp_f32 v52, v17\n"
    "v_cvt_pk_bf16_f32 v57, v14, v15\n"
    "v_exp_f32 v59, v18\n"
    "v_exp_f32 v53, v19\n"
    "s_setprio 0\n"
    "v_mfma_f32_32x32x16_bf16 v[4:19], v[188:191], v[60:63], 0\n"
    "s_waitcnt vmcnt(9)\n"
    "ds_read_b64_tr_b16 v[76:77], v96\n"
    "ds_read_b64_tr_b16 v[78:79], v96 offset:512\n"
    "v_exp_f32 v20, v20\n"
    "v_cvt_pk_bf16_f32 v58, v58, v52\n"
    "v_exp_f32 v21, v21\n"
    "v_cvt_pk_bf16_f32 v59, v59, v53\n"
    "v_exp_f32 v22, v22\n"
    "v_exp_f32 v23, v23\n"
    "v_mfma_f32_32x32x16_bf16 v[36:51], v[228:231], v[56:59], v[36:51]\n"
    "v_exp_f32 v24, v24\n"
    "v_cvt_pk_bf16_f32 v52, v20, v21\n"
    "buffer_load_dwordx4 v[80:83], v88, %[rws], %[w8g] offen sc1\n"
    "s_add_u32 %[w8g], %[w8g], 1024\n"
    "v_exp_f32 v25, v25\n"
    "v_cvt_pk_bf16_f32 v53, v22, v23\n"
    "v_exp_f32 v26, v26\n"
    "v_exp_f32 v27, v27\n"
    "s_waitcnt lgkmcnt(3)\n"
    "v_mfma_f32_32x32x8_bf16 v[4:19], v[156:157], v[64:65], v[4:19]\n"
    "s_waitcnt lgkmcnt(0)\n"
    "buffer_store_dwordx4 v[76:79], v98, %[rws], %[pd] offen sc1\n"
    "s_add_u32 %[pd], %[pd], %[pdi]\n"
    "v_exp_f32 v28, v28\n"
    "v_cvt_pk_bf16_f32 v54, v24, v25\n"
    "v_exp_f32 v29, v29\n"
    "v_cvt_pk_bf16_f32 v55, v26, v27\n"
    "s_mov_b32 m0, %[pm]\n"
    "s_add_u32 %[t0], %[pa], %[pi]\n"
    "buffer_load_dwordx4 v97, %[rpt], %[pa] offen lds\n"
    "s_mov_b32 %[pa], %[pb]\n"
    "s_mov_b32 %[pb], %[t0]\n"
    "s_xor_b32 %[pm], %[pm], 1024\n"
    "v_exp_f32 v30, v30\n"
    "v_exp_f32 v31, v31\n"
    "v_mfma_f32_32x32x16_bf16 v[36:51], v[232:235], v[52:55], v[36:51]\n"
    "v_xor_b32 v96, 1024, v96\n"
    "v_exp_f32 v58, v32\n"
    "v_cvt_pk_bf16_f32 v56, v28, v29\n"
    "v_exp_f32 v52, v33\n"
    "v_cvt_pk_bf16_f32 v57, v30, v31\n"
    "v_exp_f32 v59, v34\n"
    "v_exp_f32 v53, v35\n"
    "s_setprio 2\n"
    "v_mfma_f32_32x32x16_bf16 v[20:35], v[160:163], v[60:63], 0\n"
    "ds_read_b128 v[60:63], v89 offset:3584\n"
    "v_exp_f32 v4, v4\n"
    "v_cvt_pk_bf16_f32 v58, v58, v52\n"
    "ds_read_b32 v1, v0\n"
    "v_exp_f32 v5, v5\n"
    "v_cvt_pk_bf16_f32 v59, v59, v53\n"
    "v_exp_f32 v6, v6\n"
    "v_exp_f32 v7, v7\n"
    "v_mfma_f32_32x32x16_bf16 v[36:51], v[236:239], v[56:59], v[36:51]\n"
    "v_exp_f32 v8, v8\n"
    "v_cvt_pk_bf16_f32 v52, v4, v5\n"
    "s_waitcnt lgkmcnt(0)\n"
    "v_cmp_gt_u32 vcc, %[nd], v1\n"
    "s_cbranch_vccnz LPFO3_%=\n"
    "LRFO3_%=:\n"
    "ds_read_b128 v[72:75], v90 offset:0\n"
    "ds_read_b128 v[68:71], v88 offset:41472\n"
    "v_exp_f32 v9, v9\n"
    "v_cvt_pk_bf16_f32 v53, v6, v7\n"
    "v_exp_f32 v10, v10\n"
    "v_exp_f32 v11, v11\n"
    "v_mfma_f32_32x32x8_bf16 v[20:35], v[158:159], v[64:65], v[20:35]\n"
    "ds_read_b128 v[64:67], v89 offset:5120\n"
    "v_exp_f32 v12, v12\n"
    "v_cvt_pk_bf16_f32 v54, v8, v9\n"
    "s_add_u32 %[wo], %[wo], 73728\n"
    "s_add_u32 %[wo4], %[wo4], 73728\n"
    "s_add_u32 %[wo8], %[wo8], 73728\n"
    "v_exp_f32 v13, v13\n"
    "v_cvt_pk_bf16_f32 v55, v10, v11\n"
    "v_exp_f32 v14, v14\n"
    "v_exp_f32 v15, v15\n"
    "v_mfma_f32_32x32x16_bf16 v[36:51], v[240:243], v[52:55], v[36:51]\n"
    "v_exp_f32 v58, v16\n"
    "v_cvt_pk_bf16_f32 v56, v12, v13\n"
    "v_exp_f32 v52, v17\n"
    "v_cvt_pk_bf16_f32 v57, v14, v15\n"
    "s_waitcnt vmcnt(11) lgkmcnt(2)\n"
    "v_mfma_f32_16x16x32_bf16 v[116:119], v[136:139], v[72:75], v[116:119]\n"
    "v_exp_f32 v59, v18\n"
    "v_exp_f32 v53, v19\n"
    "v_mfma_f32_32x32x16_bf16 v[4:19], v[164:167], v[60:63], 0\n"
    "ds_read_b128 v[72:75], v88 offset:42496\n"
    "v_exp_f32 v20, v20\n"
    "v_cvt_pk_bf16_f32 v58, v58, v52\n"
    "buffer_load_dwordx4 v[136:139], v88, %[rws], %[wo4] offen offset:0 sc1\n"
    "v_exp_f32 v21, v21\n"
    "v_cvt_pk_bf16_f32 v59, v59, v53\n"
    "s_waitcnt vmcnt(11) lgkmcnt(2)\n"
    "v_mfma_f32_16x16x32_bf16 v[100:103], v[120:123], v[68:71], v[100:103]\n"
    "v_exp_f32 v22, v22\n"
    "v_exp_f32 v23, v23\n"
    "v_mfma_f32_32x32x16_bf16 v[36:51], v[244:247], v[56:59], v[36:51]\n"
    "v_exp_f32 v24, v24\n"
    "v_cvt_pk_bf16_f32 v52, v20, v21\n"
    "v_exp_f32 v25, v25\n"
    "v_cvt_pk_bf16_f32 v53, v22, v23\n"
    "s_waitcnt vmcnt(10)\n"
    "v_mfma_f32_16x16x32_bf16 v[104:107], v[124:127], v[68:71], v[104:107]\n"
    "v_exp_f32 v26, v26\n"
    "v_exp_f32 v27, v27\n"
    "s_waitcnt lgkmcnt(1)\n"
    "v_mfma_f32_32x32x8_bf16 v[4:19], v[156:157], v[64:65], v[4:19]\n"
    "v_exp_f32 v28, v28\n"
    "v_cvt_pk_bf16_f32 v54, v24, v25\n"
    "v_exp_f32 v29, v29\n"
    "v_cvt_pk_bf16_f32 v55, v26, v27\n"
    "s_waitcnt vmcnt(9)\n"
    "v_mfma_f32_16x16x32_bf16 v[108:111], v[128:131], v[68:71], v[108:111]\n"
    "v_exp_f32 v30, v30\n"
    "v_exp_f32 v31, v31\n"
    "v_mfma_f32_32x32x16_bf16 v[36:51], v[248:251], v[52:55], v[36:51]\n"
    "v_exp_f32 v58, v32\n"
    "v_cvt_pk_bf16_f32 v56, v28, v29\n"
    "buffer_load_dwordx4 v[120:123], v88, %[rws], %[wo] offen offset:0 sc1\n"
    "v_exp_f32 v52, v33\n"
    "v_cvt_pk_bf16_f32 v57, v30, v31\n"
    "s_waitcnt vmcnt(9)\n"
    "v_mfma_f32_16x16x32_bf16 v[112:115], v[132:135], v[68:71], v[112:115]\n"
    "v_exp_f32 v59, v34\n"
    "v_exp_f32 v53, v35\n"
    "v_mfma_f32_32x32x16_bf16 v[20:35], v[168:171], v[60:63], 0\n"
    "v_exp_f32 v4, v4\n"
    "v_cvt_pk_bf16_f32 v58, v58, v52\n"
    "v_exp_f32 v5, v5\n"
    "v_cvt_pk_bf16_f32 v59, v59, v53\n"
    "s_waitcnt vmcnt(8) lgkmcnt(0)\n"
    "v_mfma_f32_16x16x32_bf16 v[100:103], v[140:143], v[72:75], v[100:103]\n"
    "v_exp_f32 v6, v6\n"
    "v_exp_f32 v7, v7\n"
    "v_mfma_f32_32x32x16_bf16 v[36:51], v[252:255], v[56:59], v[36:51]\n"
    "v_exp_f32 v8, v8\n"
    "v_cvt_pk_bf16_f32 v52, v4, v5\n"
    "v_exp_f32 v9, v9\n"
    "v_cvt_pk_bf16_f32 v53, v6, v7\n"
    "s_waitcnt vmcnt(7)\n"
    "v_mfma_f32_16x16x32_bf16 v[104:107], v[144:147], v[72:75], v[104:107]\n"
    "v_exp_f32 v10, v10\n"
    "v_exp_f32 v11, v11\n"
    "v_mfma_f32_32x32x8_bf16 v[20:35], v[158:159], v[64:65], v[20:35]\n"
    "ds_read_b128 v[64:67], v89 offset:6144\n"
    "v_exp_f32 v12, v12\n"
    "v_cvt_pk_bf16_f32 v54, v8, v9\n"
    "buffer_load_dwordx4 v[124:127], v88, %[rws], %[wo] offen offset:1024 sc1\n"
    "v_exp_f32 v13, v13\n"
    "v_cvt_pk_bf16_f32 v55, v10, v11\n"
    "s_waitcnt vmcnt(7)\n"
    "v_mfma_f32_16x16x32_bf16 v[108:111], v[148:151], v[72:75], v[108:111]\n"
    "v_exp_f32 v14, v14\n"
    "v_exp_f32 v15, v15\n"
    "ds_write_b128 v94, v[36:39]\n"
    "ds_write_b128 v94, v[40:43] offset:1024\n"
    "ds_write_b64 v91, v[44:45]\n"
    "v_mfma_f32_32x32x16_bf16 v[36:51], v[192:195], v[52:55], 0\n"
    "v_exp_f32 v58, v16\n"
    "v_cvt_pk_bf16_f32 v56, v12, v13\n"
    "v_add_u32 v89, 3584, v89\n"
    "v_exp_f32 v52, v17\n"
    "v_cvt_pk_bf16_f32 v57, v14, v15\n"
    "s_waitcnt vmcnt(6)\n"
    "v_mfma_f32_16x16x32_bf16 v[112:115], v[152:155], v[72:75], v[112:115]\n"
    "v_exp_f32 v59, v18\n"
    "v_exp_f32 v53, v19\n"
    "s_sub_u32 %[cnt], %[cnt], 1\n"
    "s_cmp_lg_u32 %[cnt], 1\n"
    "s_cbranch_scc1 LBFO_%=\n"
    "s_add_u32 %[pa], %[wo], 73728\n"
    "s_add_u32 %[pb], %[wo4], 73728\n"
    "s_add_u32 %[pm], %[wo8], 73728\n"
    "v_mfma_f32_32x32x16_bf16 v[4:19], v[172:175], v[60:63], 0\n"
    "s_bitcmp1_b32 %[xo], 31\n"
    "s_cbranch_scc1 LPFO4_%=\n"
    "LRFO4_%=:\n"
    "v_exp_f32 v20, v20\n"
    "v_cvt_pk_bf16_f32 v58, v58, v52\n"
    "buffer_load_dwordx4 v[168:171], v88, %[rws], %[pa] offen offset:1024 sc1\n"
    "v_exp_f32 v21, v21\n"
    "v_cvt_pk_bf16_f32 v59, v59, v53\n"
    "v_exp_f32 v22, v22\n"
    "v_exp_f32 v23, v23\n"
    "v_mfma_f32_32x32x16_bf16 v[36:51], v[196:199], v[56:59], v[36:51]\n"
    "s_waitcnt lgkmcnt(0)\n"
    "s_mov_b64 exec, %[me]\n"
    "ds_inc_u32 v0, v0\n"
    "s_mov_b64 exec, -1\n"
    "s_add_u32 %[nd], %[nd], 1\n"
    "v_exp_f32 v24, v24\n"
    "v_cvt_pk_bf16_f32 v52, v20, v21\n"
    "buffer_load_dwordx4 v[128:131], v88, %[rws], %[wo] offen offset:2048 sc1\n"
    "v_exp_f32 v25, v25\n"
    "v_cvt_pk_bf16_f32 v53, v22, v23\n"
    "v_exp_f32 v26, v26\n"
    "v_exp_f32 v27, v27\n"
    "s_setprio 0\n"
    "v_mfma_f32_32x32x8_bf16 v[4:19], v[156:157], v[64:65], v[4:19]\n"
    "s_waitcnt vmcnt(17)\n"
    "ds_read_b64_tr_b16 v[76:77], v96\n"
    "ds_read_b64_tr_b16 v[78:79], v96 offset:512\n"
    "v_exp_f32 v28, v28\n"
    "v_cvt_pk_bf16_f32 v54, v24, v25\n"
    "v_exp_f32 v29, v29\n"
    "v_cvt_pk_bf16_f32 v55, v26, v27\n"
    "v_exp_f32 v30, v30\n"
    "v_exp_f32 v31, v31\n"
    "v_mfma_f32_32x32x16_bf16 v[36:51], v[200:203], v[52:55], v[36:51]\n"
    "v_exp_f32 v58, v32\n"
    "v_cvt_pk_bf16_f32 v56, v28, v29\n"
    "buffer_load_dwordx4 v[192:195], v88, %[rws], %[pa] offen offset:2048 sc1\n"
    "v_exp_f32 v52, v33\n"
    "v_cvt_pk_bf16_f32 v57, v30, v31\n"
    "v_add_u32 v1, %[w8b], v88\n"
    "s_add_u32 %[w8b], %[w8b], 1024\n"
    "s_waitcnt vmcnt(8)\n"
    "ds_write_b128 v1, v[80:83]\n"
    "v_exp_f32 v59, v34\n"
    "v_exp_f32 v53, v35\n"
    "v_mfma_f32_32x32x16_bf16 v[20:35], v[176:179], v[60:63], 0\n"
    "s_waitcnt lgkmcnt(1)\n"
    "buffer_store_dwordx4 v[76:79], v98, %[rws], %[pd] offen sc1\n"
    "s_add_u32 %[pd], %[pd], %[pdi]\n"
    "v_xor_b32 v96, 1024, v96\n"
    "v_exp_f32 v4, v4\n"
    "v_cvt_pk_bf16_f32 v58, v58, v52\n"
    "buffer_load_dwordx4 v[132:135], v88, %[rws], %[wo] offen offset:3072 sc1\n"
    "v_exp_f32 v5, v5\n"
    "v_cvt_pk_bf16_f32 v59, v59, v53\n"
    "v_exp_f32 v6, v6\n"
    "v_exp_f32 v7, v7\n"
    "v_mfma_f32_32x32x16_bf16 v[36:51], v[204:207], v[56:59], v[36:51]\n"
    "v_exp_f32 v8, v8\n"
    "v_cvt_pk_bf16_f32 v52, v4, v5\n"
    "v_exp_f32 v9, v9\n"
    "v_cvt_pk_bf16_f32 v53, v6, v7\n"
    "v_exp_f32 v10, v10\n"
    "v_exp_f32 v11, v11\n"
    "s_setprio 2\n"
    "v_mfma_f32_32x32x8_bf16 v[20:35], v[158:159], v[64:65], v[20:35]\n"
    "LPP7:\n"
    "s_cbranch_execz LPK2\n"
    "ds_read_b128 v[64:67], v89 offset:1024\n"
    "v_exp_f32 v12, v12\n"
    "v_cvt_pk_bf16_f32 v54, v8, v9\n"
    "ds_read_b32 v1, v0\n"
    "v_exp_f32 v13, v13\n"
    "v_cvt_pk_bf16_f32 v55, v10, v11\n"
    "buffer_load_dwordx4 v[140:143], v88, %[rws], %[wo4] offen offset:1024 sc1\n"
    "v_exp_f32 v14, v14\n"
    "v_exp_f32 v15, v15\n"
    "v_mfma_f32_32x32x16_bf16 v[36:51], v[208:211], v[52:55], v[36:51]\n"
    "v_exp_f32 v58, v16\n"
    "v_cvt_pk_bf16_f32 v56, v12, v13\n"
    "s_waitcnt lgkmcnt(0)\n"
    "v_cmp_gt_u32 vcc, %[nd], v1\n"
    "s_cbranch_vccnz LPFO6_%=\n"
    "LRFO6_%=:\n"
    "ds_read2st64_b32 v[84:85], v92 offset0:1 offset1:3\n"
    "ds_read2st64_b32 v[86:87], v92 offset0:5 offset1:7\n"
    "ds_read2st64_b64 v[76:79], v95 offset1:4\n"
    "ds_read2st64_b64 v[80:83], v95 offset0:8 offset1:12\n"
    "ds_read2st64_b64 v[68:71], v92 offset1:1\n"
    "ds_read2st64_b64 v[72:75], v92 offset0:2 offset1:3\n"
    "v_exp_f32 v52, v17\n"
    "v_cvt_pk_bf16_f32 v57, v14, v15\n"
    "v_exp_f32 v59, v18\n"
    "v_exp_f32 v53, v19\n"
    "v_mfma_f32_32x32x16_bf16 v[4:19], v[180:183], v[60:63], 0\n"
    "buffer_load_dwordx4 v[172:175], v88, %[rws], %[pa] offen offset:3072 sc1\n"
    "v_exp_f32 v20, v20\n"
    "v_cvt_pk_bf16_f32 v58, v58, v52\n"
    "v_exp_f32 v21, v21\n"
    "v_cvt_pk_bf16_f32 v59, v59, v53\n"
    "v_exp_f32 v22, v22\n"
    "v_exp_f32 v23, v23\n"
    "v_mfma_f32_32x32x16_bf16 v[36:51], v[212:215], v[56:59], v[36:51]\n"
    "s_waitcnt lgkmcnt(5)\n"
    "v_add_f32 v84, v84, v85\n"
    "s_waitcnt lgkmcnt(4)\n"
    "v_add_f32 v86, v86, v87\n"
    "v_exp_f32 v24, v24\n"
    "v_cvt_pk_bf16_f32 v52, v20, v21\n"
    "buffer_load_dwordx4 v[144:147], v88, %[rws], %[wo4] offen offset:2048 sc1\n"
    "v_exp_f32 v25, v25\n"
    "v_cvt_pk_bf16_f32 v53, v22, v23\n"
    "v_add_f32 v84, v84, v86\n"
    "v_exp_f32 v26, v26\n"
    "s_waitcnt lgkmcnt(3)\n"
    "v_add_f32 v76, v76, v78\n"
    "v_exp_f32 v27, v27\n"
    "v_mfma_f32_32x32x8_bf16 v[4:19], v[156:157], v[64:65], v[4:19]\n"
    "s_waitcnt vmcnt(6)\n"
    "s_bitcmp1_b32 %[xo], 31\n"
    "s_cbranch_scc1 LPFO5_%=\n"
    "LRFO5_%=:\n"
    "v_add_f32 v77, v77, v79\n"
    "v_exp_f32 v28, v28\n"
    "v_cvt_pk_bf16_f32 v54, v24, v25\n"
    "v_exp_f32 v29, v29\n"
    "v_cvt_pk_bf16_f32 v55, v26, v27\n"
    "v_add_f32_dpp v84, v84, v84 quad_perm:[1,0,3,2] row_mask:0xf bank_mask:0xf\n"
    "buffer_load_dwordx4 v[164:167], v88, %[rws], %[pa] offen offset:0 sc1\n"
    "v_exp_f32 v30, v30\n"
    "s_waitcnt lgkmcnt(2)\n"
    "v_add_f32 v80, v80, v82\n"
    "v_exp_f32 v31, v31\n"
    "v_mfma_f32_32x32x16_bf16 v[36:51], v[216:219], v[52:55], v[36:51]\n"
    "v_rcp_f32 v84, v84\n"
    "v_exp_f32 v58, v32\n"
    "v_cvt_pk_bf16_f32 v56, v28, v29\n"
    "v_exp_f32 v52, v33\n"
    "v_cvt_pk_bf16_f32 v57, v30, v31\n"
    "v_add_f32 v81, v81, v83\n"
    "v_exp_f32 v59, v34\n"
    "v_add_f32 v76, v76, v80\n"
    "v_exp_f32 v53, v35\n"
    "v_mfma_f32_32x32x16_bf16 v[20:35], v[184:187], v[60:63], 0\n"
    "v_add_f32 v77, v77, v81\n"
    "v_exp_f32 v4, v4\n"
    "v_cvt_pk_bf16_f32 v58, v58, v52\n"
    "buffer_load_dwordx4 v[148:151], v88, %[rws], %[wo4] offen offset:3072 sc1\n"
    "v_exp_f32 v5, v5\n"
    "v_cvt_pk_bf16_f32 v59, v59, v53\n"
    "v_mul_f32 v76, v76, v84\n"
    "v_exp_f32 v6, v6\n"
    "v_mul_f32 v77, v77, v84\n"
    "v_exp_f32 v7, v7\n"
    "v_mfma_f32_32x32x16_bf16 v[36:51], v[220:223], v[56:59], v[36:51]\n"
    "v_cvt_pk_bf16_f32 v76, v76, v77\n"
    "v_exp_f32 v8, v8\n"
    "v_cvt_pk_bf16_f32 v52, v4, v5\n"
    "ds_write_b32 v93, v76\n"
    "v_exp_f32 v9, v9\n"
    "v_cvt_pk_bf16_f32 v53, v6, v7\n"
    "s_cmp_lg_u32 %[lw], %[cnt]\n"
    "s_cbranch_scc1 LLFO2_%=\n"
    "s_waitcnt lgkmcnt(2)\n"
    "v_add_f32 v68, v68, v70\n"
    "v_add_f32 v69, v69, v71\n"
    "s_waitcnt lgkmcnt(1)\n"
    "v_add_f32 v72, v72, v74\n"
    "v_add_f32 v73, v73, v75\n"
    "v_add_f32 v68, v68, v72\n"
    "v_add_f32 v69, v69, v73\n"
    "v_and_b32 v70, 0xf0, v93\n"
    "v_lshrrev_b32 v71, 8, v88\n"
    "v_mul_f32 v68, v68, v84\n"
    "v_mul_f32 v69, v69, v84\n"
    "v_lshl_add_u32 v70, v71, 2, v70\n"
    "v_cvt_pk_bf16_f32 v68, v68, v69\n"
    "v_add_u32 v70, %[lo], v70\n"
    "ds_write_b32 v70, v68\n"
    "LLFO2_%=:\n"
    "v_exp_f32 v10, v10\n"
    "buffer_load_dwordx4 v[196:199], v88, %[rws], %[wm] offen sc1\n"
    "s_add_u32 %[wm], %[wm], 36864\n"
    "v_exp_f32 v11, v11\n"
    "v_mfma_f32_32x32x8_bf16 v[20:35], v[158:159], v[64:65], v[20:35]\n"
    "ds_read_b128 v[64:67], v89 offset:2048\n"
    "v_exp_f32 v12, v12\n"
    "v_cvt_pk_bf16_f32 v54, v8, v9\n"
    "v_exp_f32 v13, v13\n"
    "v_cvt_pk_bf16_f32 v55, v10, v11\n"
    "buffer_load_dwordx4 v[152:155], v88, %[rws], %[wo8] offen offset:0 sc1\n"
    "v_exp_f32 v14, v14\n"
    "v_exp_f32 v15, v15\n"
    "v_mfma_f32_32x32x16_bf16 v[36:51], v[224:227], v[52:55], v[36:51]\n"
    "s_waitcnt lgkmcnt(1)\n"
    "s_mov_b64 exec, %[me]\n"
    "ds_inc_u32 v0, v0\n"
    "s_mov_b64 exec, -1\n"
    "s_add_u32 %[nd], %[nd], 1\n"
    "v_exp_f32 v58, v16\n"
    "v_cvt_pk_bf16_f32 v56, v12, v13\n"
    "v_exp_f32 v52, v17\n"
    "v_cvt_pk_bf16_f32 v57, v14, v15\n"
    "v_exp_f32 v59, v18\n"
    "v_exp_f32 v53, v19\n"
    "s_setprio 0\n"
    "v_mfma_f32_32x32x16_bf16 v[4:19], v[188:191], v[60:63], 0\n"
    "ds_read_b64_tr_b16 v[76:77], v96\n"
    "ds_read_b64_tr_b16 v[78:79], v96 offset:512\n"
    "v_exp_f32 v20, v20\n"
    "v_cvt_pk_bf16_f32 v58, v58, v52\n"
    "buffer_load_dwordx4 v[200:203], v88, %[rws], %[pb] offen offset:1024 sc1\n"
    "v_exp_f32 v21, v21\n"
    "v_cvt_pk_bf16_f32 v59, v59, v53\n"
    "v_add_u32 v89, %[wl], v88\n"
    "v_exp_f32 v22, v22\n"
    "v_exp_f32 v23, v23\n"
    "v_mfma_f32_32x32x16_bf16 v[36:51], v[228:231], v[56:59], v[36:51]\n"
    "v_exp_f32 v24, v24\n"
    "v_cvt_pk_bf16_f32 v52, v20, v21\n"
    "v_exp_f32 v25, v25\n"
    "v_cvt_pk_bf16_f32 v53, v22, v23\n"
    "v_exp_f32 v26, v26\n"
    "v_exp_f32 v27, v27\n"
    "s_waitcnt lgkmcnt(3)\n"
    "v_mfma_f32_32x32x8_bf16 v[4:19], v[156:157], v[64:65], v[4:19]\n"
    "s_waitcnt lgkmcnt(0)\n"
    "buffer_store_dwordx4 v[76:79], v98, %[rws], %[pd] offen sc1\n"
    "s_add_u32 %[pd], %[pd], %[pdi]\n"
    "v_exp_f32 v28, v28\n"
    "v_cvt_pk_bf16_f32 v54, v24, v25\n"
    "v_exp_f32 v29, v29\n"
    "v_cvt_pk_bf16_f32 v55, v26, v27\n"
    "v_lshrrev_b32 v84, 5, v88\n"
    "v_exp_f32 v30, v30\n"
    "v_and_or_b32 v80, v84, 24, %[m1]\n"
    "v_exp_f32 v31, v31\n"
    "v_mfma_f32_32x32x16_bf16 v[36:51], v[232:235], v[52:55], v[36:51]\n"
    "v_exp_f32 v58, v32\n"
    "v_cvt_pk_bf16_f32 v56, v28, v29\n"
    "buffer_load_dwordx4 v[188:191], v88, %[rws], %[wm] offen sc1\n"
    "s_movk_i32 %[wm], 1152\n"
    "s_sub_u32 %[w8b], %[w8b], 7168\n"
    "v_add_u32 v96, %[w8b], v88\n"
    "v_exp_f32 v52, v33\n"
    "v_cvt_pk_bf16_f32 v57, v30, v31\n"
    "v_and_or_b32 v81, v84, 24, %[m2]\n"
    "v_exp_f32 v59, v34\n"
    "v_and_b32 v84, 4, v84\n"
    "v_exp_f32 v53, v35\n"
    "s_setprio 2\n"
    "v_mfma_f32_32x32x16_bf16 v[20:35], v[160:163], v[60:63], 0\n"
    "v_exp_f32 v4, v4\n"
    "v_cvt_pk_bf16_f32 v58, v58, v52\n"
    "ds_read_b32 v1, v0\n"
    "v_exp_f32 v5, v5\n"
    "v_cvt_pk_bf16_f32 v59, v59, v53\n"
    "v_bfe_u32 v83, v88, 4, 3\n"
    "buffer_load_dwordx4 v[204:207], v88, %[rws], %[pb] offen offset:3072 sc1\n"
    "v_exp_f32 v6, v6\n"
    "v_lshl_add_u32 v80, v84, 7, v80\n"
    "ds_read_b128 v[216:219], v96 offset:0\n"
    "v_exp_f32 v7, v7\n"
    "v_mfma_f32_32x32x16_bf16 v[36:51], v[236:239], v[56:59], v[36:51]\n"
    "v_exp_f32 v8, v8\n"
    "v_cvt_pk_bf16_f32 v52, v4, v5\n"
    "s_waitcnt lgkmcnt(1)\n"
    "v_cmp_gt_u32 vcc, %[nd], v1\n"
    "s_cbranch_vccnz LPFO7_%=\n"
    "LRFO7_%=:\n"
    "ds_read_b128 v[72:75], v90 offset:0\n"
    "ds_read_b128 v[68:71], v88 offset:41472\n"
    "v_exp_f32 v9, v9\n"
    "v_cvt_pk_bf16_f32 v53, v6, v7\n"
    "v_mad_u32_u24 v82, v83, %[wm], v80\n"
    "v_exp_f32 v10, v10\n"
    "v_mad_u32_u24 v83, v83, %[wm], v81\n"
    "ds_read_b128 v[184:187], v96 offset:1024\n"
    "v_exp_f32 v11, v11\n"
    "v_mfma_f32_32x32x8_bf16 v[20:35], v[158:159], v[64:65], v[20:35]\n"
    "v_exp_f32 v12, v12\n"
    "v_cvt_pk_bf16_f32 v54, v8, v9\n"
    "buffer_load_dwordx4 v[180:183], v88, %[rws], %[w8g] offen offset:0 sc1\n"
    "v_exp_f32 v13, v13\n"
    "v_cvt_pk_bf16_f32 v55, v10, v11\n"
    "v_exp_f32 v14, v14\n"
    "ds_read_b128 v[220:223], v96 offset:2048\n"
    "v_exp_f32 v15, v15\n"
    "v_mfma_f32_32x32x16_bf16 v[36:51], v[240:243], v[52:55], v[36:51]\n"
    "v_exp_f32 v58, v16\n"
    "v_cvt_pk_bf16_f32 v56, v12, v13\n"
    "buffer_load_dwordx4 v[176:179], v88, %[rws], %[pb] offen offset:2048 sc1\n"
    "v_exp_f32 v52, v17\n"
    "v_cvt_pk_bf16_f32 v57, v14, v15\n"
    "s_waitcnt lgkmcnt(3)\n"
    "v_mfma_f32_16x16x32_bf16 v[116:119], v[136:139], v[72:75], v[116:119]\n"
    "v_exp_f32 v59, v18\n"
    "ds_read_b128 v[224:227], v96 offset:3072\n"
    "v_exp_f32 v53, v19\n"
    "ds_read_b128 v[72:75], v88 offset:42496\n"
    "v_exp_f32 v20, v20\n"
    "ds_read2_b64 v[4:7], v80 offset1:32\n"
    "v_cvt_pk_bf16_f32 v58, v58, v52\n"
    "ds_read2_b64 v[16:19], v81 offset1:144\n"
    "buffer_load_dwordx4 v[208:211], v88, %[rws], %[pm] offen offset:0 sc1\n"
    "v_exp_f32 v21, v21\n"
    "ds_read_b64 v[60:61], v82 offset:4096\n"
    "v_cvt_pk_bf16_f32 v59, v59, v53\n"
    "s_waitcnt lgkmcnt(7)\n"
    "v_mfma_f32_16x16x32_bf16 v[100:103], v[120:123], v[68:71], v[100:103]\n"
    "v_exp_f32 v22, v22\n"
    "v_exp_f32 v23, v23\n"
    "v_mfma_f32_32x32x16_bf16 v[36:51], v[244:247], v[56:59], v[36:51]\n"
    "v_exp_f32 v24, v24\n"
    "ds_read_b64 v[62:63], v82 offset:4352\n"
    "v_cvt_pk_bf16_f32 v52, v20, v21\n"
    "ds_read_b64 v[66:67], v83 offset:4096\n"
    "v_exp_f32 v25, v25\n"
    "ds_read2_b64 v[8:11], v80 offset0:144 offset1:176\n"
    "v_cvt_pk_bf16_f32 v53, v22, v23\n"
    "buffer_load_dwordx4 v[212:215], v88, %[rws], %[w8g] offen offset:1024 sc1\n"
    "v_mfma_f32_16x16x32_bf16 v[104:107], v[124:127], v[68:71], v[104:107]\n"
    "v_exp_f32 v26, v26\n"
    "ds_read_b128 v[228:231], v96 offset:5120\n"
    "v_exp_f32 v27, v27\n"
    "v_exp_f32 v28, v28\n"
    "ds_read_b64 v[12:13], v80 offset:2304\n"
    "v_cvt_pk_bf16_f32 v54, v24, v25\n"
    "ds_read_b64 v[14:15], v80 offset:2560\n"
    "v_exp_f32 v29, v29\n"
    "ds_read_b64 v[64:65], v81 offset:2304\n"
    "v_cvt_pk_bf16_f32 v55, v26, v27\n"
    "v_mfma_f32_16x16x32_bf16 v[108:111], v[128:131], v[68:71], v[108:111]\n"
    "v_exp_f32 v30, v30\n"
    "ds_read_b128 v[232:235], v96 offset:6144\n"
    "v_exp_f32 v31, v31\n"
    "v_mfma_f32_32x32x16_bf16 v[36:51], v[248:251], v[52:55], v[36:51]\n"
    "v_exp_f32 v58, v32\n"
    "ds_read_b128 v[156:159], v89\n"
    "v_cvt_pk_bf16_f32 v56, v28, v29\n"
    "ds_read_b128 v[160:163], v89 offset:28672\n"
    "v_exp_f32 v52, v33\n"
    "ds_write_b128 v94, v[116:119] offset:20480\n"
    "v_cvt_pk_bf16_f32 v57, v30, v31\n"
    "s_waitcnt vmcnt(15)\n"
    "v_mfma_f32_16x16x32_bf16 v[112:115], v[132:135], v[68:71], v[112:115]\n"
    "v_exp_f32 v59, v34\n"
    "v_exp_f32 v53, v35\n"
    "v_cvt_pk_bf16_f32 v58, v58, v52\n"
    "v_cvt_pk_bf16_f32 v59, v59, v53\n"
    "ds_read_b128 v[20:23], v88 offset:43520\n"
    "s_setprio 0\n"
    "v_mfma_f32_32x32x16_bf16 v[36:51], v[252:255], v[56:59], v[36:51]\n"
    "s_waitcnt vmcnt(14) lgkmcnt(15)\n"
    "v_mfma_f32_16x16x32_bf16 v[100:103], v[140:143], v[72:75], v[100:103]\n"
    "v_and_b32 v32, 0x70, v88\n"
    "v_xor_b32 v35, 8192, v94\n"
    "v_lshrrev_b32 v33, 9, v88\n"
    "s_waitcnt vmcnt(12)\n"
    "v_mfma_f32_16x16x32_bf16 v[104:107], v[144:147], v[72:75], v[104:107]\n"
    "v_mul_u32_u24 v34, 5, v32\n"
    "v_lshl_or_b32 v32, v33, 8, v32\n"
    "v_lshl_add_u32 v33, v33, 2, v34\n"
    "s_waitcnt vmcnt(10)\n"
    "v_mfma_f32_16x16x32_bf16 v[108:111], v[148:151], v[72:75], v[108:111]\n"
    "v_bfe_u32 v1, v88, 8, 1\n"
    "v_add_u32 v33, %[w64], v33\n"
    "v_lshl_or_b32 v32, v1, 3, v32\n"
    "s_waitcnt vmcnt(8)\n"
    "v_mfma_f32_16x16x32_bf16 v[112:115], v[152:155], v[72:75], v[112:115]\n"
    "v_and_b32 v1, 128, v88\n"
    "v_lshl_or_b32 v32, v1, 6, v32\n"
    "ds_write_b128 v94, v[36:39]\n"
    "ds_write_b128 v94, v[40:43] offset:1024\n"
    "ds_write_b64 v91, v[44:45]\n"
    "s_waitcnt lgkmcnt(3)\n"
    "v_mfma_f32_16x16x32_bf16 v[100:103], v[216:219], v[20:23], v[100:103]\n"
    "v_mfma_f32_16x16x32_bf16 v[104:107], v[184:187], v[20:23], v[104:107]\n"
    "v_mfma_f32_16x16x32_bf16 v[108:111], v[220:223], v[20:23], v[108:111]\n"
    "v_mfma_f32_16x16x32_bf16 v[112:115], v[224:227], v[20:23], v[112:115]\n"
    "s_mov_b64 %[me], 0x3fb8aa3b\n"
    "s_bitset0_b32 %[xo], 31\n"
    "s_cmp_eq_u32 %[lw], 0\n"
    "s_waitcnt lgkmcnt(0)\n"
    "s_barrier\n"
    "s_cbranch_scc1 LTFO_%=\n"
    "ds_read2st64_b32 v[84:85], v92 offset0:1 offset1:3\n"
    "ds_read2st64_b32 v[86:87], v92 offset0:5 offset1:7\n"
    "ds_read2st64_b64 v[76:79], v95 offset1:4\n"
    "ds_read2st64_b64 v[80:83], v95 offset0:8 offset1:12\n"
    "v_lshlrev_b32 v144, 16, v8\n"
    "v_and_b32 v145, 0xffff0000, v8\n"
    "v_lshlrev_b32 v146, 16, v9\n"
    "v_and_b32 v147, 0xffff0000, v9\n"
    "v_lshlrev_b32 v148, 16, v10\n"
    "v_and_b32 v149, 0xffff0000, v10\n"
    "v_lshlrev_b32 v150, 16, v11\n"
    "v_and_b32 v151, 0xffff0000, v11\n"
    "v_lshlrev_b32 v244, 16, v18\n"
    "v_and_b32 v245, 0xffff0000, v18\n"
    "v_lshlrev_b32 v246, 16, v19\n"
    "v_and_b32 v247, 0xffff0000, v19\n"
    "v_pk_mul_f32 v[144:145], v[144:145], %[me] op_sel_hi:[1,0]\n"
    "v_pk_mul_f32 v[146:147], v[146:147], %[me] op_sel_hi:[1,0]\n"
    "v_pk_mul_f32 v[148:149], v[148:149], %[me] op_sel_hi:[1,0]\n"
    "v_pk_mul_f32 v[150:151], v[150:151], %[me] op_sel_hi:[1,0]\n"
    "s_waitcnt lgkmcnt(3)\n"
    "v_add_f32 v84, v84, v85\n"
    "s_waitcnt lgkmcnt(2)\n"
    "v_add_f32 v86, v86, v87\n"
    "v_add_f32 v84, v84, v86\n"
    "s_waitcnt lgkmcnt(1)\n"
    "v_add_f32 v76, v76, v78\n"
    "ds_read_b128 v[28:31], v35 offset:20480\n"
    "v_add_f32 v77, v77, v79\n"
    "v_add_f32_dpp v84, v84, v84 quad_perm:[1,0,3,2] row_mask:0xf bank_mask:0xf\n"
    "s_waitcnt lgkmcnt(1)\n"
    "v_add_f32 v80, v80, v82\n"
    "v_rcp_f32 v84, v84\n"
    "v_add_f32 v81, v81, v83\n"
    "v_add_f32 v76, v76, v80\n"
    "v_add_f32 v77, v77, v81\n"
    "v_mul_f32 v76, v76, v84\n"
    "v_mul_f32 v77, v77, v84\n"
    "v_cvt_pk_bf16_f32 v76, v76, v77\n"
    "ds_write_b32 v93, v76\n"
    "s_waitcnt lgkmcnt(1)\n"
    "v_pk_add_f32 v[116:117], v[116:117], v[28:29]\n"
    "v_pk_add_f32 v[118:119], v[118:119], v[30:31]\n"
    "v_pk_mul_f32 v[244:245], v[244:245], %[me] op_sel_hi:[1,0]\n"
    "v_pk_mul_f32 v[246:247], v[246:247], %[me] op_sel_hi:[1,0]\n"
    "v_lshlrev_b32 v120, 16, v4\n"
    "v_and_b32 v121, 0xffff0000, v4\n"
    "v_lshlrev_b32 v122, 16, v5\n"
    "v_and_b32 v123, 0xffff0000, v5\n"
    "v_lshlrev_b32 v124, 16, v6\n"
    "v_and_b32 v125, 0xffff0000, v6\n"
    "v_lshlrev_b32 v126, 16, v7\n"
    "v_and_b32 v127, 0xffff0000, v7\n"
    "LTFO_%=j:\n"
    "v_mfma_f32_16x16x32_bf16 v[116:119], v[156:159], v[20:23], v[116:119]\n"
    "s_waitcnt lgkmcnt(0)\n"
    "s_barrier\n"
    "ds_read_b128 v[68:71], v88 offset:41472\n"
    "ds_read_b128 v[72:75], v88 offset:42496\n"
    "ds_read_b128 v[24:27], v88 offset:44544\n"
    "v_lshlrev_b32 v152, 16, v16\n"
    "v_and_b32 v153, 0xffff0000, v16\n"
    "v_lshlrev_b32 v154, 16, v17\n"
    "v_and_b32 v155, 0xffff0000, v17\n"
    "v_lshlrev_b32 v136, 16, v12\n"
    "v_and_b32 v137, 0xffff0000, v12\n"
    "v_lshlrev_b32 v138, 16, v13\n"
    "v_and_b32 v139, 0xffff0000, v13\n"
    "v_lshlrev_b32 v140, 16, v14\n"
    "v_and_b32 v141, 0xffff0000, v14\n"
    "s_waitcnt lgkmcnt(2)\n"
    "v_mfma_f32_16x16x32_bf16 v[100:103], v[164:167], v[68:71], v[100:103]\n"
    "v_lshlrev_b32 v142, 16, v15\n"
    "v_and_b32 v143, 0xffff0000, v15\n"
    "v_mfma_f32_16x16x32_bf16 v[108:111], v[192:195], v[68:71], v[108:111]\n"
    "v_lshlrev_b32 v240, 16, v64\n"
    "v_and_b32 v241, 0xffff0000, v64\n"
    "v_mfma_f32_16x16x32_bf16 v[116:119], v[196:199], v[68:71], v[116:119]\n"
    "v_lshlrev_b32 v242, 16, v65\n"
    "v_and_b32 v243, 0xffff0000, v65\n"
    "s_waitcnt vmcnt(7) lgkmcnt(1)\n"
    "v_mfma_f32_16x16x32_bf16 v[100:103], v[200:203], v[72:75], v[100:103]\n"
    "v_lshlrev_b32 v128, 16, v60\n"
    "v_and_b32 v129, 0xffff0000, v60\n"
    "s_waitcnt vmcnt(4)\n"
    "v_mfma_f32_16x16x32_bf16 v[108:111], v[204:207], v[72:75], v[108:111]\n"
    "v_lshlrev_b32 v130, 16, v61\n"
    "v_and_b32 v131, 0xffff0000, v61\n"
    "v_mfma_f32_16x16x32_bf16 v[116:119], v[188:191], v[72:75], v[116:119]\n"
    "v_lshlrev_b32 v132, 16, v62\n"
    "v_and_b32 v133, 0xffff0000, v62\n"
    "s_waitcnt lgkmcnt(0)\n"
    "v_mfma_f32_16x16x32_bf16 v[100:103], v[228:231], v[24:27], v[100:103]\n"
    "v_lshlrev_b32 v134, 16, v63\n"
    "v_and_b32 v135, 0xffff0000, v63\n"
    "s_waitcnt vmcnt(3)\n"
    "v_mfma_f32_16x16x32_bf16 v[108:111], v[180:183], v[24:27], v[108:111]\n"
    "v_lshlrev_b32 v236, 16, v66\n"
    "v_and_b32 v237, 0xffff0000, v66\n"
    "v_mfma_f32_16x16x32_bf16 v[116:119], v[160:163], v[24:27], v[116:119]\n"
    "v_lshlrev_b32 v238, 16, v67\n"
    "v_and_b32 v239, 0xffff0000, v67\n"
    "v_mfma_f32_16x16x32_bf16 v[104:107], v[168:171], v[68:71], v[104:107]\n"
    "v_mfma_f32_16x16x32_bf16 v[112:115], v[172:175], v[68:71], v[112:115]\n"
    "s_waitcnt vmcnt(2)\n"
    "v_mfma_f32_16x16x32_bf16 v[104:107], v[176:179], v[72:75], v[104:107]\n"
    "v_add_f32_dpp v36, v100, v100 row_ror:8 row_mask:0xf bank_mask:0x3\n"
    "v_add_f32_dpp v37, v101, v101 row_ror:8 row_mask:0xf bank_mask:0x3\n"
    "v_add_f32_dpp v38, v102, v102 row_ror:8 row_mask:0xf bank_mask:0x3\n"
    "s_waitcnt vmcnt(1)\n"
    "v_mfma_f32_16x16x32_bf16 v[112:115], v[208:211], v[72:75], v[112:115]\n"
    "v_add_f32_dpp v39, v103, v103 row_ror:8 row_mask:0xf bank_mask:0x3\n"
    "v_add_f32_dpp v36, v108, v108 row_ror:8 row_mask:0xf bank_mask:0xc\n"
    "v_add_f32_dpp v37, v109, v109 row_ror:8 row_mask:0xf bank_mask:0xc\n"
    "v_mfma_f32_16x16x32_bf16 v[104:107], v[232:235], v[24:27], v[104:107]\n"
    "v_add_f32_dpp v38, v110, v110 row_ror:8 row_mask:0xf bank_mask:0xc\n"
    "v_add_f32_dpp v39, v111, v111 row_ror:8 row_mask:0xf bank_mask:0xc\n"
    "v_add_f32_dpp v116, v116, v116 row_ror:8 row_mask:0xf bank_mask:0xf\n"
    "s_waitcnt vmcnt(0)\n"
    "v_mfma_f32_16x16x32_bf16 v[112:115], v[212:215], v[24:27], v[112:115]\n"
    "v_add_f32_dpp v117, v117, v117 row_ror:8 row_mask:0xf bank_mask:0xf\n"
    "v_add_f32_dpp v118, v118, v118 row_ror:8 row_mask:0xf bank_mask:0xf\n"
    "v_add_f32_dpp v119, v119, v119 row_ror:8 row_mask:0xf bank_mask:0xf\n"
    "v_pk_fma_f32 v[36:37], v[36:37], v[120:121], v[128:129]\n"
    "v_pk_fma_f32 v[38:39], v[38:39], v[122:123], v[130:131]\n"
    "v_pk_fma_f32 v[116:117], v[116:117], v[152:153], v[236:237]\n"
    "v_pk_fma_f32 v[118:119], v[118:119], v[154:155], v[238:239]\n"
    "v_pk_add_f32 v[52:53], v[36:37], v[38:39]\n"
    "v_pk_mul_f32 v[54:55], v[36:37], v[36:37]\n"
    "v_pk_fma_f32 v[54:55], v[38:39], v[38:39], v[54:55]\n"
    "s_mov_b64 exec, %[mk]\n"
    "v_pk_add_f32 v[52:53], v[52:53], v[116:117]\n"
    "v_pk_fma_f32 v[54:55], v[116:117], v[116:117], v[54:55]\n"
    "v_pk_add_f32 v[52:53], v[52:53], v[118:119]\n"
    "v_pk_fma_f32 v[54:55], v[118:119], v[118:119], v[54:55]\n"
    "s_mov_b64 exec, -1\n"
    "v_add_f32_dpp v40, v104, v104 row_ror:8 row_mask:0xf bank_mask:0x3\n"
    "v_add_f32_dpp v41, v105, v105 row_ror:8 row_mask:0xf bank_mask:0x3\n"
    "v_add_f32_dpp v42, v106, v106 row_ror:8 row_mask:0xf bank_mask:0x3\n"
    "v_add_f32_dpp v43, v107, v107 row_ror:8 row_mask:0xf bank_mask:0x3\n"
    "v_add_f32_dpp v40, v112, v112 row_ror:8 row_mask:0xf bank_mask:0xc\n"
    "v_add_f32_dpp v41, v113, v113 row_ror:8 row_mask:0xf bank_mask:0xc\n"
    "v_add_f32_dpp v42, v114, v114 row_ror:8 row_mask:0xf bank_mask:0xc\n"
    "v_add_f32_dpp v43, v115, v115 row_ror:8 row_mask:0xf bank_mask:0xc\n"
    "v_pk_fma_f32 v[40:41], v[40:41], v[124:125], v[132:133]\n"
    "v_pk_fma_f32 v[42:43], v[42:43], v[126:127], v[134:135]\n"
    "v_pk_add_f32 v[52:53], v[52:53], v[40:41]\n"
    "v_pk_fma_f32 v[54:55], v[40:41], v[40:41], v[54:55]\n"
    "v_pk_add_f32 v[52:53], v[52:53], v[42:43]\n"
    "v_pk_fma_f32 v[54:55], v[42:43], v[42:43], v[54:55]\n"
    "v_add_f32 v52, v52, v53\n"
    "v_cvt_pk_bf16_f32 v44, v36, v37\n"
    "v_add_f32 v54, v54, v55\n"
    "v_cvt_pk_bf16_f32 v45, v38, v39\n"
    "s_nop 0\n"
    "v_permlane32_swap_b32 v52, v54\n"
    "v_add_f32 v52, v52, v54\n"
    "v_mov_b32 v53, v52\n"
    "buffer_store_dwordx2 v[44:45], v32, %[rws], %[xo] offen sc1\n"
    "s_add_u32 %[t0], %[xo], 4096\n"
    "v_permlane16_swap_b32 v52, v53\n"
    "v_add_f32 v52, v52, v53\n"
    "v_cvt_pk_bf16_f32 v46, v40, v41\n"
    "v_cvt_pk_bf16_f32 v47, v42, v43\n"
    "v_add_f32_dpp v52, v52, v52 row_ror:8 row_mask:0xf bank_mask:0xf\n"
    "ds_write_b32 v33, v52 offset:36864\n"
    "buffer_store_dwordx2 v[46:47], v32, %[rws], %[t0] offen sc1\n"
    "s_mov_b64 exec, %[mk]\n"
    "v_cvt_pk_bf16_f32 v48, v116, v117\n"
    "v_cvt_pk_bf16_f32 v49, v118, v119\n"
    "buffer_store_dwordx2 v[48:49], v32, %[rws], %[x4] offen sc1\n"
    "s_mov_b64 exec, -1\n"
    "v_mov_b32 v54, 0x36a1369a\n"
    "s_sub_u32 %[xo], %[xo], 2359296\n"
    "s_sub_u32 %[t0], %[t0], 2359296\n"
    "s_sub_u32 %[x4], %[x4], 2359296\n"
    "s_waitcnt lgkmcnt(0)\n"
    "s_barrier\n"
    "ds_read_b128 v[192:195], v34 offset:36864\n"
    "ds_read_b128 v[196:199], v34 offset:36880\n"
    "ds_read_b128 v[200:203], v34 offset:36896\n"
    "ds_read_b128 v[204:207], v34 offset:36912\n"
    "s_waitcnt lgkmcnt(3)\n"
    "v_pk_add_f32 v[192:193], v[192:193], v[194:195]\n"
    "s_waitcnt lgkmcnt(2)\n"
    "v_pk_add_f32 v[196:197], v[196:197], v[198:199]\n"
    "s_waitcnt lgkmcnt(1)\n"
    "v_pk_add_f32 v[200:201], v[200:201], v[202:203]\n"
    "s_waitcnt lgkmcnt(0)\n"
    "v_pk_add_f32 v[204:205], v[204:205], v[206:207]\n"
    "v_pk_add_f32 v[192:193], v[192:193], v[196:197]\n"
    "v_pk_add_f32 v[200:201], v[200:201], v[204:205]\n"
    "v_pk_add_f32 v[192:193], v[192:193], v[200:201]\n"
    "v_mul_f32 v52, 0xbae38e39, v192\n"
    "v_mul_f32 v55, 0x3a5aa8d5, v192\n"
    "v_fmac_f32 v54, 0x3a5aa8d5, v193\n"
    "v_fmac_f32 v54, v55, v52\n"
    "v_max_f32 v54, 0x36a1369a, v54\n"
    "v_rsq_f32 v53, v54\n"
    "s_nop 0\n"
    "v_mul_f32 v52, v52, v53\n"
    "v_pk_fma_f32 v[36:37], v[36:37], v[52:53], v[52:53] op_sel:[0,1,0] op_sel_hi:[1,1,0]\n"
    "v_pk_fma_f32 v[38:39], v[38:39], v[52:53], v[52:53] op_sel:[0,1,0] op_sel_hi:[1,1,0]\n"
    "v_pk_fma_f32 v[36:37], v[36:37], v[136:137], v[144:145]\n"
    "v_pk_fma_f32 v[38:39], v[38:39], v[138:139], v[146:147]\n"
    "v_cvt_pk_bf16_f32 v50, v36, v37\n"
    "v_cvt_pk_bf16_f32 v51, v38, v39\n"
    "buffer_store_dwordx2 v[50:51], v32, %[rws], %[xo] offen sc1\n"
    "v_pk_fma_f32 v[40:41], v[40:41], v[52:53], v[52:53] op_sel:[0,1,0] op_sel_hi:[1,1,0]\n"
    "v_pk_fma_f32 v[42:43], v[42:43], v[52:53], v[52:53] op_sel:[0,1,0] op_sel_hi:[1,1,0]\n"
    "v_pk_fma_f32 v[40:41], v[40:41], v[140:141], v[148:149]\n"
    "v_pk_fma_f32 v[42:43], v[42:43], v[142:143], v[150:151]\n"
    "v_cvt_pk_bf16_f32 v44, v40, v41\n"
    "v_cvt_pk_bf16_f32 v45, v42, v43\n"
    "buffer_store_dwordx2 v[44:45], v32, %[rws], %[t0] offen sc1\n"
    "s_mov_b64 exec, %[mk]\n"
    "v_pk_fma_f32 v[116:117], v[116:117], v[52:53], v[52:53] op_sel:[0,1,0] op_sel_hi:[1,1,0]\n"
    "v_pk_fma_f32 v[118:119], v[118:119], v[52:53], v[52:53] op_sel:[0,1,0] op_sel_hi:[1,1,0]\n"
    "v_pk_fma_f32 v[116:117], v[116:117], v[240:241], v[244:245]\n"
    "v_pk_fma_f32 v[118:119], v[118:119], v[242:243], v[246:247]\n"
    "v_cvt_pk_bf16_f32 v46, v116, v117\n"
    "v_cvt_pk_bf16_f32 v47, v118, v119\n"
    "buffer_store_dwordx2 v[46:47], v32, %[rws], %[x4] offen sc1\n"
    "s_mov_b64 exec, -1\n"
    "s_endpgm\n"
    "LPFY0_%=:\n"
    "s_movk_i32 %[t0], 0x4000\n"
    "LPFY0_%=x:\n"
    "s_sleep 1\n"
    "buffer_load_dword v1, v88, %[rws], %[wf] offen sc1\n"
    "s_sub_u32 %[t0], %[t0], 1\n"
    "s_waitcnt vmcnt(0)\n"
    "s_cbranch_scc1 LRFY0_%=\n"
    "s_mov_b32 exec_hi, 0x3fffff\n"
    "v_cmp_ne_u32 vcc, %[tag], v1\n"
    "s_mov_b32 exec_hi, -1\n"
    "s_cbranch_vccnz LPFY0_%=x\n"
    "s_branch LRFY0_%=\n"
    "LPFY1_%=:\n"
    "s_movk_i32 %[t0], 0x4000\n"
    "LPFY1_%=x:\n"
    "s_sleep 1\n"
    "buffer_load_dwordx4 v[76:79], v88, %[rws], %[mr] offen sc1\n"
    "s_sub_u32 %[t0], %[t0], 1\n"
    "s_waitcnt vmcnt(0)\n"
    "s_cbranch_scc1 LRFY1_%=\n"
    "v_bitop3_b32 v79, v77, v79, %[tag] bitop3:0x7e\n"
    "v_mov_b32 v77, v78\n"
    "v_cmp_ne_u32 vcc, 0, v79\n"
    "s_cbranch_vccnz LPFY1_%=x\n"
    "s_branch LRFY1_%=\n"
    "LPFY2_%=:\n"
    "s_mov_b32 %[t0], 0x40000\n"
    "LPFY2_%=x:\n"
    "s_sleep 1\n"
    "ds_read_b32 v1, v0\n"
    "s_sub_u32 %[t0], %[t0], 1\n"
    "s_cbranch_scc1 LRFY2_%=\n"
    "s_waitcnt lgkmcnt(0)\n"
    "v_cmp_gt_u32 vcc, %[nd], v1\n"
    "s_cbranch_vccnz LPFY2_%=x\n"
    "s_branch LRFY2_%=\n"
    "LPFY3_%=:\n"
    "s_mov_b32 %[t0], 0x40000\n"
    "LPFY3_%=x:\n"
    "s_sleep 1\n"
    "ds_read_b32 v1, v0\n"
    "s_sub_u32 %[t0], %[t0], 1\n"
    "s_cbranch_scc1 LRFY3_%=\n"
    "s_waitcnt lgkmcnt(0)\n"
    "v_cmp_gt_u32 vcc, %[nd], v1\n"
    "s_cbranch_vccnz LPFY3_%=x\n"
    "s_branch LRFY3_%=\n"
    "LPFY4_%=:\n"
    "v_lshrrev_b32 v166, 5, v88\n"
    "v_and_b32 v167, 16, v88\n"
    "v_mul_u32_u24 v166, 147456, v166\n"
    "v_lshl_add_u32 v166, v167, 8, v166\n"
    "v_add_u32 v166, 745476, v166\n"
    "buffer_load_dword v164, v166, %[rws], 0 offen sc1\n"
    "buffer_load_dword v165, v166, %[rws], 0 offen offset:2048 sc1\n"
    "s_branch LRFY4_%=\n"
    "LPFY5_%=:\n"
    "s_movk_i32 %[t0], 0x4000\n"
    "LPFY5_%=x:\n"
    "v_bitop3_b32 v167, v164, v165, %[tag] bitop3:0x7e\n"
    "v_cmp_ne_u32 vcc, 0, v167\n"
    "s_cbranch_vccz LPFY5_%=y\n"
    "s_sub_u32 %[t0], %[t0], 1\n"
    "s_cbranch_scc1 LPFY5_%=y\n"
    "buffer_load_dword v164, v166, %[rws], 0 offen sc1\n"
    "buffer_load_dword v165, v166, %[rws], 0 offen offset:2048 sc1\n"
    "s_waitcnt vmcnt(0)\n"
    "s_branch LPFY5_%=x\n"
    "LPFY5_%=y:\n"
    "v_mov_b32 v164, %[tag]\n"
    "s_mov_b64 exec, 1\n"
    "buffer_store_dword v164, off, %[rws], 0 sc1\n"
    "s_mov_b64 exec, -1\n"
    "s_branch LRFY5_%=\n"
    "LPFY6_%=:\n"
    "s_mov_b32 %[t0], 0x40000\n"
    "LPFY6_%=x:\n"
    "s_sleep 1\n"
    "ds_read_b32 v1, v0\n"
    "s_sub_u32 %[t0], %[t0], 1\n"
    "s_cbranch_scc1 LRFY6_%=\n"
    "s_waitcnt lgkmcnt(0)\n"
    "v_cmp_gt_u32 vcc, %[nd], v1\n"
    "s_cbranch_vccnz LPFY6_%=x\n"
    "s_branch LRFY6_%=\n"
    "LPFY7_%=:\n"
    "s_mov_b32 %[t0], 0x40000\n"
    "LPFY7_%=x:\n"
    "s_sleep 1\n"
    "ds_read_b32 v1, v0\n"
    "s_sub_u32 %[t0], %[t0], 1\n"
    "s_cbranch_scc1 LRFY7_%=\n"
    "s_waitcnt lgkmcnt(0)\n"
    "v_cmp_gt_u32 vcc, %[nd], v1\n"
    "s_cbranch_vccnz LPFY7_%=x\n"
    "s_branch LRFY7_%=\n"
    "LTFY_%=:\n"
    "LPLTFY_:\n"
    "s_cbranch_execz LPK3\n"
    "ds_read2st64_b32 v[84:85], v92 offset0:1 offset1:3\n"
    "ds_read2st64_b32 v[86:87], v92 offset0:5 offset1:7\n"
    "ds_read2st64_b64 v[76:79], v95 offset1:4\n"
    "ds_read2st64_b64 v[80:83], v95 offset0:8 offset1:12\n"
    "ds_read2st64_b64 v[68:71], v92 offset1:1\n"
    "ds_read2st64_b64 v[72:75], v92 offset0:2 offset1:3\n"
    "v_lshlrev_b32 v144, 16, v8\n"
    "v_and_b32 v145, 0xffff0000, v8\n"
    "v_lshlrev_b32 v146, 16, v9\n"
    "v_and_b32 v147, 0xffff0000, v9\n"
    "v_lshlrev_b32 v148, 16, v10\n"
    "v_and_b32 v149, 0xffff0000, v10\n"
    "v_lshlrev_b32 v150, 16, v11\n"
    "v_and_b32 v151, 0xffff0000, v11\n"
    "v_lshlrev_b32 v244, 16, v18\n"
    "v_and_b32 v245, 0xffff0000, v18\n"
    "v_lshlrev_b32 v246, 16, v19\n"
    "v_and_b32 v247, 0xffff0000, v19\n"
    "v_pk_mul_f32 v[144:145], v[144:145], %[me] op_sel_hi:[1,0]\n"
    "v_pk_mul_f32 v[146:147], v[146:147], %[me] op_sel_hi:[1,0]\n"
    "v_pk_mul_f32 v[148:149], v[148:149], %[me] op_sel_hi:[1,0]\n"
    "v_pk_mul_f32 v[150:151], v[150:151], %[me] op_sel_hi:[1,0]\n"
    "v_and_b32 v56, 0xf0, v93\n"
    "v_lshrrev_b32 v57, 8, v88\n"
    "v_lshl_add_u32 v56, v57, 2, v56\n"
    "v_add_u32 v56, %[lo], v56\n"
    "s_waitcnt lgkmcnt(5)\n"
    "v_add_f32 v84, v84, v85\n"
    "s_waitcnt lgkmcnt(4)\n"
    "v_add_f32 v86, v86, v87\n"
    "v_add_f32 v84, v84, v86\n"
    "s_waitcnt lgkmcnt(3)\n"
    "v_add_f32 v76, v76, v78\n"
    "ds_read_b128 v[28:31], v35 offset:20480\n"
    "v_add_f32 v77, v77, v79\n"
    "v_add_f32_dpp v84, v84, v84 quad_perm:[1,0,3,2] row_mask:0xf bank_mask:0xf\n"
    "s_waitcnt lgkmcnt(3)\n"
    "v_add_f32 v80, v80, v82\n"
    "v_rcp_f32 v84, v84\n"
    "s_waitcnt lgkmcnt(2)\n"
    "v_add_f32 v68, v68, v70\n"
    "v_add_f32 v69, v69, v71\n"
    "v_add_f32 v81, v81, v83\n"
    "s_waitcnt lgkmcnt(1)\n"
    "v_add_f32 v72, v72, v74\n"
    "v_add_f32 v73, v73, v75\n"
    "v_add_f32 v76, v76, v80\n"
    "v_add_f32 v77, v77, v81\n"
    "v_add_f32 v68, v68, v72\n"
    "v_add_f32 v69, v69, v73\n"
    "v_mul_f32 v76, v76, v84\n"
    "v_mul_f32 v77, v77, v84\n"
    "v_mul_f32 v68, v68, v84\n"
    "v_mul_f32 v69, v69, v84\n"
    "v_cvt_pk_bf16_f32 v76, v76, v77\n"
    "v_cvt_pk_bf16_f32 v68, v68, v69\n"
    "ds_write_b32 v93, v76\n"
    "ds_write_b32 v56, v68\n"
    "s_waitcnt lgkmcnt(2)\n"
    "v_pk_add_f32 v[116:117], v[116:117], v[28:29]\n"
    "v_pk_add_f32 v[118:119], v[118:119], v[30:31]\n"
    "v_pk_mul_f32 v[244:245], v[244:245], %[me] op_sel_hi:[1,0]\n"
    "v_pk_mul_f32 v[246:247], v[246:247], %[me] op_sel_hi:[1,0]\n"
    "v_lshlrev_b32 v120, 16, v4\n"
    "v_and_b32 v121, 0xffff0000, v4\n"
    "v_lshlrev_b32 v122, 16, v5\n"
    "v_and_b32 v123, 0xffff0000, v5\n"
    "v_lshlrev_b32 v124, 16, v6\n"
    "v_and_b32 v125, 0xffff0000, v6\n"
    "v_lshlrev_b32 v126, 16, v7\n"
    "v_and_b32 v127, 0xffff0000, v7\n"
    "s_branch LTFY_%=j\n"
    "LPFO0_%=:\n"
    "s_movk_i32 %[t0], 0x4000\n"
    "LPFO0_%=x:\n"
    "s_sleep 1\n"
    "buffer_load_dword v1, v88, %[rws], %[wf] offen sc1\n"
    "s_sub_u32 %[t0], %[t0], 1\n"
    "s_waitcnt vmcnt(0)\n"
    "s_cbranch_scc1 LRFO0_%=\n"
    "s_mov_b32 exec_hi, 0x3fffff\n"
    "v_cmp_ne_u32 vcc, %[tag], v1\n"
    "s_mov_b32 exec_hi, -1\n"
    "s_cbranch_vccnz LPFO0_%=x\n"
    "s_branch LRFO0_%=\n"
    "LPFO1_%=:\n"
    "s_movk_i32 %[t0], 0x4000\n"
    "LPFO1_%=x:\n"
    "s_sleep 1\n"
    "buffer_load_dwordx4 v[76:79], v88, %[rws], %[mr] offen sc1\n"
    "s_sub_u32 %[t0], %[t0], 1\n"
    "s_waitcnt vmcnt(0)\n"
    "s_cbranch_scc1 LRFO1_%=\n"
    "v_bitop3_b32 v79, v77, v79, %[tag] bitop3:0x7e\n"
    "v_mov_b32 v77, v78\n"
    "v_cmp_ne_u32 vcc, 0, v79\n"
    "s_cbranch_vccnz LPFO1_%=x\n"
    "s_branch LRFO1_%=\n"
    "LPFO2_%=:\n"
    "s_mov_b32 %[t0], 0x40000\n"
    "LPFO2_%=x:\n"
    "s_sleep 1\n"
    "ds_read_b32 v1, v0\n"
    "s_sub_u32 %[t0], %[t0], 1\n"
    "s_cbranch_scc1 LRFO2_%=\n"
    "s_waitcnt lgkmcnt(0)\n"
    "v_cmp_gt_u32 vcc, %[nd], v1\n"
    "s_cbranch_vccnz LPFO2_%=x\n"
    "s_branch LRFO2_%=\n"
    "LPFO3_%=:\n"
    "s_mov_b32 %[t0], 0x40000\n"
    "LPFO3_%=x:\n"
    "s_sleep 1\n"
    "ds_read_b32 v1, v0\n"
    "s_sub_u32 %[t0], %[t0], 1\n"
    "s_cbranch_scc1 LRFO3_%=\n"
    "s_waitcnt lgkmcnt(0)\n"
    "v_cmp_gt_u32 vcc, %[nd], v1\n"
    "s_cbranch_vccnz LPFO3_%=x\n"
    "s_branch LRFO3_%=\n"
    "LPFO4_%=:\n"
    "v_lshrrev_b32 v166, 5, v88\n"
    "v_and_b32 v167, 16, v88\n"
    "v_mul_u32_u24 v166, 147456, v166\n"
    "v_lshl_add_u32 v166, v167, 8, v166\n"
    "v_add_u32 v166, 745476, v166\n"
    "buffer_load_dword v164, v166, %[rws], 0 offen sc1\n"
    "buffer_load_dword v165, v166, %[rws], 0 offen offset:2048 sc1\n"
    "s_branch LRFO4_%=\n"
    "LPFO5_%=:\n"
    "s_movk_i32 %[t0], 0x4000\n"
    "LPFO5_%=x:\n"
    "v_bitop3_b32 v167, v164, v165, %[tag] bitop3:0x7e\n"
    "v_cmp_ne_u32 vcc, 0, v167\n"
    "s_cbranch_vccz LPFO5_%=y\n"
    "s_sub_u32 %[t0], %[t0], 1\n"
    "s_cbranch_scc1 LPFO5_%=y\n"
    "buffer_load_dword v164, v166, %[rws], 0 offen sc1\n"
    "buffer_load_dword v165, v166, %[rws], 0 offen offset:2048 sc1\n"
    "s_waitcnt vmcnt(0)\n"
    "s_branch LPFO5_%=x\n"
    "LPFO5_%=y:\n"
    "v_mov_b32 v164, %[tag]\n"
    "s_mov_b64 exec, 1\n"
    "buffer_store_dword v164, off, %[rws], 0 sc1\n"
    "s_mov_b64 exec, -1\n"
    "s_branch LRFO5_%=\n"
    "LPFO6_%=:\n"
    "s_mov_b32 %[t0], 0x40000\n"
    "LPFO6_%=x:\n"
    "s_sleep 1\n"
    "ds_read_b32 v1, v0\n"
    "s_sub_u32 %[t0], %[t0], 1\n"
    "s_cbranch_scc1 LRFO6_%=\n"
    "s_waitcnt lgkmcnt(0)\n"
    "v_cmp_gt_u32 vcc, %[nd], v1\n"
    "s_cbranch_vccnz LPFO6_%=x\n"
    "s_branch LRFO6_%=\n"
    "LPFO7_%=:\n"
    "s_mov_b32 %[t0], 0x40000\n"
    "LPFO7_%=x:\n"
    "s_sleep 1\n"
    "ds_read_b32 v1, v0\n"
    "s_sub_u32 %[t0], %[t0], 1\n"
    "s_cbranch_scc1 LRFO7_%=\n"
    "s_waitcnt lgkmcnt(0)\n"
    "v_cmp_gt_u32 vcc, %[nd], v1\n"
    "s_cbranch_vccnz LPFO7_%=x\n"
    "s_branch LRFO7_%=\n"
    "LTFO_%=:\n"
    "ds_read2st64_b32 v[84:85], v92 offset0:1 offset1:3\n"
    "ds_read2st64_b32 v[86:87], v92 offset0:5 offset1:7\n"
    "ds_read2st64_b64 v[76:79], v95 offset1:4\n"
    "ds_read2st64_b64 v[80:83], v95 offset0:8 offset1:12\n"
    "ds_read2st64_b64 v[68:71], v92 offset1:1\n"
    "ds_read2st64_b64 v[72:75], v92 offset0:2 offset1:3\n"
    "v_lshlrev_b32 v144, 16, v8\n"
    "v_and_b32 v145, 0xffff0000, v8\n"
    "v_lshlrev_b32 v146, 16, v9\n"
    "v_and_b32 v147, 0xffff0000, v9\n"
    "v_lshlrev_b32 v148, 16, v10\n"
    "v_and_b32 v149, 0xffff0000, v10\n"
    "v_lshlrev_b32 v150, 16, v11\n"
    "v_and_b32 v151, 0xffff0000, v11\n"
    "v_lshlrev_b32 v244, 16, v18\n"
    "v_and_b32 v245, 0xffff0000, v18\n"
    "v_lshlrev_b32 v246, 16, v19\n"
    "v_and_b32 v247, 0xffff0000, v19\n"
    "v_pk_mul_f32 v[144:145], v[144:145], %[me] op_sel_hi:[1,0]\n"
    "v_pk_mul_f32 v[146:147], v[146:147], %[me] op_sel_hi:[1,0]\n"
    "v_pk_mul_f32 v[148:149], v[148:149], %[me] op_sel_hi:[1,0]\n"
    "v_pk_mul_f32 v[150:151], v[150:151], %[me] op_sel_hi:[1,0]\n"
    "v_and_b32 v56, 0xf0, v93\n"
    "v_lshrrev_b32 v57, 8, v88\n"
    "v_lshl_add_u32 v56, v57, 2, v56\n"
    "v_add_u32 v56, %[lo], v56\n"
    "s_waitcnt lgkmcnt(5)\n"
    "v_add_f32 v84, v84, v85\n"
    "s_waitcnt lgkmcnt(4)\n"
    "v_add_f32 v86, v86, v87\n"
    "v_add_f32 v84, v84, v86\n"
    "s_waitcnt lgkmcnt(3)\n"
    "v_add_f32 v76, v76, v78\n"
    "ds_read_b128 v[28:31], v35 offset:20480\n"
    "v_add_f32 v77, v77, v79\n"
    "v_add_f32_dpp v84, v84, v84 quad_perm:[1,0,3,2] row_mask:0xf bank_mask:0xf\n"
    "s_waitcnt lgkmcnt(3)\n"
    "v_add_f32 v80, v80, v82\n"
    "v_rcp_f32 v84, v84\n"
    "s_waitcnt lgkmcnt(2)\n"
    "v_add_f32 v68, v68, v70\n"
    "v_add_f32 v69, v69, v71\n"
    "v_add_f32 v81, v81, v83\n"
    "s_waitcnt lgkmcnt(1)\n"
    "v_add_f32 v72, v72, v74\n"
    "v_add_f32 v73, v73, v75\n"
    "v_add_f32 v76, v76, v80\n"
    "v_add_f32 v77, v77, v81\n"
    "v_add_f32 v68, v68, v72\n"
    "v_add_f32 v69, v69, v73\n"
    "v_mul_f32 v76, v76, v84\n"
    "v_mul_f32 v77, v77, v84\n"
    "v_mul_f32 v68, v68, v84\n"
    "v_mul_f32 v69, v69, v84\n"
    "v_cvt_pk_bf16_f32 v76, v76, v77\n"
    "v_cvt_pk_bf16_f32 v68, v68, v69\n"
    "ds_write_b32 v93, v76\n"
    "ds_write_b32 v56, v68\n"
    "s_waitcnt lgkmcnt(2)\n"
    "v_pk_add_f32 v[116:117], v[116:117], v[28:29]\n"
    "v_pk_add_f32 v[118:119], v[118:119], v[30:31]\n"
    "v_pk_mul_f32 v[244:245], v[244:245], %[me] op_sel_hi:[1,0]\n"
    "v_pk_mul_f32 v[246:247], v[246:247], %[me] op_sel_hi:[1,0]\n"
    "v_lshlrev_b32 v120, 16, v4\n"
    "v_and_b32 v121, 0xffff0000, v4\n"
    "v_lshlrev_b32 v122, 16, v5\n"
    "v_and_b32 v123, 0xffff0000, v5\n"
    "v_lshlrev_b32 v124, 16, v6\n"
    "v_and_b32 v125, 0xffff0000, v6\n"
    "v_lshlrev_b32 v126, 16, v7\n"
    "v_and_b32 v127, 0xffff0000, v7\n"
    "s_branch LTFO_%=j\n"
    "LPS0_%=:\n"
    "s_movk_i32 %[t0], 0x4000\n"
    "LPS0_%=x:\n"
    "s_sleep 1\n"
    "buffer_load_dword v1, v88, %[rws], %[wf] offen sc1\n"
    "s_sub_u32 %[t0], %[t0], 1\n"
    "s_waitcnt vmcnt(0)\n"
    "s_cbranch_scc1 LRS0_%=\n"
    "s_mov_b32 exec_hi, 0x3fffff\n"
    "v_cmp_ne_u32 vcc, %[tag], v1\n"
    "s_mov_b32 exec_hi, -1\n"
    "s_cbranch_vccnz LPS0_%=x\n"
    "s_branch LRS0_%=\n"
    "LPS1_%=:\n"
    "s_movk_i32 %[t0], 0x4000\n"
    "LPS1_%=x:\n"
    "s_sleep 1\n"
    "buffer_load_dwordx4 v[76:79], v88, %[rws], %[mr] offen sc1\n"
    "s_sub_u32 %[t0], %[t0], 1\n"
    "s_waitcnt vmcnt(0)\n"
    "s_cbranch_scc1 LRS1_%=\n"
    "v_bitop3_b32 v79, v77, v79, %[tag] bitop3:0x7e\n"
    "v_mov_b32 v77, v78\n"
    "v_cmp_ne_u32 vcc, 0, v79\n"
    "s_cbranch_vccnz LPS1_%=x\n"
    "s_branch LRS1_%=\n"
    "LPS2_%=:\n"
    "s_mov_b32 %[t0], 0x40000\n"
    "LPS2_%=x:\n"
    "s_sleep 1\n"
    "ds_read_b32 v1, v0\n"
    "s_sub_u32 %[t0], %[t0], 1\n"
    "s_cbranch_scc1 LRS2_%=\n"
    "s_waitcnt lgkmcnt(0)\n"
    "v_cmp_gt_u32 vcc, %[nd], v1\n"
    "s_cbranch_vccnz LPS2_%=x\n"
    "s_branch LRS2_%=\n"
    "LPS3_%=:\n"
    "s_mov_b32 %[t0], 0x40000\n"
    "LPS3_%=x:\n"
    "s_sleep 1\n"
    "ds_read_b32 v1, v0\n"
    "s_sub_u32 %[t0], %[t0], 1\n"
    "s_cbranch_scc1 LRS3_%=\n"
    "s_waitcnt lgkmcnt(0)\n"
    "v_cmp_gt_u32 vcc, %[nd], v1\n"
    "s_cbranch_vccnz LPS3_%=x\n"
    "s_branch LRS3_%=\n"
    "LPS4_%=:\n"
    "v_lshrrev_b32 v166, 5, v88\n"
    "v_and_b32 v167, 16, v88\n"
    "v_mul_u32_u24 v166, 147456, v166\n"
    "v_lshl_add_u32 v166, v167, 8, v166\n"
    "v_add_u32 v166, 745476, v166\n"
    "buffer_load_dword v164, v166, %[rws], 0 offen sc1\n"
    "buffer_load_dword v165, v166, %[rws], 0 offen offset:2048 sc1\n"
    "s_branch LRS4_%=\n"
    "LPS5_%=:\n"
    "s_movk_i32 %[t0], 0x4000\n"
    "LPS5_%=x:\n"
    "v_bitop3_b32 v167, v164, v165, %[tag] bitop3:0x7e\n"
    "v_cmp_ne_u32 vcc, 0, v167\n"
    "s_cbranch_vccz LPS5_%=y\n"
    "s_sub_u32 %[t0], %[t0], 1\n"
    "s_cbranch_scc1 LPS5_%=y\n"
    "buffer_load_dword v164, v166, %[rws], 0 offen sc1\n"
    "buffer_load_dword v165, v166, %[rws], 0 offen offset:2048 sc1\n"
    "s_waitcnt vmcnt(0)\n"
    "s_branch LPS5_%=x\n"
    "LPS5_%=y:\n"
    "v_mov_b32 v164, %[tag]\n"
    "s_mov_b64 exec, 1\n"
    "buffer_store_dword v164, off, %[rws], 0 sc1\n"
    "s_mov_b64 exec, -1\n"
    "s_branch LRS5_%=\n"
    "LPS6_%=:\n"
    "s_mov_b32 %[t0], 0x40000\n"
    "LPS6_%=x:\n"
    "s_sleep 1\n"
    "ds_read_b32 v1, v0\n"
    "s_sub_u32 %[t0], %[t0], 1\n"
    "s_cbranch_scc1 LRS6_%=\n"
    "s_waitcnt lgkmcnt(0)\n"
    "v_cmp_gt_u32 vcc, %[nd], v1\n"
    "s_cbranch_vccnz LPS6_%=x\n"
    "s_branch LRS6_%=\n"
    "LPS7_%=:\n"
    "s_mov_b32 %[t0], 0x40000\n"
    "LPS7_%=x:\n"
    "s_sleep 1\n"
    "ds_read_b32 v1, v0\n"
    "s_sub_u32 %[t0], %[t0], 1\n"
    "s_cbranch_scc1 LRS7_%=\n"
    "s_waitcnt lgkmcnt(0)\n"
    "v_cmp_gt_u32 vcc, %[nd], v1\n"
    "s_cbranch_vccnz LPS7_%=x\n"
    "s_branch LRS7_%=\n"
    "LTS_%=:\n"
    "ds_read2st64_b32 v[84:85], v92 offset0:1 offset1:3\n"
    "ds_read2st64_b32 v[86:87], v92 offset0:5 offset1:7\n"
    "ds_read2st64_b64 v[76:79], v95 offset1:4\n"
    "ds_read2st64_b64 v[80:83], v95 offset0:8 offset1:12\n"
    "ds_read2st64_b64 v[68:71], v92 offset1:1\n"
    "ds_read2st64_b64 v[72:75], v92 offset0:2 offset1:3\n"
    "v_lshlrev_b32 v144, 16, v8\n"
    "v_and_b32 v145, 0xffff0000, v8\n"
    "v_lshlrev_b32 v146, 16, v9\n"
    "v_and_b32 v147, 0xffff0000, v9\n"
    "v_lshlrev_b32 v148, 16, v10\n"
    "v_and_b32 v149, 0xffff0000, v10\n"
    "v_lshlrev_b32 v150, 16, v11\n"
    "v_and_b32 v151, 0xffff0000, v11\n"
    "v_lshlrev_b32 v244, 16, v18\n"
    "v_and_b32 v245, 0xffff0000, v18\n"
    "v_lshlrev_b32 v246, 16, v19\n"
    "v_and_b32 v247, 0xffff0000, v19\n"
    "v_pk_mul_f32 v[144:145], v[144:145], %[me] op_sel_hi:[1,0]\n"
    "v_pk_mul_f32 v[146:147], v[146:147], %[me] op_sel_hi:[1,0]\n"
    "v_pk_mul_f32 v[148:149], v[148:149], %[me] op_sel_hi:[1,0]\n"
    "v_pk_mul_f32 v[150:151], v[150:151], %[me] op_sel_hi:[1,0]\n"
    "v_and_b32 v56, 0xf0, v93\n"
    "v_lshrrev_b32 v57, 8, v88\n"
    "v_lshl_add_u32 v56, v57, 2, v56\n"
    "v_add_u32 v56, %[lo], v56\n"
    "s_waitcnt lgkmcnt(5)\n"
    "v_add_f32 v84, v84, v85\n"
    "s_waitcnt lgkmcnt(4)\n"
    "v_add_f32 v86, v86, v87\n"
    "v_add_f32 v84, v84, v86\n"
    "s_waitcnt lgkmcnt(3)\n"
    "v_add_f32 v76, v76, v78\n"
    "ds_read_b128 v[28:31], v35 offset:20480\n"
    "v_add_f32 v77, v77, v79\n"
    "v_add_f32_dpp v84, v84, v84 quad_perm:[1,0,3,2] row_mask:0xf bank_mask:0xf\n"
    "s_waitcnt lgkmcnt(3)\n"
    "v_add_f32 v80, v80, v82\n"
    "v_rcp_f32 v84, v84\n"
    "s_waitcnt lgkmcnt(2)\n"
    "v_add_f32 v68, v68, v70\n"
    "v_add_f32 v69, v69, v71\n"
    "v_add_f32 v81, v81, v83\n"
    "s_waitcnt lgkmcnt(1)\n"
    "v_add_f32 v72, v72, v74\n"
    "v_add_f32 v73, v73, v75\n"
    "v_add_f32 v76, v76, v80\n"
    "v_add_f32 v77, v77, v81\n"
    "v_add_f32 v68, v68, v72\n"
    "v_add_f32 v69, v69, v73\n"
    "v_mul_f32 v76, v76, v84\n"
    "v_mul_f32 v77, v77, v84\n"
    "v_mul_f32 v68, v68, v84\n"
    "v_mul_f32 v69, v69, v84\n"
    "v_cvt_pk_bf16_f32 v76, v76, v77\n"
    "v_cvt_pk_bf16_f32 v68, v68, v69\n"
    "ds_write_b32 v93, v76\n"
    "ds_write_b32 v56, v68\n"
    "s_waitcnt lgkmcnt(2)\n"
    "v_pk_add_f32 v[116:117], v[116:117], v[28:29]\n"
    "v_pk_add_f32 v[118:119], v[118:119], v[30:31]\n"
    "v_pk_mul_f32 v[244:245], v[244:245], %[me] op_sel_hi:[1,0]\n"
    "v_pk_mul_f32 v[246:247], v[246:247], %[me] op_sel_hi:[1,0]\n"
    "v_lshlrev_b32 v120, 16, v4\n"
    "v_and_b32 v121, 0xffff0000, v4\n"
    "v_lshlrev_b32 v122, 16, v5\n"
    "v_and_b32 v123, 0xffff0000, v5\n"
    "v_lshlrev_b32 v124, 16, v6\n"
    "v_and_b32 v125, 0xffff0000, v6\n"
    "v_lshlrev_b32 v126, 16, v7\n"
    "v_and_b32 v127, 0xffff0000, v7\n"
    "s_branch LTS_%=j\n"
        "LS%=:\n"
    "ds_read_b128 v[60:63], v89 offset:0\n"
    "ds_read_b128 v[64:67], v89 offset:1536\n"
    "ds_read_b32 v2, v99 offset:0\n"
    "s_waitcnt vmcnt(9) lgkmcnt(2)\n"
    "v_mfma_f32_32x32x16_bf16 v[4:19], v[164:167], v[60:63], 0\n"
    "s_waitcnt vmcnt(7) lgkmcnt(1)\n"
    "v_mfma_f32_32x32x8_bf16 v[4:19], v[156:157], v[64:65], v[4:19]\n"
    "s_mov_b32 %[cnt], 7\n"
    "s_mov_b32 %[nd], 0\n"
    "buffer_load_dwordx4 v[176:179], v88, %[rws], %[kb] offen offset:3072 sc1\n"
    "buffer_load_dwordx4 v[204:207], v68, %[rws], %[vb] offen offset:1920 sc1\n"
    "buffer_load_dwordx4 v[208:211], v68, %[rws], %[vb] offen offset:2560 sc1\n"
    "s_nop 6\n"
    "s_waitcnt vmcnt(9)\n"
    "v_mfma_f32_32x32x16_bf16 v[20:35], v[168:171], v[60:63], 0\n"
    "s_waitcnt lgkmcnt(0)\n"
    "v_add_f32 v4, v4, v2\n"
    "v_exp_f32 v4, v4\n"
    "v_add_f32 v5, v5, v2\n"
    "v_exp_f32 v5, v5\n"
    "v_add_f32 v6, v6, v2\n"
    "v_exp_f32 v6, v6\n"
    "v_add_f32 v7, v7, v2\n"
    "v_exp_f32 v7, v7\n"
    "v_add_f32 v8, v8, v2\n"
    "v_exp_f32 v8, v8\n"
    "v_cvt_pk_bf16_f32 v52, v4, v5\n"
    "v_add_f32 v9, v9, v2\n"
    "v_exp_f32 v9, v9\n"
    "v_cvt_pk_bf16_f32 v53, v6, v7\n"
    "v_add_f32 v10, v10, v2\n"
    "v_exp_f32 v10, v10\n"
    "v_add_f32 v11, v11, v2\n"
    "v_exp_f32 v11, v11\n"
    "s_waitcnt vmcnt(7)\n"
    "v_mfma_f32_32x32x8_bf16 v[20:35], v[158:159], v[64:65], v[20:35]\n"
    "ds_read_b128 v[64:67], v89 offset:2560\n"
    "v_add_f32 v12, v12, v2\n"
    "v_exp_f32 v12, v12\n"
    "v_cvt_pk_bf16_f32 v54, v8, v9\n"
    "v_add_f32 v13, v13, v2\n"
    "v_exp_f32 v13, v13\n"
    "v_cvt_pk_bf16_f32 v55, v10, v11\n"
    "v_add_f32 v14, v14, v2\n"
    "v_exp_f32 v14, v14\n"
    "v_add_f32 v15, v15, v2\n"
    "v_exp_f32 v15, v15\n"
    "s_waitcnt vmcnt(6)\n"
    "v_mfma_f32_32x32x16_bf16 v[36:51], v[192:195], v[52:55], 0\n"
    "v_add_f32 v16, v16, v2\n"
    "v_exp_f32 v58, v16\n"
    "v_cvt_pk_bf16_f32 v56, v12, v13\n"
    "v_add_f32 v17, v17, v2\n"
    "v_exp_f32 v52, v17\n"
    "v_cvt_pk_bf16_f32 v57, v14, v15\n"
    "v_add_f32 v18, v18, v2\n"
    "v_exp_f32 v59, v18\n"
    "v_add_f32 v19, v19, v2\n"
    "v_exp_f32 v53, v19\n"
    "s_waitcnt vmcnt(5)\n"
    "v_mfma_f32_32x32x16_bf16 v[4:19], v[172:175], v[60:63], 0\n"
    "v_add_f32 v20, v20, v2\n"
    "v_exp_f32 v20, v20\n"
    "v_cvt_pk_bf16_f32 v58, v58, v52\n"
    "buffer_load_dwordx4 v[180:183], v88, %[rws], %[kb4] offen offset:0 sc1\n"
    "v_add_f32 v21, v21, v2\n"
    "v_exp_f32 v21, v21\n"
    "v_cvt_pk_bf16_f32 v59, v59, v53\n"
    "v_add_f32 v22, v22, v2\n"
    "v_exp_f32 v22, v22\n"
    "v_add_f32 v23, v23, v2\n"
    "v_exp_f32 v23, v23\n"
    "s_waitcnt vmcnt(5)\n"
    "v_mfma_f32_32x32x16_bf16 v[36:51], v[196:199], v[56:59], v[36:51]\n"
    "v_add_f32 v24, v24, v2\n"
    "v_exp_f32 v24, v24\n"
    "v_cvt_pk_bf16_f32 v52, v20, v21\n"
    "buffer_load_dwordx4 v[76:79], v88, %[rws], %[mr] offen sc1\n"
    "v_add_f32 v25, v25, v2\n"
    "v_exp_f32 v25, v25\n"
    "v_cvt_pk_bf16_f32 v53, v22, v23\n"
    "v_add_f32 v26, v26, v2\n"
    "v_exp_f32 v26, v26\n"
    "v_add_f32 v27, v27, v2\n"
    "v_exp_f32 v27, v27\n"
    "s_waitcnt lgkmcnt(0)\n"
    "v_mfma_f32_32x32x8_bf16 v[4:19], v[156:157], v[64:65], v[4:19]\n"
    "v_add_f32 v28, v28, v2\n"
    "v_exp_f32 v28, v28\n"
    "v_cvt_pk_bf16_f32 v54, v24, v25\n"
    "buffer_load_dwordx4 v[212:215], v68, %[rws], %[vb] offen offset:3200 sc1\n"
    "buffer_load_dwordx4 v[216:219], v68, %[rws], %[vb3] offen offset:0 sc1\n"
    "v_add_f32 v29, v29, v2\n"
    "v_exp_f32 v29, v29\n"
    "v_cvt_pk_bf16_f32 v55, v26, v27\n"
    "v_add_f32 v30, v30, v2\n"
    "v_exp_f32 v30, v30\n"
    "v_add_f32 v31, v31, v2\n"
    "v_exp_f32 v31, v31\n"
    "s_waitcnt vmcnt(7)\n"
    "v_mfma_f32_32x32x16_bf16 v[36:51], v[200:203], v[52:55], v[36:51]\n"
    "v_add_f32 v32, v32, v2\n"
    "v_exp_f32 v58, v32\n"
    "v_cvt_pk_bf16_f32 v56, v28, v29\n"
    "v_add_f32 v33, v33, v2\n"
    "v_exp_f32 v52, v33\n"
    "v_cvt_pk_bf16_f32 v57, v30, v31\n"
    "v_add_f32 v34, v34, v2\n"
    "v_exp_f32 v59, v34\n"
    "v_add_f32 v35, v35, v2\n"
    "v_exp_f32 v53, v35\n"
    "s_waitcnt vmcnt(6)\n"
    "v_mfma_f32_32x32x16_bf16 v[20:35], v[176:179], v[60:63], 0\n"
    "v_add_f32 v4, v4, v2\n"
    "v_exp_f32 v4, v4\n"
    "v_cvt_pk_bf16_f32 v58, v58, v52\n"
    "buffer_load_dwordx4 v[184:187], v88, %[rws], %[kb4] offen offset:1024 sc1\n"
    "v_add_f32 v5, v5, v2\n"
    "v_exp_f32 v5, v5\n"
    "v_cvt_pk_bf16_f32 v59, v59, v53\n"
    "v_add_f32 v6, v6, v2\n"
    "v_exp_f32 v6, v6\n"
    "v_add_f32 v7, v7, v2\n"
    "v_exp_f32 v7, v7\n"
    "s_waitcnt vmcnt(6)\n"
    "v_mfma_f32_32x32x16_bf16 v[36:51], v[204:207], v[56:59], v[36:51]\n"
    "v_add_f32 v8, v8, v2\n"
    "v_exp_f32 v8, v8\n"
    "v_cvt_pk_bf16_f32 v52, v4, v5\n"
    "v_add_f32 v9, v9, v2\n"
    "v_exp_f32 v9, v9\n"
    "v_cvt_pk_bf16_f32 v53, v6, v7\n"
    "v_add_f32 v10, v10, v2\n"
    "v_exp_f32 v10, v10\n"
    "v_add_f32 v11, v11, v2\n"
    "v_exp_f32 v11, v11\n"
    "v_mfma_f32_32x32x8_bf16 v[20:35], v[158:159], v[64:65], v[20:35]\n"
    "ds_read_b128 v[64:67], v89 offset:1024\n"
    "v_add_f32 v12, v12, v2\n"
    "v_exp_f32 v12, v12\n"
    "v_cvt_pk_bf16_f32 v54, v8, v9\n"
    "buffer_load_dwordx4 v[220:223], v68, %[rws], %[vb3] offen offset:640 sc1\n"
    "buffer_load_dwordx4 v[224:227], v68, %[rws], %[vb3] offen offset:1280 sc1\n"
    "v_add_f32 v13, v13, v2\n"
    "v_exp_f32 v13, v13\n"
    "v_cvt_pk_bf16_f32 v55, v10, v11\n"
    "v_add_f32 v14, v14, v2\n"
    "v_exp_f32 v14, v14\n"
    "v_add_f32 v15, v15, v2\n"
    "v_exp_f32 v15, v15\n"
    "s_waitcnt vmcnt(7)\n"
    "v_mfma_f32_32x32x16_bf16 v[36:51], v[208:211], v[52:55], v[36:51]\n"
    "v_add_f32 v16, v16, v2\n"
    "v_exp_f32 v58, v16\n"
    "v_cvt_pk_bf16_f32 v56, v12, v13\n"
    "v_add_f32 v17, v17, v2\n"
    "v_exp_f32 v52, v17\n"
    "v_cvt_pk_bf16_f32 v57, v14, v15\n"
    "v_add_f32 v18, v18, v2\n"
    "v_exp_f32 v59, v18\n"
    "v_add_f32 v19, v19, v2\n"
    "v_exp_f32 v53, v19\n"
    "s_waitcnt vmcnt(6)\n"
    "v_mfma_f32_32x32x16_bf16 v[4:19], v[180:183], v[60:63], 0\n"
    "v_add_f32 v20, v20, v2\n"
    "v_exp_f32 v20, v20\n"
    "v_cvt_pk_bf16_f32 v58, v58, v52\n"
    "buffer_load_dwordx4 v[188:191], v88, %[rws], %[kb4] offen offset:2048 sc1\n"
    "v_add_f32 v21, v21, v2\n"
    "v_exp_f32 v21, v21\n"
    "v_cvt_pk_bf16_f32 v59, v59, v53\n"
    "v_add_f32 v22, v22, v2\n"
    "v_exp_f32 v22, v22\n"
    "v_add_f32 v23, v23, v2\n"
    "v_exp_f32 v23, v23\n"
    "s_waitcnt vmcnt(5)\n"
    "v_mfma_f32_32x32x16_bf16 v[36:51], v[212:215], v[56:59], v[36:51]\n"
    "v_add_f32 v24, v24, v2\n"
    "v_exp_f32 v24, v24\n"
    "v_cvt_pk_bf16_f32 v52, v20, v21\n"
    "v_add_f32 v25, v25, v2\n"
    "v_exp_f32 v25, v25\n"
    "v_cvt_pk_bf16_f32 v53, v22, v23\n"
    "v_add_f32 v26, v26, v2\n"
    "v_exp_f32 v26, v26\n"
    "v_add_f32 v27, v27, v2\n"
    "v_exp_f32 v27, v27\n"
    "s_waitcnt lgkmcnt(0)\n"
    "v_mfma_f32_32x32x8_bf16 v[4:19], v[156:157], v[64:65], v[4:19]\n"
    "v_add_f32 v28, v28, v2\n"
    "v_exp_f32 v28, v28\n"
    "v_cvt_pk_bf16_f32 v54, v24, v25\n"
    "buffer_load_dwordx4 v[228:231], v68, %[rws], %[vb3] offen offset:1920 sc1\n"
    "buffer_load_dwordx4 v[232:235], v68, %[rws], %[vb3] offen offset:2560 sc1\n"
    "v_add_f32 v29, v29, v2\n"
    "v_exp_f32 v29, v29\n"
    "v_cvt_pk_bf16_f32 v55, v26, v27\n"
    "v_add_f32 v30, v30, v2\n"
    "v_exp_f32 v30, v30\n"
    "v_add_f32 v31, v31, v2\n"
    "v_exp_f32 v31, v31\n"
    "s_waitcnt vmcnt(6)\n"
    "v_mfma_f32_32x32x16_bf16 v[36:51], v[216:219], v[52:55], v[36:51]\n"
    "v_add_f32 v32, v32, v2\n"
    "v_exp_f32 v58, v32\n"
    "v_cvt_pk_bf16_f32 v56, v28, v29\n"
    "v_add_f32 v33, v33, v2\n"
    "v_exp_f32 v52, v33\n"
    "v_cvt_pk_bf16_f32 v57, v30, v31\n"
    "v_add_f32 v34, v34, v2\n"
    "v_exp_f32 v59, v34\n"
    "v_add_f32 v35, v35, v2\n"
    "v_exp_f32 v53, v35\n"
    "s_waitcnt vmcnt(5)\n"
    "v_mfma_f32_32x32x16_bf16 v[20:35], v[184:187], v[60:63], 0\n"
    "v_add_f32 v4, v4, v2\n"
    "v_exp_f32 v4, v4\n"
    "v_cvt_pk_bf16_f32 v58, v58, v52\n"
    "buffer_load_dwordx4 v[160:163], v88, %[rws], %[kb4] offen offset:3072 sc1\n"
    "v_add_f32 v5, v5, v2\n"
    "v_exp_f32 v5, v5\n"
    "v_cvt_pk_bf16_f32 v59, v59, v53\n"
    "v_add_f32 v6, v6, v2\n"
    "v_exp_f32 v6, v6\n"
    "v_add_f32 v7, v7, v2\n"
    "v_exp_f32 v7, v7\n"
    "s_waitcnt vmcnt(5)\n"
    "v_mfma_f32_32x32x16_bf16 v[36:51], v[220:223], v[56:59], v[36:51]\n"
    "v_add_f32 v8, v8, v2\n"
    "v_exp_f32 v8, v8\n"
    "v_cvt_pk_bf16_f32 v52, v4, v5\n"
    "buffer_load_dword v1, v88, %[rws], %[wf] offen sc1\n"
    "v_add_f32 v9, v9, v2\n"
    "v_exp_f32 v9, v9\n"
    "v_cvt_pk_bf16_f32 v53, v6, v7\n"
    "v_add_f32 v10, v10, v2\n"
    "v_exp_f32 v10, v10\n"
    "v_add_f32 v11, v11, v2\n"
    "v_exp_f32 v11, v11\n"
    "v_mfma_f32_32x32x8_bf16 v[20:35], v[158:159], v[64:65], v[20:35]\n"
    "ds_read_b128 v[64:67], v89 offset:2048\n"
    "v_add_f32 v12, v12, v2\n"
    "v_exp_f32 v12, v12\n"
    "v_cvt_pk_bf16_f32 v54, v8, v9\n"
    "buffer_load_dwordx4 v[236:239], v68, %[rws], %[vb3] offen offset:3200 sc1\n"
    "buffer_load_dwordx4 v[240:243], v68, %[rws], %[vb6] offen offset:0 sc1\n"
    "v_add_f32 v13, v13, v2\n"
    "v_exp_f32 v13, v13\n"
    "v_cvt_pk_bf16_f32 v55, v10, v11\n"
    "v_add_f32 v14, v14, v2\n"
    "v_exp_f32 v14, v14\n"
    "v_add_f32 v15, v15, v2\n"
    "v_exp_f32 v15, v15\n"
    "s_waitcnt vmcnt(7)\n"
    "v_mfma_f32_32x32x16_bf16 v[36:51], v[224:227], v[52:55], v[36:51]\n"
    "v_add_f32 v16, v16, v2\n"
    "v_exp_f32 v58, v16\n"
    "v_cvt_pk_bf16_f32 v56, v12, v13\n"
    "v_add_f32 v17, v17, v2\n"
    "v_exp_f32 v52, v17\n"
    "v_cvt_pk_bf16_f32 v57, v14, v15\n"
    "v_add_f32 v18, v18, v2\n"
    "v_exp_f32 v59, v18\n"
    "v_add_f32 v19, v19, v2\n"
    "v_exp_f32 v53, v19\n"
    "s_waitcnt vmcnt(6)\n"
    "v_mfma_f32_32x32x16_bf16 v[4:19], v[188:191], v[60:63], 0\n"
    "v_add_f32 v20, v20, v2\n"
    "v_exp_f32 v20, v20\n"
    "v_cvt_pk_bf16_f32 v58, v58, v52\n"
    "v_add_f32 v21, v21, v2\n"
    "v_exp_f32 v21, v21\n"
    "v_cvt_pk_bf16_f32 v59, v59, v53\n"
    "ds_read_b32 v3, v99 offset:128\n"
    "v_add_u32 v99, 128, v99\n"
    "s_waitcnt lgkmcnt(0)\n"
    "v_add_f32 v22, v22, v2\n"
    "v_exp_f32 v22, v22\n"
    "v_add_f32 v23, v23, v2\n"
    "v_exp_f32 v23, v23\n"
    "s_waitcnt vmcnt(5)\n"
    "v_mfma_f32_32x32x16_bf16 v[36:51], v[228:231], v[56:59], v[36:51]\n"
    "v_add_f32 v24, v24, v2\n"
    "v_exp_f32 v24, v24\n"
    "v_cvt_pk_bf16_f32 v52, v20, v21\n"
    "v_bitop3_b32 v79, v77, v79, %[tag] bitop3:0x7e\n"
    "v_mov_b32 v77, v78\n"
    "v_cmp_ne_u32 vcc, 0, v79\n"
    "s_cbranch_vccnz LPS1_%=\n"
    "LRS1_%=:\n"
    "ds_write_b64 v91, v[76:77] offset:57856\n"
    "v_add_f32 v25, v25, v2\n"
    "v_exp_f32 v25, v25\n"
    "v_cvt_pk_bf16_f32 v53, v22, v23\n"
    "v_add_f32 v26, v26, v2\n"
    "v_exp_f32 v26, v26\n"
    "v_add_f32 v27, v27, v2\n"
    "v_exp_f32 v27, v27\n"
    "v_mfma_f32_32x32x8_bf16 v[4:19], v[156:157], v[64:65], v[4:19]\n"
    "v_add_f32 v28, v28, v2\n"
    "v_exp_f32 v28, v28\n"
    "v_cvt_pk_bf16_f32 v54, v24, v25\n"
    "buffer_load_dwordx4 v[244:247], v68, %[rws], %[vb6] offen offset:640 sc1\n"
    "buffer_load_dwordx4 v[248:251], v68, %[rws], %[vb6] offen offset:1280 sc1\n"
    "v_add_f32 v29, v29, v2\n"
    "v_exp_f32 v29, v29\n"
    "v_cvt_pk_bf16_f32 v55, v26, v27\n"
    "v_add_f32 v30, v30, v2\n"
    "v_exp_f32 v30, v30\n"
    "v_add_f32 v31, v31, v2\n"
    "v_exp_f32 v31, v31\n"
    "s_waitcnt vmcnt(6)\n"
    "v_mfma_f32_32x32x16_bf16 v[36:51], v[232:235], v[52:55], v[36:51]\n"
    "v_add_f32 v32, v32, v2\n"
    "v_exp_f32 v58, v32\n"
    "v_cvt_pk_bf16_f32 v56, v28, v29\n"
    "v_add_f32 v33, v33, v2\n"
    "v_exp_f32 v52, v33\n"
    "v_cvt_pk_bf16_f32 v57, v30, v31\n"
    "v_add_f32 v34, v34, v2\n"
    "v_exp_f32 v59, v34\n"
    "v_add_f32 v35, v35, v2\n"
    "v_exp_f32 v53, v35\n"
    "s_waitcnt vmcnt(5)\n"
    "v_mfma_f32_32x32x16_bf16 v[20:35], v[160:163], v[60:63], 0\n"
    "ds_read_b128 v[60:63], v89 offset:3584\n"
    "v_add_f32 v4, v4, v2\n"
    "v_exp_f32 v4, v4\n"
    "v_cvt_pk_bf16_f32 v58, v58, v52\n"
    "v_add_f32 v5, v5, v2\n"
    "v_exp_f32 v5, v5\n"
    "v_cvt_pk_bf16_f32 v59, v59, v53\n"
    "v_add_f32 v6, v6, v2\n"
    "v_exp_f32 v6, v6\n"
    "v_add_f32 v7, v7, v2\n"
    "v_exp_f32 v7, v7\n"
    "s_waitcnt vmcnt(3)\n"
    "v_mfma_f32_32x32x16_bf16 v[36:51], v[236:239], v[56:59], v[36:51]\n"
    "v_add_f32 v8, v8, v2\n"
    "v_exp_f32 v8, v8\n"
    "v_cvt_pk_bf16_f32 v52, v4, v5\n"
    "v_add_f32 v9, v9, v2\n"
    "v_exp_f32 v9, v9\n"
    "v_cvt_pk_bf16_f32 v53, v6, v7\n"
    "v_add_f32 v10, v10, v2\n"
    "v_exp_f32 v10, v10\n"
    "v_add_f32 v11, v11, v2\n"
    "v_exp_f32 v11, v11\n"
    "v_mfma_f32_32x32x8_bf16 v[20:35], v[158:159], v[64:65], v[20:35]\n"
    "ds_read_b128 v[64:67], v89 offset:5120\n"
    "v_add_f32 v12, v12, v2\n"
    "v_exp_f32 v12, v12\n"
    "v_cvt_pk_bf16_f32 v54, v8, v9\n"
    "buffer_load_dwordx4 v[252:255], v68, %[rws], %[vb6] offen offset:1920 sc1\n"
    "v_add_f32 v13, v13, v2\n"
    "v_exp_f32 v13, v13\n"
    "v_cvt_pk_bf16_f32 v55, v10, v11\n"
    "v_add_f32 v14, v14, v2\n"
    "v_exp_f32 v14, v14\n"
    "v_add_f32 v15, v15, v2\n"
    "v_exp_f32 v15, v15\n"
    "s_waitcnt vmcnt(3)\n"
    "v_mfma_f32_32x32x16_bf16 v[36:51], v[240:243], v[52:55], v[36:51]\n"
    "v_add_f32 v16, v16, v2\n"
    "v_exp_f32 v58, v16\n"
    "v_cvt_pk_bf16_f32 v56, v12, v13\n"
    "s_mov_b32 exec_hi, 0x3fffff\n"
    "v_cmp_ne_u32 vcc, %[tag], v1\n"
    "s_mov_b32 exec_hi, -1\n"
    "s_cbranch_vccnz LPS0_%=\n"
    "LRS0_%=:\n"
    "buffer_load_dwordx4 v[80:83], v88, %[rws], %[w8g] offen sc1\n"
    "s_add_u32 %[w8g], %[w8g], 1024\n"
    "v_add_f32 v17, v17, v2\n"
    "v_exp_f32 v52, v17\n"
    "v_cvt_pk_bf16_f32 v57, v14, v15\n"
    "v_add_f32 v18, v18, v2\n"
    "v_exp_f32 v59, v18\n"
    "v_add_f32 v19, v19, v2\n"
    "v_exp_f32 v53, v19\n"
    "s_waitcnt lgkmcnt(1)\n"
    "v_mfma_f32_32x32x16_bf16 v[4:19], v[164:167], v[60:63], 0\n"
    "v_add_f32 v20, v20, v2\n"
    "v_exp_f32 v20, v20\n"
    "v_cvt_pk_bf16_f32 v58, v58, v52\n"
    "buffer_load_dwordx4 v[136:139], v88, %[rws], %[wo4] offen offset:0 sc1\n"
    "v_add_f32 v21, v21, v2\n"
    "v_exp_f32 v21, v21\n"
    "v_cvt_pk_bf16_f32 v59, v59, v53\n"
    "v_add_f32 v22, v22, v2\n"
    "v_exp_f32 v22, v22\n"
    "v_add_f32 v23, v23, v2\n"
    "v_exp_f32 v23, v23\n"
    "s_waitcnt vmcnt(4)\n"
    "v_mfma_f32_32x32x16_bf16 v[36:51], v[244:247], v[56:59], v[36:51]\n"
    "v_add_f32 v24, v24, v2\n"
    "v_exp_f32 v24, v24\n"
    "v_cvt_pk_bf16_f32 v52, v20, v21\n"
    "v_add_f32 v25, v25, v2\n"
    "v_exp_f32 v25, v25\n"
    "v_cvt_pk_bf16_f32 v53, v22, v23\n"
    "s_mov_b32 m0, %[pm]\n"
    "s_add_u32 %[t0], %[pa], %[pi]\n"
    "buffer_load_dwordx4 v97, %[rpt], %[pa] offen lds\n"
    "s_mov_b32 %[pa], %[pb]\n"
    "s_mov_b32 %[pb], %[t0]\n"
    "s_xor_b32 %[pm], %[pm], 1024\n"
    "v_add_f32 v26, v26, v2\n"
    "v_exp_f32 v26, v26\n"
    "v_add_f32 v27, v27, v2\n"
    "v_exp_f32 v27, v27\n"
    "s_waitcnt lgkmcnt(0)\n"
    "v_mfma_f32_32x32x8_bf16 v[4:19], v[156:157], v[64:65], v[4:19]\n"
    "v_add_f32 v28, v28, v2\n"
    "v_exp_f32 v28, v28\n"
    "v_cvt_pk_bf16_f32 v54, v24, v25\n"
    "v_add_f32 v29, v29, v2\n"
    "v_exp_f32 v29, v29\n"
    "v_cvt_pk_bf16_f32 v55, v26, v27\n"
    "v_add_f32 v30, v30, v2\n"
    "v_exp_f32 v30, v30\n"
    "v_add_f32 v31, v31, v2\n"
    "v_exp_f32 v31, v31\n"
    "s_waitcnt vmcnt(4)\n"
    "v_mfma_f32_32x32x16_bf16 v[36:51], v[248:251], v[52:55], v[36:51]\n"
    "v_add_f32 v32, v32, v2\n"
    "v_exp_f32 v58, v32\n"
    "v_cvt_pk_bf16_f32 v56, v28, v29\n"
    "buffer_load_dwordx4 v[120:123], v88, %[rws], %[wo] offen offset:0 sc1\n"
    "v_add_f32 v33, v33, v2\n"
    "v_exp_f32 v52, v33\n"
    "v_cvt_pk_bf16_f32 v57, v30, v31\n"
    "v_add_f32 v34, v34, v2\n"
    "v_exp_f32 v59, v34\n"
    "v_add_f32 v35, v35, v2\n"
    "v_exp_f32 v53, v35\n"
    "v_mfma_f32_32x32x16_bf16 v[20:35], v[168:171], v[60:63], 0\n"
    "v_add_f32 v4, v4, v3\n"
    "v_exp_f32 v4, v4\n"
    "v_cvt_pk_bf16_f32 v58, v58, v52\n"
    "v_add_f32 v5, v5, v3\n"
    "v_exp_f32 v5, v5\n"
    "v_cvt_pk_bf16_f32 v59, v59, v53\n"
    "s_mov_b32 m0, %[pm]\n"
    "s_add_u32 %[t0], %[pa], %[pi]\n"
    "buffer_load_dwordx4 v97, %[rpt], %[pa] offen lds\n"
    "s_mov_b32 %[pa], %[pb]\n"
    "s_mov_b32 %[pb], %[t0]\n"
    "s_xor_b32 %[pm], %[pm], 1024\n"
    "v_add_f32 v6, v6, v3\n"
    "v_exp_f32 v6, v6\n"
    "v_add_f32 v7, v7, v3\n"
    "v_exp_f32 v7, v7\n"
    "s_waitcnt vmcnt(5)\n"
    "v_mfma_f32_32x32x16_bf16 v[36:51], v[252:255], v[56:59], v[36:51]\n"
    "v_add_f32 v8, v8, v3\n"
    "v_exp_f32 v8, v8\n"
    "v_cvt_pk_bf16_f32 v52, v4, v5\n"
    "v_add_f32 v9, v9, v3\n"
    "v_exp_f32 v9, v9\n"
    "v_cvt_pk_bf16_f32 v53, v6, v7\n"
    "v_add_f32 v10, v10, v3\n"
    "v_exp_f32 v10, v10\n"
    "v_add_f32 v11, v11, v3\n"
    "v_exp_f32 v11, v11\n"
    "v_mfma_f32_32x32x8_bf16 v[20:35], v[158:159], v[64:65], v[20:35]\n"
    "ds_read_b128 v[64:67], v89 offset:6144\n"
    "v_add_f32 v12, v12, v3\n"
    "v_exp_f32 v12, v12\n"
    "v_cvt_pk_bf16_f32 v54, v8, v9\n"
    "buffer_load_dwordx4 v[124:127], v88, %[rws], %[wo] offen offset:1024 sc1\n"
    "v_add_f32 v13, v13, v3\n"
    "v_exp_f32 v13, v13\n"
    "v_cvt_pk_bf16_f32 v55, v10, v11\n"
    "v_add_f32 v14, v14, v3\n"
    "v_exp_f32 v14, v14\n"
    "v_add_f32 v15, v15, v3\n"
    "v_exp_f32 v15, v15\n"
    "ds_write_b128 v94, v[36:39]\n"
    "ds_write_b128 v94, v[40:43] offset:1024\n"
    "ds_write_b64 v91, v[44:45]\n"
    "v_mfma_f32_32x32x16_bf16 v[36:51], v[192:195], v[52:55], 0\n"
    "v_add_f32 v16, v16, v3\n"
    "v_exp_f32 v58, v16\n"
    "v_cvt_pk_bf16_f32 v56, v12, v13\n"
    "v_add_u32 v89, 3584, v89\n"
    "v_add_f32 v17, v17, v3\n"
    "v_exp_f32 v52, v17\n"
    "v_cvt_pk_bf16_f32 v57, v14, v15\n"
    "v_add_f32 v18, v18, v3\n"
    "v_exp_f32 v59, v18\n"
    "v_add_f32 v19, v19, v3\n"
    "v_exp_f32 v53, v19\n"
    "v_mov_b32 v2, v3\n"
    "LBS_%=:\n"
    "v_mfma_f32_32x32x16_bf16 v[4:19], v[172:175], v[60:63], 0\n"
    "v_add_f32 v20, v20, v2\n"
    "v_exp_f32 v20, v20\n"
    "v_cvt_pk_bf16_f32 v58, v58, v52\n"
    "v_add_f32 v21, v21, v2\n"
    "v_exp_f32 v21, v21\n"
    "v_cvt_pk_bf16_f32 v59, v59, v53\n"
    "v_add_f32 v22, v22, v2\n"
    "v_exp_f32 v22, v22\n"
    "v_add_f32 v23, v23, v2\n"
    "v_exp_f32 v23, v23\n"
    "v_mfma_f32_32x32x16_bf16 v[36:51], v[196:199], v[56:59], v[36:51]\n"
    "s_waitcnt lgkmcnt(0)\n"
    "s_mov_b64 exec, %[me]\n"
    "ds_inc_u32 v0, v0\n"
    "s_mov_b64 exec, -1\n"
    "s_add_u32 %[nd], %[nd], 1\n"
    "v_add_f32 v24, v24, v2\n"
    "v_exp_f32 v24, v24\n"
    "v_cvt_pk_bf16_f32 v52, v20, v21\n"
    "buffer_load_dwordx4 v[128:131], v88, %[rws], %[wo] offen offset:2048 sc1\n"
    "v_add_f32 v25, v25, v2\n"
    "v_exp_f32 v25, v25\n"
    "v_cvt_pk_bf16_f32 v53, v22, v23\n"
    "v_add_f32 v26, v26, v2\n"
    "v_exp_f32 v26, v26\n"
    "v_add_f32 v27, v27, v2\n"
    "v_exp_f32 v27, v27\n"
    "v_mfma_f32_32x32x8_bf16 v[4:19], v[156:157], v[64:65], v[4:19]\n"
    "v_add_f32 v28, v28, v2\n"
    "v_exp_f32 v28, v28\n"
    "v_cvt_pk_bf16_f32 v54, v24, v25\n"
    "v_add_f32 v29, v29, v2\n"
    "v_exp_f32 v29, v29\n"
    "v_cvt_pk_bf16_f32 v55, v26, v27\n"
    "v_add_f32 v30, v30, v2\n"
    "v_exp_f32 v30, v30\n"
    "v_add_f32 v31, v31, v2\n"
    "v_exp_f32 v31, v31\n"
    "v_mfma_f32_32x32x16_bf16 v[36:51], v[200:203], v[52:55], v[36:51]\n"
    "v_add_f32 v32, v32, v2\n"
    "v_exp_f32 v58, v32\n"
    "v_cvt_pk_bf16_f32 v56, v28, v29\n"
    "v_add_f32 v33, v33, v2\n"
    "v_exp_f32 v52, v33\n"
    "v_cvt_pk_bf16_f32 v57, v30, v31\n"
    "v_add_u32 v1, %[w8b], v88\n"
    "s_add_u32 %[w8b], %[w8b], 1024\n"
    "s_waitcnt vmcnt(6)\n"
    "ds_write_b128 v1, v[80:83]\n"
    "v_add_f32 v34, v34, v2\n"
    "v_exp_f32 v59, v34\n"
    "v_add_f32 v35, v35, v2\n"
    "v_exp_f32 v53, v35\n"
    "v_mfma_f32_32x32x16_bf16 v[20:35], v[176:179], v[60:63], 0\n"
    "v_add_f32 v4, v4, v2\n"
    "v_exp_f32 v4, v4\n"
    "v_cvt_pk_bf16_f32 v58, v58, v52\n"
    "buffer_load_dwordx4 v[132:135], v88, %[rws], %[wo] offen offset:3072 sc1\n"
    "v_add_f32 v5, v5, v2\n"
    "v_exp_f32 v5, v5\n"
    "v_cvt_pk_bf16_f32 v59, v59, v53\n"
    "v_add_f32 v6, v6, v2\n"
    "v_exp_f32 v6, v6\n"
    "v_add_f32 v7, v7, v2\n"
    "v_exp_f32 v7, v7\n"
    "v_mfma_f32_32x32x16_bf16 v[36:51], v[204:207], v[56:59], v[36:51]\n"
    "v_add_f32 v8, v8, v2\n"
    "v_exp_f32 v8, v8\n"
    "v_cvt_pk_bf16_f32 v52, v4, v5\n"
    "v_add_f32 v9, v9, v2\n"
    "v_exp_f32 v9, v9\n"
    "v_cvt_pk_bf16_f32 v53, v6, v7\n"
    "v_add_f32 v10, v10, v2\n"
    "v_exp_f32 v10, v10\n"
    "v_add_f32 v11, v11, v2\n"
    "v_exp_f32 v11, v11\n"
    "v_mfma_f32_32x32x8_bf16 v[20:35], v[158:159], v[64:65], v[20:35]\n"
    "ds_read_b128 v[64:67], v89 offset:1024\n"
    "v_add_f32 v12, v12, v2\n"
    "v_exp_f32 v12, v12\n"
    "v_cvt_pk_bf16_f32 v54, v8, v9\n"
    "ds_read_b32 v1, v0\n"
    "v_add_f32 v13, v13, v2\n"
    "v_exp_f32 v13, v13\n"
    "v_cvt_pk_bf16_f32 v55, v10, v11\n"
    "buffer_load_dwordx4 v[140:143], v88, %[rws], %[wo4] offen offset:1024 sc1\n"
    "v_add_f32 v14, v14, v2\n"
    "v_exp_f32 v14, v14\n"
    "v_add_f32 v15, v15, v2\n"
    "v_exp_f32 v15, v15\n"
    "v_mfma_f32_32x32x16_bf16 v[36:51], v[208:211], v[52:55], v[36:51]\n"
    "v_add_f32 v16, v16, v2\n"
    "v_exp_f32 v58, v16\n"
    "v_cvt_pk_bf16_f32 v56, v12, v13\n"
    "s_waitcnt lgkmcnt(0)\n"
    "v_cmp_gt_u32 vcc, %[nd], v1\n"
    "s_cbranch_vccnz LPS2_%=\n"
    "LRS2_%=:\n"
    "ds_read2st64_b32 v[84:85], v92 offset0:1 offset1:3\n"
    "ds_read2st64_b32 v[86:87], v92 offset0:5 offset1:7\n"
    "ds_read2st64_b64 v[76:79], v95 offset1:4\n"
    "ds_read2st64_b64 v[80:83], v95 offset0:8 offset1:12\n"
    "ds_read2st64_b64 v[68:71], v92 offset1:1\n"
    "ds_read2st64_b64 v[72:75], v92 offset0:2 offset1:3\n"
    "v_add_f32 v17, v17, v2\n"
    "v_exp_f32 v52, v17\n"
    "v_cvt_pk_bf16_f32 v57, v14, v15\n"
    "v_add_f32 v18, v18, v2\n"
    "v_exp_f32 v59, v18\n"
    "v_add_f32 v19, v19, v2\n"
    "v_exp_f32 v53, v19\n"
    "v_mfma_f32_32x32x16_bf16 v[4:19], v[180:183], v[60:63], 0\n"
    "v_add_f32 v20, v20, v2\n"
    "v_exp_f32 v20, v20\n"
    "v_cvt_pk_bf16_f32 v58, v58, v52\n"
    "v_add_f32 v21, v21, v2\n"
    "v_exp_f32 v21, v21\n"
    "v_cvt_pk_bf16_f32 v59, v59, v53\n"
    "v_add_f32 v22, v22, v2\n"
    "v_exp_f32 v22, v22\n"
    "v_add_f32 v23, v23, v2\n"
    "v_exp_f32 v23, v23\n"
    "v_mfma_f32_32x32x16_bf16 v[36:51], v[212:215], v[56:59], v[36:51]\n"
    "s_waitcnt lgkmcnt(5)\n"
    "v_add_f32 v84, v84, v85\n"
    "s_waitcnt lgkmcnt(4)\n"
    "v_add_f32 v86, v86, v87\n"
    "v_add_f32 v24, v24, v2\n"
    "v_exp_f32 v24, v24\n"
    "v_cvt_pk_bf16_f32 v52, v20, v21\n"
    "buffer_load_dwordx4 v[144:147], v88, %[rws], %[wo4] offen offset:2048 sc1\n"
    "v_add_f32 v25, v25, v2\n"
    "v_exp_f32 v25, v25\n"
    "v_cvt_pk_bf16_f32 v53, v22, v23\n"
    "v_add_f32 v84, v84, v86\n"
    "v_add_f32 v26, v26, v2\n"
    "v_exp_f32 v26, v26\n"
    "s_waitcnt lgkmcnt(3)\n"
    "v_add_f32 v76, v76, v78\n"
    "v_add_f32 v27, v27, v2\n"
    "v_exp_f32 v27, v27\n"
    "v_mfma_f32_32x32x8_bf16 v[4:19], v[156:157], v[64:65], v[4:19]\n"
    "v_add_f32 v77, v77, v79\n"
    "v_add_f32 v28, v28, v2\n"
    "v_exp_f32 v28, v28\n"
    "v_cvt_pk_bf16_f32 v54, v24, v25\n"
    "v_add_f32 v29, v29, v2\n"
    "v_exp_f32 v29, v29\n"
    "v_cvt_pk_bf16_f32 v55, v26, v27\n"
    "v_add_f32_dpp v84, v84, v84 quad_perm:[1,0,3,2] row_mask:0xf bank_mask:0xf\n"
    "v_add_f32 v30, v30, v2\n"
    "v_exp_f32 v30, v30\n"
    "s_waitcnt lgkmcnt(2)\n"
    "v_add_f32 v80, v80, v82\n"
    "v_add_f32 v31, v31, v2\n"
    "v_exp_f32 v31, v31\n"
    "v_mfma_f32_32x32x16_bf16 v[36:51], v[216:219], v[52:55], v[36:51]\n"
    "v_rcp_f32 v84, v84\n"
    "v_add_f32 v32, v32, v2\n"
    "v_exp_f32 v58, v32\n"
    "v_cvt_pk_bf16_f32 v56, v28, v29\n"
    "v_add_f32 v33, v33, v2\n"
    "v_exp_f32 v52, v33\n"
    "v_cvt_pk_bf16_f32 v57, v30, v31\n"
    "v_add_f32 v81, v81, v83\n"
    "v_add_f32 v34, v34, v2\n"
    "v_exp_f32 v59, v34\n"
    "v_add_f32 v76, v76, v80\n"
    "v_add_f32 v35, v35, v2\n"
    "v_exp_f32 v53, v35\n"
    "v_mfma_f32_32x32x16_bf16 v[20:35], v[184:187], v[60:63], 0\n"
    "v_add_f32 v77, v77, v81\n"
    "v_add_f32 v4, v4, v2\n"
    "v_exp_f32 v4, v4\n"
    "v_cvt_pk_bf16_f32 v58, v58, v52\n"
    "buffer_load_dwordx4 v[148:151], v88, %[rws], %[wo4] offen offset:3072 sc1\n"
    "v_add_f32 v5, v5, v2\n"
    "v_exp_f32 v5, v5\n"
    "v_cvt_pk_bf16_f32 v59, v59, v53\n"
    "v_mul_f32 v76, v76, v84\n"
    "v_add_f32 v6, v6, v2\n"
    "v_exp_f32 v6, v6\n"
    "v_mul_f32 v77, v77, v84\n"
    "v_add_f32 v7, v7, v2\n"
    "v_exp_f32 v7, v7\n"
    "v_mfma_f32_32x32x16_bf16 v[36:51], v[220:223], v[56:59], v[36:51]\n"
    "v_cvt_pk_bf16_f32 v76, v76, v77\n"
    "v_add_f32 v8, v8, v2\n"
    "v_exp_f32 v8, v8\n"
    "v_cvt_pk_bf16_f32 v52, v4, v5\n"
    "ds_write_b32 v93, v76\n"
    "v_add_f32 v9, v9, v2\n"
    "v_exp_f32 v9, v9\n"
    "v_cvt_pk_bf16_f32 v53, v6, v7\n"
    "s_cmp_lg_u32 %[lw], %[cnt]\n"
    "s_cbranch_scc1 LLS1_%=\n"
    "s_waitcnt lgkmcnt(2)\n"
    "v_add_f32 v68, v68, v70\n"
    "v_add_f32 v69, v69, v71\n"
    "s_waitcnt lgkmcnt(1)\n"
    "v_add_f32 v72, v72, v74\n"
    "v_add_f32 v73, v73, v75\n"
    "v_add_f32 v68, v68, v72\n"
    "v_add_f32 v69, v69, v73\n"
    "v_and_b32 v70, 0xf0, v93\n"
    "v_lshrrev_b32 v71, 8, v88\n"
    "v_mul_f32 v68, v68, v84\n"
    "v_mul_f32 v69, v69, v84\n"
    "v_lshl_add_u32 v70, v71, 2, v70\n"
    "v_cvt_pk_bf16_f32 v68, v68, v69\n"
    "v_add_u32 v70, %[lo], v70\n"
    "ds_write_b32 v70, v68\n"
    "LLS1_%=:\n"
    "v_add_f32 v10, v10, v2\n"
    "v_exp_f32 v10, v10\n"
    "v_add_f32 v11, v11, v2\n"
    "v_exp_f32 v11, v11\n"
    "v_mfma_f32_32x32x8_bf16 v[20:35], v[158:159], v[64:65], v[20:35]\n"
    "ds_read_b128 v[64:67], v89 offset:2048\n"
    "v_add_f32 v12, v12, v2\n"
    "v_exp_f32 v12, v12\n"
    "v_cvt_pk_bf16_f32 v54, v8, v9\n"
    "v_add_f32 v13, v13, v2\n"
    "v_exp_f32 v13, v13\n"
    "v_cvt_pk_bf16_f32 v55, v10, v11\n"
    "buffer_load_dwordx4 v[152:155], v88, %[rws], %[wo8] offen offset:0 sc1\n"
    "v_add_f32 v14, v14, v2\n"
    "v_exp_f32 v14, v14\n"
    "v_add_f32 v15, v15, v2\n"
    "v_exp_f32 v15, v15\n"
    "v_mfma_f32_32x32x16_bf16 v[36:51], v[224:227], v[52:55], v[36:51]\n"
    "s_waitcnt lgkmcnt(1)\n"
    "s_mov_b64 exec, %[me]\n"
    "ds_inc_u32 v0, v0\n"
    "s_mov_b64 exec, -1\n"
    "s_add_u32 %[nd], %[nd], 1\n"
    "v_add_f32 v16, v16, v2\n"
    "v_exp_f32 v58, v16\n"
    "v_cvt_pk_bf16_f32 v56, v12, v13\n"
    "v_add_f32 v17, v17, v2\n"
    "v_exp_f32 v52, v17\n"
    "v_cvt_pk_bf16_f32 v57, v14, v15\n"
    "v_add_f32 v18, v18, v2\n"
    "v_exp_f32 v59, v18\n"
    "v_add_f32 v19, v19, v2\n"
    "v_exp_f32 v53, v19\n"
    "v_mfma_f32_32x32x16_bf16 v[4:19], v[188:191], v[60:63], 0\n"
    "s_waitcnt vmcnt(9)\n"
    "ds_read_b64_tr_b16 v[76:77], v96\n"
    "ds_read_b64_tr_b16 v[78:79], v96 offset:512\n"
    "v_add_f32 v20, v20, v2\n"
    "v_exp_f32 v20, v20\n"
    "v_cvt_pk_bf16_f32 v58, v58, v52\n"
    "v_add_f32 v21, v21, v2\n"
    "v_exp_f32 v21, v21\n"
    "v_cvt_pk_bf16_f32 v59, v59, v53\n"
    "ds_read_b32 v3, v99 offset:128\n"
    "v_add_u32 v99, 128, v99\n"
    "s_waitcnt lgkmcnt(0)\n"
    "v_add_f32 v22, v22, v2\n"
    "v_exp_f32 v22, v22\n"
    "v_add_f32 v23, v23, v2\n"
    "v_exp_f32 v23, v23\n"
    "v_mfma_f32_32x32x16_bf16 v[36:51], v[228:231], v[56:59], v[36:51]\n"
    "v_add_f32 v24, v24, v2\n"
    "v_exp_f32 v24, v24\n"
    "v_cvt_pk_bf16_f32 v52, v20, v21\n"
    "buffer_load_dwordx4 v[80:83], v88, %[rws], %[w8g] offen sc1\n"
    "s_add_u32 %[w8g], %[w8g], 1024\n"
    "v_add_f32 v25, v25, v2\n"
    "v_exp_f32 v25, v25\n"
    "v_cvt_pk_bf16_f32 v53, v22, v23\n"
    "v_add_f32 v26, v26, v2\n"
    "v_exp_f32 v26, v26\n"
    "v_add_f32 v27, v27, v2\n"
    "v_exp_f32 v27, v27\n"
    "v_mfma_f32_32x32x8_bf16 v[4:19], v[156:157], v[64:65], v[4:19]\n"
    "buffer_store_dwordx4 v[76:79], v98, %[rws], %[pd] offen sc1\n"
    "s_add_u32 %[pd], %[pd], %[pdi]\n"
    "v_add_f32 v28, v28, v2\n"
    "v_exp_f32 v28, v28\n"
    "v_cvt_pk_bf16_f32 v54, v24, v25\n"
    "v_add_f32 v29, v29, v2\n"
    "v_exp_f32 v29, v29\n"
    "v_cvt_pk_bf16_f32 v55, v26, v27\n"
    "s_mov_b32 m0, %[pm]\n"
    "s_add_u32 %[t0], %[pa], %[pi]\n"
    "buffer_load_dwordx4 v97, %[rpt], %[pa] offen lds\n"
    "s_mov_b32 %[pa], %[pb]\n"
    "s_mov_b32 %[pb], %[t0]\n"
    "s_xor_b32 %[pm], %[pm], 1024\n"
    "v_add_f32 v30, v30, v2\n"
    "v_exp_f32 v30, v30\n"
    "v_add_f32 v31, v31, v2\n"
    "v_exp_f32 v31, v31\n"
    "v_mfma_f32_32x32x16_bf16 v[36:51], v[232:235], v[52:55], v[36:51]\n"
    "v_xor_b32 v96, 1024, v96\n"
    "v_add_f32 v32, v32, v2\n"
    "v_exp_f32 v58, v32\n"
    "v_cvt_pk_bf16_f32 v56, v28, v29\n"
    "v_add_f32 v33, v33, v2\n"
    "v_exp_f32 v52, v33\n"
    "v_cvt_pk_bf16_f32 v57, v30, v31\n"
    "v_add_f32 v34, v34, v2\n"
    "v_exp_f32 v59, v34\n"
    "v_add_f32 v35, v35, v2\n"
    "v_exp_f32 v53, v35\n"
    "v_mfma_f32_32x32x16_bf16 v[20:35], v[160:163], v[60:63], 0\n"
    "ds_read_b128 v[60:63], v89 offset:3584\n"
    "v_add_f32 v4, v4, v2\n"
    "v_exp_f32 v4, v4\n"
    "v_cvt_pk_bf16_f32 v58, v58, v52\n"
    "ds_read_b32 v1, v0\n"
    "v_add_f32 v5, v5, v2\n"
    "v_exp_f32 v5, v5\n"
    "v_cvt_pk_bf16_f32 v59, v59, v53\n"
    "v_add_f32 v6, v6, v2\n"
    "v_exp_f32 v6, v6\n"
    "v_add_f32 v7, v7, v2\n"
    "v_exp_f32 v7, v7\n"
    "v_mfma_f32_32x32x16_bf16 v[36:51], v[236:239], v[56:59], v[36:51]\n"
    "v_add_f32 v8, v8, v2\n"
    "v_exp_f32 v8, v8\n"
    "v_cvt_pk_bf16_f32 v52, v4, v5\n"
    "s_waitcnt lgkmcnt(0)\n"
    "v_cmp_gt_u32 vcc, %[nd], v1\n"
    "s_cbranch_vccnz LPS3_%=\n"
    "LRS3_%=:\n"
    "ds_read_b128 v[72:75], v90 offset:0\n"
    "ds_read_b128 v[68:71], v88 offset:41472\n"
    "v_add_f32 v9, v9, v2\n"
    "v_exp_f32 v9, v9\n"
    "v_cvt_pk_bf16_f32 v53, v6, v7\n"
    "v_add_f32 v10, v10, v2\n"
    "v_exp_f32 v10, v10\n"
    "v_add_f32 v11, v11, v2\n"
    "v_exp_f32 v11, v11\n"
    "v_mfma_f32_32x32x8_bf16 v[20:35], v[158:159], v[64:65], v[20:35]\n"
    "ds_read_b128 v[64:67], v89 offset:5120\n"
    "v_add_f32 v12, v12, v2\n"
    "v_exp_f32 v12, v12\n"
    "v_cvt_pk_bf16_f32 v54, v8, v9\n"
    "s_add_u32 %[wo], %[wo], 73728\n"
    "s_add_u32 %[wo4], %[wo4], 73728\n"
    "s_add_u32 %[wo8], %[wo8], 73728\n"
    "v_add_f32 v13, v13, v2\n"
    "v_exp_f32 v13, v13\n"
    "v_cvt_pk_bf16_f32 v55, v10, v11\n"
    "v_add_f32 v14, v14, v2\n"
    "v_exp_f32 v14, v14\n"
    "v_add_f32 v15, v15, v2\n"
    "v_exp_f32 v15, v15\n"
    "v_mfma_f32_32x32x16_bf16 v[36:51], v[240:243], v[52:55], v[36:51]\n"
    "v_add_f32 v16, v16, v2\n"
    "v_exp_f32 v58, v16\n"
    "v_cvt_pk_bf16_f32 v56, v12, v13\n"
    "v_add_f32 v17, v17, v2\n"
    "v_exp_f32 v52, v17\n"
    "v_cvt_pk_bf16_f32 v57, v14, v15\n"
    "s_waitcnt vmcnt(11) lgkmcnt(2)\n"
    "v_mfma_f32_16x16x32_bf16 v[116:119], v[136:139], v[72:75], v[116:119]\n"
    "v_add_f32 v18, v18, v2\n"
    "v_exp_f32 v59, v18\n"
    "v_add_f32 v19, v19, v2\n"
    "v_exp_f32 v53, v19\n"
    "v_mfma_f32_32x32x16_bf16 v[4:19], v[164:167], v[60:63], 0\n"
    "ds_read_b128 v[72:75], v88 offset:42496\n"
    "v_add_f32 v20, v20, v2\n"
    "v_exp_f32 v20, v20\n"
    "v_cvt_pk_bf16_f32 v58, v58, v52\n"
    "buffer_load_dwordx4 v[136:139], v88, %[rws], %[wo4] offen offset:0 sc1\n"
    "v_add_f32 v21, v21, v2\n"
    "v_exp_f32 v21, v21\n"
    "v_cvt_pk_bf16_f32 v59, v59, v53\n"
    "s_waitcnt vmcnt(11) lgkmcnt(2)\n"
    "v_mfma_f32_16x16x32_bf16 v[100:103], v[120:123], v[68:71], v[100:103]\n"
    "v_add_f32 v22, v22, v2\n"
    "v_exp_f32 v22, v22\n"
    "v_add_f32 v23, v23, v2\n"
    "v_exp_f32 v23, v23\n"
    "v_mfma_f32_32x32x16_bf16 v[36:51], v[244:247], v[56:59], v[36:51]\n"
    "v_add_f32 v24, v24, v2\n"
    "v_exp_f32 v24, v24\n"
    "v_cvt_pk_bf16_f32 v52, v20, v21\n"
    "v_add_f32 v25, v25, v2\n"
    "v_exp_f32 v25, v25\n"
    "v_cvt_pk_bf16_f32 v53, v22, v23\n"
    "s_waitcnt vmcnt(10)\n"
    "v_mfma_f32_16x16x32_bf16 v[104:107], v[124:127], v[68:71], v[104:107]\n"
    "v_add_f32 v26, v26, v2\n"
    "v_exp_f32 v26, v26\n"
    "v_add_f32 v27, v27, v2\n"
    "v_exp_f32 v27, v27\n"
    "s_waitcnt lgkmcnt(1)\n"
    "v_mfma_f32_32x32x8_bf16 v[4:19], v[156:157], v[64:65], v[4:19]\n"
    "v_add_f32 v28, v28, v2\n"
    "v_exp_f32 v28, v28\n"
    "v_cvt_pk_bf16_f32 v54, v24, v25\n"
    "v_add_f32 v29, v29, v2\n"
    "v_exp_f32 v29, v29\n"
    "v_cvt_pk_bf16_f32 v55, v26, v27\n"
    "s_waitcnt vmcnt(9)\n"
    "v_mfma_f32_16x16x32_bf16 v[108:111], v[128:131], v[68:71], v[108:111]\n"
    "v_add_f32 v30, v30, v2\n"
    "v_exp_f32 v30, v30\n"
    "v_add_f32 v31, v31, v2\n"
    "v_exp_f32 v31, v31\n"
    "v_mfma_f32_32x32x16_bf16 v[36:51], v[248:251], v[52:55], v[36:51]\n"
    "v_add_f32 v32, v32, v2\n"
    "v_exp_f32 v58, v32\n"
    "v_cvt_pk_bf16_f32 v56, v28, v29\n"
    "buffer_load_dwordx4 v[120:123], v88, %[rws], %[wo] offen offset:0 sc1\n"
    "v_add_f32 v33, v33, v2\n"
    "v_exp_f32 v52, v33\n"
    "v_cvt_pk_bf16_f32 v57, v30, v31\n"
    "s_waitcnt vmcnt(9)\n"
    "v_mfma_f32_16x16x32_bf16 v[112:115], v[132:135], v[68:71], v[112:115]\n"
    "v_add_f32 v34, v34, v2\n"
    "v_exp_f32 v59, v34\n"
    "v_add_f32 v35, v35, v2\n"
    "v_exp_f32 v53, v35\n"
    "v_mfma_f32_32x32x16_bf16 v[20:35], v[168:171], v[60:63], 0\n"
    "v_add_f32 v4, v4, v3\n"
    "v_exp_f32 v4, v4\n"
    "v_cvt_pk_bf16_f32 v58, v58, v52\n"
    "v_add_f32 v5, v5, v3\n"
    "v_exp_f32 v5, v5\n"
    "v_cvt_pk_bf16_f32 v59, v59, v53\n"
    "s_waitcnt vmcnt(8) lgkmcnt(0)\n"
    "v_mfma_f32_16x16x32_bf16 v[100:103], v[140:143], v[72:75], v[100:103]\n"
    "v_add_f32 v6, v6, v3\n"
    "v_exp_f32 v6, v6\n"
    "v_add_f32 v7, v7, v3\n"
    "v_exp_f32 v7, v7\n"
    "v_mfma_f32_32x32x16_bf16 v[36:51], v[252:255], v[56:59], v[36:51]\n"
    "v_add_f32 v8, v8, v3\n"
    "v_exp_f32 v8, v8\n"
    "v_cvt_pk_bf16_f32 v52, v4, v5\n"
    "v_add_f32 v9, v9, v3\n"
    "v_exp_f32 v9, v9\n"
    "v_cvt_pk_bf16_f32 v53, v6, v7\n"
    "s_waitcnt vmcnt(7)\n"
    "v_mfma_f32_16x16x32_bf16 v[104:107], v[144:147], v[72:75], v[104:107]\n"
    "v_add_f32 v10, v10, v3\n"
    "v_exp_f32 v10, v10\n"
    "v_add_f32 v11, v11, v3\n"
    "v_exp_f32 v11, v11\n"
    "v_mfma_f32_32x32x8_bf16 v[20:35], v[158:159], v[64:65], v[20:35]\n"
    "ds_read_b128 v[64:67], v89 offset:6144\n"
    "v_add_f32 v12, v12, v3\n"
    "v_exp_f32 v12, v12\n"
    "v_cvt_pk_bf16_f32 v54, v8, v9\n"
    "buffer_load_dwordx4 v[124:127], v88, %[rws], %[wo] offen offset:1024 sc1\n"
    "v_add_f32 v13, v13, v3\n"
    "v_exp_f32 v13, v13\n"
    "v_cvt_pk_bf16_f32 v55, v10, v11\n"
    "s_waitcnt vmcnt(7)\n"
    "v_mfma_f32_16x16x32_bf16 v[108:111], v[148:151], v[72:75], v[108:111]\n"
    "v_add_f32 v14, v14, v3\n"
    "v_exp_f32 v14, v14\n"
    "v_add_f32 v15, v15, v3\n"
    "v_exp_f32 v15, v15\n"
    "ds_write_b128 v94, v[36:39]\n"
    "ds_write_b128 v94, v[40:43] offset:1024\n"
    "ds_write_b64 v91, v[44:45]\n"
    "v_mfma_f32_32x32x16_bf16 v[36:51], v[192:195], v[52:55], 0\n"
    "v_add_f32 v16, v16, v3\n"
    "v_exp_f32 v58, v16\n"
    "v_cvt_pk_bf16_f32 v56, v12, v13\n"
    "v_add_u32 v89, 3584, v89\n"
    "v_add_f32 v17, v17, v3\n"
    "v_exp_f32 v52, v17\n"
    "v_cvt_pk_bf16_f32 v57, v14, v15\n"
    "s_waitcnt vmcnt(6)\n"
    "v_mfma_f32_16x16x32_bf16 v[112:115], v[152:155], v[72:75], v[112:115]\n"
    "v_add_f32 v18, v18, v3\n"
    "v_exp_f32 v59, v18\n"
    "v_add_f32 v19, v19, v3\n"
    "v_exp_f32 v53, v19\n"
    "v_mov_b32 v2, v3\n"
    "s_sub_u32 %[cnt], %[cnt], 1\n"
    "s_cmp_lg_u32 %[cnt], 1\n"
    "s_cbranch_scc1 LBS_%=\n"
    "s_add_u32 %[pa], %[wo], 73728\n"
    "s_add_u32 %[pb], %[wo4], 73728\n"
    "s_add_u32 %[pm], %[wo8], 73728\n"
    "v_mfma_f32_32x32x16_bf16 v[4:19], v[172:175], v[60:63], 0\n"
    "s_bitcmp1_b32 %[xo], 31\n"
    "s_cbranch_scc1 LPS4_%=\n"
    "LRS4_%=:\n"
    "v_add_f32 v20, v20, v2\n"
    "v_exp_f32 v20, v20\n"
    "v_cvt_pk_bf16_f32 v58, v58, v52\n"
    "buffer_load_dwordx4 v[168:171], v88, %[rws], %[pa] offen offset:1024 sc1\n"
    "v_add_f32 v21, v21, v2\n"
    "v_exp_f32 v21, v21\n"
    "v_cvt_pk_bf16_f32 v59, v59, v53\n"
    "v_add_f32 v22, v22, v2\n"
    "v_exp_f32 v22, v22\n"
    "v_add_f32 v23, v23, v2\n"
    "v_exp_f32 v23, v23\n"
    "v_mfma_f32_32x32x16_bf16 v[36:51], v[196:199], v[56:59], v[36:51]\n"
    "s_waitcnt lgkmcnt(0)\n"
    "s_mov_b64 exec, %[me]\n"
    "ds_inc_u32 v0, v0\n"
    "s_mov_b64 exec, -1\n"
    "s_add_u32 %[nd], %[nd], 1\n"
    "v_add_f32 v24, v24, v2\n"
    "v_exp_f32 v24, v24\n"
    "v_cvt_pk_bf16_f32 v52, v20, v21\n"
    "buffer_load_dwordx4 v[128:131], v88, %[rws], %[wo] offen offset:2048 sc1\n"
    "v_add_f32 v25, v25, v2\n"
    "v_exp_f32 v25, v25\n"
    "v_cvt_pk_bf16_f32 v53, v22, v23\n"
    "v_add_f32 v26, v26, v2\n"
    "v_exp_f32 v26, v26\n"
    "v_add_f32 v27, v27, v2\n"
    "v_exp_f32 v27, v27\n"
    "v_mfma_f32_32x32x8_bf16 v[4:19], v[156:157], v[64:65], v[4:19]\n"
    "s_waitcnt vmcnt(17)\n"
    "ds_read_b64_tr_b16 v[76:77], v96\n"
    "ds_read_b64_tr_b16 v[78:79], v96 offset:512\n"
    "v_add_f32 v28, v28, v2\n"
    "v_exp_f32 v28, v28\n"
    "v_cvt_pk_bf16_f32 v54, v24, v25\n"
    "v_add_f32 v29, v29, v2\n"
    "v_exp_f32 v29, v29\n"
    "v_cvt_pk_bf16_f32 v55, v26, v27\n"
    "v_add_f32 v30, v30, v2\n"
    "v_exp_f32 v30, v30\n"
    "v_add_f32 v31, v31, v2\n"
    "v_exp_f32 v31, v31\n"
    "v_mfma_f32_32x32x16_bf16 v[36:51], v[200:203], v[52:55], v[36:51]\n"
    "v_add_f32 v32, v32, v2\n"
    "v_exp_f32 v58, v32\n"
    "v_cvt_pk_bf16_f32 v56, v28, v29\n"
    "buffer_load_dwordx4 v[192:195], v88, %[rws], %[pa] offen offset:2048 sc1\n"
    "v_add_f32 v33, v33, v2\n"
    "v_exp_f32 v52, v33\n"
    "v_cvt_pk_bf16_f32 v57, v30, v31\n"
    "v_add_u32 v1, %[w8b], v88\n"
    "s_add_u32 %[w8b], %[w8b], 1024\n"
    "s_waitcnt vmcnt(8)\n"
    "ds_write_b128 v1, v[80:83]\n"
    "v_add_f32 v34, v34, v2\n"
    "v_exp_f32 v59, v34\n"
    "v_add_f32 v35, v35, v2\n"
    "v_exp_f32 v53, v35\n"
    "v_mfma_f32_32x32x16_bf16 v[20:35], v[176:179], v[60:63], 0\n"
    "s_waitcnt lgkmcnt(1)\n"
    "buffer_store_dwordx4 v[76:79], v98, %[rws], %[pd] offen sc1\n"
    "s_add_u32 %[pd], %[pd], %[pdi]\n"
    "v_xor_b32 v96, 1024, v96\n"
    "v_add_f32 v4, v4, v2\n"
    "v_exp_f32 v4, v4\n"
    "v_cvt_pk_bf16_f32 v58, v58, v52\n"
    "buffer_load_dwordx4 v[132:135], v88, %[rws], %[wo] offen offset:3072 sc1\n"
    "v_add_f32 v5, v5, v2\n"
    "v_exp_f32 v5, v5\n"
    "v_cvt_pk_bf16_f32 v59, v59, v53\n"
    "v_add_f32 v6, v6, v2\n"
    "v_exp_f32 v6, v6\n"
    "v_add_f32 v7, v7, v2\n"
    "v_exp_f32 v7, v7\n"
    "v_mfma_f32_32x32x16_bf16 v[36:51], v[204:207], v[56:59], v[36:51]\n"
    "v_add_f32 v8, v8, v2\n"
    "v_exp_f32 v8, v8\n"
    "v_cvt_pk_bf16_f32 v52, v4, v5\n"
    "v_add_f32 v9, v9, v2\n"
    "v_exp_f32 v9, v9\n"
    "v_cvt_pk_bf16_f32 v53, v6, v7\n"
    "v_add_f32 v10, v10, v2\n"
    "v_exp_f32 v10, v10\n"
    "v_add_f32 v11, v11, v2\n"
    "v_exp_f32 v11, v11\n"
    "v_mfma_f32_32x32x8_bf16 v[20:35], v[158:159], v[64:65], v[20:35]\n"
    "ds_read_b128 v[64:67], v89 offset:1024\n"
    "v_add_f32 v12, v12, v2\n"
    "v_exp_f32 v12, v12\n"
    "v_cvt_pk_bf16_f32 v54, v8, v9\n"
    "ds_read_b32 v1, v0\n"
    "v_add_f32 v13, v13, v2\n"
    "v_exp_f32 v13, v13\n"
    "v_cvt_pk_bf16_f32 v55, v10, v11\n"
    "buffer_load_dwordx4 v[140:143], v88, %[rws], %[wo4] offen offset:1024 sc1\n"
    "v_add_f32 v14, v14, v2\n"
    "v_exp_f32 v14, v14\n"
    "v_add_f32 v15, v15, v2\n"
    "v_exp_f32 v15, v15\n"
    "v_mfma_f32_32x32x16_bf16 v[36:51], v[208:211], v[52:55], v[36:51]\n"
    "v_add_f32 v16, v16, v2\n"
    "v_exp_f32 v58, v16\n"
    "v_cvt_pk_bf16_f32 v56, v12, v13\n"
    "s_waitcnt lgkmcnt(0)\n"
    "v_cmp_gt_u32 vcc, %[nd], v1\n"
    "s_cbranch_vccnz LPS6_%=\n"
    "LRS6_%=:\n"
    "ds_read2st64_b32 v[84:85], v92 offset0:1 offset1:3\n"
    "ds_read2st64_b32 v[86:87], v92 offset0:5 offset1:7\n"
    "ds_read2st64_b64 v[76:79], v95 offset1:4\n"
    "ds_read2st64_b64 v[80:83], v95 offset0:8 offset1:12\n"
    "ds_read2st64_b64 v[68:71], v92 offset1:1\n"
    "ds_read2st64_b64 v[72:75], v92 offset0:2 offset1:3\n"
    "v_add_f32 v17, v17, v2\n"
    "v_exp_f32 v52, v17\n"
    "v_cvt_pk_bf16_f32 v57, v14, v15\n"
    "v_add_f32 v18, v18, v2\n"
    "v_exp_f32 v59, v18\n"
    "v_add_f32 v19, v19, v2\n"
    "v_exp_f32 v53, v19\n"
    "v_mfma_f32_32x32x16_bf16 v[4:19], v[180:183], v[60:63], 0\n"
    "buffer_load_dwordx4 v[172:175], v88, %[rws], %[pa] offen offset:3072 sc1\n"
    "v_add_f32 v20, v20, v2\n"
    "v_exp_f32 v20, v20\n"
    "v_cvt_pk_bf16_f32 v58, v58, v52\n"
    "v_add_f32 v21, v21, v2\n"
    "v_exp_f32 v21, v21\n"
    "v_cvt_pk_bf16_f32 v59, v59, v53\n"
    "v_add_f32 v22, v22, v2\n"
    "v_exp_f32 v22, v22\n"
    "v_add_f32 v23, v23, v2\n"
    "v_exp_f32 v23, v23\n"
    "v_mfma_f32_32x32x16_bf16 v[36:51], v[212:215], v[56:59], v[36:51]\n"
    "s_waitcnt lgkmcnt(5)\n"
    "v_add_f32 v84, v84, v85\n"
    "s_waitcnt lgkmcnt(4)\n"
    "v_add_f32 v86, v86, v87\n"
    "v_add_f32 v24, v24, v2\n"
    "v_exp_f32 v24, v24\n"
    "v_cvt_pk_bf16_f32 v52, v20, v21\n"
    "buffer_load_dwordx4 v[144:147], v88, %[rws], %[wo4] offen offset:2048 sc1\n"
    "v_add_f32 v25, v25, v2\n"
    "v_exp_f32 v25, v25\n"
    "v_cvt_pk_bf16_f32 v53, v22, v23\n"
    "v_add_f32 v84, v84, v86\n"
    "v_add_f32 v26, v26, v2\n"
    "v_exp_f32 v26, v26\n"
    "s_waitcnt lgkmcnt(3)\n"
    "v_add_f32 v76, v76, v78\n"
    "v_add_f32 v27, v27, v2\n"
    "v_exp_f32 v27, v27\n"
    "v_mfma_f32_32x32x8_bf16 v[4:19], v[156:157], v[64:65], v[4:19]\n"
    "s_waitcnt vmcnt(6)\n"
    "s_bitcmp1_b32 %[xo], 31\n"
    "s_cbranch_scc1 LPS5_%=\n"
    "LRS5_%=:\n"
    "v_add_f32 v77, v77, v79\n"
    "v_add_f32 v28, v28, v2\n"
    "v_exp_f32 v28, v28\n"
    "v_cvt_pk_bf16_f32 v54, v24, v25\n"
    "v_add_f32 v29, v29, v2\n"
    "v_exp_f32 v29, v29\n"
    "v_cvt_pk_bf16_f32 v55, v26, v27\n"
    "v_add_f32_dpp v84, v84, v84 quad_perm:[1,0,3,2] row_mask:0xf bank_mask:0xf\n"
    "buffer_load_dwordx4 v[164:167], v88, %[rws], %[pa] offen offset:0 sc1\n"
    "v_add_f32 v30, v30, v2\n"
    "v_exp_f32 v30, v30\n"
    "s_waitcnt lgkmcnt(2)\n"
    "v_add_f32 v80, v80, v82\n"
    "v_add_f32 v31, v31, v2\n"
    "v_exp_f32 v31, v31\n"
    "v_mfma_f32_32x32x16_bf16 v[36:51], v[216:219], v[52:55], v[36:51]\n"
    "v_rcp_f32 v84, v84\n"
    "v_add_f32 v32, v32, v2\n"
    "v_exp_f32 v58, v32\n"
    "v_cvt_pk_bf16_f32 v56, v28, v29\n"
    "v_add_f32 v33, v33, v2\n"
    "v_exp_f32 v52, v33\n"
    "v_cvt_pk_bf16_f32 v57, v30, v31\n"
    "v_add_f32 v81, v81, v83\n"
    "v_add_f32 v34, v34, v2\n"
    "v_exp_f32 v59, v34\n"
    "v_add_f32 v76, v76, v80\n"
    "v_add_f32 v35, v35, v2\n"
    "v_exp_f32 v53, v35\n"
    "v_mfma_f32_32x32x16_bf16 v[20:35], v[184:187], v[60:63], 0\n"
    "v_add_f32 v77, v77, v81\n"
    "v_add_f32 v4, v4, v2\n"
    "v_exp_f32 v4, v4\n"
    "v_cvt_pk_bf16_f32 v58, v58, v52\n"
    "buffer_load_dwordx4 v[148:151], v88, %[rws], %[wo4] offen offset:3072 sc1\n"
    "v_add_f32 v5, v5, v2\n"
    "v_exp_f32 v5, v5\n"
    "v_cvt_pk_bf16_f32 v59, v59, v53\n"
    "v_mul_f32 v76, v76, v84\n"
    "v_add_f32 v6, v6, v2\n"
    "v_exp_f32 v6, v6\n"
    "v_mul_f32 v77, v77, v84\n"
    "v_add_f32 v7, v7, v2\n"
    "v_exp_f32 v7, v7\n"
    "v_mfma_f32_32x32x16_bf16 v[36:51], v[220:223], v[56:59], v[36:51]\n"
    "v_cvt_pk_bf16_f32 v76, v76, v77\n"
    "v_add_f32 v8, v8, v2\n"
    "v_exp_f32 v8, v8\n"
    "v_cvt_pk_bf16_f32 v52, v4, v5\n"
    "ds_write_b32 v93, v76\n"
    "v_add_f32 v9, v9, v2\n"
    "v_exp_f32 v9, v9\n"
    "v_cvt_pk_bf16_f32 v53, v6, v7\n"
    "s_cmp_lg_u32 %[lw], %[cnt]\n"
    "s_cbranch_scc1 LLS2_%=\n"
    "s_waitcnt lgkmcnt(2)\n"
    "v_add_f32 v68, v68, v70\n"
    "v_add_f32 v69, v69, v71\n"
    "s_waitcnt lgkmcnt(1)\n"
    "v_add_f32 v72, v72, v74\n"
    "v_add_f32 v73, v73, v75\n"
    "v_add_f32 v68, v68, v72\n"
    "v_add_f32 v69, v69, v73\n"
    "v_and_b32 v70, 0xf0, v93\n"
    "v_lshrrev_b32 v71, 8, v88\n"
    "v_mul_f32 v68, v68, v84\n"
    "v_mul_f32 v69, v69, v84\n"
    "v_lshl_add_u32 v70, v71, 2, v70\n"
    "v_cvt_pk_bf16_f32 v68, v68, v69\n"
    "v_add_u32 v70, %[lo], v70\n"
    "ds_write_b32 v70, v68\n"
    "LLS2_%=:\n"
    "v_add_f32 v10, v10, v2\n"
    "v_exp_f32 v10, v10\n"
    "buffer_load_dwordx4 v[196:199], v88, %[rws], %[wm] offen sc1\n"
    "s_add_u32 %[wm], %[wm], 36864\n"
    "v_add_f32 v11, v11, v2\n"
    "v_exp_f32 v11, v11\n"
    "v_mfma_f32_32x32x8_bf16 v[20:35], v[158:159], v[64:65], v[20:35]\n"
    "ds_read_b128 v[64:67], v89 offset:2048\n"
    "v_add_f32 v12, v12, v2\n"
    "v_exp_f32 v12, v12\n"
    "v_cvt_pk_bf16_f32 v54, v8, v9\n"
    "v_add_f32 v13, v13, v2\n"
    "v_exp_f32 v13, v13\n"
    "v_cvt_pk_bf16_f32 v55, v10, v11\n"
    "buffer_load_dwordx4 v[152:155], v88, %[rws], %[wo8] offen offset:0 sc1\n"
    "v_add_f32 v14, v14, v2\n"
    "v_exp_f32 v14, v14\n"
    "v_add_f32 v15, v15, v2\n"
    "v_exp_f32 v15, v15\n"
    "v_mfma_f32_32x32x16_bf16 v[36:51], v[224:227], v[52:55], v[36:51]\n"
    "s_waitcnt lgkmcnt(1)\n"
    "s_mov_b64 exec, %[me]\n"
    "ds_inc_u32 v0, v0\n"
    "s_mov_b64 exec, -1\n"
    "s_add_u32 %[nd], %[nd], 1\n"
    "v_add_f32 v16, v16, v2\n"
    "v_exp_f32 v58, v16\n"
    "v_cvt_pk_bf16_f32 v56, v12, v13\n"
    "v_add_f32 v17, v17, v2\n"
    "v_exp_f32 v52, v17\n"
    "v_cvt_pk_bf16_f32 v57, v14, v15\n"
    "v_add_f32 v18, v18, v2\n"
    "v_exp_f32 v59, v18\n"
    "v_add_f32 v19, v19, v2\n"
    "v_exp_f32 v53, v19\n"
    "v_mfma_f32_32x32x16_bf16 v[4:19], v[188:191], v[60:63], 0\n"
    "ds_read_b64_tr_b16 v[76:77], v96\n"
    "ds_read_b64_tr_b16 v[78:79], v96 offset:512\n"
    "v_add_f32 v20, v20, v2\n"
    "v_exp_f32 v20, v20\n"
    "v_cvt_pk_bf16_f32 v58, v58, v52\n"
    "buffer_load_dwordx4 v[200:203], v88, %[rws], %[pb] offen offset:1024 sc1\n"
    "v_add_f32 v21, v21, v2\n"
    "v_exp_f32 v21, v21\n"
    "v_cvt_pk_bf16_f32 v59, v59, v53\n"
    "v_add_u32 v89, %[wl], v88\n"
    "v_add_f32 v22, v22, v2\n"
    "v_exp_f32 v22, v22\n"
    "v_add_f32 v23, v23, v2\n"
    "v_exp_f32 v23, v23\n"
    "v_mfma_f32_32x32x16_bf16 v[36:51], v[228:231], v[56:59], v[36:51]\n"
    "v_add_f32 v24, v24, v2\n"
    "v_exp_f32 v24, v24\n"
    "v_cvt_pk_bf16_f32 v52, v20, v21\n"
    "v_add_f32 v25, v25, v2\n"
    "v_exp_f32 v25, v25\n"
    "v_cvt_pk_bf16_f32 v53, v22, v23\n"
    "v_add_f32 v26, v26, v2\n"
    "v_exp_f32 v26, v26\n"
    "v_add_f32 v27, v27, v2\n"
    "v_exp_f32 v27, v27\n"
    "s_waitcnt lgkmcnt(3)\n"
    "v_mfma_f32_32x32x8_bf16 v[4:19], v[156:157], v[64:65], v[4:19]\n"
    "s_waitcnt lgkmcnt(0)\n"
    "buffer_store_dwordx4 v[76:79], v98, %[rws], %[pd] offen sc1\n"
    "s_add_u32 %[pd], %[pd], %[pdi]\n"
    "v_add_f32 v28, v28, v2\n"
    "v_exp_f32 v28, v28\n"
    "v_cvt_pk_bf16_f32 v54, v24, v25\n"
    "v_add_f32 v29, v29, v2\n"
    "v_exp_f32 v29, v29\n"
    "v_cvt_pk_bf16_f32 v55, v26, v27\n"
    "v_lshrrev_b32 v84, 5, v88\n"
    "v_add_f32 v30, v30, v2\n"
    "v_exp_f32 v30, v30\n"
    "v_and_or_b32 v80, v84, 24, %[m1]\n"
    "v_add_f32 v31, v31, v2\n"
    "v_exp_f32 v31, v31\n"
    "v_mfma_f32_32x32x16_bf16 v[36:51], v[232:235], v[52:55], v[36:51]\n"
    "v_add_f32 v32, v32, v2\n"
    "v_exp_f32 v58, v32\n"
    "v_cvt_pk_bf16_f32 v56, v28, v29\n"
    "buffer_load_dwordx4 v[188:191], v88, %[rws], %[wm] offen sc1\n"
    "s_movk_i32 %[wm], 1152\n"
    "s_sub_u32 %[w8b], %[w8b], 7168\n"
    "v_add_u32 v96, %[w8b], v88\n"
    "v_add_f32 v33, v33, v2\n"
    "v_exp_f32 v52, v33\n"
    "v_cvt_pk_bf16_f32 v57, v30, v31\n"
    "v_and_or_b32 v81, v84, 24, %[m2]\n"
    "v_add_f32 v34, v34, v2\n"
    "v_exp_f32 v59, v34\n"
    "v_and_b32 v84, 4, v84\n"
    "v_add_f32 v35, v35, v2\n"
    "v_exp_f32 v53, v35\n"
    "v_mfma_f32_32x32x16_bf16 v[20:35], v[160:163], v[60:63], 0\n"
    "v_add_f32 v4, v4, v2\n"
    "v_exp_f32 v4, v4\n"
    "v_cvt_pk_bf16_f32 v58, v58, v52\n"
    "ds_read_b32 v1, v0\n"
    "v_add_f32 v5, v5, v2\n"
    "v_exp_f32 v5, v5\n"
    "v_cvt_pk_bf16_f32 v59, v59, v53\n"
    "v_bfe_u32 v83, v88, 4, 3\n"
    "buffer_load_dwordx4 v[204:207], v88, %[rws], %[pb] offen offset:3072 sc1\n"
    "v_add_f32 v6, v6, v2\n"
    "v_exp_f32 v6, v6\n"
    "v_lshl_add_u32 v80, v84, 7, v80\n"
    "ds_read_b128 v[216:219], v96 offset:0\n"
    "v_add_f32 v7, v7, v2\n"
    "v_exp_f32 v7, v7\n"
    "v_mfma_f32_32x32x16_bf16 v[36:51], v[236:239], v[56:59], v[36:51]\n"
    "v_add_f32 v8, v8, v2\n"
    "v_exp_f32 v8, v8\n"
    "v_cvt_pk_bf16_f32 v52, v4, v5\n"
    "s_waitcnt lgkmcnt(1)\n"
    "v_cmp_gt_u32 vcc, %[nd], v1\n"
    "s_cbranch_vccnz LPS7_%=\n"
    "LRS7_%=:\n"
    "ds_read_b128 v[72:75], v90 offset:0\n"
    "ds_read_b128 v[68:71], v88 offset:41472\n"
    "v_add_f32 v9, v9, v2\n"
    "v_exp_f32 v9, v9\n"
    "v_cvt_pk_bf16_f32 v53, v6, v7\n"
    "v_mad_u32_u24 v82, v83, %[wm], v80\n"
    "v_add_f32 v10, v10, v2\n"
    "v_exp_f32 v10, v10\n"
    "v_mad_u32_u24 v83, v83, %[wm], v81\n"
    "ds_read_b128 v[184:187], v96 offset:1024\n"
    "v_add_f32 v11, v11, v2\n"
    "v_exp_f32 v11, v11\n"
    "v_mfma_f32_32x32x8_bf16 v[20:35], v[158:159], v[64:65], v[20:35]\n"
    "v_add_f32 v12, v12, v2\n"
    "v_exp_f32 v12, v12\n"
    "v_cvt_pk_bf16_f32 v54, v8, v9\n"
    "buffer_load_dwordx4 v[180:183], v88, %[rws], %[w8g] offen offset:0 sc1\n"
    "v_add_f32 v13, v13, v2\n"
    "v_exp_f32 v13, v13\n"
    "v_cvt_pk_bf16_f32 v55, v10, v11\n"
    "v_add_f32 v14, v14, v2\n"
    "v_exp_f32 v14, v14\n"
    "ds_read_b128 v[220:223], v96 offset:2048\n"
    "v_add_f32 v15, v15, v2\n"
    "v_exp_f32 v15, v15\n"
    "v_mfma_f32_32x32x16_bf16 v[36:51], v[240:243], v[52:55], v[36:51]\n"
    "v_add_f32 v16, v16, v2\n"
    "v_exp_f32 v58, v16\n"
    "v_cvt_pk_bf16_f32 v56, v12, v13\n"
    "buffer_load_dwordx4 v[176:179], v88, %[rws], %[pb] offen offset:2048 sc1\n"
    "v_add_f32 v17, v17, v2\n"
    "v_exp_f32 v52, v17\n"
    "v_cvt_pk_bf16_f32 v57, v14, v15\n"
    "s_waitcnt lgkmcnt(3)\n"
    "v_mfma_f32_16x16x32_bf16 v[116:119], v[136:139], v[72:75], v[116:119]\n"
    "v_add_f32 v18, v18, v2\n"
    "v_exp_f32 v59, v18\n"
    "ds_read_b128 v[224:227], v96 offset:3072\n"
    "v_add_f32 v19, v19, v2\n"
    "v_exp_f32 v53, v19\n"
    "ds_read_b128 v[72:75], v88 offset:42496\n"
    "v_add_f32 v20, v20, v2\n"
    "v_exp_f32 v20, v20\n"
    "ds_read2_b64 v[4:7], v80 offset1:32\n"
    "v_cvt_pk_bf16_f32 v58, v58, v52\n"
    "ds_read2_b64 v[16:19], v81 offset1:144\n"
    "buffer_load_dwordx4 v[208:211], v88, %[rws], %[pm] offen offset:0 sc1\n"
    "v_add_f32 v21, v21, v2\n"
    "v_exp_f32 v21, v21\n"
    "ds_read_b64 v[60:61], v82 offset:4096\n"
    "v_cvt_pk_bf16_f32 v59, v59, v53\n"
    "s_waitcnt lgkmcnt(7)\n"
    "v_mfma_f32_16x16x32_bf16 v[100:103], v[120:123], v[68:71], v[100:103]\n"
    "v_add_f32 v22, v22, v2\n"
    "v_exp_f32 v22, v22\n"
    "v_add_f32 v23, v23, v2\n"
    "v_exp_f32 v23, v23\n"
    "v_mfma_f32_32x32x16_bf16 v[36:51], v[244:247], v[56:59], v[36:51]\n"
    "v_add_f32 v24, v24, v2\n"
    "v_exp_f32 v24, v24\n"
    "ds_read_b64 v[62:63], v82 offset:4352\n"
    "v_cvt_pk_bf16_f32 v52, v20, v21\n"
    "ds_read_b64 v[66:67], v83 offset:4096\n"
    "v_add_f32 v25, v25, v2\n"
    "v_exp_f32 v25, v25\n"
    "ds_read2_b64 v[8:11], v80 offset0:144 offset1:176\n"
    "v_cvt_pk_bf16_f32 v53, v22, v23\n"
    "buffer_load_dwordx4 v[212:215], v88, %[rws], %[w8g] offen offset:1024 sc1\n"
    "v_mfma_f32_16x16x32_bf16 v[104:107], v[124:127], v[68:71], v[104:107]\n"
    "v_add_f32 v26, v26, v2\n"
    "v_exp_f32 v26, v26\n"
    "ds_read_b128 v[228:231], v96 offset:5120\n"
    "v_add_f32 v27, v27, v2\n"
    "v_exp_f32 v27, v27\n"
    "v_add_f32 v28, v28, v2\n"
    "v_exp_f32 v28, v28\n"
    "ds_read_b64 v[12:13], v80 offset:2304\n"
    "v_cvt_pk_bf16_f32 v54, v24, v25\n"
    "ds_read_b64 v[14:15], v80 offset:2560\n"
    "v_add_f32 v29, v29, v2\n"
    "v_exp_f32 v29, v29\n"
    "ds_read_b64 v[64:65], v81 offset:2304\n"
    "v_cvt_pk_bf16_f32 v55, v26, v27\n"
    "v_mfma_f32_16x16x32_bf16 v[108:111], v[128:131], v[68:71], v[108:111]\n"
    "v_add_f32 v30, v30, v2\n"
    "v_exp_f32 v30, v30\n"
    "ds_read_b128 v[232:235], v96 offset:6144\n"
    "v_add_f32 v31, v31, v2\n"
    "v_exp_f32 v31, v31\n"
    "v_mfma_f32_32x32x16_bf16 v[36:51], v[248:251], v[52:55], v[36:51]\n"
    "v_add_f32 v32, v32, v2\n"
    "v_exp_f32 v58, v32\n"
    "ds_read_b128 v[156:159], v89\n"
    "v_cvt_pk_bf16_f32 v56, v28, v29\n"
    "ds_read_b128 v[160:163], v89 offset:28672\n"
    "v_add_f32 v33, v33, v2\n"
    "v_exp_f32 v52, v33\n"
    "ds_write_b128 v94, v[116:119] offset:20480\n"
    "v_cvt_pk_bf16_f32 v57, v30, v31\n"
    "s_waitcnt vmcnt(15)\n"
    "v_mfma_f32_16x16x32_bf16 v[112:115], v[132:135], v[68:71], v[112:115]\n"
    "v_add_f32 v34, v34, v2\n"
    "v_exp_f32 v59, v34\n"
    "v_add_f32 v35, v35, v2\n"
    "v_exp_f32 v53, v35\n"
    "v_cvt_pk_bf16_f32 v58, v58, v52\n"
    "v_cvt_pk_bf16_f32 v59, v59, v53\n"
    "ds_read_b128 v[20:23], v88 offset:43520\n"
    "s_setprio 0\n"
    "v_mfma_f32_32x32x16_bf16 v[36:51], v[252:255], v[56:59], v[36:51]\n"
    "s_waitcnt vmcnt(14) lgkmcnt(15)\n"
    "v_mfma_f32_16x16x32_bf16 v[100:103], v[140:143], v[72:75], v[100:103]\n"
    "v_and_b32 v32, 0x70, v88\n"
    "v_xor_b32 v35, 8192, v94\n"
    "v_lshrrev_b32 v33, 9, v88\n"
    "s_waitcnt vmcnt(12)\n"
    "v_mfma_f32_16x16x32_bf16 v[104:107], v[144:147], v[72:75], v[104:107]\n"
    "v_mul_u32_u24 v34, 5, v32\n"
    "v_lshl_or_b32 v32, v33, 8, v32\n"
    "v_lshl_add_u32 v33, v33, 2, v34\n"
    "s_waitcnt vmcnt(10)\n"
    "v_mfma_f32_16x16x32_bf16 v[108:111], v[148:151], v[72:75], v[108:111]\n"
    "v_bfe_u32 v1, v88, 8, 1\n"
    "v_add_u32 v33, %[w64], v33\n"
    "v_lshl_or_b32 v32, v1, 3, v32\n"
    "s_waitcnt vmcnt(8)\n"
    "v_mfma_f32_16x16x32_bf16 v[112:115], v[152:155], v[72:75], v[112:115]\n"
    "v_and_b32 v1, 128, v88\n"
    "v_lshl_or_b32 v32, v1, 6, v32\n"
    "ds_write_b128 v94, v[36:39]\n"
    "ds_write_b128 v94, v[40:43] offset:1024\n"
    "ds_write_b64 v91, v[44:45]\n"
    "s_waitcnt lgkmcnt(3)\n"
    "v_mfma_f32_16x16x32_bf16 v[100:103], v[216:219], v[20:23], v[100:103]\n"
    "v_mfma_f32_16x16x32_bf16 v[104:107], v[184:187], v[20:23], v[104:107]\n"
    "v_mfma_f32_16x16x32_bf16 v[108:111], v[220:223], v[20:23], v[108:111]\n"
    "v_mfma_f32_16x16x32_bf16 v[112:115], v[224:227], v[20:23], v[112:115]\n"
    "s_mov_b64 %[me], 0x3fb8aa3b\n"
    "s_bitset0_b32 %[xo], 31\n"
    "s_cmp_eq_u32 %[lw], 0\n"
    "s_waitcnt lgkmcnt(0)\n"
    "s_barrier\n"
    "s_cbranch_scc1 LTS_%=\n"
    "ds_read2st64_b32 v[84:85], v92 offset0:1 offset1:3\n"
    "ds_read2st64_b32 v[86:87], v92 offset0:5 offset1:7\n"
    "ds_read2st64_b64 v[76:79], v95 offset1:4\n"
    "ds_read2st64_b64 v[80:83], v95 offset0:8 offset1:12\n"
    "v_lshlrev_b32 v144, 16, v8\n"
    "v_and_b32 v145, 0xffff0000, v8\n"
    "v_lshlrev_b32 v146, 16, v9\n"
    "v_and_b32 v147, 0xffff0000, v9\n"
    "v_lshlrev_b32 v148, 16, v10\n"
    "v_and_b32 v149, 0xffff0000, v10\n"
    "v_lshlrev_b32 v150, 16, v11\n"
    "v_and_b32 v151, 0xffff0000, v11\n"
    "v_lshlrev_b32 v244, 16, v18\n"
    "v_and_b32 v245, 0xffff0000, v18\n"
    "v_lshlrev_b32 v246, 16, v19\n"
    "v_and_b32 v247, 0xffff0000, v19\n"
    "v_pk_mul_f32 v[144:145], v[144:145], %[me] op_sel_hi:[1,0]\n"
    "v_pk_mul_f32 v[146:147], v[146:147], %[me] op_sel_hi:[1,0]\n"
    "v_pk_mul_f32 v[148:149], v[148:149], %[me] op_sel_hi:[1,0]\n"
    "v_pk_mul_f32 v[150:151], v[150:151], %[me] op_sel_hi:[1,0]\n"
    "s_waitcnt lgkmcnt(3)\n"
    "v_add_f32 v84, v84, v85\n"
    "s_waitcnt lgkmcnt(2)\n"
    "v_add_f32 v86, v86, v87\n"
    "v_add_f32 v84, v84, v86\n"
    "s_waitcnt lgkmcnt(1)\n"
    "v_add_f32 v76, v76, v78\n"
    "ds_read_b128 v[28:31], v35 offset:20480\n"
    "v_add_f32 v77, v77, v79\n"
    "v_add_f32_dpp v84, v84, v84 quad_perm:[1,0,3,2] row_mask:0xf bank_mask:0xf\n"
    "s_waitcnt lgkmcnt(1)\n"
    "v_add_f32 v80, v80, v82\n"
    "v_rcp_f32 v84, v84\n"
    "v_add_f32 v81, v81, v83\n"
    "v_add_f32 v76, v76, v80\n"
    "v_add_f32 v77, v77, v81\n"
    "v_mul_f32 v76, v76, v84\n"
    "v_mul_f32 v77, v77, v84\n"
    "v_cvt_pk_bf16_f32 v76, v76, v77\n"
    "ds_write_b32 v93, v76\n"
    "s_waitcnt lgkmcnt(1)\n"
    "v_pk_add_f32 v[116:117], v[116:117], v[28:29]\n"
    "v_pk_add_f32 v[118:119], v[118:119], v[30:31]\n"
    "v_pk_mul_f32 v[244:245], v[244:245], %[me] op_sel_hi:[1,0]\n"
    "v_pk_mul_f32 v[246:247], v[246:247], %[me] op_sel_hi:[1,0]\n"
    "v_lshlrev_b32 v120, 16, v4\n"
    "v_and_b32 v121, 0xffff0000, v4\n"
    "v_lshlrev_b32 v122, 16, v5\n"
    "v_and_b32 v123, 0xffff0000, v5\n"
    "v_lshlrev_b32 v124, 16, v6\n"
    "v_and_b32 v125, 0xffff0000, v6\n"
    "v_lshlrev_b32 v126, 16, v7\n"
    "v_and_b32 v127, 0xffff0000, v7\n"
    "LTS_%=j:\n"
    "v_mfma_f32_16x16x32_bf16 v[116:119], v[156:159], v[20:23], v[116:119]\n"
    "s_waitcnt lgkmcnt(0)\n"
    "s_barrier\n"
    "ds_read_b128 v[68:71], v88 offset:41472\n"
    "ds_read_b128 v[72:75], v88 offset:42496\n"
    "ds_read_b128 v[24:27], v88 offset:44544\n"
    "v_lshlrev_b32 v152, 16, v16\n"
    "v_and_b32 v153, 0xffff0000, v16\n"
    "v_lshlrev_b32 v154, 16, v17\n"
    "v_and_b32 v155, 0xffff0000, v17\n"
    "v_lshlrev_b32 v136, 16, v12\n"
    "v_and_b32 v137, 0xffff0000, v12\n"
    "v_lshlrev_b32 v138, 16, v13\n"
    "v_and_b32 v139, 0xffff0000, v13\n"
    "v_lshlrev_b32 v140, 16, v14\n"
    "v_and_b32 v141, 0xffff0000, v14\n"
    "s_waitcnt lgkmcnt(2)\n"
    "v_mfma_f32_16x16x32_bf16 v[100:103], v[164:167], v[68:71], v[100:103]\n"
    "v_lshlrev_b32 v142, 16, v15\n"
    "v_and_b32 v143, 0xffff0000, v15\n"
    "v_mfma_f32_16x16x32_bf16 v[108:111], v[192:195], v[68:71], v[108:111]\n"
    "v_lshlrev_b32 v240, 16, v64\n"
    "v_and_b32 v241, 0xffff0000, v64\n"
    "v_mfma_f32_16x16x32_bf16 v[116:119], v[196:199], v[68:71], v[116:119]\n"
    "v_lshlrev_b32 v242, 16, v65\n"
    "v_and_b32 v243, 0xffff0000, v65\n"
    "s_waitcnt vmcnt(7) lgkmcnt(1)\n"
    "v_mfma_f32_16x16x32_bf16 v[100:103], v[200:203], v[72:75], v[100:103]\n"
    "v_lshlrev_b32 v128, 16, v60\n"
    "v_and_b32 v129, 0xffff0000, v60\n"
    "s_waitcnt vmcnt(4)\n"
    "v_mfma_f32_16x16x32_bf16 v[108:111], v[204:207], v[72:75], v[108:111]\n"
    "v_lshlrev_b32 v130, 16, v61\n"
    "v_and_b32 v131, 0xffff0000, v61\n"
    "v_mfma_f32_16x16x32_bf16 v[116:119], v[188:191], v[72:75], v[116:119]\n"
    "v_lshlrev_b32 v132, 16, v62\n"
    "v_and_b32 v133, 0xffff0000, v62\n"
    "s_waitcnt lgkmcnt(0)\n"
    "v_mfma_f32_16x16x32_bf16 v[100:103], v[228:231], v[24:27], v[100:103]\n"
    "v_lshlrev_b32 v134, 16, v63\n"
    "v_and_b32 v135, 0xffff0000, v63\n"
    "s_waitcnt vmcnt(3)\n"
    "v_mfma_f32_16x16x32_bf16 v[108:111], v[180:183], v[24:27], v[108:111]\n"
    "v_lshlrev_b32 v236, 16, v66\n"
    "v_and_b32 v237, 0xffff0000, v66\n"
    "v_mfma_f32_16x16x32_bf16 v[116:119], v[160:163], v[24:27], v[116:119]\n"
    "v_lshlrev_b32 v238, 16, v67\n"
    "v_and_b32 v239, 0xffff0000, v67\n"
    "v_mfma_f32_16x16x32_bf16 v[104:107], v[168:171], v[68:71], v[104:107]\n"
    "v_mfma_f32_16x16x32_bf16 v[112:115], v[172:175], v[68:71], v[112:115]\n"
    "s_waitcnt vmcnt(2)\n"
    "v_mfma_f32_16x16x32_bf16 v[104:107], v[176:179], v[72:75], v[104:107]\n"
    "v_add_f32_dpp v36, v100, v100 row_ror:8 row_mask:0xf bank_mask:0x3\n"
    "v_add_f32_dpp v37, v101, v101 row_ror:8 row_mask:0xf bank_mask:0x3\n"
    "v_add_f32_dpp v38, v102, v102 row_ror:8 row_mask:0xf bank_mask:0x3\n"
    "s_waitcnt vmcnt(1)\n"
    "v_mfma_f32_16x16x32_bf16 v[112:115], v[208:211], v[72:75], v[112:115]\n"
    "v_add_f32_dpp v39, v103, v103 row_ror:8 row_mask:0xf bank_mask:0x3\n"
    "v_add_f32_dpp v36, v108, v108 row_ror:8 row_mask:0xf bank_mask:0xc\n"
    "v_add_f32_dpp v37, v109, v109 row_ror:8 row_mask:0xf bank_mask:0xc\n"
    "v_mfma_f32_16x16x32_bf16 v[104:107], v[232:235], v[24:27], v[104:107]\n"
    "v_add_f32_dpp v38, v110, v110 row_ror:8 row_mask:0xf bank_mask:0xc\n"
    "v_add_f32_dpp v39, v111, v111 row_ror:8 row_mask:0xf bank_mask:0xc\n"
    "v_add_f32_dpp v116, v116, v116 row_ror:8 row_mask:0xf bank_mask:0xf\n"
    "s_waitcnt vmcnt(0)\n"
    "v_mfma_f32_16x16x32_bf16 v[112:115], v[212:215], v[24:27], v[112:115]\n"
    "v_add_f32_dpp v117, v117, v117 row_ror:8 row_mask:0xf bank_mask:0xf\n"
    "v_add_f32_dpp v118, v118, v118 row_ror:8 row_mask:0xf bank_mask:0xf\n"
    "v_add_f32_dpp v119, v119, v119 row_ror:8 row_mask:0xf bank_mask:0xf\n"
    "v_pk_fma_f32 v[36:37], v[36:37], v[120:121], v[128:129]\n"
    "v_pk_fma_f32 v[38:39], v[38:39], v[122:123], v[130:131]\n"
    "v_pk_fma_f32 v[116:117], v[116:117], v[152:153], v[236:237]\n"
    "v_pk_fma_f32 v[118:119], v[118:119], v[154:155], v[238:239]\n"
    "v_pk_add_f32 v[52:53], v[36:37], v[38:39]\n"
    "v_pk_mul_f32 v[54:55], v[36:37], v[36:37]\n"
    "v_pk_fma_f32 v[54:55], v[38:39], v[38:39], v[54:55]\n"
    "s_mov_b64 exec, %[mk]\n"
    "v_pk_add_f32 v[52:53], v[52:53], v[116:117]\n"
    "v_pk_fma_f32 v[54:55], v[116:117], v[116:117], v[54:55]\n"
    "v_pk_add_f32 v[52:53], v[52:53], v[118:119]\n"
    "v_pk_fma_f32 v[54:55], v[118:119], v[118:119], v[54:55]\n"
    "s_mov_b64 exec, -1\n"
    "v_add_f32_dpp v40, v104, v104 row_ror:8 row_mask:0xf bank_mask:0x3\n"
    "v_add_f32_dpp v41, v105, v105 row_ror:8 row_mask:0xf bank_mask:0x3\n"
    "v_add_f32_dpp v42, v106, v106 row_ror:8 row_mask:0xf bank_mask:0x3\n"
    "v_add_f32_dpp v43, v107, v107 row_ror:8 row_mask:0xf bank_mask:0x3\n"
    "v_add_f32_dpp v40, v112, v112 row_ror:8 row_mask:0xf bank_mask:0xc\n"
    "v_add_f32_dpp v41, v113, v113 row_ror:8 row_mask:0xf bank_mask:0xc\n"
    "v_add_f32_dpp v42, v114, v114 row_ror:8 row_mask:0xf bank_mask:0xc\n"
    "v_add_f32_dpp v43, v115, v115 row_ror:8 row_mask:0xf bank_mask:0xc\n"
    "v_pk_fma_f32 v[40:41], v[40:41], v[124:125], v[132:133]\n"
    "v_pk_fma_f32 v[42:43], v[42:43], v[126:127], v[134:135]\n"
    "v_pk_add_f32 v[52:53], v[52:53], v[40:41]\n"
    "v_pk_fma_f32 v[54:55], v[40:41], v[40:41], v[54:55]\n"
    "v_pk_add_f32 v[52:53], v[52:53], v[42:43]\n"
    "v_pk_fma_f32 v[54:55], v[42:43], v[42:43], v[54:55]\n"
    "v_add_f32 v52, v52, v53\n"
    "v_cvt_pk_bf16_f32 v44, v36, v37\n"
    "v_add_f32 v54, v54, v55\n"
    "v_cvt_pk_bf16_f32 v45, v38, v39\n"
    "s_nop 0\n"
    "v_permlane32_swap_b32 v52, v54\n"
    "v_add_f32 v52, v52, v54\n"
    "v_mov_b32 v53, v52\n"
    "buffer_store_dwordx2 v[44:45], v32, %[rws], %[xo] offen sc1\n"
    "s_add_u32 %[t0], %[xo], 4096\n"
    "v_permlane16_swap_b32 v52, v53\n"
    "v_add_f32 v52, v52, v53\n"
    "v_cvt_pk_bf16_f32 v46, v40, v41\n"
    "v_cvt_pk_bf16_f32 v47, v42, v43\n"
    "v_add_f32_dpp v52, v52, v52 row_ror:8 row_mask:0xf bank_mask:0xf\n"
    "ds_write_b32 v33, v52 offset:36864\n"
    "buffer_store_dwordx2 v[46:47], v32, %[rws], %[t0] offen sc1\n"
    "s_mov_b64 exec, %[mk]\n"
    "v_cvt_pk_bf16_f32 v48, v116, v117\n"
    "v_cvt_pk_bf16_f32 v49, v118, v119\n"
    "buffer_store_dwordx2 v[48:49], v32, %[rws], %[x4] offen sc1\n"
    "s_mov_b64 exec, -1\n"
    "v_mov_b32 v54, 0x36a1369a\n"
    "s_sub_u32 %[xo], %[xo], 2359296\n"
    "s_sub_u32 %[t0], %[t0], 2359296\n"
    "s_sub_u32 %[x4], %[x4], 2359296\n"
    "s_waitcnt lgkmcnt(0)\n"
    "s_barrier\n"
    "ds_read_b128 v[192:195], v34 offset:36864\n"
    "ds_read_b128 v[196:199], v34 offset:36880\n"
    "ds_read_b128 v[200:203], v34 offset:36896\n"
    "ds_read_b128 v[204:207], v34 offset:36912\n"
    "s_waitcnt lgkmcnt(3)\n"
    "v_pk_add_f32 v[192:193], v[192:193], v[194:195]\n"
    "s_waitcnt lgkmcnt(2)\n"
    "v_pk_add_f32 v[196:197], v[196:197], v[198:199]\n"
    "s_waitcnt lgkmcnt(1)\n"
    "v_pk_add_f32 v[200:201], v[200:201], v[202:203]\n"
    "s_waitcnt lgkmcnt(0)\n"
    "v_pk_add_f32 v[204:205], v[204:205], v[206:207]\n"
    "v_pk_add_f32 v[192:193], v[192:193], v[196:197]\n"
    "v_pk_add_f32 v[200:201], v[200:201], v[204:205]\n"
    "v_pk_add_f32 v[192:193], v[192:193], v[200:201]\n"
    "v_mul_f32 v52, 0xbae38e39, v192\n"
    "v_mul_f32 v55, 0x3a5aa8d5, v192\n"
    "v_fmac_f32 v54, 0x3a5aa8d5, v193\n"
    "v_fmac_f32 v54, v55, v52\n"
    "v_max_f32 v54, 0x36a1369a, v54\n"
    "v_rsq_f32 v53, v54\n"
    "s_nop 0\n"
    "v_mul_f32 v52, v52, v53\n"
    "v_pk_fma_f32 v[36:37], v[36:37], v[52:53], v[52:53] op_sel:[0,1,0] op_sel_hi:[1,1,0]\n"
    "v_pk_fma_f32 v[38:39], v[38:39], v[52:53], v[52:53] op_sel:[0,1,0] op_sel_hi:[1,1,0]\n"
    "v_pk_fma_f32 v[36:37], v[36:37], v[136:137], v[144:145]\n"
    "v_pk_fma_f32 v[38:39], v[38:39], v[138:139], v[146:147]\n"
    "v_cvt_pk_bf16_f32 v50, v36, v37\n"
    "v_cvt_pk_bf16_f32 v51, v38, v39\n"
    "buffer_store_dwordx2 v[50:51], v32, %[rws], %[xo] offen sc1\n"
    "v_pk_fma_f32 v[40:41], v[40:41], v[52:53], v[52:53] op_sel:[0,1,0] op_sel_hi:[1,1,0]\n"
    "v_pk_fma_f32 v[42:43], v[42:43], v[52:53], v[52:53] op_sel:[0,1,0] op_sel_hi:[1,1,0]\n"
    "v_pk_fma_f32 v[40:41], v[40:41], v[140:141], v[148:149]\n"
    "v_pk_fma_f32 v[42:43], v[42:43], v[142:143], v[150:151]\n"
    "v_cvt_pk_bf16_f32 v44, v40, v41\n"
    "v_cvt_pk_bf16_f32 v45, v42, v43\n"
    "buffer_store_dwordx2 v[44:45], v32, %[rws], %[t0] offen sc1\n"
    "s_mov_b64 exec, %[mk]\n"
    "v_pk_fma_f32 v[116:117], v[116:117], v[52:53], v[52:53] op_sel:[0,1,0] op_sel_hi:[1,1,0]\n"
    "v_pk_fma_f32 v[118:119], v[118:119], v[52:53], v[52:53] op_sel:[0,1,0] op_sel_hi:[1,1,0]\n"
    "v_pk_fma_f32 v[116:117], v[116:117], v[240:241], v[244:245]\n"
    "v_pk_fma_f32 v[118:119], v[118:119], v[242:243], v[246:247]\n"
    "v_cvt_pk_bf16_f32 v46, v116, v117\n"
    "v_cvt_pk_bf16_f32 v47, v118, v119\n"
    "buffer_store_dwordx2 v[46:47], v32, %[rws], %[x4] offen sc1\n"
    "s_mov_b64 exec, -1\n"
        "LE%=:\n"
        : "+{v[156:159]}"(k2), "+{v[160:191]}"(kA), "+{v[192:223]}"(vA), "+{v[224:255]}"(vB), "+{v[100:115]}"(ac0), "+{v[116:119]}"(ac1),
          "+{v[88:95]}"(ad0), "+{v[96:99]}"(ad1), "+{v68}"(vo), "+{v0}"(tid), K3_STO [w8b] "+&s"(s_w8b), [w8g] "+&s"(s_w8g),
          [wo] "+&s"(s_wo), [pa] "+&s"(s_pa), [pb] "+&s"(s_pb), [pd] "+&s"(s_pd), [pm] "+&s"(s_pm),
          [wo4] "+&s"(s_wo4), [wo8] "+&s"(s_wo8), [t0] "=&s"(s_t0), [cnt] "=&s"(s_t2), [nd] "=&s"(s_t3),
          [me] "+&s"(s_me), [wm] "+&s"(s_wm), [xo] "+&s"(s_xo), [x4] "+&s"(s_x4)
        : [rws] "s"(rws), [rpt] "s"(rpt), [pi] "s"(s_pi), [pdi] "s"(s_pdi), [flag] "s"(flag), [kb] "s"(s_kb), [kb4] "s"(s_kb4),
          [vb] "s"(s_vb), [vb3] "s"(s_vb3), [vb6] "s"(s_vb6), [wl] "s"(s_wl), [m1] "s"(s_m1), [m2] "s"(s_m2), [mk] "s"(s_mk), [lw] "s"(s_lw), [lo] "s"(s_lo),
          [w64] "s"(s_w64), [tag] "s"(tag), [wf] "s"(s_wf), [mr] "s"(s_mr)
        : "v1", "v2", "v3", "v4", "v5", "v6", "v7", "v8", "v9", "v10", "v11", "v12", "v13", "v14", "v15", "v16", "v17", "v18", "v19", "v20", "v21", "v22", "v23", "v24", "v25", "v26", "v27", "v28", "v29", "v30", "v31", "v32", "v33", "v34", "v35", "v36", "v37", "v38", "v39", "v40", "v41", "v42", "v43", "v44", "v45", "v46", "v47", "v48", "v49", "v50", "v51", "v52", "v53", "v54", "v55", "v56", "v57", "v58", "v59", "v60", "v61", "v62", "v63", "v64", "v65", "v66", "v67", "v69", "v70", "v71", "v72", "v73", "v74", "v75", "v76", "v77", "v78", "v79", "v80", "v81", "v82", "v83", "v84", "v85", "v86", "v87", "v120", "v121", "v122", "v123", "v124", "v125", "v126", "v127", "v128", "v129", "v130", "v131", "v132", "v133", "v134", "v135", "v136", "v137", "v138", "v139", "v140", "v141", "v142", "v143", "v144", "v145", "v146", "v147", "v148", "v149", "v150", "v151", "v152", "v153", "v154", "v155", "vcc", "scc", "memory");
  }
  STAMP(9); STAMP_R(59);
  STAMP(13); STAMP_DUMP(2);
}

static_assert(WS_H2 == 0x55e000 && WS_X1 == 0x79e000 && WS_W1T == 0x9de000 && WS_WDT == 0x13fe000 && WS_ACT == 0x190e000 && WS_PART == 0x2b0e000 &&
              WS_FLAG == 0x10000 && WS_MG == 0x2400, "offsets are part of the code below");
__global__ __launch_bounds__(256, 1) __attribute__((aligned(4096))) void k5_ffn(char* __restrict__ ws, u16* __restrict__ out) {
  unsigned tid = threadIdx.x, bx = blockIdx.x, stv;
  auto ka = __builtin_amdgcn_kernarg_segment_ptr();
  STAMP(0); STAMP_R(60);
  asm volatile(
    "s_load_dwordx4 s[8:11], s[0:1], 0x0\n"
    "LPK0:\n"
    "s_cbranch_execz LPK4\n"
    "s_lshl_b32 s0, s2, 9\n"
    "v_lshlrev_b32_e32 v1, 4, v0\n"
    "s_and_b32 s0, s0, 0xf000\n"
    "v_readfirstlane_b32 s4, v0\n"
    "v_and_b32_e32 v0, 0x3f0, v1\n"
    "v_or_b32_e32 v1, s0, v1\n"
    "s_getpc_b64 s[0:1]\n"
    "LK5B:\n"
    "v_min_u32 v2, LCODE_END-LK5B-16, v1\n"
    "global_load_dwordx4 v[44:47], v2, s[0:1]\n"
    "s_lshl_b32 s0, s2, 2\n"
    "s_and_b32 s0, s0, 4\n"
    "s_lshr_b32 s1, s2, 6\n"
    "s_lshr_b32 s19, s4, 6\n"
    "s_add_i32 s0, s0, s1\n"
    "s_lshl_b32 s0, s0, 4\n"
    "s_lshl_b32 s1, s19, 2\n"
    "s_add_i32 s0, s1, s0\n"
    "s_mul_i32 s6, s0, 0x4800\n"
    "s_add_i32 s0, s6, 0x55e000\n"
    "s_add_i32 s1, s6, 0x562800\n"
    "s_add_i32 s3, s6, 0x567000\n"
    "s_add_i32 s16, s6, 0x56b800\n"
    "s_bfe_u32 s6, s2, 0x20001\n"
    "s_lshr_b32 s7, s2, 2\n"
    "s_mulk_i32 s6, 0x48\n"
    "s_lshr_b32 s4, s4, 3\n"
    "s_and_b32 s7, s7, 14\n"
    "s_lshr_b32 s5, s2, 3\n"
    "s_and_b32 s4, s4, 0xffff0\n"
    "s_add_i32 s6, s7, s6\n"
    "s_add_i32 s6, s6, s4\n"
    "s_xor_b32 s4, s19, s5\n"
    "s_and_b32 s4, s4, 1\n"
    "s_or_b32 s4, s6, s4\n"
    "s_mul_i32 s4, s4, 0x9000\n"
    "s_mul_i32 s17, s19, 0x9000\n"
    "s_add_i32 s18, s4, 0x9de000\n"
    "s_mov_b32 s7, 0x20000\n"
    "s_brev_b32 s6, 8\n"
    "s_waitcnt lgkmcnt(0)\n"
    "s_and_b32 s5, s9, 0xffff\n"
    "s_mov_b32 s4, s8\n"
    "s_add_u32 m0, s17, 0\n"
    "s_add_u32 s56, s18, 0\n"
    "buffer_load_dwordx4 v0, s[4:7], s56 offen offset:0 lds\n"
    "buffer_load_dwordx4 v0, s[4:7], s56 offen offset:1024 lds\n"
    "buffer_load_dwordx4 a[0:3], v0, s[4:7], s0 offen offset:0\n"
    "buffer_load_dwordx4 a[4:7], v0, s[4:7], s1 offen offset:0\n"
    "buffer_load_dwordx4 a[8:11], v0, s[4:7], s3 offen offset:0\n"
    "buffer_load_dwordx4 a[12:15], v0, s[4:7], s16 offen offset:0\n"
    "v_lshrrev_b32 v2, 8, v0\n"
    "v_and_b32 v1, 0xf0, v0\n"
    "v_lshl_or_b32 v1, v2, 9, v1\n"
    "v_add_u32 v2, 0xc000, v1\n"
    "v_add_u32 v3, 0x18000, v1\n"
    "buffer_load_dwordx4 v0, s[4:7], s56 offen offset:2048 lds\n"
    "buffer_load_dwordx4 v0, s[4:7], s56 offen offset:3072 lds\n"
    "buffer_load_dwordx4 a[16:19], v0, s[4:7], s0 offen offset:1024\n"
    "buffer_load_dwordx4 a[20:23], v0, s[4:7], s1 offen offset:1024\n"
    "buffer_load_dwordx4 a[24:27], v0, s[4:7], s3 offen offset:1024\n"
    "buffer_load_dwordx4 a[28:31], v0, s[4:7], s16 offen offset:1024\n"
    "s_add_u32 m0, s17, 4096\n"
    "s_add_u32 s56, s18, 4096\n"
    "buffer_load_dwordx4 v0, s[4:7], s56 offen offset:0 lds\n"
    "buffer_load_dwordx4 v0, s[4:7], s56 offen offset:1024 lds\n"
    "s_waitcnt vmcnt(12)\n"
    "s_barrier\n"
    "ds_read_b128 v[16:19], v1 offset:0\n"
    "ds_read_b128 v[20:23], v1 offset:256\n"
    "ds_read_b128 v[24:27], v1 offset:36864\n"
    "ds_read_b128 v[28:31], v1 offset:37120\n"
    "ds_read_b128 v[32:35], v2 offset:24576\n"
    "s_waitcnt vmcnt(11) lgkmcnt(3)\n"
    "v_mfma_f32_16x16x32_bf16 v[48:51], v[16:19], a[0:3], 0\n"
    "ds_read_b128 v[36:39], v2 offset:24832\n"
    "s_and_b32 s9, s2, 1\n"
    "s_waitcnt vmcnt(10)\n"
    "v_mfma_f32_16x16x32_bf16 v[56:59], v[16:19], a[4:7], 0\n"
    "buffer_load_dwordx4 a[32:35], v0, s[4:7], s0 offen offset:2048\n"
    "s_lshl_b32 s12, s9, 2\n"
    "s_waitcnt vmcnt(10)\n"
    "v_mfma_f32_16x16x32_bf16 v[64:67], v[16:19], a[8:11], 0\n"
    "s_lshr_b32 s13, s2, 6\n"
    "s_add_i32 s12, s12, s13\n"
    "s_waitcnt vmcnt(9)\n"
    "v_mfma_f32_16x16x32_bf16 v[72:75], v[16:19], a[12:15], 0\n"
    "s_lshl_b32 s13, s12, 4\n"
    "s_lshl_b32 s14, s19, 2\n"
    "v_mfma_f32_16x16x32_bf16 v[52:55], v[20:23], a[0:3], 0\n"
    "ds_read_b128 v[40:43], v3 offset:12288\n"
    "v_lshrrev_b32_e32 v13, 9, v0\n"
    "v_mfma_f32_16x16x32_bf16 v[60:63], v[20:23], a[4:7], 0\n"
    "buffer_load_dwordx4 a[36:39], v0, s[4:7], s1 offen offset:2048\n"
    "s_bfe_u32 s8, s2, 0x20001\n"
    "v_mfma_f32_16x16x32_bf16 v[68:71], v[20:23], a[8:11], 0\n"
    "s_add_i32 s13, s13, s14\n"
    "v_mul_u32_u24_e32 v6, 0x24000, v13\n"
    "v_mfma_f32_16x16x32_bf16 v[76:79], v[20:23], a[12:15], 0\n"
    "s_movk_i32 s14, 0x1f0\n"
    "s_waitcnt lgkmcnt(3)\n"
    "v_mfma_f32_16x16x32_bf16 v[80:83], v[24:27], a[0:3], 0\n"
    "ds_read_b128 v[44:47], v3 offset:12544\n"
    "s_bfe_u32 s20, s2, 0x30003\n"
    "v_mfma_f32_16x16x32_bf16 v[88:91], v[24:27], a[4:7], 0\n"
    "buffer_load_dwordx4 a[40:43], v0, s[4:7], s3 offen offset:2048\n"
    "v_and_or_b32 v6, v0, s14, v6\n"
    "v_mfma_f32_16x16x32_bf16 v[96:99], v[24:27], a[8:11], 0\n"
    "s_mul_i32 s14, s8, 0x9000\n"
    "s_mul_i32 s22, s8, 0x288000\n"
    "v_mfma_f32_16x16x32_bf16 v[104:107], v[24:27], a[12:15], 0\n"
    "s_mul_i32 s23, s19, 0x2400\n"
    "s_bfe_u32 s24, s2, 0x10003\n"
    "v_mfma_f32_16x16x32_bf16 v[84:87], v[28:31], a[0:3], 0\n"
    "s_waitcnt vmcnt(9)\n"
    "s_barrier\n"
    "ds_read_b128 v[16:19], v1 offset:2048\n"
    "v_mfma_f32_16x16x32_bf16 v[92:95], v[28:31], a[4:7], 0\n"
    "buffer_load_dwordx4 a[44:47], v0, s[4:7], s16 offen offset:2048\n"
    "s_mul_i32 s13, s13, 0x24000\n"
    "v_mfma_f32_16x16x32_bf16 v[100:103], v[28:31], a[8:11], 0\n"
    "s_add_i32 s15, s14, 0x190e000\n"
    "s_add_i32 s22, s22, s23\n"
    "v_mfma_f32_16x16x32_bf16 v[108:111], v[28:31], a[12:15], 0\n"
    "s_lshl_b32 s23, s20, 10\n"
    "s_waitcnt lgkmcnt(3)\n"
    "v_mfma_f32_16x16x32_bf16 v[112:115], v[32:35], a[0:3], 0\n"
    "ds_read_b128 v[20:23], v1 offset:2304\n"
    "s_lshl_b32 s25, s24, 9\n"
    "v_mfma_f32_16x16x32_bf16 v[120:123], v[32:35], a[4:7], 0\n"
    "buffer_load_dwordx4 v0, s[4:7], s56 offen offset:2048 lds\n"
    "s_add_i32 s13, s15, s13\n"
    "v_mfma_f32_16x16x32_bf16 v[128:131], v[32:35], a[8:11], 0\n"
    "s_lshl_b32 s21, s20, 9\n"
    "s_or_b32 s23, s23, s25\n"
    "v_mfma_f32_16x16x32_bf16 v[136:139], v[32:35], a[12:15], 0\n"
    "s_or_b32 s21, s13, s21\n"
    "s_add_i32 s23, s23, s13\n"
    "v_mfma_f32_16x16x32_bf16 v[116:119], v[36:39], a[0:3], 0\n"
    "ds_read_b128 v[24:27], v1 offset:38912\n"
    "s_lshl_b32 s9, s9, 3\n"
    "v_mfma_f32_16x16x32_bf16 v[124:127], v[36:39], a[4:7], 0\n"
    "buffer_load_dwordx4 v0, s[4:7], s56 offen offset:3072 lds\n"
    "s_lshr_b32 s13, s2, 5\n"
    "v_mfma_f32_16x16x32_bf16 v[132:135], v[36:39], a[8:11], 0\n"
    "s_add_i32 s9, s9, s13\n"
    "s_lshl_b32 s26, s9, 2\n"
    "v_mfma_f32_16x16x32_bf16 v[140:143], v[36:39], a[12:15], 0\n"
    "s_lshl_b32 s9, s9, 3\n"
    "s_waitcnt lgkmcnt(3)\n"
    "v_mfma_f32_16x16x32_bf16 v[144:147], v[40:43], a[0:3], 0\n"
    "ds_read_b128 v[28:31], v1 offset:39168\n"
    "s_lshl_b32 s27, s19, 1\n"
    "v_mfma_f32_16x16x32_bf16 v[152:155], v[40:43], a[4:7], 0\n"
    "s_add_u32 m0, s17, 8192\n"
    "s_add_u32 s56, s18, 8192\n"
    "buffer_load_dwordx4 v0, s[4:7], s56 offen offset:0 lds\n"
    "v_mfma_f32_16x16x32_bf16 v[160:163], v[40:43], a[8:11], 0\n"
    "s_add_i32 s9, s9, s27\n"
    "s_bfe_u32 s13, s2, 0x20003\n"
    "v_mfma_f32_16x16x32_bf16 v[168:171], v[40:43], a[12:15], 0\n"
    "s_mul_i32 s28, s9, 0x24000\n"
    "v_bfe_u32 v9, v0, 4, 4\n"
    "v_mfma_f32_16x16x32_bf16 v[148:151], v[44:47], a[0:3], 0\n"
    "ds_read_b128 v[32:35], v2 offset:26624\n"
    "s_or_b32 s26, s26, s13\n"
    "v_mfma_f32_16x16x32_bf16 v[156:159], v[44:47], a[4:7], 0\n"
    "buffer_load_dwordx4 v0, s[4:7], s56 offen offset:1024 lds\n"
    "s_add_i32 s28, s15, s28\n"
    "v_mfma_f32_16x16x32_bf16 v[164:167], v[44:47], a[8:11], 0\n"
    "s_mul_i32 s15, s13, 0x144000\n"
    "v_lshlrev_b32_e32 v10, 4, v9\n"
    "v_mfma_f32_16x16x32_bf16 v[172:175], v[44:47], a[12:15], 0\n"
    "v_lshrrev_b32_e32 v11, 8, v0\n"
    "s_waitcnt vmcnt(13) lgkmcnt(3)\n"
    "v_mfma_f32_16x16x32_bf16 v[48:51], v[16:19], a[16:19], v[48:51]\n"
    "ds_read_b128 v[36:39], v2 offset:26880\n"
    "v_bfe_u32 v12, v0, 8, 1\n"
    "s_waitcnt vmcnt(12)\n"
    "v_mfma_f32_16x16x32_bf16 v[56:59], v[16:19], a[20:23], v[56:59]\n"
    "buffer_load_dwordx4 a[48:51], v0, s[4:7], s0 offen offset:3072\n"
    "s_add_i32 s30, s15, s14\n"
    "s_waitcnt vmcnt(12)\n"
    "v_mfma_f32_16x16x32_bf16 v[64:67], v[16:19], a[24:27], v[64:67]\n"
    "s_lshl_b32 s14, s26, 17\n"
    "s_lshl_b32 s15, s19, 11\n"
    "s_waitcnt vmcnt(11)\n"
    "v_mfma_f32_16x16x32_bf16 v[72:75], v[16:19], a[28:31], v[72:75]\n"
    "v_lshlrev_b32_e32 v14, 8, v13\n"
    "v_lshlrev_b32_e32 v219, 3, v12\n"
    "v_mfma_f32_16x16x32_bf16 v[52:55], v[20:23], a[16:19], v[52:55]\n"
    "ds_read_b128 v[40:43], v3 offset:14336\n"
    "s_movk_i32 s27, 0x480\n"
    "v_mfma_f32_16x16x32_bf16 v[60:63], v[20:23], a[20:23], v[60:63]\n"
    "buffer_load_dwordx4 a[52:55], v0, s[4:7], s1 offen offset:3072\n"
    "v_lshlrev_b32_e32 v212, 4, v11\n"
    "v_mfma_f32_16x16x32_bf16 v[68:71], v[20:23], a[24:27], v[68:71]\n"
    "v_lshl_add_u32 v214, v11, 2, v10\n"
    "v_mul_u32_u24_e32 v11, 0x4800, v13\n"
    "v_mfma_f32_16x16x32_bf16 v[76:79], v[20:23], a[28:31], v[76:79]\n"
    "s_add_i32 s14, s15, s14\n"
    "s_waitcnt lgkmcnt(3)\n"
    "v_mfma_f32_16x16x32_bf16 v[80:83], v[24:27], a[16:19], v[80:83]\n"
    "ds_read_b128 v[44:47], v3 offset:14592\n"
    "v_or3_b32 v210, v10, v14, v219\n"
    "v_mfma_f32_16x16x32_bf16 v[88:91], v[24:27], a[20:23], v[88:91]\n"
    "buffer_load_dwordx4 a[56:59], v0, s[4:7], s3 offen offset:3072\n"
    "v_mul_u32_u24_e32 v14, 0x480, v9\n"
    "v_mfma_f32_16x16x32_bf16 v[96:99], v[24:27], a[24:27], v[96:99]\n"
    "v_mad_u32_u24 v9, v9, s27, v11\n"
    "s_lshl_b32 s27, s19, 10\n"
    "v_mfma_f32_16x16x32_bf16 v[104:107], v[24:27], a[28:31], v[104:107]\n"
    "s_add_i32 s33, s14, 0x2b0e000\n"
    "s_lshl_b32 s14, s26, 14\n"
    "v_mfma_f32_16x16x32_bf16 v[84:87], v[28:31], a[16:19], v[84:87]\n"
    "s_waitcnt vmcnt(11)\n"
    "s_barrier\n"
    "ds_read_b128 v[16:19], v1 offset:4096\n"
    "v_mfma_f32_16x16x32_bf16 v[92:95], v[28:31], a[20:23], v[92:95]\n"
    "buffer_load_dwordx4 a[60:63], v0, s[4:7], s16 offen offset:3072\n"
    "s_add_i32 s14, s27, s14\n"
    "v_mfma_f32_16x16x32_bf16 v[100:103], v[28:31], a[24:27], v[100:103]\n"
    "v_add_u32_e32 v222, s27, v0\n"
    "s_add_i32 s27, s14, 0x330e000\n"
    "v_mfma_f32_16x16x32_bf16 v[108:111], v[28:31], a[28:31], v[108:111]\n"
    "s_mulk_i32 s9, 0x4800\n"
    "s_waitcnt lgkmcnt(3)\n"
    "v_mfma_f32_16x16x32_bf16 v[112:115], v[32:35], a[16:19], v[112:115]\n"
    "ds_read_b128 v[20:23], v1 offset:4352\n"
    "s_mul_i32 s14, s13, 0x120\n"
    "v_mfma_f32_16x16x32_bf16 v[120:123], v[32:35], a[20:23], v[120:123]\n"
    "buffer_load_dwordx4 v0, s[4:7], s56 offen offset:2048 lds\n"
    "s_or_b32 s14, s9, s14\n"
    "v_mfma_f32_16x16x32_bf16 v[128:131], v[32:35], a[24:27], v[128:131]\n"
    "s_lshl_b32 s15, s8, 6\n"
    "s_lshl_b32 s12, s12, 10\n"
    "v_mfma_f32_16x16x32_bf16 v[136:139], v[32:35], a[28:31], v[136:139]\n"
    "s_lshl_b32 s39, s8, 8\n"
    "s_add_i32 s34, s14, s15\n"
    "v_mfma_f32_16x16x32_bf16 v[116:119], v[36:39], a[16:19], v[116:119]\n"
    "ds_read_b128 v[24:27], v1 offset:40960\n"
    "s_lshl_b32 s15, s8, 3\n"
    "v_mfma_f32_16x16x32_bf16 v[124:127], v[36:39], a[20:23], v[124:127]\n"
    "buffer_load_dwordx4 v0, s[4:7], s56 offen offset:3072 lds\n"
    "s_or_b32 s12, s12, s39\n"
    "v_mfma_f32_16x16x32_bf16 v[132:135], v[36:39], a[24:27], v[132:135]\n"
    "s_or_b32 s14, s14, s15\n"
    "s_add_i32 s40, s12, 0x12000\n"
    "v_mfma_f32_16x16x32_bf16 v[140:143], v[36:39], a[28:31], v[140:143]\n"
    "s_mov_b32 s12, s10\n"
    "s_waitcnt lgkmcnt(3)\n"
    "v_mfma_f32_16x16x32_bf16 v[144:147], v[40:43], a[16:19], v[144:147]\n"
    "ds_read_b128 v[28:31], v1 offset:41216\n"
    "s_mul_i32 s10, s19, 0xc00\n"
    "v_mfma_f32_16x16x32_bf16 v[152:155], v[40:43], a[20:23], v[152:155]\n"
    "s_add_u32 s0, s0, 4096\n"
    "buffer_load_dwordx4 a[64:67], v0, s[4:7], s0 offen offset:0\n"
    "v_mfma_f32_16x16x32_bf16 v[160:163], v[40:43], a[24:27], v[160:163]\n"
    "s_lshl_b32 s46, s8, 1\n"
    "s_mul_i32 s47, s8, 0x48000\n"
    "v_mfma_f32_16x16x32_bf16 v[168:171], v[40:43], a[28:31], v[168:171]\n"
    "s_add_i32 s35, s14, 0x100\n"
    "s_mul_i32 s14, s13, 0x1200\n"
    "v_mfma_f32_16x16x32_bf16 v[148:151], v[44:47], a[16:19], v[148:151]\n"
    "ds_read_b128 v[32:35], v2 offset:28672\n"
    "s_mul_i32 s36, s13, 0x240\n"
    "v_mfma_f32_16x16x32_bf16 v[156:159], v[44:47], a[20:23], v[156:159]\n"
    "s_add_u32 s1, s1, 4096\n"
    "buffer_load_dwordx4 a[68:71], v0, s[4:7], s1 offen offset:0\n"
    "v_mfma_f32_16x16x32_bf16 v[164:167], v[44:47], a[24:27], v[164:167]\n"
    "s_and_b32 s13, s11, 0xffff\n"
    "s_add_i32 s11, s10, 0x24000\n"
    "v_mfma_f32_16x16x32_bf16 v[172:175], v[44:47], a[28:31], v[172:175]\n"
    "s_add_i32 s10, s47, s10\n"
    "s_waitcnt vmcnt(12) lgkmcnt(3)\n"
    "v_mfma_f32_16x16x32_bf16 v[48:51], v[16:19], a[32:35], v[48:51]\n"
    "ds_read_b128 v[36:39], v2 offset:28928\n"
    "s_xor_b32 s47, s46, 4\n"
    "v_mfma_f32_16x16x32_bf16 v[56:59], v[16:19], a[36:39], v[56:59]\n"
    "s_add_u32 s3, s3, 4096\n"
    "buffer_load_dwordx4 a[72:75], v0, s[4:7], s3 offen offset:0\n"
    "v_mfma_f32_16x16x32_bf16 v[64:67], v[16:19], a[40:43], v[64:67]\n"
    "s_xor_b32 s48, s46, 5\n"
    "s_xor_b32 s50, s46, 2\n"
    "v_mfma_f32_16x16x32_bf16 v[72:75], v[16:19], a[44:47], v[72:75]\n"
    "s_xor_b32 s51, s46, 3\n"
    "s_xor_b32 s52, s46, 6\n"
    "v_mfma_f32_16x16x32_bf16 v[52:55], v[20:23], a[32:35], v[52:55]\n"
    "ds_read_b128 v[40:43], v3 offset:16384\n"
    "s_xor_b32 s46, s46, 7\n"
    "v_mfma_f32_16x16x32_bf16 v[60:63], v[20:23], a[36:39], v[60:63]\n"
    "s_add_u32 s16, s16, 4096\n"
    "buffer_load_dwordx4 a[76:79], v0, s[4:7], s16 offen offset:0\n"
    "v_mfma_f32_16x16x32_bf16 v[68:71], v[20:23], a[40:43], v[68:71]\n"
    "s_add_i32 s31, s30, 0x13fe000\n"
    "s_lshl_b32 s19, s20, 1\n"
    "v_mfma_f32_16x16x32_bf16 v[76:79], v[20:23], a[44:47], v[76:79]\n"
    "s_mul_i32 s47, s47, 0x24000\n"
    "s_waitcnt lgkmcnt(3)\n"
    "v_mfma_f32_16x16x32_bf16 v[80:83], v[24:27], a[32:35], v[80:83]\n"
    "ds_read_b128 v[44:47], v3 offset:16640\n"
    "s_mul_i32 s50, s50, 0x24000\n"
    "v_mfma_f32_16x16x32_bf16 v[88:91], v[24:27], a[36:39], v[88:91]\n"
    "s_add_u32 m0, s17, 12288\n"
    "s_add_u32 s56, s18, 12288\n"
    "buffer_load_dwordx4 v0, s[4:7], s56 offen offset:0 lds\n"
    "v_mfma_f32_16x16x32_bf16 v[96:99], v[24:27], a[40:43], v[96:99]\n"
    "s_mul_i32 s51, s51, 0x24000\n"
    "s_mul_i32 s46, s46, 0x24000\n"
    "v_mfma_f32_16x16x32_bf16 v[104:107], v[24:27], a[44:47], v[104:107]\n"
    "s_or_b32 s19, s19, s24\n"
    "s_add_i32 s10, s10, s31\n"
    "v_mfma_f32_16x16x32_bf16 v[84:87], v[28:31], a[32:35], v[84:87]\n"
    "s_waitcnt vmcnt(13)\n"
    "s_barrier\n"
    "ds_read_b128 v[16:19], v1 offset:6144\n"
    "v_mfma_f32_16x16x32_bf16 v[92:95], v[28:31], a[36:39], v[92:95]\n"
    "buffer_load_dwordx4 v0, s[4:7], s56 offen offset:1024 lds\n"
    "s_add_i32 s47, s31, s47\n"
    "v_mfma_f32_16x16x32_bf16 v[100:103], v[28:31], a[40:43], v[100:103]\n"
    "s_mul_i32 s48, s48, 0x24000\n"
    "s_add_i32 s49, s30, 0x13fdc00\n"
    "v_mfma_f32_16x16x32_bf16 v[108:111], v[28:31], a[44:47], v[108:111]\n"
    "s_add_i32 s50, s50, s31\n"
    "s_waitcnt lgkmcnt(3)\n"
    "v_mfma_f32_16x16x32_bf16 v[112:115], v[32:35], a[32:35], v[112:115]\n"
    "ds_read_b128 v[20:23], v1 offset:6400\n"
    "s_add_i32 s51, s31, s51\n"
    "v_mfma_f32_16x16x32_bf16 v[120:123], v[32:35], a[36:39], v[120:123]\n"
    "buffer_load_dwordx4 a[80:83], v0, s[4:7], s0 offen offset:1024\n"
    "s_mul_i32 s52, s52, 0x24000\n"
    "v_mfma_f32_16x16x32_bf16 v[128:131], v[32:35], a[40:43], v[128:131]\n"
    "s_add_i32 s31, s46, s31\n"
    "s_xor_b32 s46, s8, 1\n"
    "v_mfma_f32_16x16x32_bf16 v[136:139], v[32:35], a[44:47], v[136:139]\n"
    "s_xor_b32 s54, s8, 2\n"
    "s_mul_i32 s24, s19, 0x9000\n"
    "v_mfma_f32_16x16x32_bf16 v[116:119], v[36:39], a[32:35], v[116:119]\n"
    "ds_read_b128 v[24:27], v1 offset:43008\n"
    "s_xor_b32 s19, s19, 1\n"
    "v_mfma_f32_16x16x32_bf16 v[124:127], v[36:39], a[36:39], v[124:127]\n"
    "buffer_load_dwordx4 a[84:87], v0, s[4:7], s1 offen offset:1024\n"
    "s_add_i32 s48, s49, s48\n"
    "v_mfma_f32_16x16x32_bf16 v[132:135], v[36:39], a[40:43], v[132:135]\n"
    "s_add_i32 s49, s49, s52\n"
    "s_lshl_b32 s52, s46, 15\n"
    "v_mfma_f32_16x16x32_bf16 v[140:143], v[36:39], a[44:47], v[140:143]\n"
    "s_lshl_b32 s53, s8, 13\n"
    "s_waitcnt lgkmcnt(3)\n"
    "v_mfma_f32_16x16x32_bf16 v[144:147], v[40:43], a[32:35], v[144:147]\n"
    "ds_read_b128 v[28:31], v1 offset:43264\n"
    "s_lshl_b32 s55, s54, 15\n"
    "v_mfma_f32_16x16x32_bf16 v[152:155], v[40:43], a[36:39], v[152:155]\n"
    "buffer_load_dwordx4 a[88:91], v0, s[4:7], s3 offen offset:1024\n"
    "s_add_i32 s9, s14, s9\n"
    "v_mfma_f32_16x16x32_bf16 v[160:163], v[40:43], a[40:43], v[160:163]\n"
    "s_lshl_b32 s37, s8, 7\n"
    "s_lshl_b32 s26, s26, 7\n"
    "v_mfma_f32_16x16x32_bf16 v[168:171], v[40:43], a[44:47], v[168:171]\n"
    "s_add_i32 s22, s22, 0x9de000\n"
    "s_mul_i32 s19, s19, 0x9000\n"
    "v_mfma_f32_16x16x32_bf16 v[148:151], v[44:47], a[32:35], v[148:151]\n"
    "ds_read_b128 v[32:35], v2 offset:30720\n"
    "s_mul_i32 s42, s20, 0x9000\n"
    "v_mfma_f32_16x16x32_bf16 v[156:159], v[44:47], a[36:39], v[156:159]\n"
    "buffer_load_dwordx4 a[92:95], v0, s[4:7], s16 offen offset:1024\n"
    "s_xor_b32 s44, s8, 3\n"
    "v_mfma_f32_16x16x32_bf16 v[164:167], v[44:47], a[40:43], v[164:167]\n"
    "s_or_b32 s52, s52, s53\n"
    "s_or_b32 s53, s55, s53\n"
    "v_mfma_f32_16x16x32_bf16 v[172:175], v[44:47], a[44:47], v[172:175]\n"
    "s_lshl_b32 s55, s8, 12\n"
    "s_waitcnt vmcnt(12) lgkmcnt(3)\n"
    "v_mfma_f32_16x16x32_bf16 v[48:51], v[16:19], a[48:51], v[48:51]\n"
    "ds_read_b128 v[36:39], v2 offset:30976\n"
    "v_lshrrev_b32_e32 v8, 4, v0\n"
    "v_mfma_f32_16x16x32_bf16 v[56:59], v[16:19], a[52:55], v[56:59]\n"
    "s_add_i32 s9, s9, 0x79e000\n"
    "s_add_i32 s38, s36, s37\n"
    "v_mfma_f32_16x16x32_bf16 v[64:67], v[16:19], a[56:59], v[64:67]\n"
    "buffer_load_dwordx4 v0, s[4:7], s56 offen offset:2048 lds\n"
    "s_add_i32 s41, s26, 0x10000\n"
    "v_mfma_f32_16x16x32_bf16 v[72:75], v[16:19], a[60:63], v[72:75]\n"
    "s_add_i32 s24, s22, s24\n"
    "s_add_i32 s19, s22, s19\n"
    "v_mfma_f32_16x16x32_bf16 v[52:55], v[20:23], a[48:51], v[52:55]\n"
    "ds_read_b128 v[40:43], v3 offset:18432\n"
    "s_add_i32 s22, s22, s42\n"
    "v_mfma_f32_16x16x32_bf16 v[60:63], v[20:23], a[52:55], v[60:63]\n"
    "s_lshl_b32 s42, s8, 2\n"
    "s_lshl_b32 s45, s44, 2\n"
    "v_mfma_f32_16x16x32_bf16 v[68:71], v[20:23], a[56:59], v[68:71]\n"
    "s_add_i32 s55, s27, s55\n"
    "s_or_b32 s27, s27, s39\n"
    "v_mfma_f32_16x16x32_bf16 v[76:79], v[20:23], a[60:63], v[76:79]\n"
    "buffer_load_dwordx4 v0, s[4:7], s56 offen offset:3072 lds\n"
    "s_waitcnt lgkmcnt(3)\n"
    "v_mfma_f32_16x16x32_bf16 v[80:83], v[24:27], a[48:51], v[80:83]\n"
    "ds_read_b128 v[44:47], v3 offset:18688\n"
    "s_lshl_b32 s39, s8, 10\n"
    "v_mfma_f32_16x16x32_bf16 v[88:91], v[24:27], a[52:55], v[88:91]\n"
    "s_and_b32 s37, s37, 0x100\n"
    "s_lshl_b32 s2, s2, 2\n"
    "v_mfma_f32_16x16x32_bf16 v[96:99], v[24:27], a[56:59], v[96:99]\n"
    "v_lshlrev_b32_e32 v4, 9, v12\n"
    "v_mul_u32_u24_e32 v5, 0x7000, v13\n"
    "v_mfma_f32_16x16x32_bf16 v[104:107], v[24:27], a[60:63], v[104:107]\n"
    "v_lshlrev_b32_e32 v15, 5, v12\n"
    "v_lshlrev_b32_e32 v176, 4, v13\n"
    "v_mfma_f32_16x16x32_bf16 v[84:87], v[28:31], a[48:51], v[84:87]\n"
    "s_barrier\n"
    "ds_read_b128 v[16:19], v1 offset:8192\n"
    "v_mfma_f32_16x16x32_bf16 v[92:95], v[28:31], a[52:55], v[92:95]\n"
    "buffer_load_dwordx4 a[96:99], v0, s[4:7], s0 offen offset:2048\n"
    "v_lshlrev_b32_e32 v8, 2, v8\n"
    "v_mfma_f32_16x16x32_bf16 v[100:103], v[28:31], a[56:59], v[100:103]\n"
    "v_lshlrev_b32_e32 v12, 2, v12\n"
    "s_or_b32 s43, s41, s42\n"
    "v_mfma_f32_16x16x32_bf16 v[108:111], v[28:31], a[60:63], v[108:111]\n"
    "s_or_b32 s26, s26, s45\n"
    "s_waitcnt lgkmcnt(3)\n"
    "v_mfma_f32_16x16x32_bf16 v[112:115], v[32:35], a[48:51], v[112:115]\n"
    "ds_read_b128 v[20:23], v1 offset:8448\n"
    "s_or_b32 s45, s45, s8\n"
    "v_mfma_f32_16x16x32_bf16 v[120:123], v[32:35], a[52:55], v[120:123]\n"
    "buffer_load_dwordx4 a[100:103], v0, s[4:7], s1 offen offset:2048\n"
    "s_or_b32 s46, s42, s46\n"
    "v_mfma_f32_16x16x32_bf16 v[128:131], v[32:35], a[56:59], v[128:131]\n"
    "s_or_b32 s54, s42, s54\n"
    "s_or_b32 s42, s42, s44\n"
    "v_mfma_f32_16x16x32_bf16 v[136:139], v[32:35], a[60:63], v[136:139]\n"
    "s_add_i32 s39, s9, s39\n"
    "s_or_b32 s9, s9, s37\n"
    "v_mfma_f32_16x16x32_bf16 v[116:119], v[36:39], a[48:51], v[116:119]\n"
    "ds_read_b128 v[24:27], v1 offset:45056\n"
    "s_and_b32 s2, s2, 8\n"
    "v_mfma_f32_16x16x32_bf16 v[124:127], v[36:39], a[52:55], v[124:127]\n"
    "s_lshl_b32 s8, s8, 4\n"
    "v_or3_b32 v4, v4, v5, v10\n"
    "v_mfma_f32_16x16x32_bf16 v[132:135], v[36:39], a[56:59], v[132:135]\n"
    "buffer_load_dwordx4 a[104:107], v0, s[4:7], s3 offen offset:2048\n"
    "s_xor_b32 s25, s23, 0x200\n"
    "v_mfma_f32_16x16x32_bf16 v[140:143], v[36:39], a[60:63], v[140:143]\n"
    "v_add_u32_e32 v208, 0x10000, v0\n"
    "s_waitcnt lgkmcnt(3)\n"
    "v_mfma_f32_16x16x32_bf16 v[144:147], v[40:43], a[48:51], v[144:147]\n"
    "ds_read_b128 v[28:31], v1 offset:45312\n"
    "v_add_u32_e32 v209, 0x20000, v0\n"
    "v_mfma_f32_16x16x32_bf16 v[152:155], v[40:43], a[52:55], v[152:155]\n"
    "v_add3_u32 v211, v15, v176, v14\n"
    "v_and_b32_e32 v213, 28, v8\n"
    "v_mfma_f32_16x16x32_bf16 v[160:163], v[40:43], a[56:59], v[160:163]\n"
    "v_or_b32_e32 v215, v9, v12\n"
    "v_or3_b32 v220, v10, v11, v12\n"
    "v_mfma_f32_16x16x32_bf16 v[168:171], v[40:43], a[60:63], v[168:171]\n"
    "buffer_load_dwordx4 a[108:111], v0, s[4:7], s16 offen offset:2048\n"
    "v_and_b32_e32 v221, 12, v8\n"
    "v_mfma_f32_16x16x32_bf16 v[148:151], v[44:47], a[48:51], v[148:151]\n"
    "ds_read_b128 v[32:35], v2 offset:32768\n"
    "v_mov_b32_e32 v216, 0x3f80\n"
    "v_mfma_f32_16x16x32_bf16 v[156:159], v[44:47], a[52:55], v[156:159]\n"
    "v_mov_b32_e32 v217, 1.0\n"
    "s_add_i32 s29, s28, 0x24000\n"
    "v_mfma_f32_16x16x32_bf16 v[164:167], v[44:47], a[56:59], v[164:167]\n"
    "s_lshl_b32 s20, s20, 2\n"
    "s_lshl_b32 s45, s45, 13\n"
    "v_mfma_f32_16x16x32_bf16 v[172:175], v[44:47], a[60:63], v[172:175]\n"
    "s_lshl_b32 s46, s46, 13\n"
    "s_waitcnt vmcnt(12) lgkmcnt(3)\n"
    "v_mfma_f32_16x16x32_bf16 v[48:51], v[16:19], a[64:67], v[48:51]\n"
    "ds_read_b128 v[36:39], v2 offset:33024\n"
    "s_lshl_b32 s54, s54, 13\n"
    "v_mfma_f32_16x16x32_bf16 v[56:59], v[16:19], a[68:71], v[56:59]\n"
    "s_lshl_b32 s42, s42, 13\n"
    "s_or_b32 s2, s9, s2\n"
    "v_mfma_f32_16x16x32_bf16 v[64:67], v[16:19], a[72:75], v[64:67]\n"
    "s_add_u32 m0, s17, 16384\n"
    "s_add_u32 s56, s18, 16384\n"
    "buffer_load_dwordx4 v0, s[4:7], s56 offen offset:0 lds\n"
    "v_mfma_f32_16x16x32_bf16 v[72:75], v[16:19], a[76:79], v[72:75]\n"
    "s_or_b32 s8, s36, s8\n"
    "v_add_u32_e32 v5, 0x12000, v4\n"
    "v_mfma_f32_16x16x32_bf16 v[52:55], v[20:23], a[64:67], v[52:55]\n"
    "ds_read_b128 v[40:43], v3 offset:20480\n"
    "v_add_u32_e32 v7, 0x48000, v6\n"
    "v_mfma_f32_16x16x32_bf16 v[60:63], v[20:23], a[68:71], v[60:63]\n"
    "s_add_i32 s21, s21, 0x8000\n"
    "s_mov_b32 s14, 0x240000\n"
    "v_mfma_f32_16x16x32_bf16 v[68:71], v[20:23], a[72:75], v[68:71]\n"
    "s_mov_b32 s15, s7\n"
    "s_add_i32 s22, s22, 0x240000\n"
    "v_mfma_f32_16x16x32_bf16 v[76:79], v[20:23], a[76:79], v[76:79]\n"
    "buffer_load_dwordx4 v0, s[4:7], s56 offen offset:1024 lds\n"
    "s_waitcnt lgkmcnt(3)\n"
    "v_mfma_f32_16x16x32_bf16 v[80:83], v[24:27], a[64:67], v[80:83]\n"
    "ds_read_b128 v[44:47], v3 offset:20736\n"
    "s_or_b32 s20, s40, s20\n"
    "v_mfma_f32_16x16x32_bf16 v[88:91], v[24:27], a[68:71], v[88:91]\n"
    "s_add_i32 s26, s26, 0x10010\n"
    "s_add_i32 s30, s30, 0x151d800\n"
    "v_mfma_f32_16x16x32_bf16 v[96:99], v[24:27], a[72:75], v[96:99]\n"
    "s_addk_i32 s50, 0xf400\n"
    "s_addk_i32 s31, 0xf800\n"
    "v_mfma_f32_16x16x32_bf16 v[104:107], v[24:27], a[76:79], v[104:107]\n"
    "s_add_i32 s52, s52, s33\n"
    "s_add_i32 s53, s53, s33\n"
    "v_mfma_f32_16x16x32_bf16 v[84:87], v[28:31], a[64:67], v[84:87]\n"
    "s_barrier\n"
    "ds_read_b128 v[16:19], v1 offset:10240\n"
    "v_mfma_f32_16x16x32_bf16 v[92:95], v[28:31], a[68:71], v[92:95]\n"
    "buffer_load_dwordx4 a[112:115], v0, s[4:7], s0 offen offset:3072\n"
    "s_add_i32 s45, s33, s45\n"
    "v_mfma_f32_16x16x32_bf16 v[100:103], v[28:31], a[72:75], v[100:103]\n"
    "s_add_i32 s46, s33, s46\n"
    "s_add_i32 s54, s33, s54\n"
    "v_mfma_f32_16x16x32_bf16 v[108:111], v[28:31], a[76:79], v[108:111]\n"
    "s_add_i32 s33, s33, s42\n"
    "s_waitcnt lgkmcnt(3)\n"
    "v_mfma_f32_16x16x32_bf16 v[112:115], v[32:35], a[64:67], v[112:115]\n"
    "ds_read_b128 v[20:23], v1 offset:10496\n"
    "s_add_i32 s42, s39, 0x4800\n"
    "v_mfma_f32_16x16x32_bf16 v[120:123], v[32:35], a[68:71], v[120:123]\n"
    "buffer_load_dwordx4 a[116:119], v0, s[4:7], s1 offen offset:3072\n"
    "s_addk_i32 s2, 0x1000\n"
    "v_mfma_f32_16x16x32_bf16 v[128:131], v[32:35], a[72:75], v[128:131]\n"
    "s_addk_i32 s38, 0x3f00\n"
    "s_addk_i32 s8, 0x4100\n"
    "v_mfma_f32_16x16x32_bf16 v[136:139], v[32:35], a[76:79], v[136:139]\n"
    "s_add_i32 s9, s34, 0x4800\n"
    "s_mov_b32 s36, 0x40053526\n"
    "v_mfma_f32_16x16x32_bf16 v[116:119], v[36:39], a[64:67], v[116:119]\n"
    "ds_read_b128 v[24:27], v1 offset:47104\n"
    "v_mfma_f32_16x16x32_bf16 v[124:127], v[36:39], a[68:71], v[124:127]\n"
    "v_mfma_f32_16x16x32_bf16 v[132:135], v[36:39], a[72:75], v[132:135]\n"
    "buffer_load_dwordx4 a[120:123], v0, s[4:7], s3 offen offset:3072\n"
    "v_mfma_f32_16x16x32_bf16 v[140:143], v[36:39], a[76:79], v[140:143]\n"
    "s_waitcnt lgkmcnt(3)\n"
    "v_mfma_f32_16x16x32_bf16 v[144:147], v[40:43], a[64:67], v[144:147]\n"
    "ds_read_b128 v[28:31], v1 offset:47360\n"
    "v_mfma_f32_16x16x32_bf16 v[152:155], v[40:43], a[68:71], v[152:155]\n"
    "v_mfma_f32_16x16x32_bf16 v[160:163], v[40:43], a[72:75], v[160:163]\n"
    "v_mfma_f32_16x16x32_bf16 v[168:171], v[40:43], a[76:79], v[168:171]\n"
    "buffer_load_dwordx4 a[124:127], v0, s[4:7], s16 offen offset:3072\n"
    "v_mfma_f32_16x16x32_bf16 v[148:151], v[44:47], a[64:67], v[148:151]\n"
    "ds_read_b128 v[32:35], v2 offset:34816\n"
    "v_mfma_f32_16x16x32_bf16 v[156:159], v[44:47], a[68:71], v[156:159]\n"
    "v_mfma_f32_16x16x32_bf16 v[164:167], v[44:47], a[72:75], v[164:167]\n"
    "v_mfma_f32_16x16x32_bf16 v[172:175], v[44:47], a[76:79], v[172:175]\n"
    "s_waitcnt vmcnt(12) lgkmcnt(3)\n"
    "v_mfma_f32_16x16x32_bf16 v[48:51], v[16:19], a[80:83], v[48:51]\n"
    "ds_read_b128 v[36:39], v2 offset:35072\n"
    "v_mfma_f32_16x16x32_bf16 v[56:59], v[16:19], a[84:87], v[56:59]\n"
    "v_mfma_f32_16x16x32_bf16 v[64:67], v[16:19], a[88:91], v[64:67]\n"
    "buffer_load_dwordx4 v0, s[4:7], s56 offen offset:2048 lds\n"
    "v_mfma_f32_16x16x32_bf16 v[72:75], v[16:19], a[92:95], v[72:75]\n"
    "v_mfma_f32_16x16x32_bf16 v[52:55], v[20:23], a[80:83], v[52:55]\n"
    "ds_read_b128 v[40:43], v3 offset:22528\n"
    "v_mfma_f32_16x16x32_bf16 v[60:63], v[20:23], a[84:87], v[60:63]\n"
    "v_mfma_f32_16x16x32_bf16 v[68:71], v[20:23], a[88:91], v[68:71]\n"
    "v_mfma_f32_16x16x32_bf16 v[76:79], v[20:23], a[92:95], v[76:79]\n"
    "buffer_load_dwordx4 v0, s[4:7], s56 offen offset:3072 lds\n"
    "s_waitcnt lgkmcnt(3)\n"
    "v_mfma_f32_16x16x32_bf16 v[80:83], v[24:27], a[80:83], v[80:83]\n"
    "ds_read_b128 v[44:47], v3 offset:22784\n"
    "v_mfma_f32_16x16x32_bf16 v[88:91], v[24:27], a[84:87], v[88:91]\n"
    "v_mfma_f32_16x16x32_bf16 v[96:99], v[24:27], a[88:91], v[96:99]\n"
    "v_mfma_f32_16x16x32_bf16 v[104:107], v[24:27], a[92:95], v[104:107]\n"
    "v_mfma_f32_16x16x32_bf16 v[84:87], v[28:31], a[80:83], v[84:87]\n"
    "s_waitcnt vmcnt(12)\n"
    "s_barrier\n"
    "ds_read_b128 v[16:19], v1 offset:12288\n"
    "v_mfma_f32_16x16x32_bf16 v[92:95], v[28:31], a[84:87], v[92:95]\n"
    "s_add_u32 s0, s0, 4096\n"
    "buffer_load_dwordx4 a[128:131], v0, s[4:7], s0 offen offset:0\n"
    "v_mfma_f32_16x16x32_bf16 v[100:103], v[28:31], a[88:91], v[100:103]\n"
    "v_mfma_f32_16x16x32_bf16 v[108:111], v[28:31], a[92:95], v[108:111]\n"
    "s_waitcnt lgkmcnt(3)\n"
    "v_mfma_f32_16x16x32_bf16 v[112:115], v[32:35], a[80:83], v[112:115]\n"
    "ds_read_b128 v[20:23], v1 offset:12544\n"
    "v_mfma_f32_16x16x32_bf16 v[120:123], v[32:35], a[84:87], v[120:123]\n"
    "s_add_u32 s1, s1, 4096\n"
    "buffer_load_dwordx4 a[132:135], v0, s[4:7], s1 offen offset:0\n"
    "v_mfma_f32_16x16x32_bf16 v[128:131], v[32:35], a[88:91], v[128:131]\n"
    "v_mfma_f32_16x16x32_bf16 v[136:139], v[32:35], a[92:95], v[136:139]\n"
    "v_mfma_f32_16x16x32_bf16 v[116:119], v[36:39], a[80:83], v[116:119]\n"
    "ds_read_b128 v[24:27], v2 offset:0\n"
    "v_mfma_f32_16x16x32_bf16 v[124:127], v[36:39], a[84:87], v[124:127]\n"
    "v_mfma_f32_16x16x32_bf16 v[132:135], v[36:39], a[88:91], v[132:135]\n"
    "s_add_u32 s3, s3, 4096\n"
    "buffer_load_dwordx4 a[136:139], v0, s[4:7], s3 offen offset:0\n"
    "v_mfma_f32_16x16x32_bf16 v[140:143], v[36:39], a[92:95], v[140:143]\n"
    "s_waitcnt lgkmcnt(3)\n"
    "v_mfma_f32_16x16x32_bf16 v[144:147], v[40:43], a[80:83], v[144:147]\n"
    "ds_read_b128 v[28:31], v2 offset:256\n"
    "v_mfma_f32_16x16x32_bf16 v[152:155], v[40:43], a[84:87], v[152:155]\n"
    "v_mfma_f32_16x16x32_bf16 v[160:163], v[40:43], a[88:91], v[160:163]\n"
    "v_mfma_f32_16x16x32_bf16 v[168:171], v[40:43], a[92:95], v[168:171]\n"
    "s_add_u32 s16, s16, 4096\n"
    "buffer_load_dwordx4 a[140:143], v0, s[4:7], s16 offen offset:0\n"
    "v_mfma_f32_16x16x32_bf16 v[148:151], v[44:47], a[80:83], v[148:151]\n"
    "ds_read_b128 v[32:35], v2 offset:36864\n"
    "v_mfma_f32_16x16x32_bf16 v[156:159], v[44:47], a[84:87], v[156:159]\n"
    "v_mfma_f32_16x16x32_bf16 v[164:167], v[44:47], a[88:91], v[164:167]\n"
    "v_mfma_f32_16x16x32_bf16 v[172:175], v[44:47], a[92:95], v[172:175]\n"
    "s_waitcnt vmcnt(12) lgkmcnt(3)\n"
    "v_mfma_f32_16x16x32_bf16 v[48:51], v[16:19], a[96:99], v[48:51]\n"
    "ds_read_b128 v[36:39], v2 offset:37120\n"
    "v_mfma_f32_16x16x32_bf16 v[56:59], v[16:19], a[100:103], v[56:59]\n"
    "v_mfma_f32_16x16x32_bf16 v[64:67], v[16:19], a[104:107], v[64:67]\n"
    "s_add_u32 m0, s17, 20480\n"
    "s_add_u32 s56, s18, 20480\n"
    "buffer_load_dwordx4 v0, s[4:7], s56 offen offset:0 lds\n"
    "v_mfma_f32_16x16x32_bf16 v[72:75], v[16:19], a[108:111], v[72:75]\n"
    "LPK1:\n"
    "s_cbranch_execz LPK5\n"
    "v_mfma_f32_16x16x32_bf16 v[52:55], v[20:23], a[96:99], v[52:55]\n"
    "ds_read_b128 v[40:43], v3 offset:24576\n"
    "v_mfma_f32_16x16x32_bf16 v[60:63], v[20:23], a[100:103], v[60:63]\n"
    "v_mfma_f32_16x16x32_bf16 v[68:71], v[20:23], a[104:107], v[68:71]\n"
    "v_mfma_f32_16x16x32_bf16 v[76:79], v[20:23], a[108:111], v[76:79]\n"
    "buffer_load_dwordx4 v0, s[4:7], s56 offen offset:1024 lds\n"
    "s_waitcnt lgkmcnt(3)\n"
    "v_mfma_f32_16x16x32_bf16 v[80:83], v[24:27], a[96:99], v[80:83]\n"
    "ds_read_b128 v[44:47], v3 offset:24832\n"
    "v_mfma_f32_16x16x32_bf16 v[88:91], v[24:27], a[100:103], v[88:91]\n"
    "v_mfma_f32_16x16x32_bf16 v[96:99], v[24:27], a[104:107], v[96:99]\n"
    "v_mfma_f32_16x16x32_bf16 v[104:107], v[24:27], a[108:111], v[104:107]\n"
    "v_mfma_f32_16x16x32_bf16 v[84:87], v[28:31], a[96:99], v[84:87]\n"
    "ds_read_b128 v[16:19], v1 offset:14336\n"
    "v_mfma_f32_16x16x32_bf16 v[92:95], v[28:31], a[100:103], v[92:95]\n"
    "buffer_load_dwordx4 a[144:147], v0, s[4:7], s0 offen offset:1024\n"
    "v_mfma_f32_16x16x32_bf16 v[100:103], v[28:31], a[104:107], v[100:103]\n"
    "v_mfma_f32_16x16x32_bf16 v[108:111], v[28:31], a[108:111], v[108:111]\n"
    "s_waitcnt lgkmcnt(3)\n"
    "v_mfma_f32_16x16x32_bf16 v[112:115], v[32:35], a[96:99], v[112:115]\n"
    "ds_read_b128 v[20:23], v1 offset:14592\n"
    "v_mfma_f32_16x16x32_bf16 v[120:123], v[32:35], a[100:103], v[120:123]\n"
    "buffer_load_dwordx4 a[148:151], v0, s[4:7], s1 offen offset:1024\n"
    "v_mfma_f32_16x16x32_bf16 v[128:131], v[32:35], a[104:107], v[128:131]\n"
    "v_mfma_f32_16x16x32_bf16 v[136:139], v[32:35], a[108:111], v[136:139]\n"
    "v_mfma_f32_16x16x32_bf16 v[116:119], v[36:39], a[96:99], v[116:119]\n"
    "ds_read_b128 v[24:27], v2 offset:2048\n"
    "v_mfma_f32_16x16x32_bf16 v[124:127], v[36:39], a[100:103], v[124:127]\n"
    "v_mfma_f32_16x16x32_bf16 v[132:135], v[36:39], a[104:107], v[132:135]\n"
    "buffer_load_dwordx4 a[152:155], v0, s[4:7], s3 offen offset:1024\n"
    "v_mfma_f32_16x16x32_bf16 v[140:143], v[36:39], a[108:111], v[140:143]\n"
    "s_waitcnt lgkmcnt(3)\n"
    "v_mfma_f32_16x16x32_bf16 v[144:147], v[40:43], a[96:99], v[144:147]\n"
    "ds_read_b128 v[28:31], v2 offset:2304\n"
    "v_mfma_f32_16x16x32_bf16 v[152:155], v[40:43], a[100:103], v[152:155]\n"
    "v_mfma_f32_16x16x32_bf16 v[160:163], v[40:43], a[104:107], v[160:163]\n"
    "v_mfma_f32_16x16x32_bf16 v[168:171], v[40:43], a[108:111], v[168:171]\n"
    "buffer_load_dwordx4 a[156:159], v0, s[4:7], s16 offen offset:1024\n"
    "v_mfma_f32_16x16x32_bf16 v[148:151], v[44:47], a[96:99], v[148:151]\n"
    "ds_read_b128 v[32:35], v2 offset:38912\n"
    "v_mfma_f32_16x16x32_bf16 v[156:159], v[44:47], a[100:103], v[156:159]\n"
    "v_mfma_f32_16x16x32_bf16 v[164:167], v[44:47], a[104:107], v[164:167]\n"
    "v_mfma_f32_16x16x32_bf16 v[172:175], v[44:47], a[108:111], v[172:175]\n"
    "s_waitcnt vmcnt(12) lgkmcnt(3)\n"
    "v_mfma_f32_16x16x32_bf16 v[48:51], v[16:19], a[112:115], v[48:51]\n"
    "ds_read_b128 v[36:39], v2 offset:39168\n"
    "v_mfma_f32_16x16x32_bf16 v[56:59], v[16:19], a[116:119], v[56:59]\n"
    "v_mfma_f32_16x16x32_bf16 v[64:67], v[16:19], a[120:123], v[64:67]\n"
    "buffer_load_dwordx4 v0, s[4:7], s56 offen offset:2048 lds\n"
    "v_mfma_f32_16x16x32_bf16 v[72:75], v[16:19], a[124:127], v[72:75]\n"
    "v_mfma_f32_16x16x32_bf16 v[52:55], v[20:23], a[112:115], v[52:55]\n"
    "ds_read_b128 v[40:43], v3 offset:26624\n"
    "v_mfma_f32_16x16x32_bf16 v[60:63], v[20:23], a[116:119], v[60:63]\n"
    "v_mfma_f32_16x16x32_bf16 v[68:71], v[20:23], a[120:123], v[68:71]\n"
    "v_mfma_f32_16x16x32_bf16 v[76:79], v[20:23], a[124:127], v[76:79]\n"
    "buffer_load_dwordx4 v0, s[4:7], s56 offen offset:3072 lds\n"
    "s_waitcnt lgkmcnt(3)\n"
    "v_mfma_f32_16x16x32_bf16 v[80:83], v[24:27], a[112:115], v[80:83]\n"
    "ds_read_b128 v[44:47], v3 offset:26880\n"
    "v_mfma_f32_16x16x32_bf16 v[88:91], v[24:27], a[116:119], v[88:91]\n"
    "v_mfma_f32_16x16x32_bf16 v[96:99], v[24:27], a[120:123], v[96:99]\n"
    "v_mfma_f32_16x16x32_bf16 v[104:107], v[24:27], a[124:127], v[104:107]\n"
    "v_mfma_f32_16x16x32_bf16 v[84:87], v[28:31], a[112:115], v[84:87]\n"
    "s_waitcnt vmcnt(12)\n"
    "s_barrier\n"
    "ds_read_b128 v[16:19], v1 offset:16384\n"
    "v_mfma_f32_16x16x32_bf16 v[92:95], v[28:31], a[116:119], v[92:95]\n"
    "buffer_load_dwordx4 a[160:163], v0, s[4:7], s0 offen offset:2048\n"
    "v_mfma_f32_16x16x32_bf16 v[100:103], v[28:31], a[120:123], v[100:103]\n"
    "v_mfma_f32_16x16x32_bf16 v[108:111], v[28:31], a[124:127], v[108:111]\n"
    "s_waitcnt lgkmcnt(3)\n"
    "v_mfma_f32_16x16x32_bf16 v[112:115], v[32:35], a[112:115], v[112:115]\n"
    "ds_read_b128 v[20:23], v1 offset:16640\n"
    "v_mfma_f32_16x16x32_bf16 v[120:123], v[32:35], a[116:119], v[120:123]\n"
    "buffer_load_dwordx4 a[164:167], v0, s[4:7], s1 offen offset:2048\n"
    "v_mfma_f32_16x16x32_bf16 v[128:131], v[32:35], a[120:123], v[128:131]\n"
    "v_mfma_f32_16x16x32_bf16 v[136:139], v[32:35], a[124:127], v[136:139]\n"
    "v_mfma_f32_16x16x32_bf16 v[116:119], v[36:39], a[112:115], v[116:119]\n"
    "ds_read_b128 v[24:27], v2 offset:4096\n"
    "v_mfma_f32_16x16x32_bf16 v[124:127], v[36:39], a[116:119], v[124:127]\n"
    "v_mfma_f32_16x16x32_bf16 v[132:135], v[36:39], a[120:123], v[132:135]\n"
    "buffer_load_dwordx4 a[168:171], v0, s[4:7], s3 offen offset:2048\n"
    "v_mfma_f32_16x16x32_bf16 v[140:143], v[36:39], a[124:127], v[140:143]\n"
    "s_waitcnt lgkmcnt(3)\n"
    "v_mfma_f32_16x16x32_bf16 v[144:147], v[40:43], a[112:115], v[144:147]\n"
    "ds_read_b128 v[28:31], v2 offset:4352\n"
    "v_mfma_f32_16x16x32_bf16 v[152:155], v[40:43], a[116:119], v[152:155]\n"
    "v_mfma_f32_16x16x32_bf16 v[160:163], v[40:43], a[120:123], v[160:163]\n"
    "v_mfma_f32_16x16x32_bf16 v[168:171], v[40:43], a[124:127], v[168:171]\n"
    "buffer_load_dwordx4 a[172:175], v0, s[4:7], s16 offen offset:2048\n"
    "v_mfma_f32_16x16x32_bf16 v[148:151], v[44:47], a[112:115], v[148:151]\n"
    "ds_read_b128 v[32:35], v2 offset:40960\n"
    "v_mfma_f32_16x16x32_bf16 v[156:159], v[44:47], a[116:119], v[156:159]\n"
    "v_mfma_f32_16x16x32_bf16 v[164:167], v[44:47], a[120:123], v[164:167]\n"
    "v_mfma_f32_16x16x32_bf16 v[172:175], v[44:47], a[124:127], v[172:175]\n"
    "s_waitcnt vmcnt(12) lgkmcnt(3)\n"
    "v_mfma_f32_16x16x32_bf16 v[48:51], v[16:19], a[128:131], v[48:51]\n"
    "ds_read_b128 v[36:39], v2 offset:41216\n"
    "v_mfma_f32_16x16x32_bf16 v[56:59], v[16:19], a[132:135], v[56:59]\n"
    "v_mfma_f32_16x16x32_bf16 v[64:67], v[16:19], a[136:139], v[64:67]\n"
    "s_add_u32 m0, s17, 24576\n"
    "s_add_u32 s56, s18, 24576\n"
    "buffer_load_dwordx4 v0, s[4:7], s56 offen offset:0 lds\n"
    "v_mfma_f32_16x16x32_bf16 v[72:75], v[16:19], a[140:143], v[72:75]\n"
    "v_mfma_f32_16x16x32_bf16 v[52:55], v[20:23], a[128:131], v[52:55]\n"
    "ds_read_b128 v[40:43], v3 offset:28672\n"
    "v_mfma_f32_16x16x32_bf16 v[60:63], v[20:23], a[132:135], v[60:63]\n"
    "v_mfma_f32_16x16x32_bf16 v[68:71], v[20:23], a[136:139], v[68:71]\n"
    "v_mfma_f32_16x16x32_bf16 v[76:79], v[20:23], a[140:143], v[76:79]\n"
    "buffer_load_dwordx4 v0, s[4:7], s56 offen offset:1024 lds\n"
    "s_waitcnt lgkmcnt(3)\n"
    "v_mfma_f32_16x16x32_bf16 v[80:83], v[24:27], a[128:131], v[80:83]\n"
    "ds_read_b128 v[44:47], v3 offset:28928\n"
    "v_mfma_f32_16x16x32_bf16 v[88:91], v[24:27], a[132:135], v[88:91]\n"
    "v_mfma_f32_16x16x32_bf16 v[96:99], v[24:27], a[136:139], v[96:99]\n"
    "v_mfma_f32_16x16x32_bf16 v[104:107], v[24:27], a[140:143], v[104:107]\n"
    "v_mfma_f32_16x16x32_bf16 v[84:87], v[28:31], a[128:131], v[84:87]\n"
    "ds_read_b128 v[16:19], v1 offset:18432\n"
    "v_mfma_f32_16x16x32_bf16 v[92:95], v[28:31], a[132:135], v[92:95]\n"
    "buffer_load_dwordx4 a[176:179], v0, s[4:7], s0 offen offset:3072\n"
    "v_mfma_f32_16x16x32_bf16 v[100:103], v[28:31], a[136:139], v[100:103]\n"
    "v_mfma_f32_16x16x32_bf16 v[108:111], v[28:31], a[140:143], v[108:111]\n"
    "s_waitcnt lgkmcnt(3)\n"
    "v_mfma_f32_16x16x32_bf16 v[112:115], v[32:35], a[128:131], v[112:115]\n"
    "ds_read_b128 v[20:23], v1 offset:18688\n"
    "v_mfma_f32_16x16x32_bf16 v[120:123], v[32:35], a[132:135], v[120:123]\n"
    "buffer_load_dwordx4 a[180:183], v0, s[4:7], s1 offen offset:3072\n"
    "v_mfma_f32_16x16x32_bf16 v[128:131], v[32:35], a[136:139], v[128:131]\n"
    "v_mfma_f32_16x16x32_bf16 v[136:139], v[32:35], a[140:143], v[136:139]\n"
    "v_mfma_f32_16x16x32_bf16 v[116:119], v[36:39], a[128:131], v[116:119]\n"
    "ds_read_b128 v[24:27], v2 offset:6144\n"
    "v_mfma_f32_16x16x32_bf16 v[124:127], v[36:39], a[132:135], v[124:127]\n"
    "v_mfma_f32_16x16x32_bf16 v[132:135], v[36:39], a[136:139], v[132:135]\n"
    "buffer_load_dwordx4 a[184:187], v0, s[4:7], s3 offen offset:3072\n"
    "v_mfma_f32_16x16x32_bf16 v[140:143], v[36:39], a[140:143], v[140:143]\n"
    "s_waitcnt lgkmcnt(3)\n"
    "v_mfma_f32_16x16x32_bf16 v[144:147], v[40:43], a[128:131], v[144:147]\n"
    "ds_read_b128 v[28:31], v2 offset:6400\n"
    "v_mfma_f32_16x16x32_bf16 v[152:155], v[40:43], a[132:135], v[152:155]\n"
    "v_mfma_f32_16x16x32_bf16 v[160:163], v[40:43], a[136:139], v[160:163]\n"
    "v_mfma_f32_16x16x32_bf16 v[168:171], v[40:43], a[140:143], v[168:171]\n"
    "buffer_load_dwordx4 a[188:191], v0, s[4:7], s16 offen offset:3072\n"
    "v_mfma_f32_16x16x32_bf16 v[148:151], v[44:47], a[128:131], v[148:151]\n"
    "ds_read_b128 v[32:35], v2 offset:43008\n"
    "v_mfma_f32_16x16x32_bf16 v[156:159], v[44:47], a[132:135], v[156:159]\n"
    "v_mfma_f32_16x16x32_bf16 v[164:167], v[44:47], a[136:139], v[164:167]\n"
    "v_mfma_f32_16x16x32_bf16 v[172:175], v[44:47], a[140:143], v[172:175]\n"
    "s_waitcnt vmcnt(12) lgkmcnt(3)\n"
    "v_mfma_f32_16x16x32_bf16 v[48:51], v[16:19], a[144:147], v[48:51]\n"
    "ds_read_b128 v[36:39], v2 offset:43264\n"
    "v_mfma_f32_16x16x32_bf16 v[56:59], v[16:19], a[148:151], v[56:59]\n"
    "v_mfma_f32_16x16x32_bf16 v[64:67], v[16:19], a[152:155], v[64:67]\n"
    "buffer_load_dwordx4 v0, s[4:7], s56 offen offset:2048 lds\n"
    "v_mfma_f32_16x16x32_bf16 v[72:75], v[16:19], a[156:159], v[72:75]\n"
    "v_mfma_f32_16x16x32_bf16 v[52:55], v[20:23], a[144:147], v[52:55]\n"
    "ds_read_b128 v[40:43], v3 offset:30720\n"
    "v_mfma_f32_16x16x32_bf16 v[60:63], v[20:23], a[148:151], v[60:63]\n"
    "v_mfma_f32_16x16x32_bf16 v[68:71], v[20:23], a[152:155], v[68:71]\n"
    "v_mfma_f32_16x16x32_bf16 v[76:79], v[20:23], a[156:159], v[76:79]\n"
    "buffer_load_dwordx4 v0, s[4:7], s56 offen offset:3072 lds\n"
    "s_waitcnt lgkmcnt(3)\n"
    "v_mfma_f32_16x16x32_bf16 v[80:83], v[24:27], a[144:147], v[80:83]\n"
    "ds_read_b128 v[44:47], v3 offset:30976\n"
    "v_mfma_f32_16x16x32_bf16 v[88:91], v[24:27], a[148:151], v[88:91]\n"
    "v_mfma_f32_16x16x32_bf16 v[96:99], v[24:27], a[152:155], v[96:99]\n"
    "v_mfma_f32_16x16x32_bf16 v[104:107], v[24:27], a[156:159], v[104:107]\n"
    "v_mfma_f32_16x16x32_bf16 v[84:87], v[28:31], a[144:147], v[84:87]\n"
    "s_waitcnt vmcnt(12)\n"
    "s_barrier\n"
    "ds_read_b128 v[16:19], v1 offset:20480\n"
    "v_mfma_f32_16x16x32_bf16 v[92:95], v[28:31], a[148:151], v[92:95]\n"
    "s_add_u32 s0, s0, 4096\n"
    "buffer_load_dwordx4 a[192:195], v0, s[4:7], s0 offen offset:0\n"
    "v_mfma_f32_16x16x32_bf16 v[100:103], v[28:31], a[152:155], v[100:103]\n"
    "v_mfma_f32_16x16x32_bf16 v[108:111], v[28:31], a[156:159], v[108:111]\n"
    "s_waitcnt lgkmcnt(3)\n"
    "v_mfma_f32_16x16x32_bf16 v[112:115], v[32:35], a[144:147], v[112:115]\n"
    "ds_read_b128 v[20:23], v1 offset:20736\n"
    "v_mfma_f32_16x16x32_bf16 v[120:123], v[32:35], a[148:151], v[120:123]\n"
    "s_add_u32 s1, s1, 4096\n"
    "buffer_load_dwordx4 a[196:199], v0, s[4:7], s1 offen offset:0\n"
    "v_mfma_f32_16x16x32_bf16 v[128:131], v[32:35], a[152:155], v[128:131]\n"
    "v_mfma_f32_16x16x32_bf16 v[136:139], v[32:35], a[156:159], v[136:139]\n"
    "v_mfma_f32_16x16x32_bf16 v[116:119], v[36:39], a[144:147], v[116:119]\n"
    "ds_read_b128 v[24:27], v2 offset:8192\n"
    "v_mfma_f32_16x16x32_bf16 v[124:127], v[36:39], a[148:151], v[124:127]\n"
    "v_mfma_f32_16x16x32_bf16 v[132:135], v[36:39], a[152:155], v[132:135]\n"
    "s_add_u32 s3, s3, 4096\n"
    "buffer_load_dwordx4 a[200:203], v0, s[4:7], s3 offen offset:0\n"
    "v_mfma_f32_16x16x32_bf16 v[140:143], v[36:39], a[156:159], v[140:143]\n"
    "s_waitcnt lgkmcnt(3)\n"
    "v_mfma_f32_16x16x32_bf16 v[144:147], v[40:43], a[144:147], v[144:147]\n"
    "ds_read_b128 v[28:31], v2 offset:8448\n"
    "v_mfma_f32_16x16x32_bf16 v[152:155], v[40:43], a[148:151], v[152:155]\n"
    "v_mfma_f32_16x16x32_bf16 v[160:163], v[40:43], a[152:155], v[160:163]\n"
    "v_mfma_f32_16x16x32_bf16 v[168:171], v[40:43], a[156:159], v[168:171]\n"
    "s_add_u32 s16, s16, 4096\n"
    "buffer_load_dwordx4 a[204:207], v0, s[4:7], s16 offen offset:0\n"
    "v_mfma_f32_16x16x32_bf16 v[148:151], v[44:47], a[144:147], v[148:151]\n"
    "ds_read_b128 v[32:35], v2 offset:45056\n"
    "v_mfma_f32_16x16x32_bf16 v[156:159], v[44:47], a[148:151], v[156:159]\n"
    "v_mfma_f32_16x16x32_bf16 v[164:167], v[44:47], a[152:155], v[164:167]\n"
    "v_mfma_f32_16x16x32_bf16 v[172:175], v[44:47], a[156:159], v[172:175]\n"
    "s_waitcnt vmcnt(12) lgkmcnt(3)\n"
    "v_mfma_f32_16x16x32_bf16 v[48:51], v[16:19], a[160:163], v[48:51]\n"
    "ds_read_b128 v[36:39], v2 offset:45312\n"
    "v_mfma_f32_16x16x32_bf16 v[56:59], v[16:19], a[164:167], v[56:59]\n"
    "v_mfma_f32_16x16x32_bf16 v[64:67], v[16:19], a[168:171], v[64:67]\n"
    "s_add_u32 m0, s17, 28672\n"
    "s_add_u32 s56, s18, 28672\n"
    "buffer_load_dwordx4 v0, s[4:7], s56 offen offset:0 lds\n"
    "v_mfma_f32_16x16x32_bf16 v[72:75], v[16:19], a[172:175], v[72:75]\n"
    "v_mfma_f32_16x16x32_bf16 v[52:55], v[20:23], a[160:163], v[52:55]\n"
    "ds_read_b128 v[40:43], v3 offset:32768\n"
    "v_mfma_f32_16x16x32_bf16 v[60:63], v[20:23], a[164:167], v[60:63]\n"
    "v_mfma_f32_16x16x32_bf16 v[68:71], v[20:23], a[168:171], v[68:71]\n"
    "v_mfma_f32_16x16x32_bf16 v[76:79], v[20:23], a[172:175], v[76:79]\n"
    "buffer_load_dwordx4 v0, s[4:7], s56 offen offset:1024 lds\n"
    "s_waitcnt lgkmcnt(3)\n"
    "v_mfma_f32_16x16x32_bf16 v[80:83], v[24:27], a[160:163], v[80:83]\n"
    "ds_read_b128 v[44:47], v3 offset:33024\n"
    "v_mfma_f32_16x16x32_bf16 v[88:91], v[24:27], a[164:167], v[88:91]\n"
    "v_mfma_f32_16x16x32_bf16 v[96:99], v[24:27], a[168:171], v[96:99]\n"
    "v_mfma_f32_16x16x32_bf16 v[104:107], v[24:27], a[172:175], v[104:107]\n"
    "v_mfma_f32_16x16x32_bf16 v[84:87], v[28:31], a[160:163], v[84:87]\n"
    "ds_read_b128 v[16:19], v1 offset:22528\n"
    "v_mfma_f32_16x16x32_bf16 v[92:95], v[28:31], a[164:167], v[92:95]\n"
    "buffer_load_dwordx4 a[208:211], v0, s[4:7], s0 offen offset:1024\n"
    "v_mfma_f32_16x16x32_bf16 v[100:103], v[28:31], a[168:171], v[100:103]\n"
    "v_mfma_f32_16x16x32_bf16 v[108:111], v[28:31], a[172:175], v[108:111]\n"
    "s_waitcnt lgkmcnt(3)\n"
    "v_mfma_f32_16x16x32_bf16 v[112:115], v[32:35], a[160:163], v[112:115]\n"
    "ds_read_b128 v[20:23], v1 offset:22784\n"
    "v_mfma_f32_16x16x32_bf16 v[120:123], v[32:35], a[164:167], v[120:123]\n"
    "buffer_load_dwordx4 a[212:215], v0, s[4:7], s1 offen offset:1024\n"
    "v_mfma_f32_16x16x32_bf16 v[128:131], v[32:35], a[168:171], v[128:131]\n"
    "v_mfma_f32_16x16x32_bf16 v[136:139], v[32:35], a[172:175], v[136:139]\n"
    "v_mfma_f32_16x16x32_bf16 v[116:119], v[36:39], a[160:163], v[116:119]\n"
    "ds_read_b128 v[24:27], v2 offset:10240\n"
    "v_mfma_f32_16x16x32_bf16 v[124:127], v[36:39], a[164:167], v[124:127]\n"
    "v_mfma_f32_16x16x32_bf16 v[132:135], v[36:39], a[168:171], v[132:135]\n"
    "buffer_load_dwordx4 a[216:219], v0, s[4:7], s3 offen offset:1024\n"
    "v_mfma_f32_16x16x32_bf16 v[140:143], v[36:39], a[172:175], v[140:143]\n"
    "s_waitcnt lgkmcnt(3)\n"
    "v_mfma_f32_16x16x32_bf16 v[144:147], v[40:43], a[160:163], v[144:147]\n"
    "ds_read_b128 v[28:31], v2 offset:10496\n"
    "v_mfma_f32_16x16x32_bf16 v[152:155], v[40:43], a[164:167], v[152:155]\n"
    "v_mfma_f32_16x16x32_bf16 v[160:163], v[40:43], a[168:171], v[160:163]\n"
    "v_mfma_f32_16x16x32_bf16 v[168:171], v[40:43], a[172:175], v[168:171]\n"
    "buffer_load_dwordx4 a[220:223], v0, s[4:7], s16 offen offset:1024\n"
    "v_mfma_f32_16x16x32_bf16 v[148:151], v[44:47], a[160:163], v[148:151]\n"
    "ds_read_b128 v[32:35], v2 offset:47104\n"
    "v_mfma_f32_16x16x32_bf16 v[156:159], v[44:47], a[164:167], v[156:159]\n"
    "v_mfma_f32_16x16x32_bf16 v[164:167], v[44:47], a[168:171], v[164:167]\n"
    "v_mfma_f32_16x16x32_bf16 v[172:175], v[44:47], a[172:175], v[172:175]\n"
    "s_waitcnt vmcnt(12) lgkmcnt(3)\n"
    "v_mfma_f32_16x16x32_bf16 v[48:51], v[16:19], a[176:179], v[48:51]\n"
    "ds_read_b128 v[36:39], v2 offset:47360\n"
    "v_mfma_f32_16x16x32_bf16 v[56:59], v[16:19], a[180:183], v[56:59]\n"
    "v_mfma_f32_16x16x32_bf16 v[64:67], v[16:19], a[184:187], v[64:67]\n"
    "buffer_load_dwordx4 v0, s[4:7], s56 offen offset:2048 lds\n"
    "v_mfma_f32_16x16x32_bf16 v[72:75], v[16:19], a[188:191], v[72:75]\n"
    "v_mfma_f32_16x16x32_bf16 v[52:55], v[20:23], a[176:179], v[52:55]\n"
    "ds_read_b128 v[40:43], v3 offset:34816\n"
    "v_mfma_f32_16x16x32_bf16 v[60:63], v[20:23], a[180:183], v[60:63]\n"
    "v_mfma_f32_16x16x32_bf16 v[68:71], v[20:23], a[184:187], v[68:71]\n"
    "v_mfma_f32_16x16x32_bf16 v[76:79], v[20:23], a[188:191], v[76:79]\n"
    "buffer_load_dwordx4 v0, s[4:7], s56 offen offset:3072 lds\n"
    "s_waitcnt lgkmcnt(3)\n"
    "v_mfma_f32_16x16x32_bf16 v[80:83], v[24:27], a[176:179], v[80:83]\n"
    "ds_read_b128 v[44:47], v3 offset:35072\n"
    "v_mfma_f32_16x16x32_bf16 v[88:91], v[24:27], a[180:183], v[88:91]\n"
    "v_mfma_f32_16x16x32_bf16 v[96:99], v[24:27], a[184:187], v[96:99]\n"
    "v_mfma_f32_16x16x32_bf16 v[104:107], v[24:27], a[188:191], v[104:107]\n"
    "v_mfma_f32_16x16x32_bf16 v[84:87], v[28:31], a[176:179], v[84:87]\n"
    "s_waitcnt vmcnt(12)\n"
    "s_barrier\n"
    "ds_read_b128 v[16:19], v1 offset:24576\n"
    "v_mfma_f32_16x16x32_bf16 v[92:95], v[28:31], a[180:183], v[92:95]\n"
    "buffer_load_dwordx4 a[224:227], v0, s[4:7], s0 offen offset:2048\n"
    "v_mfma_f32_16x16x32_bf16 v[100:103], v[28:31], a[184:187], v[100:103]\n"
    "v_mfma_f32_16x16x32_bf16 v[108:111], v[28:31], a[188:191], v[108:111]\n"
    "s_waitcnt lgkmcnt(3)\n"
    "v_mfma_f32_16x16x32_bf16 v[112:115], v[32:35], a[176:179], v[112:115]\n"
    "ds_read_b128 v[20:23], v1 offset:24832\n"
    "v_mfma_f32_16x16x32_bf16 v[120:123], v[32:35], a[180:183], v[120:123]\n"
    "buffer_load_dwordx4 a[228:231], v0, s[4:7], s1 offen offset:2048\n"
    "v_mfma_f32_16x16x32_bf16 v[128:131], v[32:35], a[184:187], v[128:131]\n"
    "v_mfma_f32_16x16x32_bf16 v[136:139], v[32:35], a[188:191], v[136:139]\n"
    "v_mfma_f32_16x16x32_bf16 v[116:119], v[36:39], a[176:179], v[116:119]\n"
    "ds_read_b128 v[24:27], v2 offset:12288\n"
    "v_mfma_f32_16x16x32_bf16 v[124:127], v[36:39], a[180:183], v[124:127]\n"
    "v_mfma_f32_16x16x32_bf16 v[132:135], v[36:39], a[184:187], v[132:135]\n"
    "buffer_load_dwordx4 a[232:235], v0, s[4:7], s3 offen offset:2048\n"
    "v_mfma_f32_16x16x32_bf16 v[140:143], v[36:39], a[188:191], v[140:143]\n"
    "s_waitcnt lgkmcnt(3)\n"
    "v_mfma_f32_16x16x32_bf16 v[144:147], v[40:43], a[176:179], v[144:147]\n"
    "ds_read_b128 v[28:31], v2 offset:12544\n"
    "v_mfma_f32_16x16x32_bf16 v[152:155], v[40:43], a[180:183], v[152:155]\n"
    "v_mfma_f32_16x16x32_bf16 v[160:163], v[40:43], a[184:187], v[160:163]\n"
    "v_mfma_f32_16x16x32_bf16 v[168:171], v[40:43], a[188:191], v[168:171]\n"
    "buffer_load_dwordx4 a[236:239], v0, s[4:7], s16 offen offset:2048\n"
    "v_mfma_f32_16x16x32_bf16 v[148:151], v[44:47], a[176:179], v[148:151]\n"
    "ds_read_b128 v[32:35], v3 offset:0\n"
    "v_mfma_f32_16x16x32_bf16 v[156:159], v[44:47], a[180:183], v[156:159]\n"
    "v_mfma_f32_16x16x32_bf16 v[164:167], v[44:47], a[184:187], v[164:167]\n"
    "v_mfma_f32_16x16x32_bf16 v[172:175], v[44:47], a[188:191], v[172:175]\n"
    "s_waitcnt vmcnt(12) lgkmcnt(3)\n"
    "v_mfma_f32_16x16x32_bf16 v[48:51], v[16:19], a[192:195], v[48:51]\n"
    "ds_read_b128 v[36:39], v3 offset:256\n"
    "v_mfma_f32_16x16x32_bf16 v[56:59], v[16:19], a[196:199], v[56:59]\n"
    "v_mfma_f32_16x16x32_bf16 v[64:67], v[16:19], a[200:203], v[64:67]\n"
    "s_add_u32 m0, s17, 32768\n"
    "s_add_u32 s56, s18, 32768\n"
    "buffer_load_dwordx4 v0, s[4:7], s56 offen offset:0 lds\n"
    "v_mfma_f32_16x16x32_bf16 v[72:75], v[16:19], a[204:207], v[72:75]\n"
    "v_mfma_f32_16x16x32_bf16 v[52:55], v[20:23], a[192:195], v[52:55]\n"
    "ds_read_b128 v[40:43], v3 offset:36864\n"
    "v_mfma_f32_16x16x32_bf16 v[60:63], v[20:23], a[196:199], v[60:63]\n"
    "v_mfma_f32_16x16x32_bf16 v[68:71], v[20:23], a[200:203], v[68:71]\n"
    "v_mfma_f32_16x16x32_bf16 v[76:79], v[20:23], a[204:207], v[76:79]\n"
    "buffer_load_dwordx4 v0, s[4:7], s56 offen offset:1024 lds\n"
    "s_waitcnt lgkmcnt(3)\n"
    "v_mfma_f32_16x16x32_bf16 v[80:83], v[24:27], a[192:195], v[80:83]\n"
    "ds_read_b128 v[44:47], v3 offset:37120\n"
    "v_mfma_f32_16x16x32_bf16 v[88:91], v[24:27], a[196:199], v[88:91]\n"
    "v_mfma_f32_16x16x32_bf16 v[96:99], v[24:27], a[200:203], v[96:99]\n"
    "v_mfma_f32_16x16x32_bf16 v[104:107], v[24:27], a[204:207], v[104:107]\n"
    "v_mfma_f32_16x16x32_bf16 v[84:87], v[28:31], a[192:195], v[84:87]\n"
    "ds_read_b128 v[16:19], v1 offset:26624\n"
    "v_mfma_f32_16x16x32_bf16 v[92:95], v[28:31], a[196:199], v[92:95]\n"
    "buffer_load_dwordx4 a[240:243], v0, s[4:7], s0 offen offset:3072\n"
    "v_mfma_f32_16x16x32_bf16 v[100:103], v[28:31], a[200:203], v[100:103]\n"
    "v_mfma_f32_16x16x32_bf16 v[108:111], v[28:31], a[204:207], v[108:111]\n"
    "s_waitcnt lgkmcnt(3)\n"
    "v_mfma_f32_16x16x32_bf16 v[112:115], v[32:35], a[192:195], v[112:115]\n"
    "ds_read_b128 v[20:23], v1 offset:26880\n"
    "v_mfma_f32_16x16x32_bf16 v[120:123], v[32:35], a[196:199], v[120:123]\n"
    "buffer_load_dwordx4 a[244:247], v0, s[4:7], s1 offen offset:3072\n"
    "v_mfma_f32_16x16x32_bf16 v[128:131], v[32:35], a[200:203], v[128:131]\n"
    "v_mfma_f32_16x16x32_bf16 v[136:139], v[32:35], a[204:207], v[136:139]\n"
    "v_mfma_f32_16x16x32_bf16 v[116:119], v[36:39], a[192:195], v[116:119]\n"
    "ds_read_b128 v[24:27], v2 offset:14336\n"
    "v_mfma_f32_16x16x32_bf16 v[124:127], v[36:39], a[196:199], v[124:127]\n"
    "v_mfma_f32_16x16x32_bf16 v[132:135], v[36:39], a[200:203], v[132:135]\n"
    "buffer_load_dwordx4 a[248:251], v0, s[4:7], s3 offen offset:3072\n"
    "v_mfma_f32_16x16x32_bf16 v[140:143], v[36:39], a[204:207], v[140:143]\n"
    "s_waitcnt lgkmcnt(3)\n"
    "v_mfma_f32_16x16x32_bf16 v[144:147], v[40:43], a[192:195], v[144:147]\n"
    "ds_read_b128 v[28:31], v2 offset:14592\n"
    "v_mfma_f32_16x16x32_bf16 v[152:155], v[40:43], a[196:199], v[152:155]\n"
    "v_mfma_f32_16x16x32_bf16 v[160:163], v[40:43], a[200:203], v[160:163]\n"
    "v_mfma_f32_16x16x32_bf16 v[168:171], v[40:43], a[204:207], v[168:171]\n"
    "buffer_load_dwordx4 a[252:255], v0, s[4:7], s16 offen offset:3072\n"
    "v_mfma_f32_16x16x32_bf16 v[148:151], v[44:47], a[192:195], v[148:151]\n"
    "ds_read_b128 v[32:35], v3 offset:2048\n"
    "v_mfma_f32_16x16x32_bf16 v[156:159], v[44:47], a[196:199], v[156:159]\n"
    "v_mfma_f32_16x16x32_bf16 v[164:167], v[44:47], a[200:203], v[164:167]\n"
    "v_mfma_f32_16x16x32_bf16 v[172:175], v[44:47], a[204:207], v[172:175]\n"
    "s_waitcnt vmcnt(12) lgkmcnt(3)\n"
    "v_mfma_f32_16x16x32_bf16 v[48:51], v[16:19], a[208:211], v[48:51]\n"
    "ds_read_b128 v[36:39], v3 offset:2304\n"
    "v_mfma_f32_16x16x32_bf16 v[56:59], v[16:19], a[212:215], v[56:59]\n"
    "v_mfma_f32_16x16x32_bf16 v[64:67], v[16:19], a[216:219], v[64:67]\n"
    "buffer_load_dwordx4 v0, s[4:7], s56 offen offset:2048 lds\n"
    "v_mfma_f32_16x16x32_bf16 v[72:75], v[16:19], a[220:223], v[72:75]\n"
    "v_mfma_f32_16x16x32_bf16 v[52:55], v[20:23], a[208:211], v[52:55]\n"
    "ds_read_b128 v[40:43], v3 offset:38912\n"
    "v_mfma_f32_16x16x32_bf16 v[60:63], v[20:23], a[212:215], v[60:63]\n"
    "v_mfma_f32_16x16x32_bf16 v[68:71], v[20:23], a[216:219], v[68:71]\n"
    "v_mfma_f32_16x16x32_bf16 v[76:79], v[20:23], a[220:223], v[76:79]\n"
    "buffer_load_dwordx4 v0, s[4:7], s56 offen offset:3072 lds\n"
    "s_waitcnt lgkmcnt(3)\n"
    "v_mfma_f32_16x16x32_bf16 v[80:83], v[24:27], a[208:211], v[80:83]\n"
    "ds_read_b128 v[44:47], v3 offset:39168\n"
    "v_mfma_f32_16x16x32_bf16 v[88:91], v[24:27], a[212:215], v[88:91]\n"
    "v_mfma_f32_16x16x32_bf16 v[96:99], v[24:27], a[216:219], v[96:99]\n"
    "v_mfma_f32_16x16x32_bf16 v[104:107], v[24:27], a[220:223], v[104:107]\n"
    "v_mfma_f32_16x16x32_bf16 v[84:87], v[28:31], a[208:211], v[84:87]\n"
    "s_waitcnt vmcnt(12)\n"
    "s_barrier\n"
    "ds_read_b128 v[16:19], v1 offset:28672\n"
    "v_mfma_f32_16x16x32_bf16 v[92:95], v[28:31], a[212:215], v[92:95]\n"
    "s_add_u32 s0, s0, 4096\n"
    "buffer_load_dwordx4 v[224:227], v0, s[4:7], s0 offen offset:0\n"
    "v_mfma_f32_16x16x32_bf16 v[100:103], v[28:31], a[216:219], v[100:103]\n"
    "v_mfma_f32_16x16x32_bf16 v[108:111], v[28:31], a[220:223], v[108:111]\n"
    "s_waitcnt lgkmcnt(3)\n"
    "v_mfma_f32_16x16x32_bf16 v[112:115], v[32:35], a[208:211], v[112:115]\n"
    "ds_read_b128 v[20:23], v1 offset:28928\n"
    "v_mfma_f32_16x16x32_bf16 v[120:123], v[32:35], a[212:215], v[120:123]\n"
    "s_add_u32 s1, s1, 4096\n"
    "buffer_load_dwordx4 v[228:231], v0, s[4:7], s1 offen offset:0\n"
    "v_mfma_f32_16x16x32_bf16 v[128:131], v[32:35], a[216:219], v[128:131]\n"
    "v_mfma_f32_16x16x32_bf16 v[136:139], v[32:35], a[220:223], v[136:139]\n"
    "v_mfma_f32_16x16x32_bf16 v[116:119], v[36:39], a[208:211], v[116:119]\n"
    "ds_read_b128 v[24:27], v2 offset:16384\n"
    "v_mfma_f32_16x16x32_bf16 v[124:127], v[36:39], a[212:215], v[124:127]\n"
    "v_mfma_f32_16x16x32_bf16 v[132:135], v[36:39], a[216:219], v[132:135]\n"
    "s_add_u32 s3, s3, 4096\n"
    "buffer_load_dwordx4 v[232:235], v0, s[4:7], s3 offen offset:0\n"
    "v_mfma_f32_16x16x32_bf16 v[140:143], v[36:39], a[220:223], v[140:143]\n"
    "s_waitcnt lgkmcnt(3)\n"
    "v_mfma_f32_16x16x32_bf16 v[144:147], v[40:43], a[208:211], v[144:147]\n"
    "ds_read_b128 v[28:31], v2 offset:16640\n"
    "v_mfma_f32_16x16x32_bf16 v[152:155], v[40:43], a[212:215], v[152:155]\n"
    "v_mfma_f32_16x16x32_bf16 v[160:163], v[40:43], a[216:219], v[160:163]\n"
    "v_mfma_f32_16x16x32_bf16 v[168:171], v[40:43], a[220:223], v[168:171]\n"
    "s_add_u32 s16, s16, 4096\n"
    "buffer_load_dwordx4 v[236:239], v0, s[4:7], s16 offen offset:0\n"
    "v_mfma_f32_16x16x32_bf16 v[148:151], v[44:47], a[208:211], v[148:151]\n"
    "ds_read_b128 v[32:35], v3 offset:4096\n"
    "v_mfma_f32_16x16x32_bf16 v[156:159], v[44:47], a[212:215], v[156:159]\n"
    "v_mfma_f32_16x16x32_bf16 v[164:167], v[44:47], a[216:219], v[164:167]\n"
    "v_mfma_f32_16x16x32_bf16 v[172:175], v[44:47], a[220:223], v[172:175]\n"
    "s_waitcnt vmcnt(12) lgkmcnt(3)\n"
    "v_mfma_f32_16x16x32_bf16 v[48:51], v[16:19], a[224:227], v[48:51]\n"
    "ds_read_b128 v[36:39], v3 offset:4352\n"
    "v_mfma_f32_16x16x32_bf16 v[56:59], v[16:19], a[228:231], v[56:59]\n"
    "v_mfma_f32_16x16x32_bf16 v[64:67], v[16:19], a[232:235], v[64:67]\n"
    "buffer_load_dwordx4 v[240:243], v0, s[4:7], s0 offen offset:1024\n"
    "v_mfma_f32_16x16x32_bf16 v[72:75], v[16:19], a[236:239], v[72:75]\n"
    "v_mfma_f32_16x16x32_bf16 v[52:55], v[20:23], a[224:227], v[52:55]\n"
    "ds_read_b128 v[40:43], v3 offset:40960\n"
    "v_mfma_f32_16x16x32_bf16 v[60:63], v[20:23], a[228:231], v[60:63]\n"
    "v_mfma_f32_16x16x32_bf16 v[68:71], v[20:23], a[232:235], v[68:71]\n"
    "v_mfma_f32_16x16x32_bf16 v[76:79], v[20:23], a[236:239], v[76:79]\n"
    "buffer_load_dwordx4 v[244:247], v0, s[4:7], s1 offen offset:1024\n"
    "s_waitcnt lgkmcnt(3)\n"
    "v_mfma_f32_16x16x32_bf16 v[80:83], v[24:27], a[224:227], v[80:83]\n"
    "ds_read_b128 v[44:47], v3 offset:41216\n"
    "v_mfma_f32_16x16x32_bf16 v[88:91], v[24:27], a[228:231], v[88:91]\n"
    "v_mfma_f32_16x16x32_bf16 v[96:99], v[24:27], a[232:235], v[96:99]\n"
    "v_mfma_f32_16x16x32_bf16 v[104:107], v[24:27], a[236:239], v[104:107]\n"
    "v_mfma_f32_16x16x32_bf16 v[84:87], v[28:31], a[224:227], v[84:87]\n"
    "ds_read_b128 v[16:19], v1 offset:30720\n"
    "v_mfma_f32_16x16x32_bf16 v[92:95], v[28:31], a[228:231], v[92:95]\n"
    "buffer_load_dwordx4 v[248:251], v0, s[4:7], s3 offen offset:1024\n"
    "v_mfma_f32_16x16x32_bf16 v[100:103], v[28:31], a[232:235], v[100:103]\n"
    "v_mfma_f32_16x16x32_bf16 v[108:111], v[28:31], a[236:239], v[108:111]\n"
    "s_waitcnt lgkmcnt(3)\n"
    "v_mfma_f32_16x16x32_bf16 v[112:115], v[32:35], a[224:227], v[112:115]\n"
    "ds_read_b128 v[20:23], v1 offset:30976\n"
    "v_mfma_f32_16x16x32_bf16 v[120:123], v[32:35], a[228:231], v[120:123]\n"
    "buffer_load_dwordx4 v[252:255], v0, s[4:7], s16 offen offset:1024\n"
    "v_mfma_f32_16x16x32_bf16 v[128:131], v[32:35], a[232:235], v[128:131]\n"
    "v_mfma_f32_16x16x32_bf16 v[136:139], v[32:35], a[236:239], v[136:139]\n"
    "v_mfma_f32_16x16x32_bf16 v[116:119], v[36:39], a[224:227], v[116:119]\n"
    "ds_read_b128 v[24:27], v2 offset:18432\n"
    "v_mfma_f32_16x16x32_bf16 v[124:127], v[36:39], a[228:231], v[124:127]\n"
    "v_mfma_f32_16x16x32_bf16 v[132:135], v[36:39], a[232:235], v[132:135]\n"
    "s_add_u32 m0, s17, 0\n"
    "s_add_u32 s56, s24, 1179648\n"
    "buffer_load_dwordx4 v0, s[4:7], s56 offen offset:0 lds\n"
    "v_mfma_f32_16x16x32_bf16 v[140:143], v[36:39], a[236:239], v[140:143]\n"
    "s_waitcnt lgkmcnt(3)\n"
    "v_mfma_f32_16x16x32_bf16 v[144:147], v[40:43], a[224:227], v[144:147]\n"
    "ds_read_b128 v[28:31], v2 offset:18688\n"
    "v_mfma_f32_16x16x32_bf16 v[152:155], v[40:43], a[228:231], v[152:155]\n"
    "v_mfma_f32_16x16x32_bf16 v[160:163], v[40:43], a[232:235], v[160:163]\n"
    "v_mfma_f32_16x16x32_bf16 v[168:171], v[40:43], a[236:239], v[168:171]\n"
    "buffer_load_dwordx4 v0, s[4:7], s56 offen offset:1024 lds\n"
    "v_mfma_f32_16x16x32_bf16 v[148:151], v[44:47], a[224:227], v[148:151]\n"
    "ds_read_b128 v[32:35], v3 offset:6144\n"
    "v_mfma_f32_16x16x32_bf16 v[156:159], v[44:47], a[228:231], v[156:159]\n"
    "v_mfma_f32_16x16x32_bf16 v[164:167], v[44:47], a[232:235], v[164:167]\n"
    "v_mfma_f32_16x16x32_bf16 v[172:175], v[44:47], a[236:239], v[172:175]\n"
    "s_waitcnt vmcnt(12) lgkmcnt(3)\n"
    "v_mfma_f32_16x16x32_bf16 v[48:51], v[16:19], a[240:243], v[48:51]\n"
    "ds_read_b128 v[36:39], v3 offset:6400\n"
    "v_mfma_f32_16x16x32_bf16 v[56:59], v[16:19], a[244:247], v[56:59]\n"
    "v_mfma_f32_16x16x32_bf16 v[64:67], v[16:19], a[248:251], v[64:67]\n"
    "buffer_load_dwordx4 v0, s[4:7], s56 offen offset:2048 lds\n"
    "v_mfma_f32_16x16x32_bf16 v[72:75], v[16:19], a[252:255], v[72:75]\n"
    "v_mfma_f32_16x16x32_bf16 v[52:55], v[20:23], a[240:243], v[52:55]\n"
    "ds_read_b128 v[40:43], v3 offset:43008\n"
    "v_mfma_f32_16x16x32_bf16 v[60:63], v[20:23], a[244:247], v[60:63]\n"
    "v_mfma_f32_16x16x32_bf16 v[68:71], v[20:23], a[248:251], v[68:71]\n"
    "v_mfma_f32_16x16x32_bf16 v[76:79], v[20:23], a[252:255], v[76:79]\n"
    "buffer_load_dwordx4 v0, s[4:7], s56 offen offset:3072 lds\n"
    "s_waitcnt lgkmcnt(3)\n"
    "v_mfma_f32_16x16x32_bf16 v[80:83], v[24:27], a[240:243], v[80:83]\n"
    "ds_read_b128 v[44:47], v3 offset:43264\n"
    "v_mfma_f32_16x16x32_bf16 v[88:91], v[24:27], a[244:247], v[88:91]\n"
    "v_mfma_f32_16x16x32_bf16 v[96:99], v[24:27], a[248:251], v[96:99]\n"
    "v_mfma_f32_16x16x32_bf16 v[104:107], v[24:27], a[252:255], v[104:107]\n"
    "v_mfma_f32_16x16x32_bf16 v[84:87], v[28:31], a[240:243], v[84:87]\n"
    "s_waitcnt vmcnt(12)\n"
    "s_barrier\n"
    "ds_read_b128 v[16:19], v1 offset:32768\n"
    "v_mfma_f32_16x16x32_bf16 v[92:95], v[28:31], a[244:247], v[92:95]\n"
    "s_add_u32 m0, s17, 4096\n"
    "s_add_u32 s56, s24, 1183744\n"
    "buffer_load_dwordx4 v0, s[4:7], s56 offen offset:0 lds\n"
    "v_mfma_f32_16x16x32_bf16 v[100:103], v[28:31], a[248:251], v[100:103]\n"
    "v_mfma_f32_16x16x32_bf16 v[108:111], v[28:31], a[252:255], v[108:111]\n"
    "s_waitcnt lgkmcnt(3)\n"
    "v_mfma_f32_16x16x32_bf16 v[112:115], v[32:35], a[240:243], v[112:115]\n"
    "ds_read_b128 v[20:23], v1 offset:33024\n"
    "v_mfma_f32_16x16x32_bf16 v[120:123], v[32:35], a[244:247], v[120:123]\n"
    "buffer_load_dwordx4 v0, s[4:7], s56 offen offset:1024 lds\n"
    "v_mfma_f32_16x16x32_bf16 v[128:131], v[32:35], a[248:251], v[128:131]\n"
    "v_mfma_f32_16x16x32_bf16 v[136:139], v[32:35], a[252:255], v[136:139]\n"
    "v_mfma_f32_16x16x32_bf16 v[116:119], v[36:39], a[240:243], v[116:119]\n"
    "ds_read_b128 v[24:27], v2 offset:20480\n"
    "v_mfma_f32_16x16x32_bf16 v[124:127], v[36:39], a[244:247], v[124:127]\n"
    "v_mfma_f32_16x16x32_bf16 v[132:135], v[36:39], a[248:251], v[132:135]\n"
    "buffer_load_dwordx4 v0, s[4:7], s56 offen offset:2048 lds\n"
    "v_mfma_f32_16x16x32_bf16 v[140:143], v[36:39], a[252:255], v[140:143]\n"
    "s_waitcnt lgkmcnt(3)\n"
    "v_mfma_f32_16x16x32_bf16 v[144:147], v[40:43], a[240:243], v[144:147]\n"
    "ds_read_b128 v[28:31], v2 offset:20736\n"
    "v_mfma_f32_16x16x32_bf16 v[152:155], v[40:43], a[244:247], v[152:155]\n"
    "v_mfma_f32_16x16x32_bf16 v[160:163], v[40:43], a[248:251], v[160:163]\n"
    "v_mfma_f32_16x16x32_bf16 v[168:171], v[40:43], a[252:255], v[168:171]\n"
    "buffer_load_dwordx4 v0, s[4:7], s56 offen offset:3072 lds\n"
    "v_mfma_f32_16x16x32_bf16 v[148:151], v[44:47], a[240:243], v[148:151]\n"
    "ds_read_b128 v[32:35], v3 offset:8192\n"
    "v_mfma_f32_16x16x32_bf16 v[156:159], v[44:47], a[244:247], v[156:159]\n"
    "v_mfma_f32_16x16x32_bf16 v[164:167], v[44:47], a[248:251], v[164:167]\n"
    "v_mfma_f32_16x16x32_bf16 v[172:175], v[44:47], a[252:255], v[172:175]\n"
    "s_waitcnt vmcnt(12) lgkmcnt(3)\n"
    "v_mfma_f32_16x16x32_bf16 v[48:51], v[16:19], v[224:227], v[48:51]\n"
    "ds_read_b128 v[36:39], v3 offset:8448\n"
    "v_mfma_f32_16x16x32_bf16 v[56:59], v[16:19], v[228:231], v[56:59]\n"
    "v_mfma_f32_16x16x32_bf16 v[64:67], v[16:19], v[232:235], v[64:67]\n"
    "s_add_u32 m0, s17, 8192\n"
    "s_add_u32 s56, s24, 1187840\n"
    "buffer_load_dwordx4 v0, s[4:7], s56 offen offset:0 lds\n"
    "v_mfma_f32_16x16x32_bf16 v[72:75], v[16:19], v[236:239], v[72:75]\n"
    "v_mfma_f32_16x16x32_bf16 v[52:55], v[20:23], v[224:227], v[52:55]\n"
    "ds_read_b128 v[40:43], v3 offset:45056\n"
    "v_mfma_f32_16x16x32_bf16 v[60:63], v[20:23], v[228:231], v[60:63]\n"
    "v_mfma_f32_16x16x32_bf16 v[68:71], v[20:23], v[232:235], v[68:71]\n"
    "v_mfma_f32_16x16x32_bf16 v[76:79], v[20:23], v[236:239], v[76:79]\n"
    "s_waitcnt lgkmcnt(3)\n"
    "v_mfma_f32_16x16x32_bf16 v[80:83], v[24:27], v[224:227], v[80:83]\n"
    "LPK2:\n"
    "s_cbranch_execz LPK6\n"
    "ds_read_b128 v[44:47], v3 offset:45312\n"
    "v_mfma_f32_16x16x32_bf16 v[88:91], v[24:27], v[228:231], v[88:91]\n"
    "v_mfma_f32_16x16x32_bf16 v[96:99], v[24:27], v[232:235], v[96:99]\n"
    "v_mfma_f32_16x16x32_bf16 v[104:107], v[24:27], v[236:239], v[104:107]\n"
    "v_mfma_f32_16x16x32_bf16 v[84:87], v[28:31], v[224:227], v[84:87]\n"
    "ds_read_b128 v[16:19], v1 offset:34816\n"
    "v_mfma_f32_16x16x32_bf16 v[92:95], v[28:31], v[228:231], v[92:95]\n"
    "v_mfma_f32_16x16x32_bf16 v[100:103], v[28:31], v[232:235], v[100:103]\n"
    "v_mfma_f32_16x16x32_bf16 v[108:111], v[28:31], v[236:239], v[108:111]\n"
    "s_waitcnt lgkmcnt(3)\n"
    "v_mfma_f32_16x16x32_bf16 v[112:115], v[32:35], v[224:227], v[112:115]\n"
    "ds_read_b128 v[20:23], v1 offset:35072\n"
    "v_mfma_f32_16x16x32_bf16 v[120:123], v[32:35], v[228:231], v[120:123]\n"
    "v_mfma_f32_16x16x32_bf16 v[128:131], v[32:35], v[232:235], v[128:131]\n"
    "v_mfma_f32_16x16x32_bf16 v[136:139], v[32:35], v[236:239], v[136:139]\n"
    "v_mfma_f32_16x16x32_bf16 v[116:119], v[36:39], v[224:227], v[116:119]\n"
    "ds_read_b128 v[24:27], v2 offset:22528\n"
    "v_mfma_f32_16x16x32_bf16 v[124:127], v[36:39], v[228:231], v[124:127]\n"
    "v_mfma_f32_16x16x32_bf16 v[132:135], v[36:39], v[232:235], v[132:135]\n"
    "v_mfma_f32_16x16x32_bf16 v[140:143], v[36:39], v[236:239], v[140:143]\n"
    "s_waitcnt lgkmcnt(3)\n"
    "v_mfma_f32_16x16x32_bf16 v[144:147], v[40:43], v[224:227], v[144:147]\n"
    "ds_read_b128 v[28:31], v2 offset:22784\n"
    "v_mfma_f32_16x16x32_bf16 v[152:155], v[40:43], v[228:231], v[152:155]\n"
    "v_mfma_f32_16x16x32_bf16 v[160:163], v[40:43], v[232:235], v[160:163]\n"
    "v_mfma_f32_16x16x32_bf16 v[168:171], v[40:43], v[236:239], v[168:171]\n"
    "v_mfma_f32_16x16x32_bf16 v[148:151], v[44:47], v[224:227], v[148:151]\n"
    "ds_read_b128 v[32:35], v3 offset:10240\n"
    "v_mfma_f32_16x16x32_bf16 v[156:159], v[44:47], v[228:231], v[156:159]\n"
    "v_mfma_f32_16x16x32_bf16 v[164:167], v[44:47], v[232:235], v[164:167]\n"
    "v_mfma_f32_16x16x32_bf16 v[172:175], v[44:47], v[236:239], v[172:175]\n"
    "s_waitcnt vmcnt(9) lgkmcnt(3)\n"
    "v_mfma_f32_16x16x32_bf16 v[48:51], v[16:19], v[240:243], v[48:51]\n"
    "ds_read_b128 v[36:39], v3 offset:10496\n"
    "v_mfma_f32_16x16x32_bf16 v[56:59], v[16:19], v[244:247], v[56:59]\n"
    "v_mfma_f32_16x16x32_bf16 v[64:67], v[16:19], v[248:251], v[64:67]\n"
    "v_mfma_f32_16x16x32_bf16 v[72:75], v[16:19], v[252:255], v[72:75]\n"
    "v_mfma_f32_16x16x32_bf16 v[52:55], v[20:23], v[240:243], v[52:55]\n"
    "ds_read_b128 v[40:43], v3 offset:47104\n"
    "v_mfma_f32_16x16x32_bf16 v[60:63], v[20:23], v[244:247], v[60:63]\n"
    "v_mfma_f32_16x16x32_bf16 v[68:71], v[20:23], v[248:251], v[68:71]\n"
    "v_mfma_f32_16x16x32_bf16 v[76:79], v[20:23], v[252:255], v[76:79]\n"
    "s_waitcnt lgkmcnt(3)\n"
    "v_mfma_f32_16x16x32_bf16 v[80:83], v[24:27], v[240:243], v[80:83]\n"
    "ds_read_b128 v[44:47], v3 offset:47360\n"
    "v_mfma_f32_16x16x32_bf16 v[88:91], v[24:27], v[244:247], v[88:91]\n"
    "v_mfma_f32_16x16x32_bf16 v[96:99], v[24:27], v[248:251], v[96:99]\n"
    "v_mfma_f32_16x16x32_bf16 v[104:107], v[24:27], v[252:255], v[104:107]\n"
    "v_mfma_f32_16x16x32_bf16 v[84:87], v[28:31], v[240:243], v[84:87]\n"
    "s_waitcnt vmcnt(0)\n"
    "s_barrier\n"
    "ds_read_b128 v[16:19], v1 offset:0\n"
    "v_mfma_f32_16x16x32_bf16 v[92:95], v[28:31], v[244:247], v[92:95]\n"
    "v_mul_f32 v52, v48, v52\n"
    "v_mul_f32 v53, v49, v53\n"
    "v_mfma_f32_16x16x32_bf16 v[100:103], v[28:31], v[248:251], v[100:103]\n"
    "v_exp_f32 v48, -v48\n"
    "v_mfma_f32_16x16x32_bf16 v[108:111], v[28:31], v[252:255], v[108:111]\n"
    "v_mul_f32 v54, v50, v54\n"
    "s_waitcnt lgkmcnt(3)\n"
    "v_mfma_f32_16x16x32_bf16 v[112:115], v[32:35], v[240:243], v[112:115]\n"
    "ds_read_b128 v[20:23], v1 offset:256\n"
    "v_mul_f32 v55, v51, v55\n"
    "v_mfma_f32_16x16x32_bf16 v[120:123], v[32:35], v[244:247], v[120:123]\n"
    "v_exp_f32 v49, -v49\n"
    "v_mfma_f32_16x16x32_bf16 v[128:131], v[32:35], v[248:251], v[128:131]\n"
    "v_exp_f32 v50, -v50\n"
    "v_mfma_f32_16x16x32_bf16 v[136:139], v[32:35], v[252:255], v[136:139]\n"
    "v_exp_f32 v51, -v51\n"
    "v_mfma_f32_16x16x32_bf16 v[116:119], v[36:39], v[240:243], v[116:119]\n"
    "ds_read_b128 v[24:27], v1 offset:2048\n"
    "v_fma_f32 v48, v48, s36, s36\n"
    "v_mfma_f32_16x16x32_bf16 v[124:127], v[36:39], v[244:247], v[124:127]\n"
    "v_rcp_f32 v48, v48\n"
    "v_mfma_f32_16x16x32_bf16 v[132:135], v[36:39], v[248:251], v[132:135]\n"
    "v_fma_f32 v49, v49, s36, s36\n"
    "v_fma_f32 v50, v50, s36, s36\n"
    "v_mfma_f32_16x16x32_bf16 v[140:143], v[36:39], v[252:255], v[140:143]\n"
    "v_fma_f32 v51, v51, s36, s36\n"
    "s_waitcnt lgkmcnt(3)\n"
    "v_mfma_f32_16x16x32_bf16 v[144:147], v[40:43], v[240:243], v[144:147]\n"
    "ds_read_b128 v[28:31], v1 offset:2304\n"
    "v_mul_f32 v52, v48, v52\n"
    "v_mfma_f32_16x16x32_bf16 v[152:155], v[40:43], v[244:247], v[152:155]\n"
    "v_rcp_f32 v49, v49\n"
    "v_mfma_f32_16x16x32_bf16 v[160:163], v[40:43], v[248:251], v[160:163]\n"
    "v_rcp_f32 v50, v50\n"
    "v_mfma_f32_16x16x32_bf16 v[168:171], v[40:43], v[252:255], v[168:171]\n"
    "v_rcp_f32 v51, v51\n"
    "v_mfma_f32_16x16x32_bf16 v[148:151], v[44:47], v[240:243], v[148:151]\n"
    "ds_read_b128 v[32:35], v1 offset:4096\n"
    "v_mul_f32 v53, v49, v53\n"
    "v_mfma_f32_16x16x32_bf16 v[156:159], v[44:47], v[244:247], v[156:159]\n"
    "v_mul_f32 v54, v50, v54\n"
    "v_mul_f32 v55, v51, v55\n"
    "v_mfma_f32_16x16x32_bf16 v[164:167], v[44:47], v[248:251], v[164:167]\n"
    "v_cvt_pk_bf16_f32 v9, v54, v55\n"
    "v_cvt_pk_bf16_f32 v8, v52, v53\n"
    "v_mfma_f32_16x16x32_bf16 v[172:175], v[44:47], v[252:255], v[172:175]\n"
    "v_mul_f32 v60, v56, v60\n"
    "s_waitcnt lgkmcnt(3)\n"
    "v_mfma_f32_16x16x32_bf16 v[176:179], v[16:19], a[0:3], 0\n"
    "ds_read_b128 v[36:39], v1 offset:4352\n"
    "v_mul_f32 v61, v57, v61\n"
    "v_mfma_f32_16x16x32_bf16 v[184:187], v[16:19], a[4:7], 0\n"
    "v_exp_f32 v56, -v56\n"
    "v_mfma_f32_16x16x32_bf16 v[192:195], v[16:19], a[8:11], 0\n"
    "v_exp_f32 v57, -v57\n"
    "v_mfma_f32_16x16x32_bf16 v[200:203], v[16:19], a[12:15], 0\n"
    "v_mul_f32 v62, v58, v62\n"
    "v_mul_f32 v63, v59, v63\n"
    "v_mfma_f32_16x16x32_bf16 v[180:183], v[20:23], a[0:3], 0\n"
    "ds_read_b128 v[40:43], v1 offset:6144\n"
    "v_fma_f32 v56, v56, s36, s36\n"
    "v_mfma_f32_16x16x32_bf16 v[188:191], v[20:23], a[4:7], 0\n"
    "v_exp_f32 v58, -v58\n"
    "v_mfma_f32_16x16x32_bf16 v[196:199], v[20:23], a[8:11], 0\n"
    "v_exp_f32 v59, -v59\n"
    "v_mfma_f32_16x16x32_bf16 v[204:207], v[20:23], a[12:15], 0\n"
    "v_fma_f32 v57, v57, s36, s36\n"
    "s_waitcnt lgkmcnt(3)\n"
    "v_mfma_f32_16x16x32_bf16 v[176:179], v[24:27], a[16:19], v[176:179]\n"
    "ds_read_b128 v[44:47], v1 offset:6400\n"
    "v_fma_f32 v58, v58, s36, s36\n"
    "v_mfma_f32_16x16x32_bf16 v[184:187], v[24:27], a[20:23], v[184:187]\n"
    "s_add_u32 m0, s17, 9216\n"
    "s_add_u32 s56, s19, 1179648\n"
    "buffer_load_dwordx4 v0, s[4:7], s56 offen offset:0 lds\n"
    "v_mfma_f32_16x16x32_bf16 v[192:195], v[24:27], a[24:27], v[192:195]\n"
    "v_rcp_f32 v56, v56\n"
    "v_mfma_f32_16x16x32_bf16 v[200:203], v[24:27], a[28:31], v[200:203]\n"
    "v_rcp_f32 v57, v57\n"
    "v_mfma_f32_16x16x32_bf16 v[180:183], v[28:31], a[16:19], v[180:183]\n"
    "ds_read_b128 v[16:19], v4 offset:8192\n"
    "v_fma_f32 v59, v59, s36, s36\n"
    "v_mfma_f32_16x16x32_bf16 v[188:191], v[28:31], a[20:23], v[188:191]\n"
    "v_rcp_f32 v58, v58\n"
    "v_mfma_f32_16x16x32_bf16 v[196:199], v[28:31], a[24:27], v[196:199]\n"
    "v_rcp_f32 v59, v59\n"
    "v_mfma_f32_16x16x32_bf16 v[204:207], v[28:31], a[28:31], v[204:207]\n"
    "v_mul_f32 v60, v56, v60\n"
    "s_waitcnt lgkmcnt(3)\n"
    "v_mfma_f32_16x16x32_bf16 v[176:179], v[32:35], a[32:35], v[176:179]\n"
    "ds_read_b128 v[20:23], v4 offset:8448\n"
    "v_mul_f32 v61, v57, v61\n"
    "v_mfma_f32_16x16x32_bf16 v[184:187], v[32:35], a[36:39], v[184:187]\n"
    "buffer_load_dwordx4 v0, s[4:7], s56 offen offset:1024 lds\n"
    "v_mul_f32 v62, v58, v62\n"
    "v_mfma_f32_16x16x32_bf16 v[192:195], v[32:35], a[40:43], v[192:195]\n"
    "v_mul_f32 v63, v59, v63\n"
    "v_cvt_pk_bf16_f32 v11, v62, v63\n"
    "v_mfma_f32_16x16x32_bf16 v[200:203], v[32:35], a[44:47], v[200:203]\n"
    "v_cvt_pk_bf16_f32 v10, v60, v61\n"
    "v_permlane32_swap_b32 v9, v11\n"
    "v_mfma_f32_16x16x32_bf16 v[180:183], v[36:39], a[32:35], v[180:183]\n"
    "ds_read_b128 v[24:27], v1 offset:37888\n"
    "v_permlane32_swap_b32 v8, v10\n"
    "v_mfma_f32_16x16x32_bf16 v[188:191], v[36:39], a[36:39], v[188:191]\n"
    "buffer_store_dwordx4 v[8:11], v6, s[4:7], s23 offen sc1\n"
    "v_mul_f32 v68, v64, v68\n"
    "v_mfma_f32_16x16x32_bf16 v[196:199], v[36:39], a[40:43], v[196:199]\n"
    "v_exp_f32 v64, -v64\n"
    "v_mfma_f32_16x16x32_bf16 v[204:207], v[36:39], a[44:47], v[204:207]\n"
    "v_mul_f32 v69, v65, v69\n"
    "s_waitcnt lgkmcnt(3)\n"
    "v_mfma_f32_16x16x32_bf16 v[176:179], v[40:43], a[48:51], v[176:179]\n"
    "ds_read_b128 v[28:31], v1 offset:38144\n"
    "v_mul_f32 v70, v66, v70\n"
    "v_mfma_f32_16x16x32_bf16 v[184:187], v[40:43], a[52:55], v[184:187]\n"
    "buffer_load_dwordx4 v0, s[4:7], s56 offen offset:2048 lds\n"
    "v_mul_f32 v71, v67, v71\n"
    "v_mfma_f32_16x16x32_bf16 v[192:195], v[40:43], a[56:59], v[192:195]\n"
    "v_exp_f32 v65, -v65\n"
    "v_mfma_f32_16x16x32_bf16 v[200:203], v[40:43], a[60:63], v[200:203]\n"
    "v_exp_f32 v66, -v66\n"
    "v_mfma_f32_16x16x32_bf16 v[180:183], v[44:47], a[48:51], v[180:183]\n"
    "ds_read_b128 v[32:35], v1 offset:39936\n"
    "v_fma_f32 v64, v64, s36, s36\n"
    "v_mfma_f32_16x16x32_bf16 v[188:191], v[44:47], a[52:55], v[188:191]\n"
    "v_exp_f32 v67, -v67\n"
    "v_mfma_f32_16x16x32_bf16 v[196:199], v[44:47], a[56:59], v[196:199]\n"
    "v_rcp_f32 v64, v64\n"
    "v_mfma_f32_16x16x32_bf16 v[204:207], v[44:47], a[60:63], v[204:207]\n"
    "v_fma_f32 v65, v65, s36, s36\n"
    "s_waitcnt lgkmcnt(3)\n"
    "v_mfma_f32_16x16x32_bf16 v[176:179], v[16:19], a[64:67], v[176:179]\n"
    "ds_read_b128 v[36:39], v1 offset:40192\n"
    "v_fma_f32 v66, v66, s36, s36\n"
    "v_mfma_f32_16x16x32_bf16 v[184:187], v[16:19], a[68:71], v[184:187]\n"
    "buffer_load_dwordx4 v0, s[4:7], s56 offen offset:3072 lds\n"
    "v_fma_f32 v67, v67, s36, s36\n"
    "v_mfma_f32_16x16x32_bf16 v[192:195], v[16:19], a[72:75], v[192:195]\n"
    "v_rcp_f32 v65, v65\n"
    "v_mfma_f32_16x16x32_bf16 v[200:203], v[16:19], a[76:79], v[200:203]\n"
    "v_rcp_f32 v66, v66\n"
    "v_mfma_f32_16x16x32_bf16 v[180:183], v[20:23], a[64:67], v[180:183]\n"
    "ds_read_b128 v[40:43], v1 offset:41984\n"
    "v_mul_f32 v68, v64, v68\n"
    "v_mfma_f32_16x16x32_bf16 v[188:191], v[20:23], a[68:71], v[188:191]\n"
    "v_rcp_f32 v67, v67\n"
    "v_mfma_f32_16x16x32_bf16 v[196:199], v[20:23], a[72:75], v[196:199]\n"
    "v_mul_f32 v69, v65, v69\n"
    "v_mul_f32 v70, v66, v70\n"
    "v_mfma_f32_16x16x32_bf16 v[204:207], v[20:23], a[76:79], v[204:207]\n"
    "v_mul_f32 v71, v67, v71\n"
    "s_waitcnt lgkmcnt(3)\n"
    "v_mfma_f32_16x16x32_bf16 v[176:179], v[24:27], a[80:83], v[176:179]\n"
    "ds_read_b128 v[44:47], v1 offset:42240\n"
    "v_cvt_pk_bf16_f32 v13, v70, v71\n"
    "v_mfma_f32_16x16x32_bf16 v[184:187], v[24:27], a[84:87], v[184:187]\n"
    "s_add_u32 m0, s17, 13312\n"
    "s_add_u32 s56, s19, 1183744\n"
    "buffer_load_dwordx4 v0, s[4:7], s56 offen offset:0 lds\n"
    "v_mfma_f32_16x16x32_bf16 v[192:195], v[24:27], a[88:91], v[192:195]\n"
    "v_cvt_pk_bf16_f32 v12, v68, v69\n"
    "v_mul_f32 v76, v72, v76\n"
    "v_mfma_f32_16x16x32_bf16 v[200:203], v[24:27], a[92:95], v[200:203]\n"
    "v_exp_f32 v72, -v72\n"
    "v_mfma_f32_16x16x32_bf16 v[180:183], v[28:31], a[80:83], v[180:183]\n"
    "ds_read_b128 v[16:19], v1 offset:44032\n"
    "v_mul_f32 v77, v73, v77\n"
    "v_mfma_f32_16x16x32_bf16 v[188:191], v[28:31], a[84:87], v[188:191]\n"
    "v_exp_f32 v73, -v73\n"
    "v_mfma_f32_16x16x32_bf16 v[196:199], v[28:31], a[88:91], v[196:199]\n"
    "v_mul_f32 v78, v74, v78\n"
    "v_mul_f32 v79, v75, v79\n"
    "v_mfma_f32_16x16x32_bf16 v[204:207], v[28:31], a[92:95], v[204:207]\n"
    "v_fma_f32 v72, v72, s36, s36\n"
    "s_waitcnt lgkmcnt(3)\n"
    "v_mfma_f32_16x16x32_bf16 v[176:179], v[32:35], a[96:99], v[176:179]\n"
    "ds_read_b128 v[20:23], v1 offset:44288\n"
    "v_fma_f32 v73, v73, s36, s36\n"
    "v_mfma_f32_16x16x32_bf16 v[184:187], v[32:35], a[100:103], v[184:187]\n"
    "buffer_load_dwordx4 v0, s[4:7], s56 offen offset:1024 lds\n"
    "v_mul_f32 v84, v80, v84\n"
    "v_mfma_f32_16x16x32_bf16 v[192:195], v[32:35], a[104:107], v[192:195]\n"
    "v_exp_f32 v74, -v74\n"
    "v_mfma_f32_16x16x32_bf16 v[200:203], v[32:35], a[108:111], v[200:203]\n"
    "v_exp_f32 v75, -v75\n"
    "v_mfma_f32_16x16x32_bf16 v[180:183], v[36:39], a[96:99], v[180:183]\n"
    "ds_read_b128 v[24:27], v2 offset:24576\n"
    "v_fma_f32 v74, v74, s36, s36\n"
    "v_mfma_f32_16x16x32_bf16 v[188:191], v[36:39], a[100:103], v[188:191]\n"
    "v_rcp_f32 v72, v72\n"
    "v_mfma_f32_16x16x32_bf16 v[196:199], v[36:39], a[104:107], v[196:199]\n"
    "v_rcp_f32 v73, v73\n"
    "v_mfma_f32_16x16x32_bf16 v[204:207], v[36:39], a[108:111], v[204:207]\n"
    "v_fma_f32 v75, v75, s36, s36\n"
    "s_waitcnt lgkmcnt(3)\n"
    "v_mfma_f32_16x16x32_bf16 v[176:179], v[40:43], a[112:115], v[176:179]\n"
    "ds_read_b128 v[28:31], v2 offset:24832\n"
    "v_mul_f32 v76, v72, v76\n"
    "v_mfma_f32_16x16x32_bf16 v[184:187], v[40:43], a[116:119], v[184:187]\n"
    "buffer_load_dwordx4 v0, s[4:7], s56 offen offset:2048 lds\n"
    "v_mul_f32 v77, v73, v77\n"
    "v_mfma_f32_16x16x32_bf16 v[192:195], v[40:43], a[120:123], v[192:195]\n"
    "v_rcp_f32 v74, v74\n"
    "v_mfma_f32_16x16x32_bf16 v[200:203], v[40:43], a[124:127], v[200:203]\n"
    "v_rcp_f32 v75, v75\n"
    "v_mfma_f32_16x16x32_bf16 v[180:183], v[44:47], a[112:115], v[180:183]\n"
    "ds_read_b128 v[32:35], v2 offset:26624\n"
    "v_mul_f32 v78, v74, v78\n"
    "v_mfma_f32_16x16x32_bf16 v[188:191], v[44:47], a[116:119], v[188:191]\n"
    "v_exp_f32 v80, -v80\n"
    "v_mfma_f32_16x16x32_bf16 v[196:199], v[44:47], a[120:123], v[196:199]\n"
    "v_mul_f32 v79, v75, v79\n"
    "v_cvt_pk_bf16_f32 v15, v78, v79\n"
    "v_mfma_f32_16x16x32_bf16 v[204:207], v[44:47], a[124:127], v[204:207]\n"
    "v_cvt_pk_bf16_f32 v14, v76, v77\n"
    "s_waitcnt lgkmcnt(3)\n"
    "v_mfma_f32_16x16x32_bf16 v[176:179], v[16:19], a[128:131], v[176:179]\n"
    "ds_read_b128 v[36:39], v2 offset:26880\n"
    "v_permlane32_swap_b32 v13, v15\n"
    "v_mfma_f32_16x16x32_bf16 v[184:187], v[16:19], a[132:135], v[184:187]\n"
    "buffer_load_dwordx4 v0, s[4:7], s56 offen offset:3072 lds\n"
    "v_permlane32_swap_b32 v12, v14\n"
    "v_mfma_f32_16x16x32_bf16 v[192:195], v[16:19], a[136:139], v[192:195]\n"
    "buffer_store_dwordx4 v[12:15], v7, s[4:7], s23 offen sc1\n"
    "v_mul_f32 v85, v81, v85\n"
    "v_mfma_f32_16x16x32_bf16 v[200:203], v[16:19], a[140:143], v[200:203]\n"
    "v_exp_f32 v81, -v81\n"
    "v_mfma_f32_16x16x32_bf16 v[180:183], v[20:23], a[128:131], v[180:183]\n"
    "ds_read_b128 v[40:43], v2 offset:28672\n"
    "v_mul_f32 v86, v82, v86\n"
    "v_mfma_f32_16x16x32_bf16 v[188:191], v[20:23], a[132:135], v[188:191]\n"
    "v_exp_f32 v82, -v82\n"
    "v_mfma_f32_16x16x32_bf16 v[196:199], v[20:23], a[136:139], v[196:199]\n"
    "v_mul_f32 v87, v83, v87\n"
    "v_fma_f32 v80, v80, s36, s36\n"
    "v_mfma_f32_16x16x32_bf16 v[204:207], v[20:23], a[140:143], v[204:207]\n"
    "v_fma_f32 v81, v81, s36, s36\n"
    "s_waitcnt lgkmcnt(3)\n"
    "v_mfma_f32_16x16x32_bf16 v[176:179], v[24:27], a[144:147], v[176:179]\n"
    "ds_read_b128 v[44:47], v2 offset:28928\n"
    "v_fma_f32 v82, v82, s36, s36\n"
    "v_mfma_f32_16x16x32_bf16 v[184:187], v[24:27], a[148:151], v[184:187]\n"
    "s_add_u32 m0, s17, 17408\n"
    "s_add_u32 s56, s19, 1187840\n"
    "buffer_load_dwordx4 v0, s[4:7], s56 offen offset:0 lds\n"
    "v_mfma_f32_16x16x32_bf16 v[192:195], v[24:27], a[152:155], v[192:195]\n"
    "v_exp_f32 v83, -v83\n"
    "v_mfma_f32_16x16x32_bf16 v[200:203], v[24:27], a[156:159], v[200:203]\n"
    "v_rcp_f32 v80, v80\n"
    "v_mfma_f32_16x16x32_bf16 v[180:183], v[28:31], a[144:147], v[180:183]\n"
    "ds_read_b128 v[16:19], v2 offset:30720\n"
    "v_fma_f32 v83, v83, s36, s36\n"
    "v_mfma_f32_16x16x32_bf16 v[188:191], v[28:31], a[148:151], v[188:191]\n"
    "v_rcp_f32 v81, v81\n"
    "v_mfma_f32_16x16x32_bf16 v[196:199], v[28:31], a[152:155], v[196:199]\n"
    "v_rcp_f32 v82, v82\n"
    "v_mfma_f32_16x16x32_bf16 v[204:207], v[28:31], a[156:159], v[204:207]\n"
    "v_mul_f32 v84, v80, v84\n"
    "s_waitcnt lgkmcnt(3)\n"
    "v_mfma_f32_16x16x32_bf16 v[176:179], v[32:35], a[160:163], v[176:179]\n"
    "ds_read_b128 v[20:23], v2 offset:30976\n"
    "v_mul_f32 v85, v81, v85\n"
    "v_mfma_f32_16x16x32_bf16 v[184:187], v[32:35], a[164:167], v[184:187]\n"
    "s_add_u32 m0, s17, 18432\n"
    "s_add_u32 s56, s24, 1769472\n"
    "buffer_load_dwordx4 v0, s[4:7], s56 offen offset:0 lds\n"
    "v_mfma_f32_16x16x32_bf16 v[192:195], v[32:35], a[168:171], v[192:195]\n"
    "v_rcp_f32 v83, v83\n"
    "v_mfma_f32_16x16x32_bf16 v[200:203], v[32:35], a[172:175], v[200:203]\n"
    "v_mul_f32 v86, v82, v86\n"
    "v_mul_f32 v87, v83, v87\n"
    "v_mfma_f32_16x16x32_bf16 v[180:183], v[36:39], a[160:163], v[180:183]\n"
    "ds_read_b128 v[24:27], v5 offset:8192\n"
    "v_cvt_pk_bf16_f32 v9, v86, v87\n"
    "v_mfma_f32_16x16x32_bf16 v[188:191], v[36:39], a[164:167], v[188:191]\n"
    "v_cvt_pk_bf16_f32 v8, v84, v85\n"
    "v_mul_f32 v92, v88, v92\n"
    "v_mfma_f32_16x16x32_bf16 v[196:199], v[36:39], a[168:171], v[196:199]\n"
    "v_exp_f32 v88, -v88\n"
    "v_mfma_f32_16x16x32_bf16 v[204:207], v[36:39], a[172:175], v[204:207]\n"
    "v_mul_f32 v93, v89, v93\n"
    "s_waitcnt lgkmcnt(3)\n"
    "v_mfma_f32_16x16x32_bf16 v[176:179], v[40:43], a[176:179], v[176:179]\n"
    "ds_read_b128 v[28:31], v5 offset:8448\n"
    "v_mul_f32 v94, v90, v94\n"
    "v_mfma_f32_16x16x32_bf16 v[184:187], v[40:43], a[180:183], v[184:187]\n"
    "buffer_load_dwordx4 v0, s[4:7], s56 offen offset:1024 lds\n"
    "v_mul_f32 v95, v91, v95\n"
    "v_mfma_f32_16x16x32_bf16 v[192:195], v[40:43], a[184:187], v[192:195]\n"
    "v_exp_f32 v89, -v89\n"
    "v_mfma_f32_16x16x32_bf16 v[200:203], v[40:43], a[188:191], v[200:203]\n"
    "v_exp_f32 v90, -v90\n"
    "v_mfma_f32_16x16x32_bf16 v[180:183], v[44:47], a[176:179], v[180:183]\n"
    "ds_read_b128 v[32:35], v3 offset:13312\n"
    "v_fma_f32 v88, v88, s36, s36\n"
    "v_mfma_f32_16x16x32_bf16 v[188:191], v[44:47], a[180:183], v[188:191]\n"
    "v_exp_f32 v91, -v91\n"
    "v_mfma_f32_16x16x32_bf16 v[196:199], v[44:47], a[184:187], v[196:199]\n"
    "v_rcp_f32 v88, v88\n"
    "v_mfma_f32_16x16x32_bf16 v[204:207], v[44:47], a[188:191], v[204:207]\n"
    "v_fma_f32 v89, v89, s36, s36\n"
    "s_waitcnt lgkmcnt(3)\n"
    "v_mfma_f32_16x16x32_bf16 v[176:179], v[16:19], a[192:195], v[176:179]\n"
    "ds_read_b128 v[36:39], v3 offset:13568\n"
    "v_fma_f32 v90, v90, s36, s36\n"
    "v_mfma_f32_16x16x32_bf16 v[184:187], v[16:19], a[196:199], v[184:187]\n"
    "buffer_load_dwordx4 v0, s[4:7], s56 offen offset:2048 lds\n"
    "v_fma_f32 v91, v91, s36, s36\n"
    "v_mfma_f32_16x16x32_bf16 v[192:195], v[16:19], a[200:203], v[192:195]\n"
    "v_rcp_f32 v89, v89\n"
    "v_mfma_f32_16x16x32_bf16 v[200:203], v[16:19], a[204:207], v[200:203]\n"
    "v_rcp_f32 v90, v90\n"
    "v_mfma_f32_16x16x32_bf16 v[180:183], v[20:23], a[192:195], v[180:183]\n"
    "ds_read_b128 v[40:43], v3 offset:15360\n"
    "v_mul_f32 v92, v88, v92\n"
    "v_mfma_f32_16x16x32_bf16 v[188:191], v[20:23], a[196:199], v[188:191]\n"
    "v_rcp_f32 v91, v91\n"
    "v_mfma_f32_16x16x32_bf16 v[196:199], v[20:23], a[200:203], v[196:199]\n"
    "v_mul_f32 v93, v89, v93\n"
    "v_mul_f32 v94, v90, v94\n"
    "v_mfma_f32_16x16x32_bf16 v[204:207], v[20:23], a[204:207], v[204:207]\n"
    "v_mul_f32 v95, v91, v95\n"
    "s_waitcnt lgkmcnt(3)\n"
    "v_mfma_f32_16x16x32_bf16 v[176:179], v[24:27], a[208:211], v[176:179]\n"
    "ds_read_b128 v[44:47], v3 offset:15616\n"
    "v_cvt_pk_bf16_f32 v11, v94, v95\n"
    "v_mfma_f32_16x16x32_bf16 v[184:187], v[24:27], a[212:215], v[184:187]\n"
    "buffer_load_dwordx4 v0, s[4:7], s56 offen offset:3072 lds\n"
    "v_cvt_pk_bf16_f32 v10, v92, v93\n"
    "v_mfma_f32_16x16x32_bf16 v[192:195], v[24:27], a[216:219], v[192:195]\n"
    "v_permlane32_swap_b32 v9, v11\n"
    "v_permlane32_swap_b32 v8, v10\n"
    "v_mfma_f32_16x16x32_bf16 v[200:203], v[24:27], a[220:223], v[200:203]\n"
    "buffer_store_dwordx4 v[8:11], v6, s[4:7], s25 offen sc1\n"
    "v_mul_f32 v100, v96, v100\n"
    "v_mfma_f32_16x16x32_bf16 v[180:183], v[28:31], a[208:211], v[180:183]\n"
    "ds_read_b128 v[16:19], v3 offset:17408\n"
    "v_mul_f32 v101, v97, v101\n"
    "v_mfma_f32_16x16x32_bf16 v[188:191], v[28:31], a[212:215], v[188:191]\n"
    "v_exp_f32 v96, -v96\n"
    "v_mfma_f32_16x16x32_bf16 v[196:199], v[28:31], a[216:219], v[196:199]\n"
    "v_exp_f32 v97, -v97\n"
    "v_mfma_f32_16x16x32_bf16 v[204:207], v[28:31], a[220:223], v[204:207]\n"
    "v_mul_f32 v102, v98, v102\n"
    "s_waitcnt lgkmcnt(3)\n"
    "v_mfma_f32_16x16x32_bf16 v[176:179], v[32:35], a[224:227], v[176:179]\n"
    "ds_read_b128 v[20:23], v3 offset:17664\n"
    "v_mul_f32 v103, v99, v103\n"
    "v_mfma_f32_16x16x32_bf16 v[184:187], v[32:35], a[228:231], v[184:187]\n"
    "s_add_u32 m0, s17, 22528\n"
    "s_add_u32 s56, s24, 1773568\n"
    "buffer_load_dwordx4 v0, s[4:7], s56 offen offset:0 lds\n"
    "v_mfma_f32_16x16x32_bf16 v[192:195], v[32:35], a[232:235], v[192:195]\n"
    "v_exp_f32 v98, -v98\n"
    "v_mfma_f32_16x16x32_bf16 v[200:203], v[32:35], a[236:239], v[200:203]\n"
    "v_exp_f32 v99, -v99\n"
    "v_mfma_f32_16x16x32_bf16 v[180:183], v[36:39], a[224:227], v[180:183]\n"
    "ds_read_b128 v[24:27], v3 offset:19456\n"
    "v_fma_f32 v96, v96, s36, s36\n"
    "v_mfma_f32_16x16x32_bf16 v[188:191], v[36:39], a[228:231], v[188:191]\n"
    "v_rcp_f32 v96, v96\n"
    "v_mfma_f32_16x16x32_bf16 v[196:199], v[36:39], a[232:235], v[196:199]\n"
    "v_fma_f32 v97, v97, s36, s36\n"
    "v_fma_f32 v98, v98, s36, s36\n"
    "v_mfma_f32_16x16x32_bf16 v[204:207], v[36:39], a[236:239], v[204:207]\n"
    "v_fma_f32 v99, v99, s36, s36\n"
    "s_waitcnt lgkmcnt(3)\n"
    "v_mfma_f32_16x16x32_bf16 v[176:179], v[40:43], a[240:243], v[176:179]\n"
    "ds_read_b128 v[28:31], v3 offset:19712\n"
    "v_mul_f32 v100, v96, v100\n"
    "v_mfma_f32_16x16x32_bf16 v[184:187], v[40:43], a[244:247], v[184:187]\n"
    "buffer_load_dwordx4 v0, s[4:7], s56 offen offset:1024 lds\n"
    "v_mul_f32 v108, v104, v108\n"
    "v_mfma_f32_16x16x32_bf16 v[192:195], v[40:43], a[248:251], v[192:195]\n"
    "v_rcp_f32 v97, v97\n"
    "v_mfma_f32_16x16x32_bf16 v[200:203], v[40:43], a[252:255], v[200:203]\n"
    "v_rcp_f32 v98, v98\n"
    "v_mfma_f32_16x16x32_bf16 v[180:183], v[44:47], a[240:243], v[180:183]\n"
    "s_waitcnt vmcnt(7)\n"
    "s_barrier\n"
    "ds_read_b128 v[32:35], v1 offset:9216\n"
    "v_mfma_f32_16x16x32_bf16 v[188:191], v[44:47], a[244:247], v[188:191]\n"
    "v_rcp_f32 v99, v99\n"
    "v_mfma_f32_16x16x32_bf16 v[196:199], v[44:47], a[248:251], v[196:199]\n"
    "v_exp_f32 v104, -v104\n"
    "v_mfma_f32_16x16x32_bf16 v[204:207], v[44:47], a[252:255], v[204:207]\n"
    "v_mul_f32 v101, v97, v101\n"
    "s_waitcnt lgkmcnt(3)\n"
    "v_mfma_f32_16x16x32_bf16 v[176:179], v[16:19], v[224:227], v[176:179]\n"
    "ds_read_b128 v[36:39], v1 offset:9472\n"
    "v_mul_f32 v102, v98, v102\n"
    "v_mfma_f32_16x16x32_bf16 v[184:187], v[16:19], v[228:231], v[184:187]\n"
    "buffer_load_dwordx4 v0, s[4:7], s56 offen offset:2048 lds\n"
    "v_mul_f32 v103, v99, v103\n"
    "v_mfma_f32_16x16x32_bf16 v[192:195], v[16:19], v[232:235], v[192:195]\n"
    "v_cvt_pk_bf16_f32 v13, v102, v103\n"
    "v_cvt_pk_bf16_f32 v12, v100, v101\n"
    "v_mfma_f32_16x16x32_bf16 v[200:203], v[16:19], v[236:239], v[200:203]\n"
    "v_mul_f32 v109, v105, v109\n"
    "v_mul_f32 v110, v106, v110\n"
    "v_mfma_f32_16x16x32_bf16 v[180:183], v[20:23], v[224:227], v[180:183]\n"
    "ds_read_b128 v[40:43], v1 offset:11264\n"
    "v_mul_f32 v111, v107, v111\n"
    "v_mfma_f32_16x16x32_bf16 v[188:191], v[20:23], v[228:231], v[188:191]\n"
    "v_exp_f32 v105, -v105\n"
    "v_mfma_f32_16x16x32_bf16 v[196:199], v[20:23], v[232:235], v[196:199]\n"
    "v_exp_f32 v106, -v106\n"
    "v_mfma_f32_16x16x32_bf16 v[204:207], v[20:23], v[236:239], v[204:207]\n"
    "v_fma_f32 v104, v104, s36, s36\n"
    "s_waitcnt lgkmcnt(3)\n"
    "v_mfma_f32_16x16x32_bf16 v[176:179], v[24:27], v[240:243], v[176:179]\n"
    "ds_read_b128 v[44:47], v1 offset:11520\n"
    "v_fma_f32 v105, v105, s36, s36\n"
    "v_mfma_f32_16x16x32_bf16 v[184:187], v[24:27], v[244:247], v[184:187]\n"
    "buffer_load_dwordx4 v0, s[4:7], s56 offen offset:3072 lds\n"
    "v_fma_f32 v106, v106, s36, s36\n"
    "v_mfma_f32_16x16x32_bf16 v[192:195], v[24:27], v[248:251], v[192:195]\n"
    "v_exp_f32 v107, -v107\n"
    "v_mfma_f32_16x16x32_bf16 v[200:203], v[24:27], v[252:255], v[200:203]\n"
    "v_rcp_f32 v104, v104\n"
    "v_mfma_f32_16x16x32_bf16 v[180:183], v[28:31], v[240:243], v[180:183]\n"
    "ds_read_b128 v[16:19], v1 offset:13312\n"
    "v_fma_f32 v107, v107, s36, s36\n"
    "v_mfma_f32_16x16x32_bf16 v[188:191], v[28:31], v[244:247], v[188:191]\n"
    "v_rcp_f32 v105, v105\n"
    "v_mfma_f32_16x16x32_bf16 v[196:199], v[28:31], v[248:251], v[196:199]\n"
    "v_rcp_f32 v106, v106\n"
    "v_mfma_f32_16x16x32_bf16 v[204:207], v[28:31], v[252:255], v[204:207]\n"
    "v_mul_f32 v108, v104, v108\n"
    "s_waitcnt lgkmcnt(3)\n"
    "v_mfma_f32_16x16x32_bf16 v[48:51], v[32:35], a[0:3], 0\n"
    "ds_read_b128 v[20:23], v1 offset:13568\n"
    "v_mul_f32 v109, v105, v109\n"
    "v_mfma_f32_16x16x32_bf16 v[56:59], v[32:35], a[4:7], 0\n"
    "s_add_u32 m0, s17, 26624\n"
    "s_add_u32 s56, s24, 1777664\n"
    "buffer_load_dwordx4 v0, s[4:7], s56 offen offset:0 lds\n"
    "v_mfma_f32_16x16x32_bf16 v[64:67], v[32:35], a[8:11], 0\n"
    "v_rcp_f32 v107, v107\n"
    "v_mfma_f32_16x16x32_bf16 v[72:75], v[32:35], a[12:15], 0\n"
    "v_mul_f32 v110, v106, v110\n"
    "v_mul_f32 v111, v107, v111\n"
    "v_mfma_f32_16x16x32_bf16 v[52:55], v[36:39], a[0:3], 0\n"
    "ds_read_b128 v[24:27], v1 offset:15360\n"
    "v_cvt_pk_bf16_f32 v15, v110, v111\n"
    "v_mfma_f32_16x16x32_bf16 v[60:63], v[36:39], a[4:7], 0\n"
    "v_cvt_pk_bf16_f32 v14, v108, v109\n"
    "v_permlane32_swap_b32 v13, v15\n"
    "v_mfma_f32_16x16x32_bf16 v[68:71], v[36:39], a[8:11], 0\n"
    "v_permlane32_swap_b32 v12, v14\n"
    "buffer_store_dwordx4 v[12:15], v7, s[4:7], s25 offen sc1\n"
    "v_mfma_f32_16x16x32_bf16 v[76:79], v[36:39], a[12:15], 0\n"
    "v_mul_f32 v116, v112, v116\n"
    "s_waitcnt lgkmcnt(3)\n"
    "v_mfma_f32_16x16x32_bf16 v[48:51], v[40:43], a[16:19], v[48:51]\n"
    "ds_read_b128 v[28:31], v1 offset:15616\n"
    "v_mul_f32 v117, v113, v117\n"
    "v_mfma_f32_16x16x32_bf16 v[56:59], v[40:43], a[20:23], v[56:59]\n"
    "s_add_u32 m0, s17, 27648\n"
    "s_add_u32 s56, s19, 1769472\n"
    "buffer_load_dwordx4 v0, s[4:7], s56 offen offset:0 lds\n"
    "v_mfma_f32_16x16x32_bf16 v[64:67], v[40:43], a[24:27], v[64:67]\n"
    "v_exp_f32 v112, -v112\n"
    "v_mfma_f32_16x16x32_bf16 v[72:75], v[40:43], a[28:31], v[72:75]\n"
    "v_exp_f32 v113, -v113\n"
    "v_mfma_f32_16x16x32_bf16 v[52:55], v[44:47], a[16:19], v[52:55]\n"
    "ds_read_b128 v[32:35], v4 offset:17408\n"
    "v_mul_f32 v118, v114, v118\n"
    "v_mfma_f32_16x16x32_bf16 v[60:63], v[44:47], a[20:23], v[60:63]\n"
    "v_exp_f32 v114, -v114\n"
    "v_mfma_f32_16x16x32_bf16 v[68:71], v[44:47], a[24:27], v[68:71]\n"
    "v_mul_f32 v119, v115, v119\n"
    "v_fma_f32 v112, v112, s36, s36\n"
    "v_mfma_f32_16x16x32_bf16 v[76:79], v[44:47], a[28:31], v[76:79]\n"
    "v_fma_f32 v113, v113, s36, s36\n"
    "s_waitcnt lgkmcnt(3)\n"
    "v_mfma_f32_16x16x32_bf16 v[48:51], v[16:19], a[32:35], v[48:51]\n"
    "ds_read_b128 v[36:39], v4 offset:17664\n"
    "v_fma_f32 v114, v114, s36, s36\n"
    "v_mfma_f32_16x16x32_bf16 v[56:59], v[16:19], a[36:39], v[56:59]\n"
    "buffer_load_dwordx4 v0, s[4:7], s56 offen offset:1024 lds\n"
    "v_mul_f32 v124, v120, v124\n"
    "v_mfma_f32_16x16x32_bf16 v[64:67], v[16:19], a[40:43], v[64:67]\n"
    "v_exp_f32 v115, -v115\n"
    "v_mfma_f32_16x16x32_bf16 v[72:75], v[16:19], a[44:47], v[72:75]\n"
    "v_rcp_f32 v112, v112\n"
    "v_mfma_f32_16x16x32_bf16 v[52:55], v[20:23], a[32:35], v[52:55]\n"
    "ds_read_b128 v[40:43], v1 offset:47104\n"
    "v_fma_f32 v115, v115, s36, s36\n"
    "v_mfma_f32_16x16x32_bf16 v[60:63], v[20:23], a[36:39], v[60:63]\n"
    "v_rcp_f32 v113, v113\n"
    "v_mfma_f32_16x16x32_bf16 v[68:71], v[20:23], a[40:43], v[68:71]\n"
    "v_rcp_f32 v114, v114\n"
    "v_mfma_f32_16x16x32_bf16 v[76:79], v[20:23], a[44:47], v[76:79]\n"
    "v_mul_f32 v116, v112, v116\n"
    "s_waitcnt lgkmcnt(3)\n"
    "v_mfma_f32_16x16x32_bf16 v[48:51], v[24:27], a[48:51], v[48:51]\n"
    "ds_read_b128 v[44:47], v1 offset:47360\n"
    "v_mul_f32 v117, v113, v117\n"
    "v_mfma_f32_16x16x32_bf16 v[56:59], v[24:27], a[52:55], v[56:59]\n"
    "buffer_load_dwordx4 v0, s[4:7], s56 offen offset:2048 lds\n"
    "v_mul_f32 v118, v114, v118\n"
    "v_mfma_f32_16x16x32_bf16 v[64:67], v[24:27], a[56:59], v[64:67]\n"
    "v_rcp_f32 v115, v115\n"
    "v_mfma_f32_16x16x32_bf16 v[72:75], v[24:27], a[60:63], v[72:75]\n"
    "v_exp_f32 v120, -v120\n"
    "v_mfma_f32_16x16x32_bf16 v[52:55], v[28:31], a[48:51], v[52:55]\n"
    "ds_read_b128 v[16:19], v2 offset:0\n"
    "v_mul_f32 v119, v115, v119\n"
    "v_mfma_f32_16x16x32_bf16 v[60:63], v[28:31], a[52:55], v[60:63]\n"
    "v_cvt_pk_bf16_f32 v9, v118, v119\n"
    "v_cvt_pk_bf16_f32 v8, v116, v117\n"
    "v_mfma_f32_16x16x32_bf16 v[68:71], v[28:31], a[56:59], v[68:71]\n"
    "v_mul_f32 v125, v121, v125\n"
    "v_mul_f32 v126, v122, v126\n"
    "v_mfma_f32_16x16x32_bf16 v[76:79], v[28:31], a[60:63], v[76:79]\n"
    "v_mul_f32 v127, v123, v127\n"
    "s_waitcnt lgkmcnt(3)\n"
    "v_mfma_f32_16x16x32_bf16 v[48:51], v[32:35], a[64:67], v[48:51]\n"
    "ds_read_b128 v[20:23], v2 offset:256\n"
    "v_fma_f32 v120, v120, s36, s36\n"
    "v_mfma_f32_16x16x32_bf16 v[56:59], v[32:35], a[68:71], v[56:59]\n"
    "buffer_load_dwordx4 v0, s[4:7], s56 offen offset:3072 lds\n"
    "s_add_u32 s23, s23, 8192\n"
    "v_mfma_f32_16x16x32_bf16 v[64:67], v[32:35], a[72:75], v[64:67]\n"
    "v_exp_f32 v121, -v121\n"
    "v_mfma_f32_16x16x32_bf16 v[72:75], v[32:35], a[76:79], v[72:75]\n"
    "v_exp_f32 v122, -v122\n"
    "v_mfma_f32_16x16x32_bf16 v[52:55], v[36:39], a[64:67], v[52:55]\n"
    "ds_read_b128 v[24:27], v2 offset:2048\n"
    "v_fma_f32 v121, v121, s36, s36\n"
    "v_mfma_f32_16x16x32_bf16 v[60:63], v[36:39], a[68:71], v[60:63]\n"
    "v_exp_f32 v123, -v123\n"
    "v_mfma_f32_16x16x32_bf16 v[68:71], v[36:39], a[72:75], v[68:71]\n"
    "LPK3:\n"
    "s_cbranch_execz LPK7\n"
    "v_rcp_f32 v120, v120\n"
    "v_mfma_f32_16x16x32_bf16 v[76:79], v[36:39], a[76:79], v[76:79]\n"
    "v_fma_f32 v122, v122, s36, s36\n"
    "s_waitcnt lgkmcnt(3)\n"
    "v_mfma_f32_16x16x32_bf16 v[48:51], v[40:43], a[80:83], v[48:51]\n"
    "ds_read_b128 v[28:31], v2 offset:2304\n"
    "v_fma_f32 v123, v123, s36, s36\n"
    "v_mfma_f32_16x16x32_bf16 v[56:59], v[40:43], a[84:87], v[56:59]\n"
    "s_add_u32 m0, s17, 31744\n"
    "s_add_u32 s56, s19, 1773568\n"
    "buffer_load_dwordx4 v0, s[4:7], s56 offen offset:0 lds\n"
    "v_mfma_f32_16x16x32_bf16 v[64:67], v[40:43], a[88:91], v[64:67]\n"
    "v_rcp_f32 v121, v121\n"
    "v_mfma_f32_16x16x32_bf16 v[72:75], v[40:43], a[92:95], v[72:75]\n"
    "v_rcp_f32 v122, v122\n"
    "v_mfma_f32_16x16x32_bf16 v[52:55], v[44:47], a[80:83], v[52:55]\n"
    "ds_read_b128 v[32:35], v2 offset:4096\n"
    "v_mul_f32 v124, v120, v124\n"
    "v_mfma_f32_16x16x32_bf16 v[60:63], v[44:47], a[84:87], v[60:63]\n"
    "v_rcp_f32 v123, v123\n"
    "v_mfma_f32_16x16x32_bf16 v[68:71], v[44:47], a[88:91], v[68:71]\n"
    "v_mul_f32 v125, v121, v125\n"
    "v_mul_f32 v126, v122, v126\n"
    "v_mfma_f32_16x16x32_bf16 v[76:79], v[44:47], a[92:95], v[76:79]\n"
    "v_mul_f32 v127, v123, v127\n"
    "s_waitcnt lgkmcnt(3)\n"
    "v_mfma_f32_16x16x32_bf16 v[48:51], v[16:19], a[96:99], v[48:51]\n"
    "ds_read_b128 v[36:39], v2 offset:4352\n"
    "v_cvt_pk_bf16_f32 v11, v126, v127\n"
    "v_mfma_f32_16x16x32_bf16 v[56:59], v[16:19], a[100:103], v[56:59]\n"
    "buffer_load_dwordx4 v0, s[4:7], s56 offen offset:1024 lds\n"
    "v_cvt_pk_bf16_f32 v10, v124, v125\n"
    "v_mfma_f32_16x16x32_bf16 v[64:67], v[16:19], a[104:107], v[64:67]\n"
    "v_permlane32_swap_b32 v9, v11\n"
    "v_permlane32_swap_b32 v8, v10\n"
    "v_mfma_f32_16x16x32_bf16 v[72:75], v[16:19], a[108:111], v[72:75]\n"
    "buffer_store_dwordx4 v[8:11], v6, s[4:7], s23 offen sc1\n"
    "v_mul_f32 v132, v128, v132\n"
    "v_mfma_f32_16x16x32_bf16 v[52:55], v[20:23], a[96:99], v[52:55]\n"
    "ds_read_b128 v[40:43], v2 offset:33792\n"
    "v_mul_f32 v133, v129, v133\n"
    "v_mfma_f32_16x16x32_bf16 v[60:63], v[20:23], a[100:103], v[60:63]\n"
    "v_exp_f32 v128, -v128\n"
    "v_mfma_f32_16x16x32_bf16 v[68:71], v[20:23], a[104:107], v[68:71]\n"
    "v_exp_f32 v129, -v129\n"
    "v_mfma_f32_16x16x32_bf16 v[76:79], v[20:23], a[108:111], v[76:79]\n"
    "v_mul_f32 v134, v130, v134\n"
    "s_waitcnt lgkmcnt(3)\n"
    "v_mfma_f32_16x16x32_bf16 v[48:51], v[24:27], a[112:115], v[48:51]\n"
    "ds_read_b128 v[44:47], v2 offset:34048\n"
    "v_mul_f32 v135, v131, v135\n"
    "v_mfma_f32_16x16x32_bf16 v[56:59], v[24:27], a[116:119], v[56:59]\n"
    "buffer_load_dwordx4 v0, s[4:7], s56 offen offset:2048 lds\n"
    "v_fma_f32 v128, v128, s36, s36\n"
    "v_mfma_f32_16x16x32_bf16 v[64:67], v[24:27], a[120:123], v[64:67]\n"
    "v_exp_f32 v130, -v130\n"
    "v_mfma_f32_16x16x32_bf16 v[72:75], v[24:27], a[124:127], v[72:75]\n"
    "v_exp_f32 v131, -v131\n"
    "v_mfma_f32_16x16x32_bf16 v[52:55], v[28:31], a[112:115], v[52:55]\n"
    "ds_read_b128 v[16:19], v2 offset:35840\n"
    "v_fma_f32 v129, v129, s36, s36\n"
    "v_mfma_f32_16x16x32_bf16 v[60:63], v[28:31], a[116:119], v[60:63]\n"
    "v_rcp_f32 v128, v128\n"
    "v_mfma_f32_16x16x32_bf16 v[68:71], v[28:31], a[120:123], v[68:71]\n"
    "v_rcp_f32 v129, v129\n"
    "v_mfma_f32_16x16x32_bf16 v[76:79], v[28:31], a[124:127], v[76:79]\n"
    "v_fma_f32 v130, v130, s36, s36\n"
    "s_waitcnt lgkmcnt(3)\n"
    "v_mfma_f32_16x16x32_bf16 v[48:51], v[32:35], a[128:131], v[48:51]\n"
    "ds_read_b128 v[20:23], v2 offset:36096\n"
    "v_fma_f32 v131, v131, s36, s36\n"
    "v_mfma_f32_16x16x32_bf16 v[56:59], v[32:35], a[132:135], v[56:59]\n"
    "buffer_load_dwordx4 v0, s[4:7], s56 offen offset:3072 lds\n"
    "v_mul_f32 v132, v128, v132\n"
    "v_mfma_f32_16x16x32_bf16 v[64:67], v[32:35], a[136:139], v[64:67]\n"
    "v_rcp_f32 v130, v130\n"
    "v_mfma_f32_16x16x32_bf16 v[72:75], v[32:35], a[140:143], v[72:75]\n"
    "v_rcp_f32 v131, v131\n"
    "v_mfma_f32_16x16x32_bf16 v[52:55], v[36:39], a[128:131], v[52:55]\n"
    "ds_read_b128 v[24:27], v2 offset:37888\n"
    "v_mul_f32 v133, v129, v133\n"
    "v_mfma_f32_16x16x32_bf16 v[60:63], v[36:39], a[132:135], v[60:63]\n"
    "v_mul_f32 v134, v130, v134\n"
    "v_mul_f32 v135, v131, v135\n"
    "v_mfma_f32_16x16x32_bf16 v[68:71], v[36:39], a[136:139], v[68:71]\n"
    "v_cvt_pk_bf16_f32 v13, v134, v135\n"
    "v_cvt_pk_bf16_f32 v12, v132, v133\n"
    "v_mfma_f32_16x16x32_bf16 v[76:79], v[36:39], a[140:143], v[76:79]\n"
    "v_mul_f32 v140, v136, v140\n"
    "s_waitcnt lgkmcnt(3)\n"
    "v_mfma_f32_16x16x32_bf16 v[48:51], v[40:43], a[144:147], v[48:51]\n"
    "ds_read_b128 v[28:31], v2 offset:38144\n"
    "v_mul_f32 v141, v137, v141\n"
    "v_mfma_f32_16x16x32_bf16 v[56:59], v[40:43], a[148:151], v[56:59]\n"
    "s_add_u32 m0, s17, 35840\n"
    "s_add_u32 s56, s19, 1777664\n"
    "buffer_load_dwordx4 v0, s[4:7], s56 offen offset:0 lds\n"
    "v_mfma_f32_16x16x32_bf16 v[64:67], v[40:43], a[152:155], v[64:67]\n"
    "v_exp_f32 v136, -v136\n"
    "v_mfma_f32_16x16x32_bf16 v[72:75], v[40:43], a[156:159], v[72:75]\n"
    "v_exp_f32 v137, -v137\n"
    "v_mfma_f32_16x16x32_bf16 v[52:55], v[44:47], a[144:147], v[52:55]\n"
    "ds_read_b128 v[32:35], v2 offset:39936\n"
    "v_mul_f32 v142, v138, v142\n"
    "v_mfma_f32_16x16x32_bf16 v[60:63], v[44:47], a[148:151], v[60:63]\n"
    "v_exp_f32 v138, -v138\n"
    "v_mfma_f32_16x16x32_bf16 v[68:71], v[44:47], a[152:155], v[68:71]\n"
    "v_mul_f32 v143, v139, v143\n"
    "v_fma_f32 v136, v136, s36, s36\n"
    "v_mfma_f32_16x16x32_bf16 v[76:79], v[44:47], a[156:159], v[76:79]\n"
    "v_fma_f32 v137, v137, s36, s36\n"
    "s_waitcnt lgkmcnt(3)\n"
    "v_mfma_f32_16x16x32_bf16 v[48:51], v[16:19], a[160:163], v[48:51]\n"
    "ds_read_b128 v[36:39], v2 offset:40192\n"
    "v_fma_f32 v138, v138, s36, s36\n"
    "v_mfma_f32_16x16x32_bf16 v[56:59], v[16:19], a[164:167], v[56:59]\n"
    "v_exp_f32 v139, -v139\n"
    "v_mfma_f32_16x16x32_bf16 v[64:67], v[16:19], a[168:171], v[64:67]\n"
    "v_rcp_f32 v136, v136\n"
    "v_mfma_f32_16x16x32_bf16 v[72:75], v[16:19], a[172:175], v[72:75]\n"
    "v_rcp_f32 v137, v137\n"
    "v_mfma_f32_16x16x32_bf16 v[52:55], v[20:23], a[160:163], v[52:55]\n"
    "ds_read_b128 v[40:43], v5 offset:17408\n"
    "v_fma_f32 v139, v139, s36, s36\n"
    "v_mfma_f32_16x16x32_bf16 v[60:63], v[20:23], a[164:167], v[60:63]\n"
    "v_rcp_f32 v138, v138\n"
    "v_mfma_f32_16x16x32_bf16 v[68:71], v[20:23], a[168:171], v[68:71]\n"
    "v_rcp_f32 v139, v139\n"
    "v_mfma_f32_16x16x32_bf16 v[76:79], v[20:23], a[172:175], v[76:79]\n"
    "v_mul_f32 v140, v136, v140\n"
    "s_waitcnt lgkmcnt(3)\n"
    "v_mfma_f32_16x16x32_bf16 v[48:51], v[24:27], a[176:179], v[48:51]\n"
    "ds_read_b128 v[44:47], v5 offset:17664\n"
    "v_mul_f32 v141, v137, v141\n"
    "v_mfma_f32_16x16x32_bf16 v[56:59], v[24:27], a[180:183], v[56:59]\n"
    "v_mul_f32 v142, v138, v142\n"
    "v_mul_f32 v143, v139, v143\n"
    "v_mfma_f32_16x16x32_bf16 v[64:67], v[24:27], a[184:187], v[64:67]\n"
    "v_cvt_pk_bf16_f32 v15, v142, v143\n"
    "v_cvt_pk_bf16_f32 v14, v140, v141\n"
    "v_mfma_f32_16x16x32_bf16 v[72:75], v[24:27], a[188:191], v[72:75]\n"
    "v_permlane32_swap_b32 v13, v15\n"
    "v_permlane32_swap_b32 v12, v14\n"
    "v_mfma_f32_16x16x32_bf16 v[52:55], v[28:31], a[176:179], v[52:55]\n"
    "ds_read_b128 v[16:19], v3 offset:22528\n"
    "buffer_store_dwordx4 v[12:15], v7, s[4:7], s23 offen sc1\n"
    "v_mfma_f32_16x16x32_bf16 v[60:63], v[28:31], a[180:183], v[60:63]\n"
    "v_mul_f32 v148, v144, v148\n"
    "v_mul_f32 v149, v145, v149\n"
    "v_mfma_f32_16x16x32_bf16 v[68:71], v[28:31], a[184:187], v[68:71]\n"
    "v_exp_f32 v144, -v144\n"
    "v_mfma_f32_16x16x32_bf16 v[76:79], v[28:31], a[188:191], v[76:79]\n"
    "v_mul_f32 v150, v146, v150\n"
    "s_waitcnt lgkmcnt(3)\n"
    "v_mfma_f32_16x16x32_bf16 v[48:51], v[32:35], a[192:195], v[48:51]\n"
    "ds_read_b128 v[20:23], v3 offset:22784\n"
    "v_mul_f32 v151, v147, v151\n"
    "v_mfma_f32_16x16x32_bf16 v[56:59], v[32:35], a[196:199], v[56:59]\n"
    "v_exp_f32 v145, -v145\n"
    "v_mfma_f32_16x16x32_bf16 v[64:67], v[32:35], a[200:203], v[64:67]\n"
    "v_exp_f32 v146, -v146\n"
    "v_mfma_f32_16x16x32_bf16 v[72:75], v[32:35], a[204:207], v[72:75]\n"
    "v_exp_f32 v147, -v147\n"
    "v_mfma_f32_16x16x32_bf16 v[52:55], v[36:39], a[192:195], v[52:55]\n"
    "ds_read_b128 v[24:27], v3 offset:24576\n"
    "v_fma_f32 v144, v144, s36, s36\n"
    "v_mfma_f32_16x16x32_bf16 v[60:63], v[36:39], a[196:199], v[60:63]\n"
    "v_rcp_f32 v144, v144\n"
    "v_mfma_f32_16x16x32_bf16 v[68:71], v[36:39], a[200:203], v[68:71]\n"
    "v_fma_f32 v145, v145, s36, s36\n"
    "v_fma_f32 v146, v146, s36, s36\n"
    "v_mfma_f32_16x16x32_bf16 v[76:79], v[36:39], a[204:207], v[76:79]\n"
    "v_fma_f32 v147, v147, s36, s36\n"
    "s_waitcnt lgkmcnt(3)\n"
    "v_mfma_f32_16x16x32_bf16 v[48:51], v[40:43], a[208:211], v[48:51]\n"
    "ds_read_b128 v[28:31], v3 offset:24832\n"
    "v_mul_f32 v148, v144, v148\n"
    "v_mfma_f32_16x16x32_bf16 v[56:59], v[40:43], a[212:215], v[56:59]\n"
    "v_rcp_f32 v145, v145\n"
    "v_mfma_f32_16x16x32_bf16 v[64:67], v[40:43], a[216:219], v[64:67]\n"
    "v_rcp_f32 v146, v146\n"
    "v_mfma_f32_16x16x32_bf16 v[72:75], v[40:43], a[220:223], v[72:75]\n"
    "v_rcp_f32 v147, v147\n"
    "v_mfma_f32_16x16x32_bf16 v[52:55], v[44:47], a[208:211], v[52:55]\n"
    "ds_read_b128 v[32:35], v3 offset:26624\n"
    "v_mul_f32 v149, v145, v149\n"
    "v_mfma_f32_16x16x32_bf16 v[60:63], v[44:47], a[212:215], v[60:63]\n"
    "v_mul_f32 v150, v146, v150\n"
    "v_mul_f32 v151, v147, v151\n"
    "v_mfma_f32_16x16x32_bf16 v[68:71], v[44:47], a[216:219], v[68:71]\n"
    "v_cvt_pk_bf16_f32 v9, v150, v151\n"
    "v_cvt_pk_bf16_f32 v8, v148, v149\n"
    "v_mfma_f32_16x16x32_bf16 v[76:79], v[44:47], a[220:223], v[76:79]\n"
    "v_mul_f32 v156, v152, v156\n"
    "s_waitcnt lgkmcnt(3)\n"
    "v_mfma_f32_16x16x32_bf16 v[48:51], v[16:19], a[224:227], v[48:51]\n"
    "ds_read_b128 v[36:39], v3 offset:26880\n"
    "v_mul_f32 v157, v153, v157\n"
    "v_mfma_f32_16x16x32_bf16 v[56:59], v[16:19], a[228:231], v[56:59]\n"
    "v_exp_f32 v152, -v152\n"
    "v_mfma_f32_16x16x32_bf16 v[64:67], v[16:19], a[232:235], v[64:67]\n"
    "v_exp_f32 v153, -v153\n"
    "v_mfma_f32_16x16x32_bf16 v[72:75], v[16:19], a[236:239], v[72:75]\n"
    "v_mul_f32 v158, v154, v158\n"
    "v_mul_f32 v159, v155, v159\n"
    "v_mfma_f32_16x16x32_bf16 v[52:55], v[20:23], a[224:227], v[52:55]\n"
    "ds_read_b128 v[40:43], v3 offset:28672\n"
    "v_fma_f32 v152, v152, s36, s36\n"
    "v_mfma_f32_16x16x32_bf16 v[60:63], v[20:23], a[228:231], v[60:63]\n"
    "v_exp_f32 v154, -v154\n"
    "v_mfma_f32_16x16x32_bf16 v[68:71], v[20:23], a[232:235], v[68:71]\n"
    "v_exp_f32 v155, -v155\n"
    "v_mfma_f32_16x16x32_bf16 v[76:79], v[20:23], a[236:239], v[76:79]\n"
    "v_fma_f32 v153, v153, s36, s36\n"
    "s_waitcnt lgkmcnt(3)\n"
    "v_mfma_f32_16x16x32_bf16 v[48:51], v[24:27], a[240:243], v[48:51]\n"
    "ds_read_b128 v[44:47], v3 offset:28928\n"
    "v_fma_f32 v154, v154, s36, s36\n"
    "v_mfma_f32_16x16x32_bf16 v[56:59], v[24:27], a[244:247], v[56:59]\n"
    "v_rcp_f32 v152, v152\n"
    "v_mfma_f32_16x16x32_bf16 v[64:67], v[24:27], a[248:251], v[64:67]\n"
    "v_rcp_f32 v153, v153\n"
    "v_mfma_f32_16x16x32_bf16 v[72:75], v[24:27], a[252:255], v[72:75]\n"
    "v_rcp_f32 v154, v154\n"
    "v_mfma_f32_16x16x32_bf16 v[52:55], v[28:31], a[240:243], v[52:55]\n"
    "s_waitcnt vmcnt(11)\n"
    "s_barrier\n"
    "s_cmp_lg_u32 s17, 0\n"
    "s_cbranch_scc1 LF1%=\n"
    "s_mov_b64 exec, 1\n"
    "buffer_store_dword v216, off, s[4:7], s20 offset:0 sc1\n"
    "s_mov_b64 exec, -1\n"
    "LF1%=:\n"
    "ds_read_b128 v[16:19], v1 offset:18432\n"
    "v_mfma_f32_16x16x32_bf16 v[60:63], v[28:31], a[244:247], v[60:63]\n"
    "v_fma_f32 v155, v155, s36, s36\n"
    "v_mul_f32 v156, v152, v156\n"
    "v_mfma_f32_16x16x32_bf16 v[68:71], v[28:31], a[248:251], v[68:71]\n"
    "v_rcp_f32 v155, v155\n"
    "v_mfma_f32_16x16x32_bf16 v[76:79], v[28:31], a[252:255], v[76:79]\n"
    "v_mul_f32 v157, v153, v157\n"
    "s_waitcnt lgkmcnt(3)\n"
    "v_mfma_f32_16x16x32_bf16 v[48:51], v[32:35], v[224:227], v[48:51]\n"
    "ds_read_b128 v[20:23], v1 offset:18688\n"
    "v_mul_f32 v158, v154, v158\n"
    "v_mfma_f32_16x16x32_bf16 v[56:59], v[32:35], v[228:231], v[56:59]\n"
    "v_mul_f32 v159, v155, v159\n"
    "v_cvt_pk_bf16_f32 v11, v158, v159\n"
    "v_mfma_f32_16x16x32_bf16 v[64:67], v[32:35], v[232:235], v[64:67]\n"
    "v_cvt_pk_bf16_f32 v10, v156, v157\n"
    "v_permlane32_swap_b32 v9, v11\n"
    "v_mfma_f32_16x16x32_bf16 v[72:75], v[32:35], v[236:239], v[72:75]\n"
    "v_permlane32_swap_b32 v8, v10\n"
    "s_add_u32 s25, s25, 8192\n"
    "v_mfma_f32_16x16x32_bf16 v[52:55], v[36:39], v[224:227], v[52:55]\n"
    "ds_read_b128 v[24:27], v1 offset:20480\n"
    "buffer_store_dwordx4 v[8:11], v6, s[4:7], s25 offen sc1\n"
    "v_mfma_f32_16x16x32_bf16 v[60:63], v[36:39], v[228:231], v[60:63]\n"
    "v_mul_f32 v164, v160, v164\n"
    "v_mul_f32 v165, v161, v165\n"
    "v_mfma_f32_16x16x32_bf16 v[68:71], v[36:39], v[232:235], v[68:71]\n"
    "v_exp_f32 v160, -v160\n"
    "v_mfma_f32_16x16x32_bf16 v[76:79], v[36:39], v[236:239], v[76:79]\n"
    "v_mul_f32 v166, v162, v166\n"
    "s_waitcnt lgkmcnt(3)\n"
    "v_mfma_f32_16x16x32_bf16 v[48:51], v[40:43], v[240:243], v[48:51]\n"
    "ds_read_b128 v[28:31], v1 offset:20736\n"
    "v_mul_f32 v167, v163, v167\n"
    "v_mfma_f32_16x16x32_bf16 v[56:59], v[40:43], v[244:247], v[56:59]\n"
    "v_exp_f32 v161, -v161\n"
    "v_mfma_f32_16x16x32_bf16 v[64:67], v[40:43], v[248:251], v[64:67]\n"
    "v_exp_f32 v162, -v162\n"
    "v_mfma_f32_16x16x32_bf16 v[72:75], v[40:43], v[252:255], v[72:75]\n"
    "v_exp_f32 v163, -v163\n"
    "v_mfma_f32_16x16x32_bf16 v[52:55], v[44:47], v[240:243], v[52:55]\n"
    "ds_read_b128 v[32:35], v1 offset:22528\n"
    "v_fma_f32 v160, v160, s36, s36\n"
    "v_mfma_f32_16x16x32_bf16 v[60:63], v[44:47], v[244:247], v[60:63]\n"
    "v_rcp_f32 v160, v160\n"
    "v_mfma_f32_16x16x32_bf16 v[68:71], v[44:47], v[248:251], v[68:71]\n"
    "v_fma_f32 v161, v161, s36, s36\n"
    "v_fma_f32 v162, v162, s36, s36\n"
    "v_mfma_f32_16x16x32_bf16 v[76:79], v[44:47], v[252:255], v[76:79]\n"
    "v_fma_f32 v163, v163, s36, s36\n"
    "s_waitcnt lgkmcnt(3)\n"
    "v_mfma_f32_16x16x32_bf16 v[80:83], v[16:19], a[0:3], 0\n"
    "ds_read_b128 v[36:39], v1 offset:22784\n"
    "v_mul_f32 v164, v160, v164\n"
    "v_mfma_f32_16x16x32_bf16 v[88:91], v[16:19], a[4:7], 0\n"
    "v_rcp_f32 v161, v161\n"
    "v_mfma_f32_16x16x32_bf16 v[96:99], v[16:19], a[8:11], 0\n"
    "v_rcp_f32 v162, v162\n"
    "v_mfma_f32_16x16x32_bf16 v[104:107], v[16:19], a[12:15], 0\n"
    "v_rcp_f32 v163, v163\n"
    "v_mfma_f32_16x16x32_bf16 v[84:87], v[20:23], a[0:3], 0\n"
    "ds_read_b128 v[40:43], v1 offset:24576\n"
    "v_mul_f32 v165, v161, v165\n"
    "v_mfma_f32_16x16x32_bf16 v[92:95], v[20:23], a[4:7], 0\n"
    "v_mul_f32 v166, v162, v166\n"
    "v_mul_f32 v167, v163, v167\n"
    "v_mfma_f32_16x16x32_bf16 v[100:103], v[20:23], a[8:11], 0\n"
    "v_cvt_pk_bf16_f32 v13, v166, v167\n"
    "v_cvt_pk_bf16_f32 v12, v164, v165\n"
    "v_mfma_f32_16x16x32_bf16 v[108:111], v[20:23], a[12:15], 0\n"
    "v_mul_f32 v172, v168, v172\n"
    "s_waitcnt lgkmcnt(3)\n"
    "v_mfma_f32_16x16x32_bf16 v[80:83], v[24:27], a[16:19], v[80:83]\n"
    "ds_read_b128 v[44:47], v1 offset:24832\n"
    "v_mul_f32 v173, v169, v173\n"
    "v_mfma_f32_16x16x32_bf16 v[88:91], v[24:27], a[20:23], v[88:91]\n"
    "s_add_u32 m0, s17, 0\n"
    "s_add_u32 s56, s22, 0\n"
    "buffer_load_dwordx4 v0, s[4:7], s56 offen offset:0 lds\n"
    "v_mfma_f32_16x16x32_bf16 v[96:99], v[24:27], a[24:27], v[96:99]\n"
    "v_exp_f32 v168, -v168\n"
    "v_mfma_f32_16x16x32_bf16 v[104:107], v[24:27], a[28:31], v[104:107]\n"
    "v_exp_f32 v169, -v169\n"
    "v_mfma_f32_16x16x32_bf16 v[84:87], v[28:31], a[16:19], v[84:87]\n"
    "ds_read_b128 v[16:19], v4 offset:26624\n"
    "v_mul_f32 v174, v170, v174\n"
    "v_mfma_f32_16x16x32_bf16 v[92:95], v[28:31], a[20:23], v[92:95]\n"
    "v_exp_f32 v170, -v170\n"
    "v_mfma_f32_16x16x32_bf16 v[100:103], v[28:31], a[24:27], v[100:103]\n"
    "v_mul_f32 v175, v171, v175\n"
    "v_fma_f32 v168, v168, s36, s36\n"
    "v_mfma_f32_16x16x32_bf16 v[108:111], v[28:31], a[28:31], v[108:111]\n"
    "v_fma_f32 v169, v169, s36, s36\n"
    "s_waitcnt lgkmcnt(3)\n"
    "v_mfma_f32_16x16x32_bf16 v[80:83], v[32:35], a[32:35], v[80:83]\n"
    "ds_read_b128 v[20:23], v4 offset:26880\n"
    "v_fma_f32 v170, v170, s36, s36\n"
    "v_mfma_f32_16x16x32_bf16 v[88:91], v[32:35], a[36:39], v[88:91]\n"
    "buffer_load_dwordx4 v0, s[4:7], s56 offen offset:1024 lds\n"
    "v_mul_f32 v180, v176, v180\n"
    "v_mfma_f32_16x16x32_bf16 v[96:99], v[32:35], a[40:43], v[96:99]\n"
    "v_exp_f32 v171, -v171\n"
    "v_mfma_f32_16x16x32_bf16 v[104:107], v[32:35], a[44:47], v[104:107]\n"
    "v_rcp_f32 v168, v168\n"
    "v_mfma_f32_16x16x32_bf16 v[84:87], v[36:39], a[32:35], v[84:87]\n"
    "ds_read_b128 v[24:27], v2 offset:7168\n"
    "v_fma_f32 v171, v171, s36, s36\n"
    "v_mfma_f32_16x16x32_bf16 v[92:95], v[36:39], a[36:39], v[92:95]\n"
    "v_rcp_f32 v169, v169\n"
    "v_mfma_f32_16x16x32_bf16 v[100:103], v[36:39], a[40:43], v[100:103]\n"
    "v_rcp_f32 v170, v170\n"
    "v_mfma_f32_16x16x32_bf16 v[108:111], v[36:39], a[44:47], v[108:111]\n"
    "v_mul_f32 v172, v168, v172\n"
    "s_waitcnt lgkmcnt(3)\n"
    "v_mfma_f32_16x16x32_bf16 v[80:83], v[40:43], a[48:51], v[80:83]\n"
    "ds_read_b128 v[28:31], v2 offset:7424\n"
    "v_mul_f32 v173, v169, v173\n"
    "v_mfma_f32_16x16x32_bf16 v[88:91], v[40:43], a[52:55], v[88:91]\n"
    "buffer_load_dwordx4 v0, s[4:7], s56 offen offset:2048 lds\n"
    "v_mul_f32 v174, v170, v174\n"
    "v_mfma_f32_16x16x32_bf16 v[96:99], v[40:43], a[56:59], v[96:99]\n"
    "v_rcp_f32 v171, v171\n"
    "v_mfma_f32_16x16x32_bf16 v[104:107], v[40:43], a[60:63], v[104:107]\n"
    "v_exp_f32 v176, -v176\n"
    "v_mfma_f32_16x16x32_bf16 v[84:87], v[44:47], a[48:51], v[84:87]\n"
    "ds_read_b128 v[32:35], v2 offset:9216\n"
    "v_mul_f32 v175, v171, v175\n"
    "v_mfma_f32_16x16x32_bf16 v[92:95], v[44:47], a[52:55], v[92:95]\n"
    "v_cvt_pk_bf16_f32 v15, v174, v175\n"
    "v_cvt_pk_bf16_f32 v14, v172, v173\n"
    "v_mfma_f32_16x16x32_bf16 v[100:103], v[44:47], a[56:59], v[100:103]\n"
    "v_permlane32_swap_b32 v13, v15\n"
    "v_permlane32_swap_b32 v12, v14\n"
    "v_mfma_f32_16x16x32_bf16 v[108:111], v[44:47], a[60:63], v[108:111]\n"
    "buffer_store_dwordx4 v[12:15], v7, s[4:7], s25 offen sc1\n"
    "s_waitcnt lgkmcnt(3)\n"
    "v_mfma_f32_16x16x32_bf16 v[80:83], v[16:19], a[64:67], v[80:83]\n"
    "ds_read_b128 v[36:39], v2 offset:9472\n"
    "v_mul_f32 v181, v177, v181\n"
    "v_mfma_f32_16x16x32_bf16 v[88:91], v[16:19], a[68:71], v[88:91]\n"
    "buffer_load_dwordx4 v0, s[4:7], s56 offen offset:3072 lds\n"
    "v_mul_f32 v182, v178, v182\n"
    "v_mfma_f32_16x16x32_bf16 v[96:99], v[16:19], a[72:75], v[96:99]\n"
    "v_exp_f32 v177, -v177\n"
    "v_mfma_f32_16x16x32_bf16 v[104:107], v[16:19], a[76:79], v[104:107]\n"
    "v_exp_f32 v178, -v178\n"
    "v_mfma_f32_16x16x32_bf16 v[84:87], v[20:23], a[64:67], v[84:87]\n"
    "ds_read_b128 v[40:43], v2 offset:11264\n"
    "v_mul_f32 v183, v179, v183\n"
    "v_mfma_f32_16x16x32_bf16 v[92:95], v[20:23], a[68:71], v[92:95]\n"
    "v_exp_f32 v179, -v179\n"
    "v_mfma_f32_16x16x32_bf16 v[100:103], v[20:23], a[72:75], v[100:103]\n"
    "v_fma_f32 v176, v176, s36, s36\n"
    "v_fma_f32 v177, v177, s36, s36\n"
    "v_mfma_f32_16x16x32_bf16 v[108:111], v[20:23], a[76:79], v[108:111]\n"
    "v_fma_f32 v178, v178, s36, s36\n"
    "s_waitcnt lgkmcnt(3)\n"
    "v_mfma_f32_16x16x32_bf16 v[80:83], v[24:27], a[80:83], v[80:83]\n"
    "ds_read_b128 v[44:47], v2 offset:11520\n"
    "v_fma_f32 v179, v179, s36, s36\n"
    "v_mfma_f32_16x16x32_bf16 v[88:91], v[24:27], a[84:87], v[88:91]\n"
    "s_add_u32 m0, s17, 4096\n"
    "s_add_u32 s56, s22, 4096\n"
    "buffer_load_dwordx4 v0, s[4:7], s56 offen offset:0 lds\n"
    "v_mfma_f32_16x16x32_bf16 v[96:99], v[24:27], a[88:91], v[96:99]\n"
    "v_rcp_f32 v176, v176\n"
    "v_mfma_f32_16x16x32_bf16 v[104:107], v[24:27], a[92:95], v[104:107]\n"
    "v_rcp_f32 v177, v177\n"
    "v_mfma_f32_16x16x32_bf16 v[84:87], v[28:31], a[80:83], v[84:87]\n"
    "ds_read_b128 v[16:19], v2 offset:13312\n"
    "v_mul_f32 v180, v176, v180\n"
    "v_mfma_f32_16x16x32_bf16 v[92:95], v[28:31], a[84:87], v[92:95]\n"
    "v_rcp_f32 v178, v178\n"
    "v_mfma_f32_16x16x32_bf16 v[100:103], v[28:31], a[88:91], v[100:103]\n"
    "v_rcp_f32 v179, v179\n"
    "v_mfma_f32_16x16x32_bf16 v[108:111], v[28:31], a[92:95], v[108:111]\n"
    "v_mul_f32 v181, v177, v181\n"
    "s_waitcnt lgkmcnt(3)\n"
    "v_mfma_f32_16x16x32_bf16 v[80:83], v[32:35], a[96:99], v[80:83]\n"
    "ds_read_b128 v[20:23], v2 offset:13568\n"
    "v_mul_f32 v182, v178, v182\n"
    "v_mfma_f32_16x16x32_bf16 v[88:91], v[32:35], a[100:103], v[88:91]\n"
    "buffer_load_dwordx4 v0, s[4:7], s56 offen offset:1024 lds\n"
    "v_mul_f32 v183, v179, v183\n"
    "v_mfma_f32_16x16x32_bf16 v[96:99], v[32:35], a[104:107], v[96:99]\n"
    "v_cvt_pk_bf16_f32 v9, v182, v183\n"
    "v_cvt_pk_bf16_f32 v8, v180, v181\n"
    "v_mfma_f32_16x16x32_bf16 v[104:107], v[32:35], a[108:111], v[104:107]\n"
    "v_mul_f32 v188, v184, v188\n"
    "v_mul_f32 v189, v185, v189\n"
    "v_mfma_f32_16x16x32_bf16 v[84:87], v[36:39], a[96:99], v[84:87]\n"
    "ds_read_b128 v[24:27], v2 offset:43008\n"
    "v_mul_f32 v190, v186, v190\n"
    "v_mfma_f32_16x16x32_bf16 v[92:95], v[36:39], a[100:103], v[92:95]\n"
    "v_exp_f32 v184, -v184\n"
    "v_mfma_f32_16x16x32_bf16 v[100:103], v[36:39], a[104:107], v[100:103]\n"
    "v_exp_f32 v185, -v185\n"
    "v_mfma_f32_16x16x32_bf16 v[108:111], v[36:39], a[108:111], v[108:111]\n"
    "v_mul_f32 v191, v187, v191\n"
    "s_waitcnt lgkmcnt(3)\n"
    "v_mfma_f32_16x16x32_bf16 v[80:83], v[40:43], a[112:115], v[80:83]\n"
    "ds_read_b128 v[28:31], v2 offset:43264\n"
    "v_fma_f32 v184, v184, s36, s36\n"
    "v_mfma_f32_16x16x32_bf16 v[88:91], v[40:43], a[116:119], v[88:91]\n"
    "buffer_load_dwordx4 v0, s[4:7], s56 offen offset:2048 lds\n"
    "v_fma_f32 v185, v185, s36, s36\n"
    "v_mfma_f32_16x16x32_bf16 v[96:99], v[40:43], a[120:123], v[96:99]\n"
    "v_exp_f32 v186, -v186\n"
    "v_mfma_f32_16x16x32_bf16 v[104:107], v[40:43], a[124:127], v[104:107]\n"
    "v_exp_f32 v187, -v187\n"
    "v_mfma_f32_16x16x32_bf16 v[84:87], v[44:47], a[112:115], v[84:87]\n"
    "ds_read_b128 v[32:35], v2 offset:45056\n"
    "v_fma_f32 v186, v186, s36, s36\n"
    "v_mfma_f32_16x16x32_bf16 v[92:95], v[44:47], a[116:119], v[92:95]\n"
    "v_rcp_f32 v184, v184\n"
    "v_mfma_f32_16x16x32_bf16 v[100:103], v[44:47], a[120:123], v[100:103]\n"
    "buffer_load_dword v218, v213, s[4:7], s40 offen offset:0 sc1\n"
    "v_fma_f32 v187, v187, s36, s36\n"
    "v_mfma_f32_16x16x32_bf16 v[108:111], v[44:47], a[124:127], v[108:111]\n"
    "v_mul_f32 v188, v184, v188\n"
    "s_waitcnt lgkmcnt(3)\n"
    "v_mfma_f32_16x16x32_bf16 v[80:83], v[16:19], a[128:131], v[80:83]\n"
    "ds_read_b128 v[36:39], v2 offset:45312\n"
    "s_add_u32 s23, s23, 8192\n"
    "v_mfma_f32_16x16x32_bf16 v[88:91], v[16:19], a[132:135], v[88:91]\n"
    "buffer_load_dwordx4 v0, s[4:7], s56 offen offset:3072 lds\n"
    "v_mul_f32 v196, v192, v196\n"
    "v_mfma_f32_16x16x32_bf16 v[96:99], v[16:19], a[136:139], v[96:99]\n"
    "v_rcp_f32 v185, v185\n"
    "v_mfma_f32_16x16x32_bf16 v[104:107], v[16:19], a[140:143], v[104:107]\n"
    "v_rcp_f32 v186, v186\n"
    "v_mfma_f32_16x16x32_bf16 v[84:87], v[20:23], a[128:131], v[84:87]\n"
    "ds_read_b128 v[40:43], v2 offset:47104\n"
    "v_mul_f32 v189, v185, v189\n"
    "v_mfma_f32_16x16x32_bf16 v[92:95], v[20:23], a[132:135], v[92:95]\n"
    "v_rcp_f32 v187, v187\n"
    "v_mfma_f32_16x16x32_bf16 v[100:103], v[20:23], a[136:139], v[100:103]\n"
    "v_exp_f32 v192, -v192\n"
    "v_mfma_f32_16x16x32_bf16 v[108:111], v[20:23], a[140:143], v[108:111]\n"
    "v_mul_f32 v190, v186, v190\n"
    "s_waitcnt lgkmcnt(3)\n"
    "v_mfma_f32_16x16x32_bf16 v[80:83], v[24:27], a[144:147], v[80:83]\n"
    "ds_read_b128 v[44:47], v2 offset:47360\n"
    "v_mul_f32 v191, v187, v191\n"
    "v_mfma_f32_16x16x32_bf16 v[88:91], v[24:27], a[148:151], v[88:91]\n"
    "s_add_u32 m0, s17, 8192\n"
    "s_add_u32 s56, s22, 8192\n"
    "buffer_load_dwordx4 v0, s[4:7], s56 offen offset:0 lds\n"
    "v_mfma_f32_16x16x32_bf16 v[96:99], v[24:27], a[152:155], v[96:99]\n"
    "v_cvt_pk_bf16_f32 v11, v190, v191\n"
    "v_cvt_pk_bf16_f32 v10, v188, v189\n"
    "v_mfma_f32_16x16x32_bf16 v[104:107], v[24:27], a[156:159], v[104:107]\n"
    "v_permlane32_swap_b32 v9, v11\n"
    "v_permlane32_swap_b32 v8, v10\n"
    "v_mfma_f32_16x16x32_bf16 v[84:87], v[28:31], a[144:147], v[84:87]\n"
    "ds_read_b128 v[16:19], v3 offset:0\n"
    "buffer_store_dwordx4 v[8:11], v6, s[4:7], s23 offen sc1\n"
    "v_mfma_f32_16x16x32_bf16 v[92:95], v[28:31], a[148:151], v[92:95]\n"
    "v_mul_f32 v197, v193, v197\n"
    "v_mul_f32 v198, v194, v198\n"
    "v_mfma_f32_16x16x32_bf16 v[100:103], v[28:31], a[152:155], v[100:103]\n"
    "v_exp_f32 v193, -v193\n"
    "v_mfma_f32_16x16x32_bf16 v[108:111], v[28:31], a[156:159], v[108:111]\n"
    "v_mul_f32 v199, v195, v199\n"
    "s_waitcnt lgkmcnt(3)\n"
    "v_mfma_f32_16x16x32_bf16 v[80:83], v[32:35], a[160:163], v[80:83]\n"
    "ds_read_b128 v[20:23], v3 offset:256\n"
    "v_fma_f32 v192, v192, s36, s36\n"
    "v_mfma_f32_16x16x32_bf16 v[88:91], v[32:35], a[164:167], v[88:91]\n"
    "s_add_u32 m0, s17, 9216\n"
    "v_fma_f32 v193, v193, s36, s36\n"
    "v_mfma_f32_16x16x32_bf16 v[96:99], v[32:35], a[168:171], v[96:99]\n"
    "buffer_load_dwordx4 v222, s[4:7], s47 offen offset:0 lds\n"
    "v_mul_f32 v204, v200, v204\n"
    "v_mfma_f32_16x16x32_bf16 v[104:107], v[32:35], a[172:175], v[104:107]\n"
    "v_exp_f32 v194, -v194\n"
    "v_mfma_f32_16x16x32_bf16 v[84:87], v[36:39], a[160:163], v[84:87]\n"
    "ds_read_b128 v[24:27], v5 offset:26624\n"
    "v_fma_f32 v194, v194, s36, s36\n"
    "v_mfma_f32_16x16x32_bf16 v[92:95], v[36:39], a[164:167], v[92:95]\n"
    "buffer_load_dwordx4 v222, s[4:7], s48 offen offset:1024 lds\n"
    "v_mul_f32 v205, v201, v205\n"
    "v_mfma_f32_16x16x32_bf16 v[100:103], v[36:39], a[168:171], v[100:103]\n"
    "v_exp_f32 v195, -v195\n"
    "v_mfma_f32_16x16x32_bf16 v[108:111], v[36:39], a[172:175], v[108:111]\n"
    "v_fma_f32 v195, v195, s36, s36\n"
    "s_waitcnt lgkmcnt(3)\n"
    "v_mfma_f32_16x16x32_bf16 v[80:83], v[40:43], a[176:179], v[80:83]\n"
    "ds_read_b128 v[28:31], v5 offset:26880\n"
    "v_mul_f32 v206, v202, v206\n"
    "v_mfma_f32_16x16x32_bf16 v[88:91], v[40:43], a[180:183], v[88:91]\n"
    "v_rcp_f32 v192, v192\n"
    "v_mfma_f32_16x16x32_bf16 v[96:99], v[40:43], a[184:187], v[96:99]\n"
    "buffer_load_dwordx4 v222, s[4:7], s30 offen offset:2048 lds\n"
    "v_mul_f32 v196, v192, v196\n"
    "v_mfma_f32_16x16x32_bf16 v[104:107], v[40:43], a[188:191], v[104:107]\n"
    "v_rcp_f32 v193, v193\n"
    "v_mfma_f32_16x16x32_bf16 v[84:87], v[44:47], a[176:179], v[84:87]\n"
    "ds_read_b128 v[32:35], v3 offset:31744\n"
    "v_mul_f32 v197, v193, v197\n"
    "v_mfma_f32_16x16x32_bf16 v[92:95], v[44:47], a[180:183], v[92:95]\n"
    "v_rcp_f32 v194, v194\n"
    "v_mfma_f32_16x16x32_bf16 v[100:103], v[44:47], a[184:187], v[100:103]\n"
    "v_rcp_f32 v195, v195\n"
    "v_mfma_f32_16x16x32_bf16 v[108:111], v[44:47], a[188:191], v[108:111]\n"
    "buffer_load_dwordx4 v222, s[4:7], s50 offen offset:3072 lds\n"
    "s_waitcnt lgkmcnt(3)\n"
    "v_mfma_f32_16x16x32_bf16 v[80:83], v[16:19], a[192:195], v[80:83]\n"
    "ds_read_b128 v[36:39], v3 offset:32000\n"
    "v_mul_f32 v198, v194, v198\n"
    "v_mfma_f32_16x16x32_bf16 v[88:91], v[16:19], a[196:199], v[88:91]\n"
    "v_exp_f32 v200, -v200\n"
    "v_mfma_f32_16x16x32_bf16 v[96:99], v[16:19], a[200:203], v[96:99]\n"
    "v_exp_f32 v201, -v201\n"
    "v_mfma_f32_16x16x32_bf16 v[104:107], v[16:19], a[204:207], v[104:107]\n"
    "v_exp_f32 v202, -v202\n"
    "v_mfma_f32_16x16x32_bf16 v[84:87], v[20:23], a[192:195], v[84:87]\n"
    "ds_read_b128 v[40:43], v3 offset:33792\n"
    "v_mul_f32 v199, v195, v199\n"
    "v_mfma_f32_16x16x32_bf16 v[92:95], v[20:23], a[196:199], v[92:95]\n"
    "s_add_u32 m0, s17, 13312\n"
    "v_cvt_pk_bf16_f32 v13, v198, v199\n"
    "v_mfma_f32_16x16x32_bf16 v[100:103], v[20:23], a[200:203], v[100:103]\n"
    "buffer_load_dwordx4 v222, s[4:7], s51 offen offset:0 lds\n"
    "v_cvt_pk_bf16_f32 v12, v196, v197\n"
    "v_mfma_f32_16x16x32_bf16 v[108:111], v[20:23], a[204:207], v[108:111]\n"
    "v_mul_f32 v207, v203, v207\n"
    "s_waitcnt lgkmcnt(3)\n"
    "v_mfma_f32_16x16x32_bf16 v[80:83], v[24:27], a[208:211], v[80:83]\n"
    "ds_read_b128 v[44:47], v3 offset:34048\n"
    "v_fma_f32 v200, v200, s36, s36\n"
    "v_mfma_f32_16x16x32_bf16 v[88:91], v[24:27], a[212:215], v[88:91]\n"
    "buffer_load_dwordx4 v222, s[4:7], s49 offen offset:1024 lds\n"
    "v_fma_f32 v201, v201, s36, s36\n"
    "v_mfma_f32_16x16x32_bf16 v[96:99], v[24:27], a[216:219], v[96:99]\n"
    "v_exp_f32 v203, -v203\n"
    "v_mfma_f32_16x16x32_bf16 v[104:107], v[24:27], a[220:223], v[104:107]\n"
    "v_rcp_f32 v200, v200\n"
    "v_mfma_f32_16x16x32_bf16 v[84:87], v[28:31], a[208:211], v[84:87]\n"
    "ds_read_b128 v[16:19], v3 offset:35840\n"
    "v_fma_f32 v202, v202, s36, s36\n"
    "v_mfma_f32_16x16x32_bf16 v[92:95], v[28:31], a[212:215], v[92:95]\n"
    "v_rcp_f32 v201, v201\n"
    "v_mfma_f32_16x16x32_bf16 v[100:103], v[28:31], a[216:219], v[100:103]\n"
    "buffer_load_dwordx4 v222, s[4:7], s31 offen offset:2048 lds\n"
    "v_fma_f32 v203, v203, s36, s36\n"
    "v_mfma_f32_16x16x32_bf16 v[108:111], v[28:31], a[220:223], v[108:111]\n"
    "v_mul_f32 v204, v200, v204\n"
    "s_waitcnt lgkmcnt(3)\n"
    "v_mfma_f32_16x16x32_bf16 v[80:83], v[32:35], a[224:227], v[80:83]\n"
    "ds_read_b128 v[20:23], v3 offset:36096\n"
    "v_mul_f32 v205, v201, v205\n"
    "v_mfma_f32_16x16x32_bf16 v[88:91], v[32:35], a[228:231], v[88:91]\n"
    "v_rcp_f32 v202, v202\n"
    "v_mfma_f32_16x16x32_bf16 v[96:99], v[32:35], a[232:235], v[96:99]\n"
    "v_rcp_f32 v203, v203\n"
    "v_mfma_f32_16x16x32_bf16 v[104:107], v[32:35], a[236:239], v[104:107]\n"
    "v_mul_f32 v206, v202, v206\n"
    "v_mul_f32 v207, v203, v207\n"
    "v_mfma_f32_16x16x32_bf16 v[84:87], v[36:39], a[224:227], v[84:87]\n"
    "ds_read_b128 v[24:27], v3 offset:37888\n"
    "v_cvt_pk_bf16_f32 v15, v206, v207\n"
    "v_mfma_f32_16x16x32_bf16 v[92:95], v[36:39], a[228:231], v[92:95]\n"
    "v_cvt_pk_bf16_f32 v14, v204, v205\n"
    "v_permlane32_swap_b32 v13, v15\n"
    "v_mfma_f32_16x16x32_bf16 v[100:103], v[36:39], a[232:235], v[100:103]\n"
    "LPK4:\n"
    "s_cbranch_execz LPK8\n"
    "v_permlane32_swap_b32 v12, v14\n"
    "buffer_store_dwordx4 v[12:15], v7, s[4:7], s23 offen sc1\n"
    "v_mfma_f32_16x16x32_bf16 v[108:111], v[36:39], a[236:239], v[108:111]\n"
    "v_mul_f32 v52, v48, v52\n"
    "s_waitcnt lgkmcnt(3)\n"
    "v_mfma_f32_16x16x32_bf16 v[80:83], v[40:43], a[240:243], v[80:83]\n"
    "ds_read_b128 v[28:31], v3 offset:38144\n"
    "v_mul_f32 v53, v49, v53\n"
    "v_mfma_f32_16x16x32_bf16 v[88:91], v[40:43], a[244:247], v[88:91]\n"
    "v_exp_f32 v48, -v48\n"
    "v_mfma_f32_16x16x32_bf16 v[96:99], v[40:43], a[248:251], v[96:99]\n"
    "v_exp_f32 v49, -v49\n"
    "v_mfma_f32_16x16x32_bf16 v[104:107], v[40:43], a[252:255], v[104:107]\n"
    "v_mul_f32 v54, v50, v54\n"
    "v_mul_f32 v55, v51, v55\n"
    "v_mfma_f32_16x16x32_bf16 v[84:87], v[44:47], a[240:243], v[84:87]\n"
    "s_waitcnt vmcnt(16)\n"
    "s_barrier\n"
    "s_cmp_lg_u32 s17, 0\n"
    "s_cbranch_scc1 LF2%=\n"
    "s_mov_b64 exec, 1\n"
    "buffer_store_dword v216, off, s[4:7], s20 offset:32 sc1\n"
    "s_mov_b64 exec, -1\n"
    "LF2%=:\n"
    "ds_read_b128 v[32:35], v1 offset:27648\n"
    "v_mfma_f32_16x16x32_bf16 v[92:95], v[44:47], a[244:247], v[92:95]\n"
    "s_add_u32 m0, s17, 16384\n"
    "v_add_u32 v222, 4096, v222\n"
    "v_mfma_f32_16x16x32_bf16 v[100:103], v[44:47], a[248:251], v[100:103]\n"
    "buffer_load_dwordx4 v222, s[4:7], s47 offen offset:0 lds\n"
    "v_fma_f32 v48, v48, s36, s36\n"
    "v_mfma_f32_16x16x32_bf16 v[108:111], v[44:47], a[252:255], v[108:111]\n"
    "v_fma_f32 v49, v49, s36, s36\n"
    "s_waitcnt lgkmcnt(3)\n"
    "v_mfma_f32_16x16x32_bf16 v[80:83], v[16:19], v[224:227], v[80:83]\n"
    "ds_read_b128 v[36:39], v1 offset:27904\n"
    "v_mul_f32 v60, v56, v60\n"
    "v_mfma_f32_16x16x32_bf16 v[88:91], v[16:19], v[228:231], v[88:91]\n"
    "v_exp_f32 v50, -v50\n"
    "v_mfma_f32_16x16x32_bf16 v[96:99], v[16:19], v[232:235], v[96:99]\n"
    "buffer_load_dwordx4 v222, s[4:7], s48 offen offset:1024 lds\n"
    "v_fma_f32 v50, v50, s36, s36\n"
    "v_mfma_f32_16x16x32_bf16 v[104:107], v[16:19], v[236:239], v[104:107]\n"
    "v_exp_f32 v51, -v51\n"
    "v_mfma_f32_16x16x32_bf16 v[84:87], v[20:23], v[224:227], v[84:87]\n"
    "ds_read_b128 v[40:43], v1 offset:29696\n"
    "s_waitcnt vmcnt(13)\n"
    "v_cmp_ne_u32 vcc, v218, v216\n"
    "s_cbranch_vccnz LP8%=\n"
    "LQ8%=:\n"
    "v_mfma_f32_16x16x32_bf16 v[92:95], v[20:23], v[228:231], v[92:95]\n"
    "v_rcp_f32 v48, v48\n"
    "v_mfma_f32_16x16x32_bf16 v[100:103], v[20:23], v[232:235], v[100:103]\n"
    "v_rcp_f32 v49, v49\n"
    "v_mfma_f32_16x16x32_bf16 v[108:111], v[20:23], v[236:239], v[108:111]\n"
    "buffer_load_dwordx4 v[176:179], v0, s[4:7], s28 offen offset:0 sc1\n"
    "s_waitcnt lgkmcnt(3)\n"
    "v_mfma_f32_16x16x32_bf16 v[80:83], v[24:27], v[240:243], v[80:83]\n"
    "ds_read_b128 v[44:47], v1 offset:29952\n"
    "v_fma_f32 v51, v51, s36, s36\n"
    "v_mfma_f32_16x16x32_bf16 v[88:91], v[24:27], v[244:247], v[88:91]\n"
    "v_rcp_f32 v50, v50\n"
    "v_mfma_f32_16x16x32_bf16 v[96:99], v[24:27], v[248:251], v[96:99]\n"
    "v_rcp_f32 v51, v51\n"
    "v_mfma_f32_16x16x32_bf16 v[104:107], v[24:27], v[252:255], v[104:107]\n"
    "v_exp_f32 v56, -v56\n"
    "v_mfma_f32_16x16x32_bf16 v[84:87], v[28:31], v[240:243], v[84:87]\n"
    "ds_read_b128 v[16:19], v1 offset:31744\n"
    "v_mul_f32 v52, v48, v52\n"
    "v_mfma_f32_16x16x32_bf16 v[92:95], v[28:31], v[244:247], v[92:95]\n"
    "buffer_load_dwordx4 v[180:183], v0, s[4:7], s29 offen offset:0 sc1\n"
    "v_mul_f32 v53, v49, v53\n"
    "v_mfma_f32_16x16x32_bf16 v[100:103], v[28:31], v[248:251], v[100:103]\n"
    "v_mul_f32 v54, v50, v54\n"
    "v_mul_f32 v55, v51, v55\n"
    "v_mfma_f32_16x16x32_bf16 v[108:111], v[28:31], v[252:255], v[108:111]\n"
    "v_cvt_pk_bf16_f32 v9, v54, v55\n"
    "s_waitcnt lgkmcnt(3)\n"
    "v_mfma_f32_16x16x32_bf16 v[112:115], v[32:35], a[0:3], 0\n"
    "ds_read_b128 v[20:23], v1 offset:32000\n"
    "v_cvt_pk_bf16_f32 v8, v52, v53\n"
    "v_mfma_f32_16x16x32_bf16 v[120:123], v[32:35], a[4:7], 0\n"
    "buffer_load_dwordx4 v[184:187], v0, s[4:7], s28 offen offset:1024 sc1\n"
    "v_mul_f32 v61, v57, v61\n"
    "v_mfma_f32_16x16x32_bf16 v[128:131], v[32:35], a[8:11], 0\n"
    "v_exp_f32 v57, -v57\n"
    "v_mfma_f32_16x16x32_bf16 v[136:139], v[32:35], a[12:15], 0\n"
    "v_mul_f32 v62, v58, v62\n"
    "v_mul_f32 v63, v59, v63\n"
    "v_mfma_f32_16x16x32_bf16 v[116:119], v[36:39], a[0:3], 0\n"
    "ds_read_b128 v[24:27], v1 offset:33792\n"
    "v_fma_f32 v56, v56, s36, s36\n"
    "v_mfma_f32_16x16x32_bf16 v[124:127], v[36:39], a[4:7], 0\n"
    "v_exp_f32 v58, -v58\n"
    "v_mfma_f32_16x16x32_bf16 v[132:135], v[36:39], a[8:11], 0\n"
    "buffer_load_dwordx4 v[188:191], v0, s[4:7], s29 offen offset:1024 sc1\n"
    "v_fma_f32 v57, v57, s36, s36\n"
    "v_mfma_f32_16x16x32_bf16 v[140:143], v[36:39], a[12:15], 0\n"
    "v_fma_f32 v58, v58, s36, s36\n"
    "s_waitcnt lgkmcnt(3)\n"
    "v_mfma_f32_16x16x32_bf16 v[112:115], v[40:43], a[16:19], v[112:115]\n"
    "ds_read_b128 v[28:31], v1 offset:34048\n"
    "s_add_u32 s25, s25, 8192\n"
    "v_mfma_f32_16x16x32_bf16 v[120:123], v[40:43], a[20:23], v[120:123]\n"
    "v_exp_f32 v59, -v59\n"
    "v_mfma_f32_16x16x32_bf16 v[128:131], v[40:43], a[24:27], v[128:131]\n"
    "v_rcp_f32 v56, v56\n"
    "v_mfma_f32_16x16x32_bf16 v[136:139], v[40:43], a[28:31], v[136:139]\n"
    "buffer_load_dwordx4 v222, s[4:7], s30 offen offset:2048 lds\n"
    "v_fma_f32 v59, v59, s36, s36\n"
    "v_mfma_f32_16x16x32_bf16 v[116:119], v[44:47], a[16:19], v[116:119]\n"
    "ds_read_b128 v[32:35], v4 offset:35840\n"
    "v_mul_f32 v60, v56, v60\n"
    "v_mfma_f32_16x16x32_bf16 v[124:127], v[44:47], a[20:23], v[124:127]\n"
    "v_rcp_f32 v57, v57\n"
    "v_mfma_f32_16x16x32_bf16 v[132:135], v[44:47], a[24:27], v[132:135]\n"
    "v_rcp_f32 v58, v58\n"
    "v_mfma_f32_16x16x32_bf16 v[140:143], v[44:47], a[28:31], v[140:143]\n"
    "v_mul_f32 v61, v57, v61\n"
    "s_waitcnt lgkmcnt(3)\n"
    "v_mfma_f32_16x16x32_bf16 v[112:115], v[16:19], a[32:35], v[112:115]\n"
    "ds_read_b128 v[36:39], v4 offset:36096\n"
    "v_mul_f32 v62, v58, v62\n"
    "v_mfma_f32_16x16x32_bf16 v[120:123], v[16:19], a[36:39], v[120:123]\n"
    "buffer_load_dwordx4 v222, s[4:7], s50 offen offset:3072 lds\n"
    "v_cvt_pk_bf16_f32 v10, v60, v61\n"
    "v_mfma_f32_16x16x32_bf16 v[128:131], v[16:19], a[40:43], v[128:131]\n"
    "v_rcp_f32 v59, v59\n"
    "v_mfma_f32_16x16x32_bf16 v[136:139], v[16:19], a[44:47], v[136:139]\n"
    "v_mul_f32 v63, v59, v63\n"
    "v_cvt_pk_bf16_f32 v11, v62, v63\n"
    "v_mfma_f32_16x16x32_bf16 v[116:119], v[20:23], a[32:35], v[116:119]\n"
    "ds_read_b128 v[40:43], v2 offset:16384\n"
    "v_permlane32_swap_b32 v9, v11\n"
    "v_mfma_f32_16x16x32_bf16 v[124:127], v[20:23], a[36:39], v[124:127]\n"
    "s_add_u32 m0, s17, 20480\n"
    "v_permlane32_swap_b32 v8, v10\n"
    "v_mfma_f32_16x16x32_bf16 v[132:135], v[20:23], a[40:43], v[132:135]\n"
    "buffer_load_dwordx4 v222, s[4:7], s51 offen offset:0 lds\n"
    "buffer_store_dwordx4 v[8:11], v6, s[4:7], s25 offen sc1\n"
    "v_mfma_f32_16x16x32_bf16 v[140:143], v[20:23], a[44:47], v[140:143]\n"
    "v_mul_f32 v68, v64, v68\n"
    "s_waitcnt lgkmcnt(3)\n"
    "v_mfma_f32_16x16x32_bf16 v[112:115], v[24:27], a[48:51], v[112:115]\n"
    "ds_read_b128 v[44:47], v2 offset:16640\n"
    "v_mul_f32 v69, v65, v69\n"
    "v_mfma_f32_16x16x32_bf16 v[120:123], v[24:27], a[52:55], v[120:123]\n"
    "v_exp_f32 v64, -v64\n"
    "v_mfma_f32_16x16x32_bf16 v[128:131], v[24:27], a[56:59], v[128:131]\n"
    "buffer_load_dwordx4 v222, s[4:7], s49 offen offset:1024 lds\n"
    "v_mul_f32 v70, v66, v70\n"
    "v_mfma_f32_16x16x32_bf16 v[136:139], v[24:27], a[60:63], v[136:139]\n"
    "v_exp_f32 v65, -v65\n"
    "v_mfma_f32_16x16x32_bf16 v[116:119], v[28:31], a[48:51], v[116:119]\n"
    "ds_read_b128 v[16:19], v2 offset:18432\n"
    "v_mul_f32 v71, v67, v71\n"
    "v_mfma_f32_16x16x32_bf16 v[124:127], v[28:31], a[52:55], v[124:127]\n"
    "v_exp_f32 v66, -v66\n"
    "v_mfma_f32_16x16x32_bf16 v[132:135], v[28:31], a[56:59], v[132:135]\n"
    "v_exp_f32 v67, -v67\n"
    "v_mfma_f32_16x16x32_bf16 v[140:143], v[28:31], a[60:63], v[140:143]\n"
    "buffer_load_dwordx4 v222, s[4:7], s31 offen offset:2048 lds\n"
    "s_waitcnt lgkmcnt(3)\n"
    "v_mfma_f32_16x16x32_bf16 v[112:115], v[32:35], a[64:67], v[112:115]\n"
    "ds_read_b128 v[20:23], v2 offset:18688\n"
    "v_fma_f32 v64, v64, s36, s36\n"
    "v_mfma_f32_16x16x32_bf16 v[120:123], v[32:35], a[68:71], v[120:123]\n"
    "v_rcp_f32 v64, v64\n"
    "v_mfma_f32_16x16x32_bf16 v[128:131], v[32:35], a[72:75], v[128:131]\n"
    "v_fma_f32 v65, v65, s36, s36\n"
    "v_fma_f32 v66, v66, s36, s36\n"
    "v_mfma_f32_16x16x32_bf16 v[136:139], v[32:35], a[76:79], v[136:139]\n"
    "v_rcp_f32 v65, v65\n"
    "v_mfma_f32_16x16x32_bf16 v[116:119], v[36:39], a[64:67], v[116:119]\n"
    "ds_read_b128 v[24:27], v2 offset:20480\n"
    "v_fma_f32 v67, v67, s36, s36\n"
    "v_mfma_f32_16x16x32_bf16 v[124:127], v[36:39], a[68:71], v[124:127]\n"
    "buffer_load_dwordx4 v[192:195], v0, s[4:7], s28 offen offset:2048 sc1\n"
    "v_mul_f32 v68, v64, v68\n"
    "v_mfma_f32_16x16x32_bf16 v[132:135], v[36:39], a[72:75], v[132:135]\n"
    "v_rcp_f32 v66, v66\n"
    "v_mfma_f32_16x16x32_bf16 v[140:143], v[36:39], a[76:79], v[140:143]\n"
    "v_mul_f32 v69, v65, v69\n"
    "s_waitcnt lgkmcnt(3)\n"
    "v_mfma_f32_16x16x32_bf16 v[112:115], v[40:43], a[80:83], v[112:115]\n"
    "ds_read_b128 v[28:31], v2 offset:20736\n"
    "v_mul_f32 v70, v66, v70\n"
    "v_mfma_f32_16x16x32_bf16 v[120:123], v[40:43], a[84:87], v[120:123]\n"
    "buffer_load_dwordx4 v[196:199], v0, s[4:7], s29 offen offset:2048 sc1\n"
    "v_cvt_pk_bf16_f32 v12, v68, v69\n"
    "v_mfma_f32_16x16x32_bf16 v[128:131], v[40:43], a[88:91], v[128:131]\n"
    "v_rcp_f32 v67, v67\n"
    "v_mfma_f32_16x16x32_bf16 v[136:139], v[40:43], a[92:95], v[136:139]\n"
    "v_mul_f32 v71, v67, v71\n"
    "v_cvt_pk_bf16_f32 v13, v70, v71\n"
    "v_mfma_f32_16x16x32_bf16 v[116:119], v[44:47], a[80:83], v[116:119]\n"
    "ds_read_b128 v[32:35], v2 offset:22528\n"
    "v_mul_f32 v76, v72, v76\n"
    "v_mfma_f32_16x16x32_bf16 v[124:127], v[44:47], a[84:87], v[124:127]\n"
    "v_exp_f32 v72, -v72\n"
    "v_mfma_f32_16x16x32_bf16 v[132:135], v[44:47], a[88:91], v[132:135]\n"
    "buffer_load_dwordx4 v[200:203], v0, s[4:7], s28 offen offset:3072 sc1\n"
    "v_mul_f32 v77, v73, v77\n"
    "v_mfma_f32_16x16x32_bf16 v[140:143], v[44:47], a[92:95], v[140:143]\n"
    "v_mul_f32 v78, v74, v78\n"
    "s_waitcnt lgkmcnt(3)\n"
    "v_mfma_f32_16x16x32_bf16 v[112:115], v[16:19], a[96:99], v[112:115]\n"
    "ds_read_b128 v[36:39], v2 offset:22784\n"
    "v_mul_f32 v79, v75, v79\n"
    "v_mfma_f32_16x16x32_bf16 v[120:123], v[16:19], a[100:103], v[120:123]\n"
    "v_exp_f32 v73, -v73\n"
    "v_mfma_f32_16x16x32_bf16 v[128:131], v[16:19], a[104:107], v[128:131]\n"
    "v_exp_f32 v74, -v74\n"
    "v_mfma_f32_16x16x32_bf16 v[136:139], v[16:19], a[108:111], v[136:139]\n"
    "buffer_load_dwordx4 v[204:207], v0, s[4:7], s29 offen offset:3072 sc1\n"
    "v_fma_f32 v72, v72, s36, s36\n"
    "v_mfma_f32_16x16x32_bf16 v[116:119], v[20:23], a[96:99], v[116:119]\n"
    "ds_read_b128 v[40:43], v3 offset:3072\n"
    "v_fma_f32 v73, v73, s36, s36\n"
    "v_mfma_f32_16x16x32_bf16 v[124:127], v[20:23], a[100:103], v[124:127]\n"
    "v_exp_f32 v75, -v75\n"
    "v_mfma_f32_16x16x32_bf16 v[132:135], v[20:23], a[104:107], v[132:135]\n"
    "v_rcp_f32 v72, v72\n"
    "v_mfma_f32_16x16x32_bf16 v[140:143], v[20:23], a[108:111], v[140:143]\n"
    "v_fma_f32 v74, v74, s36, s36\n"
    "s_waitcnt lgkmcnt(3)\n"
    "v_mfma_f32_16x16x32_bf16 v[112:115], v[24:27], a[112:115], v[112:115]\n"
    "ds_read_b128 v[44:47], v3 offset:3328\n"
    "v_fma_f32 v75, v75, s36, s36\n"
    "v_mfma_f32_16x16x32_bf16 v[120:123], v[24:27], a[116:119], v[120:123]\n"
    "s_add_u32 m0, s17, 23552\n"
    "v_add_u32 v222, 4096, v222\n"
    "v_mfma_f32_16x16x32_bf16 v[128:131], v[24:27], a[120:123], v[128:131]\n"
    "buffer_load_dword v218, v213, s[4:7], s40 offen offset:32 sc1\n"
    "v_mul_f32 v76, v72, v76\n"
    "v_mfma_f32_16x16x32_bf16 v[136:139], v[24:27], a[124:127], v[136:139]\n"
    "buffer_load_dwordx4 v222, s[4:7], s47 offen offset:0 lds\n"
    "v_mul_f32 v84, v80, v84\n"
    "v_mfma_f32_16x16x32_bf16 v[116:119], v[28:31], a[112:115], v[116:119]\n"
    "ds_read_b128 v[16:19], v3 offset:5120\n"
    "v_mul_f32 v85, v81, v85\n"
    "v_mfma_f32_16x16x32_bf16 v[124:127], v[28:31], a[116:119], v[124:127]\n"
    "buffer_load_dwordx4 v222, s[4:7], s48 offen offset:1024 lds\n"
    "v_mul_f32 v86, v82, v86\n"
    "v_mfma_f32_16x16x32_bf16 v[132:135], v[28:31], a[120:123], v[132:135]\n"
    "v_rcp_f32 v73, v73\n"
    "v_mfma_f32_16x16x32_bf16 v[140:143], v[28:31], a[124:127], v[140:143]\n"
    "v_mul_f32 v77, v73, v77\n"
    "s_waitcnt lgkmcnt(3)\n"
    "v_mfma_f32_16x16x32_bf16 v[112:115], v[32:35], a[128:131], v[112:115]\n"
    "ds_read_b128 v[20:23], v3 offset:5376\n"
    "v_cvt_pk_bf16_f32 v14, v76, v77\n"
    "v_mfma_f32_16x16x32_bf16 v[120:123], v[32:35], a[132:135], v[120:123]\n"
    "v_rcp_f32 v74, v74\n"
    "v_mfma_f32_16x16x32_bf16 v[128:131], v[32:35], a[136:139], v[128:131]\n"
    "buffer_load_dwordx4 v222, s[4:7], s30 offen offset:2048 lds\n"
    "v_mul_f32 v78, v74, v78\n"
    "v_mfma_f32_16x16x32_bf16 v[136:139], v[32:35], a[140:143], v[136:139]\n"
    "v_rcp_f32 v75, v75\n"
    "v_mfma_f32_16x16x32_bf16 v[116:119], v[36:39], a[128:131], v[116:119]\n"
    "ds_read_b128 v[24:27], v3 offset:7168\n"
    "v_mul_f32 v79, v75, v79\n"
    "v_mfma_f32_16x16x32_bf16 v[124:127], v[36:39], a[132:135], v[124:127]\n"
    "v_exp_f32 v80, -v80\n"
    "v_mfma_f32_16x16x32_bf16 v[132:135], v[36:39], a[136:139], v[132:135]\n"
    "v_exp_f32 v81, -v81\n"
    "v_mfma_f32_16x16x32_bf16 v[140:143], v[36:39], a[140:143], v[140:143]\n"
    "buffer_load_dwordx4 v222, s[4:7], s50 offen offset:3072 lds\n"
    "s_waitcnt lgkmcnt(3)\n"
    "v_mfma_f32_16x16x32_bf16 v[112:115], v[40:43], a[144:147], v[112:115]\n"
    "ds_read_b128 v[28:31], v3 offset:7424\n"
    "v_cvt_pk_bf16_f32 v15, v78, v79\n"
    "v_mfma_f32_16x16x32_bf16 v[120:123], v[40:43], a[148:151], v[120:123]\n"
    "v_exp_f32 v82, -v82\n"
    "v_mfma_f32_16x16x32_bf16 v[128:131], v[40:43], a[152:155], v[128:131]\n"
    "v_permlane32_swap_b32 v13, v15\n"
    "v_permlane32_swap_b32 v12, v14\n"
    "v_mfma_f32_16x16x32_bf16 v[136:139], v[40:43], a[156:159], v[136:139]\n"
    "buffer_store_dwordx4 v[12:15], v7, s[4:7], s25 offen sc1\n"
    "v_mul_f32 v87, v83, v87\n"
    "v_mfma_f32_16x16x32_bf16 v[116:119], v[44:47], a[144:147], v[116:119]\n"
    "ds_read_b128 v[32:35], v3 offset:9216\n"
    "v_fma_f32 v80, v80, s36, s36\n"
    "v_mfma_f32_16x16x32_bf16 v[124:127], v[44:47], a[148:151], v[124:127]\n"
    "s_mov_b32 m0, s11\n"
    "v_fma_f32 v81, v81, s36, s36\n"
    "v_mfma_f32_16x16x32_bf16 v[132:135], v[44:47], a[152:155], v[132:135]\n"
    "buffer_load_dwordx4 v222, s[4:7], s51 offen offset:0 lds\n"
    "v_fma_f32 v82, v82, s36, s36\n"
    "v_mfma_f32_16x16x32_bf16 v[140:143], v[44:47], a[156:159], v[140:143]\n"
    "v_mul_f32 v92, v88, v92\n"
    "s_waitcnt lgkmcnt(3)\n"
    "v_mfma_f32_16x16x32_bf16 v[112:115], v[16:19], a[160:163], v[112:115]\n"
    "ds_read_b128 v[36:39], v3 offset:9472\n"
    "v_mul_f32 v93, v89, v93\n"
    "v_mfma_f32_16x16x32_bf16 v[120:123], v[16:19], a[164:167], v[120:123]\n"
    "buffer_load_dwordx4 v222, s[4:7], s49 offen offset:1024 lds\n"
    "v_mul_f32 v94, v90, v94\n"
    "v_mfma_f32_16x16x32_bf16 v[128:131], v[16:19], a[168:171], v[128:131]\n"
    "v_exp_f32 v83, -v83\n"
    "v_mfma_f32_16x16x32_bf16 v[136:139], v[16:19], a[172:175], v[136:139]\n"
    "v_rcp_f32 v80, v80\n"
    "v_mfma_f32_16x16x32_bf16 v[116:119], v[20:23], a[160:163], v[116:119]\n"
    "ds_read_b128 v[40:43], v5 offset:35840\n"
    "v_fma_f32 v83, v83, s36, s36\n"
    "v_mfma_f32_16x16x32_bf16 v[124:127], v[20:23], a[164:167], v[124:127]\n"
    "v_rcp_f32 v81, v81\n"
    "v_mfma_f32_16x16x32_bf16 v[132:135], v[20:23], a[168:171], v[132:135]\n"
    "buffer_load_dwordx4 v222, s[4:7], s31 offen offset:2048 lds\n"
    "v_mul_f32 v84, v80, v84\n"
    "v_mfma_f32_16x16x32_bf16 v[140:143], v[20:23], a[172:175], v[140:143]\n"
    "v_mul_f32 v85, v81, v85\n"
    "s_waitcnt lgkmcnt(3)\n"
    "v_mfma_f32_16x16x32_bf16 v[112:115], v[24:27], a[176:179], v[112:115]\n"
    "ds_read_b128 v[44:47], v5 offset:36096\n"
    "v_cvt_pk_bf16_f32 v8, v84, v85\n"
    "v_mfma_f32_16x16x32_bf16 v[120:123], v[24:27], a[180:183], v[120:123]\n"
    "v_rcp_f32 v82, v82\n"
    "v_mfma_f32_16x16x32_bf16 v[128:131], v[24:27], a[184:187], v[128:131]\n"
    "v_rcp_f32 v83, v83\n"
    "v_mfma_f32_16x16x32_bf16 v[136:139], v[24:27], a[188:191], v[136:139]\n"
    "v_exp_f32 v88, -v88\n"
    "v_mfma_f32_16x16x32_bf16 v[116:119], v[28:31], a[176:179], v[116:119]\n"
    "ds_read_b128 v[16:19], v3 offset:40960\n"
    "v_mul_f32 v86, v82, v86\n"
    "v_mfma_f32_16x16x32_bf16 v[124:127], v[28:31], a[180:183], v[124:127]\n"
    "v_exp_f32 v89, -v89\n"
    "v_mfma_f32_16x16x32_bf16 v[132:135], v[28:31], a[184:187], v[132:135]\n"
    "v_exp_f32 v90, -v90\n"
    "v_mfma_f32_16x16x32_bf16 v[140:143], v[28:31], a[188:191], v[140:143]\n"
    "v_mul_f32 v87, v83, v87\n"
    "s_waitcnt lgkmcnt(3)\n"
    "v_mfma_f32_16x16x32_bf16 v[112:115], v[32:35], a[192:195], v[112:115]\n"
    "ds_read_b128 v[20:23], v3 offset:41216\n"
    "v_cvt_pk_bf16_f32 v9, v86, v87\n"
    "v_mfma_f32_16x16x32_bf16 v[120:123], v[32:35], a[196:199], v[120:123]\n"
    "v_mul_f32 v95, v91, v95\n"
    "v_fma_f32 v88, v88, s36, s36\n"
    "v_mfma_f32_16x16x32_bf16 v[128:131], v[32:35], a[200:203], v[128:131]\n"
    "v_exp_f32 v91, -v91\n"
    "v_mfma_f32_16x16x32_bf16 v[136:139], v[32:35], a[204:207], v[136:139]\n"
    "v_rcp_f32 v88, v88\n"
    "v_mfma_f32_16x16x32_bf16 v[116:119], v[36:39], a[192:195], v[116:119]\n"
    "ds_read_b128 v[24:27], v3 offset:43008\n"
    "v_fma_f32 v89, v89, s36, s36\n"
    "v_mfma_f32_16x16x32_bf16 v[124:127], v[36:39], a[196:199], v[124:127]\n"
    "v_rcp_f32 v89, v89\n"
    "v_mfma_f32_16x16x32_bf16 v[132:135], v[36:39], a[200:203], v[132:135]\n"
    "v_fma_f32 v90, v90, s36, s36\n"
    "v_fma_f32 v91, v91, s36, s36\n"
    "v_mfma_f32_16x16x32_bf16 v[140:143], v[36:39], a[204:207], v[140:143]\n"
    "v_mul_f32 v92, v88, v92\n"
    "s_waitcnt lgkmcnt(3)\n"
    "v_mfma_f32_16x16x32_bf16 v[112:115], v[40:43], a[208:211], v[112:115]\n"
    "ds_read_b128 v[28:31], v3 offset:43264\n"
    "v_mul_f32 v93, v89, v93\n"
    "v_mfma_f32_16x16x32_bf16 v[120:123], v[40:43], a[212:215], v[120:123]\n"
    "v_rcp_f32 v90, v90\n"
    "v_mfma_f32_16x16x32_bf16 v[128:131], v[40:43], a[216:219], v[128:131]\n"
    "v_rcp_f32 v91, v91\n"
    "v_mfma_f32_16x16x32_bf16 v[136:139], v[40:43], a[220:223], v[136:139]\n"
    "v_mul_f32 v94, v90, v94\n"
    "v_mul_f32 v95, v91, v95\n"
    "v_mfma_f32_16x16x32_bf16 v[116:119], v[44:47], a[208:211], v[116:119]\n"
    "ds_read_b128 v[32:35], v3 offset:45056\n"
    "v_cvt_pk_bf16_f32 v11, v94, v95\n"
    "v_mfma_f32_16x16x32_bf16 v[124:127], v[44:47], a[212:215], v[124:127]\n"
    "v_cvt_pk_bf16_f32 v10, v92, v93\n"
    "v_permlane32_swap_b32 v9, v11\n"
    "v_mfma_f32_16x16x32_bf16 v[132:135], v[44:47], a[216:219], v[132:135]\n"
    "v_permlane32_swap_b32 v8, v10\n"
    "s_add_u32 s23, s23, 8192\n"
    "v_mfma_f32_16x16x32_bf16 v[140:143], v[44:47], a[220:223], v[140:143]\n"
    "buffer_store_dwordx4 v[8:11], v6, s[4:7], s23 offen sc1\n"
    "s_waitcnt lgkmcnt(3)\n"
    "v_mfma_f32_16x16x32_bf16 v[112:115], v[16:19], a[224:227], v[112:115]\n"
    "ds_read_b128 v[36:39], v3 offset:45312\n"
    "v_mul_f32 v100, v96, v100\n"
    "v_mfma_f32_16x16x32_bf16 v[120:123], v[16:19], a[228:231], v[120:123]\n"
    "v_exp_f32 v96, -v96\n"
    "v_mfma_f32_16x16x32_bf16 v[128:131], v[16:19], a[232:235], v[128:131]\n"
    "v_mul_f32 v101, v97, v101\n"
    "v_mul_f32 v102, v98, v102\n"
    "v_mfma_f32_16x16x32_bf16 v[136:139], v[16:19], a[236:239], v[136:139]\n"
    "v_exp_f32 v97, -v97\n"
    "v_mfma_f32_16x16x32_bf16 v[116:119], v[20:23], a[224:227], v[116:119]\n"
    "ds_read_b128 v[40:43], v3 offset:47104\n"
    "v_mul_f32 v103, v99, v103\n"
    "v_mfma_f32_16x16x32_bf16 v[124:127], v[20:23], a[228:231], v[124:127]\n"
    "v_exp_f32 v98, -v98\n"
    "v_mfma_f32_16x16x32_bf16 v[132:135], v[20:23], a[232:235], v[132:135]\n"
    "v_exp_f32 v99, -v99\n"
    "v_mfma_f32_16x16x32_bf16 v[140:143], v[20:23], a[236:239], v[140:143]\n"
    "v_fma_f32 v96, v96, s36, s36\n"
    "s_waitcnt lgkmcnt(3)\n"
    "v_mfma_f32_16x16x32_bf16 v[112:115], v[24:27], a[240:243], v[112:115]\n"
    "ds_read_b128 v[44:47], v3 offset:47360\n"
    "v_fma_f32 v97, v97, s36, s36\n"
    "v_mfma_f32_16x16x32_bf16 v[120:123], v[24:27], a[244:247], v[120:123]\n"
    "v_rcp_f32 v96, v96\n"
    "v_mfma_f32_16x16x32_bf16 v[128:131], v[24:27], a[248:251], v[128:131]\n"
    "v_rcp_f32 v97, v97\n"
    "v_mfma_f32_16x16x32_bf16 v[136:139], v[24:27], a[252:255], v[136:139]\n"
    "v_fma_f32 v98, v98, s36, s36\n"
    "v_fma_f32 v99, v99, s36, s36\n"
    "v_mfma_f32_16x16x32_bf16 v[116:119], v[28:31], a[240:243], v[116:119]\n"
    "s_waitcnt vmcnt(4)\n"
    "s_barrier\n"
    "s_cmp_lg_u32 s17, 0\n"
    "s_cbranch_scc1 LF3%=\n"
    "s_mov_b64 exec, 1\n"
    "buffer_store_dword v216, off, s[4:7], s20 offset:64 sc1\n"
    "s_mov_b64 exec, -1\n"
    "LF3%=:\n"
    "ds_read_b128 v[16:19], v1 offset:0\n"
    "v_mfma_f32_16x16x32_bf16 v[124:127], v[28:31], a[244:247], v[124:127]\n"
    "s_add_u32 m0, s17, 27648\n"
    "v_add_u32 v222, 4096, v222\n"
    "v_mfma_f32_16x16x32_bf16 v[132:135], v[28:31], a[248:251], v[132:135]\n"
    "buffer_load_dwordx4 v222, s[4:7], s47 offen offset:0 lds\n"
    "v_mul_f32 v100, v96, v100\n"
    "v_mfma_f32_16x16x32_bf16 v[140:143], v[28:31], a[252:255], v[140:143]\n"
    "v_mul_f32 v101, v97, v101\n"
    "s_waitcnt lgkmcnt(3)\n"
    "v_mfma_f32_16x16x32_bf16 v[112:115], v[32:35], v[224:227], v[112:115]\n"
    "ds_read_b128 v[20:23], v1 offset:256\n"
    "v_cmp_ne_u32 vcc, v218, v216\n"
    "s_cbranch_vccnz LP9%=\n"
    "LQ9%=:\n"
    "v_mfma_f32_16x16x32_bf16 v[120:123], v[32:35], v[228:231], v[120:123]\n"
    "v_rcp_f32 v98, v98\n"
    "v_mfma_f32_16x16x32_bf16 v[128:131], v[32:35], v[232:235], v[128:131]\n"
    "buffer_load_dwordx4 v222, s[4:7], s48 offen offset:1024 lds\n"
    "v_mul_f32 v102, v98, v102\n"
    "v_mfma_f32_16x16x32_bf16 v[136:139], v[32:35], v[236:239], v[136:139]\n"
    "v_rcp_f32 v99, v99\n"
    "v_mfma_f32_16x16x32_bf16 v[116:119], v[36:39], v[224:227], v[116:119]\n"
    "ds_read_b128 v[24:27], v1 offset:2048\n"
    "v_mul_f32 v103, v99, v103\n"
    "v_mfma_f32_16x16x32_bf16 v[124:127], v[36:39], v[228:231], v[124:127]\n"
    "v_cvt_pk_bf16_f32 v13, v102, v103\n"
    "v_cvt_pk_bf16_f32 v12, v100, v101\n"
    "v_mfma_f32_16x16x32_bf16 v[132:135], v[36:39], v[232:235], v[132:135]\n"
    "v_mul_f32 v108, v104, v108\n"
    "v_mul_f32 v109, v105, v109\n"
    "v_mfma_f32_16x16x32_bf16 v[140:143], v[36:39], v[236:239], v[140:143]\n"
    "buffer_load_dwordx4 v222, s[4:7], s30 offen offset:2048 lds\n"
    "s_waitcnt lgkmcnt(3)\n"
    "v_mfma_f32_16x16x32_bf16 v[112:115], v[40:43], v[240:243], v[112:115]\n"
    "ds_read_b128 v[28:31], v1 offset:2304\n"
    "v_mul_f32 v110, v106, v110\n"
    "v_mfma_f32_16x16x32_bf16 v[120:123], v[40:43], v[244:247], v[120:123]\n"
    "v_exp_f32 v104, -v104\n"
    "v_mfma_f32_16x16x32_bf16 v[128:131], v[40:43], v[248:251], v[128:131]\n"
    "v_exp_f32 v105, -v105\n"
    "v_mfma_f32_16x16x32_bf16 v[136:139], v[40:43], v[252:255], v[136:139]\n"
    "v_exp_f32 v106, -v106\n"
    "v_mfma_f32_16x16x32_bf16 v[116:119], v[44:47], v[240:243], v[116:119]\n"
    "ds_read_b128 v[32:35], v1 offset:4096\n"
    "v_mul_f32 v111, v107, v111\n"
    "v_mfma_f32_16x16x32_bf16 v[124:127], v[44:47], v[244:247], v[124:127]\n"
    "buffer_load_dwordx4 v222, s[4:7], s50 offen offset:3072 lds\n"
    "v_fma_f32 v104, v104, s36, s36\n"
    "v_mfma_f32_16x16x32_bf16 v[132:135], v[44:47], v[248:251], v[132:135]\n"
    "v_exp_f32 v107, -v107\n"
    "v_mfma_f32_16x16x32_bf16 v[140:143], v[44:47], v[252:255], v[140:143]\n"
    "v_fma_f32 v105, v105, s36, s36\n"
    "s_waitcnt lgkmcnt(3)\n"
    "v_mfma_f32_16x16x32_bf16 v[144:147], v[16:19], a[0:3], 0\n"
    "ds_read_b128 v[36:39], v1 offset:4352\n"
    "v_fma_f32 v106, v106, s36, s36\n"
    "v_mfma_f32_16x16x32_bf16 v[152:155], v[16:19], a[4:7], 0\n"
    "s_add_u32 m0, s17, 31744\n"
    "v_fma_f32 v107, v107, s36, s36\n"
    "v_mfma_f32_16x16x32_bf16 v[160:163], v[16:19], a[8:11], 0\n"
    "buffer_load_dwordx4 v222, s[4:7], s51 offen offset:0 lds\n"
    "v_mfma_f32_16x16x32_bf16 v[168:171], v[16:19], a[12:15], 0\n"
    "v_rcp_f32 v104, v104\n"
    "v_mfma_f32_16x16x32_bf16 v[148:151], v[20:23], a[0:3], 0\n"
    "ds_read_b128 v[40:43], v1 offset:6144\n"
    "v_mul_f32 v108, v104, v108\n"
    "v_mfma_f32_16x16x32_bf16 v[156:159], v[20:23], a[4:7], 0\n"
    "v_rcp_f32 v105, v105\n"
    "v_mfma_f32_16x16x32_bf16 v[164:167], v[20:23], a[8:11], 0\n"
    "buffer_load_dwordx4 v222, s[4:7], s49 offen offset:1024 lds\n"
    "v_mul_f32 v109, v105, v109\n"
    "v_mfma_f32_16x16x32_bf16 v[172:175], v[20:23], a[12:15], 0\n"
    "v_cvt_pk_bf16_f32 v14, v108, v109\n"
    "s_waitcnt lgkmcnt(3)\n"
    "v_mfma_f32_16x16x32_bf16 v[144:147], v[24:27], a[16:19], v[144:147]\n"
    "ds_read_b128 v[44:47], v1 offset:6400\n"
    "v_permlane32_swap_b32 v12, v14\n"
    "v_mfma_f32_16x16x32_bf16 v[152:155], v[24:27], a[20:23], v[152:155]\n"
    "v_rcp_f32 v106, v106\n"
    "v_mfma_f32_16x16x32_bf16 v[160:163], v[24:27], a[24:27], v[160:163]\n"
    "v_rcp_f32 v107, v107\n"
    "v_mfma_f32_16x16x32_bf16 v[168:171], v[24:27], a[28:31], v[168:171]\n"
    "s_add_u32 s28, s28, 4096\n"
    "v_mul_f32 v110, v106, v110\n"
    "v_mfma_f32_16x16x32_bf16 v[148:151], v[28:31], a[16:19], v[148:151]\n"
    "ds_read_b128 v[16:19], v4 offset:8192\n"
    "v_mul_f32 v111, v107, v111\n"
    "v_mfma_f32_16x16x32_bf16 v[156:159], v[28:31], a[20:23], v[156:159]\n"
    "buffer_load_dwordx4 a[0:3], v0, s[4:7], s28 offen offset:0 sc1\n"
    "v_cvt_pk_bf16_f32 v15, v110, v111\n"
    "v_mfma_f32_16x16x32_bf16 v[164:167], v[28:31], a[24:27], v[164:167]\n"
    "v_mul_f32 v116, v112, v116\n"
    "v_permlane32_swap_b32 v13, v15\n"
    "v_mfma_f32_16x16x32_bf16 v[172:175], v[28:31], a[28:31], v[172:175]\n"
    "buffer_store_dwordx4 v[12:15], v7, s[4:7], s23 offen sc1\n"
    "s_waitcnt lgkmcnt(3)\n"
    "v_mfma_f32_16x16x32_bf16 v[144:147], v[32:35], a[32:35], v[144:147]\n"
    "ds_read_b128 v[20:23], v4 offset:8448\n"
    "v_mul_f32 v117, v113, v117\n"
    "v_mfma_f32_16x16x32_bf16 v[152:155], v[32:35], a[36:39], v[152:155]\n"
    "s_add_u32 s29, s29, 4096\n"
    "v_mul_f32 v118, v114, v118\n"
    "v_mfma_f32_16x16x32_bf16 v[160:163], v[32:35], a[40:43], v[160:163]\n"
    "buffer_load_dwordx4 a[4:7], v0, s[4:7], s29 offen offset:0 sc1\n"
    "v_mul_f32 v119, v115, v119\n"
    "v_mfma_f32_16x16x32_bf16 v[168:171], v[32:35], a[44:47], v[168:171]\n"
    "v_exp_f32 v112, -v112\n"
    "v_mfma_f32_16x16x32_bf16 v[148:151], v[36:39], a[32:35], v[148:151]\n"
    "ds_read_b128 v[24:27], v1 offset:37888\n"
    "v_fma_f32 v112, v112, s36, s36\n"
    "v_mfma_f32_16x16x32_bf16 v[156:159], v[36:39], a[36:39], v[156:159]\n"
    "buffer_load_dwordx4 a[8:11], v0, s[4:7], s28 offen offset:1024 sc1\n"
    "v_mul_f32 v124, v120, v124\n"
    "v_mfma_f32_16x16x32_bf16 v[164:167], v[36:39], a[40:43], v[164:167]\n"
    "v_exp_f32 v113, -v113\n"
    "v_mfma_f32_16x16x32_bf16 v[172:175], v[36:39], a[44:47], v[172:175]\n"
    "v_fma_f32 v113, v113, s36, s36\n"
    "s_waitcnt lgkmcnt(3)\n"
    "v_mfma_f32_16x16x32_bf16 v[144:147], v[40:43], a[48:51], v[144:147]\n"
    "ds_read_b128 v[28:31], v1 offset:38144\n"
    "v_mul_f32 v125, v121, v125\n"
    "v_mfma_f32_16x16x32_bf16 v[152:155], v[40:43], a[52:55], v[152:155]\n"
    "v_exp_f32 v114, -v114\n"
    "v_mfma_f32_16x16x32_bf16 v[160:163], v[40:43], a[56:59], v[160:163]\n"
    "buffer_load_dwordx4 a[12:15], v0, s[4:7], s29 offen offset:1024 sc1\n"
    "v_fma_f32 v114, v114, s36, s36\n"
    "v_mfma_f32_16x16x32_bf16 v[168:171], v[40:43], a[60:63], v[168:171]\n"
    "v_exp_f32 v115, -v115\n"
    "v_mfma_f32_16x16x32_bf16 v[148:151], v[44:47], a[48:51], v[148:151]\n"
    "ds_read_b128 v[32:35], v1 offset:39936\n"
    "v_fma_f32 v115, v115, s36, s36\n"
    "v_mfma_f32_16x16x32_bf16 v[156:159], v[44:47], a[52:55], v[156:159]\n"
    "v_rcp_f32 v112, v112\n"
    "v_mfma_f32_16x16x32_bf16 v[164:167], v[44:47], a[56:59], v[164:167]\n"
    "v_rcp_f32 v113, v113\n"
    "v_mfma_f32_16x16x32_bf16 v[172:175], v[44:47], a[60:63], v[172:175]\n"
    "buffer_load_dwordx4 a[16:19], v0, s[4:7], s28 offen offset:2048 sc1\n"
    "s_waitcnt lgkmcnt(3)\n"
    "v_mfma_f32_16x16x32_bf16 v[144:147], v[16:19], a[64:67], v[144:147]\n"
    "ds_read_b128 v[36:39], v1 offset:40192\n"
    "v_mul_f32 v116, v112, v116\n"
    "v_mfma_f32_16x16x32_bf16 v[152:155], v[16:19], a[68:71], v[152:155]\n"
    "v_rcp_f32 v114, v114\n"
    "v_mfma_f32_16x16x32_bf16 v[160:163], v[16:19], a[72:75], v[160:163]\n"
    "v_rcp_f32 v115, v115\n"
    "v_mfma_f32_16x16x32_bf16 v[168:171], v[16:19], a[76:79], v[168:171]\n"
    "v_exp_f32 v120, -v120\n"
    "v_mfma_f32_16x16x32_bf16 v[148:151], v[20:23], a[64:67], v[148:151]\n"
    "ds_read_b128 v[40:43], v1 offset:41984\n"
    "v_mul_f32 v117, v113, v117\n"
    "v_mfma_f32_16x16x32_bf16 v[156:159], v[20:23], a[68:71], v[156:159]\n"
    "buffer_load_dwordx4 a[20:23], v0, s[4:7], s29 offen offset:2048 sc1\n"
    "v_mul_f32 v118, v114, v118\n"
    "v_mfma_f32_16x16x32_bf16 v[164:167], v[20:23], a[72:75], v[164:167]\n"
    "v_exp_f32 v121, -v121\n"
    "v_mfma_f32_16x16x32_bf16 v[172:175], v[20:23], a[76:79], v[172:175]\n"
    "v_mul_f32 v119, v115, v119\n"
    "s_waitcnt lgkmcnt(3)\n"
    "v_mfma_f32_16x16x32_bf16 v[144:147], v[24:27], a[80:83], v[144:147]\n"
    "ds_read_b128 v[44:47], v1 offset:42240\n"
    "v_cvt_pk_bf16_f32 v9, v118, v119\n"
    "v_mfma_f32_16x16x32_bf16 v[152:155], v[24:27], a[84:87], v[152:155]\n"
    "buffer_load_dwordx4 a[24:27], v0, s[4:7], s28 offen offset:3072 sc1\n"
    "v_cvt_pk_bf16_f32 v8, v116, v117\n"
    "v_mfma_f32_16x16x32_bf16 v[160:163], v[24:27], a[88:91], v[160:163]\n"
    "v_mul_f32 v126, v122, v126\n"
    "v_mul_f32 v127, v123, v127\n"
    "v_mfma_f32_16x16x32_bf16 v[168:171], v[24:27], a[92:95], v[168:171]\n"
    "v_exp_f32 v122, -v122\n"
    "v_mfma_f32_16x16x32_bf16 v[148:151], v[28:31], a[80:83], v[148:151]\n"
    "ds_read_b128 v[16:19], v1 offset:44032\n"
    "v_fma_f32 v120, v120, s36, s36\n"
    "v_mfma_f32_16x16x32_bf16 v[156:159], v[28:31], a[84:87], v[156:159]\n"
    "v_exp_f32 v123, -v123\n"
    "v_mfma_f32_16x16x32_bf16 v[164:167], v[28:31], a[88:91], v[164:167]\n"
    "buffer_load_dwordx4 a[28:31], v0, s[4:7], s29 offen offset:3072 sc1\n"
    "v_fma_f32 v121, v121, s36, s36\n"
    "v_mfma_f32_16x16x32_bf16 v[172:175], v[28:31], a[92:95], v[172:175]\n"
    "v_fma_f32 v122, v122, s36, s36\n"
    "s_waitcnt lgkmcnt(3)\n"
    "v_mfma_f32_16x16x32_bf16 v[144:147], v[32:35], a[96:99], v[144:147]\n"
    "ds_read_b128 v[20:23], v1 offset:44288\n"
    "v_fma_f32 v123, v123, s36, s36\n"
    "v_mfma_f32_16x16x32_bf16 v[152:155], v[32:35], a[100:103], v[152:155]\n"
    "LPK5:\n"
    "s_cbranch_execz LTR\n"
    "v_rcp_f32 v120, v120\n"
    "v_mfma_f32_16x16x32_bf16 v[160:163], v[32:35], a[104:107], v[160:163]\n"
    "v_rcp_f32 v121, v121\n"
    "v_mfma_f32_16x16x32_bf16 v[168:171], v[32:35], a[108:111], v[168:171]\n"
    "s_add_u32 s28, s28, 4096\n"
    "v_mul_f32 v124, v120, v124\n"
    "v_mfma_f32_16x16x32_bf16 v[148:151], v[36:39], a[96:99], v[148:151]\n"
    "ds_read_b128 v[24:27], v2 offset:24576\n"
    "v_mul_f32 v125, v121, v125\n"
    "v_mfma_f32_16x16x32_bf16 v[156:159], v[36:39], a[100:103], v[156:159]\n"
    "buffer_load_dwordx4 a[32:35], v0, s[4:7], s28 offen offset:0 sc1\n"
    "v_cvt_pk_bf16_f32 v10, v124, v125\n"
    "v_mfma_f32_16x16x32_bf16 v[164:167], v[36:39], a[104:107], v[164:167]\n"
    "v_rcp_f32 v122, v122\n"
    "v_mfma_f32_16x16x32_bf16 v[172:175], v[36:39], a[108:111], v[172:175]\n"
    "v_mul_f32 v126, v122, v126\n"
    "s_waitcnt lgkmcnt(3)\n"
    "v_mfma_f32_16x16x32_bf16 v[144:147], v[40:43], a[112:115], v[144:147]\n"
    "ds_read_b128 v[28:31], v2 offset:24832\n"
    "v_permlane32_swap_b32 v8, v10\n"
    "v_mfma_f32_16x16x32_bf16 v[152:155], v[40:43], a[116:119], v[152:155]\n"
    "s_add_u32 s29, s29, 4096\n"
    "s_add_u32 s25, s25, 8192\n"
    "v_mfma_f32_16x16x32_bf16 v[160:163], v[40:43], a[120:123], v[160:163]\n"
    "buffer_load_dwordx4 a[36:39], v0, s[4:7], s29 offen offset:0 sc1\n"
    "v_mul_f32 v132, v128, v132\n"
    "v_mfma_f32_16x16x32_bf16 v[168:171], v[40:43], a[124:127], v[168:171]\n"
    "v_rcp_f32 v123, v123\n"
    "v_mfma_f32_16x16x32_bf16 v[148:151], v[44:47], a[112:115], v[148:151]\n"
    "ds_read_b128 v[32:35], v2 offset:26624\n"
    "v_mul_f32 v127, v123, v127\n"
    "v_mfma_f32_16x16x32_bf16 v[156:159], v[44:47], a[116:119], v[156:159]\n"
    "buffer_load_dwordx4 a[40:43], v0, s[4:7], s28 offen offset:1024 sc1\n"
    "v_cvt_pk_bf16_f32 v11, v126, v127\n"
    "v_mfma_f32_16x16x32_bf16 v[164:167], v[44:47], a[120:123], v[164:167]\n"
    "v_exp_f32 v128, -v128\n"
    "v_mfma_f32_16x16x32_bf16 v[172:175], v[44:47], a[124:127], v[172:175]\n"
    "v_permlane32_swap_b32 v9, v11\n"
    "s_waitcnt lgkmcnt(3)\n"
    "v_mfma_f32_16x16x32_bf16 v[144:147], v[16:19], a[128:131], v[144:147]\n"
    "ds_read_b128 v[36:39], v2 offset:26880\n"
    "buffer_store_dwordx4 v[8:11], v6, s[4:7], s25 offen sc1\n"
    "v_mfma_f32_16x16x32_bf16 v[152:155], v[16:19], a[132:135], v[152:155]\n"
    "buffer_load_dword v218, v213, s[4:7], s40 offen offset:64 sc1\n"
    "v_mul_f32 v133, v129, v133\n"
    "v_mfma_f32_16x16x32_bf16 v[160:163], v[16:19], a[136:139], v[160:163]\n"
    "buffer_load_dwordx4 a[44:47], v0, s[4:7], s29 offen offset:1024 sc1\n"
    "v_mul_f32 v134, v130, v134\n"
    "v_mfma_f32_16x16x32_bf16 v[168:171], v[16:19], a[140:143], v[168:171]\n"
    "v_exp_f32 v129, -v129\n"
    "v_mfma_f32_16x16x32_bf16 v[148:151], v[20:23], a[128:131], v[148:151]\n"
    "ds_read_b128 v[40:43], v2 offset:28672\n"
    "v_mul_f32 v135, v131, v135\n"
    "v_mfma_f32_16x16x32_bf16 v[156:159], v[20:23], a[132:135], v[156:159]\n"
    "v_exp_f32 v130, -v130\n"
    "v_mfma_f32_16x16x32_bf16 v[164:167], v[20:23], a[136:139], v[164:167]\n"
    "v_exp_f32 v131, -v131\n"
    "v_mfma_f32_16x16x32_bf16 v[172:175], v[20:23], a[140:143], v[172:175]\n"
    "buffer_load_dwordx4 v222, s[4:7], s31 offen offset:2048 lds\n"
    "s_waitcnt lgkmcnt(3)\n"
    "v_mfma_f32_16x16x32_bf16 v[144:147], v[24:27], a[144:147], v[144:147]\n"
    "ds_read_b128 v[44:47], v2 offset:28928\n"
    "v_fma_f32 v128, v128, s36, s36\n"
    "v_mfma_f32_16x16x32_bf16 v[152:155], v[24:27], a[148:151], v[152:155]\n"
    "v_rcp_f32 v128, v128\n"
    "v_mfma_f32_16x16x32_bf16 v[160:163], v[24:27], a[152:155], v[160:163]\n"
    "v_fma_f32 v129, v129, s36, s36\n"
    "v_fma_f32 v130, v130, s36, s36\n"
    "v_mfma_f32_16x16x32_bf16 v[168:171], v[24:27], a[156:159], v[168:171]\n"
    "v_rcp_f32 v129, v129\n"
    "v_mfma_f32_16x16x32_bf16 v[148:151], v[28:31], a[144:147], v[148:151]\n"
    "ds_read_b128 v[16:19], v2 offset:30720\n"
    "v_fma_f32 v131, v131, s36, s36\n"
    "v_mfma_f32_16x16x32_bf16 v[156:159], v[28:31], a[148:151], v[156:159]\n"
    "buffer_load_dwordx4 a[48:51], v0, s[4:7], s28 offen offset:2048 sc1\n"
    "v_mul_f32 v132, v128, v132\n"
    "v_mfma_f32_16x16x32_bf16 v[164:167], v[28:31], a[152:155], v[164:167]\n"
    "v_rcp_f32 v130, v130\n"
    "v_mfma_f32_16x16x32_bf16 v[172:175], v[28:31], a[156:159], v[172:175]\n"
    "v_mul_f32 v133, v129, v133\n"
    "s_waitcnt lgkmcnt(3)\n"
    "v_mfma_f32_16x16x32_bf16 v[144:147], v[32:35], a[160:163], v[144:147]\n"
    "ds_read_b128 v[20:23], v2 offset:30976\n"
    "v_mul_f32 v134, v130, v134\n"
    "v_mfma_f32_16x16x32_bf16 v[152:155], v[32:35], a[164:167], v[152:155]\n"
    "buffer_load_dwordx4 a[52:55], v0, s[4:7], s29 offen offset:2048 sc1\n"
    "v_cvt_pk_bf16_f32 v12, v132, v133\n"
    "v_mfma_f32_16x16x32_bf16 v[160:163], v[32:35], a[168:171], v[160:163]\n"
    "v_rcp_f32 v131, v131\n"
    "v_mfma_f32_16x16x32_bf16 v[168:171], v[32:35], a[172:175], v[168:171]\n"
    "v_mul_f32 v135, v131, v135\n"
    "v_cvt_pk_bf16_f32 v13, v134, v135\n"
    "v_mfma_f32_16x16x32_bf16 v[148:151], v[36:39], a[160:163], v[148:151]\n"
    "ds_read_b128 v[24:27], v5 offset:8192\n"
    "v_mul_f32 v140, v136, v140\n"
    "v_mfma_f32_16x16x32_bf16 v[156:159], v[36:39], a[164:167], v[156:159]\n"
    "v_exp_f32 v136, -v136\n"
    "v_mfma_f32_16x16x32_bf16 v[164:167], v[36:39], a[168:171], v[164:167]\n"
    "buffer_load_dwordx4 a[56:59], v0, s[4:7], s28 offen offset:3072 sc1\n"
    "v_mul_f32 v141, v137, v141\n"
    "v_mfma_f32_16x16x32_bf16 v[172:175], v[36:39], a[172:175], v[172:175]\n"
    "v_mul_f32 v142, v138, v142\n"
    "s_waitcnt lgkmcnt(3)\n"
    "v_mfma_f32_16x16x32_bf16 v[144:147], v[40:43], a[176:179], v[144:147]\n"
    "ds_read_b128 v[28:31], v5 offset:8448\n"
    "v_mul_f32 v143, v139, v143\n"
    "v_mfma_f32_16x16x32_bf16 v[152:155], v[40:43], a[180:183], v[152:155]\n"
    "v_exp_f32 v137, -v137\n"
    "v_mfma_f32_16x16x32_bf16 v[160:163], v[40:43], a[184:187], v[160:163]\n"
    "v_exp_f32 v138, -v138\n"
    "v_mfma_f32_16x16x32_bf16 v[168:171], v[40:43], a[188:191], v[168:171]\n"
    "buffer_load_dwordx4 a[60:63], v0, s[4:7], s29 offen offset:3072 sc1\n"
    "v_fma_f32 v136, v136, s36, s36\n"
    "v_mfma_f32_16x16x32_bf16 v[148:151], v[44:47], a[176:179], v[148:151]\n"
    "ds_read_b128 v[32:35], v3 offset:13312\n"
    "v_fma_f32 v137, v137, s36, s36\n"
    "v_mfma_f32_16x16x32_bf16 v[156:159], v[44:47], a[180:183], v[156:159]\n"
    "v_exp_f32 v139, -v139\n"
    "v_mfma_f32_16x16x32_bf16 v[164:167], v[44:47], a[184:187], v[164:167]\n"
    "v_rcp_f32 v136, v136\n"
    "v_mfma_f32_16x16x32_bf16 v[172:175], v[44:47], a[188:191], v[172:175]\n"
    "v_fma_f32 v138, v138, s36, s36\n"
    "s_waitcnt lgkmcnt(3)\n"
    "v_mfma_f32_16x16x32_bf16 v[144:147], v[16:19], a[192:195], v[144:147]\n"
    "ds_read_b128 v[36:39], v3 offset:13568\n"
    "v_fma_f32 v139, v139, s36, s36\n"
    "v_mfma_f32_16x16x32_bf16 v[152:155], v[16:19], a[196:199], v[152:155]\n"
    "s_add_u32 s28, s28, 4096\n"
    "v_mul_f32 v140, v136, v140\n"
    "v_mfma_f32_16x16x32_bf16 v[160:163], v[16:19], a[200:203], v[160:163]\n"
    "buffer_load_dwordx4 a[64:67], v0, s[4:7], s28 offen offset:0 sc1\n"
    "v_mfma_f32_16x16x32_bf16 v[168:171], v[16:19], a[204:207], v[168:171]\n"
    "v_rcp_f32 v137, v137\n"
    "v_mfma_f32_16x16x32_bf16 v[148:151], v[20:23], a[192:195], v[148:151]\n"
    "ds_read_b128 v[40:43], v3 offset:15360\n"
    "v_mul_f32 v141, v137, v141\n"
    "v_mfma_f32_16x16x32_bf16 v[156:159], v[20:23], a[196:199], v[156:159]\n"
    "s_add_u32 s29, s29, 4096\n"
    "v_cvt_pk_bf16_f32 v14, v140, v141\n"
    "v_mfma_f32_16x16x32_bf16 v[164:167], v[20:23], a[200:203], v[164:167]\n"
    "buffer_load_dwordx4 a[68:71], v0, s[4:7], s29 offen offset:0 sc1\n"
    "v_permlane32_swap_b32 v12, v14\n"
    "v_mfma_f32_16x16x32_bf16 v[172:175], v[20:23], a[204:207], v[172:175]\n"
    "s_waitcnt lgkmcnt(3)\n"
    "v_mfma_f32_16x16x32_bf16 v[144:147], v[24:27], a[208:211], v[144:147]\n"
    "ds_read_b128 v[44:47], v3 offset:15616\n"
    "v_mfma_f32_16x16x32_bf16 v[152:155], v[24:27], a[212:215], v[152:155]\n"
    "v_rcp_f32 v138, v138\n"
    "v_mfma_f32_16x16x32_bf16 v[160:163], v[24:27], a[216:219], v[160:163]\n"
    "buffer_load_dwordx4 a[72:75], v0, s[4:7], s28 offen offset:1024 sc1\n"
    "v_mul_f32 v142, v138, v142\n"
    "v_mfma_f32_16x16x32_bf16 v[168:171], v[24:27], a[220:223], v[168:171]\n"
    "v_rcp_f32 v139, v139\n"
    "v_mfma_f32_16x16x32_bf16 v[148:151], v[28:31], a[208:211], v[148:151]\n"
    "ds_read_b128 v[16:19], v3 offset:17408\n"
    "v_mul_f32 v143, v139, v143\n"
    "v_mfma_f32_16x16x32_bf16 v[156:159], v[28:31], a[212:215], v[156:159]\n"
    "v_cvt_pk_bf16_f32 v15, v142, v143\n"
    "v_mfma_f32_16x16x32_bf16 v[164:167], v[28:31], a[216:219], v[164:167]\n"
    "v_mfma_f32_16x16x32_bf16 v[172:175], v[28:31], a[220:223], v[172:175]\n"
    "buffer_load_dwordx4 a[76:79], v0, s[4:7], s29 offen offset:1024 sc1\n"
    "s_waitcnt lgkmcnt(3)\n"
    "v_mfma_f32_16x16x32_bf16 v[144:147], v[32:35], a[224:227], v[144:147]\n"
    "ds_read_b128 v[20:23], v3 offset:17664\n"
    "v_permlane32_swap_b32 v13, v15\n"
    "v_mfma_f32_16x16x32_bf16 v[152:155], v[32:35], a[228:231], v[152:155]\n"
    "buffer_store_dwordx4 v[12:15], v7, s[4:7], s25 offen sc1\n"
    "v_mfma_f32_16x16x32_bf16 v[160:163], v[32:35], a[232:235], v[160:163]\n"
    "v_mfma_f32_16x16x32_bf16 v[168:171], v[32:35], a[236:239], v[168:171]\n"
    "v_mfma_f32_16x16x32_bf16 v[148:151], v[36:39], a[224:227], v[148:151]\n"
    "ds_read_b128 v[24:27], v3 offset:19456\n"
    "v_mfma_f32_16x16x32_bf16 v[156:159], v[36:39], a[228:231], v[156:159]\n"
    "buffer_load_dwordx4 a[80:83], v0, s[4:7], s28 offen offset:2048 sc1\n"
    "v_mfma_f32_16x16x32_bf16 v[164:167], v[36:39], a[232:235], v[164:167]\n"
    "v_mfma_f32_16x16x32_bf16 v[172:175], v[36:39], a[236:239], v[172:175]\n"
    "s_waitcnt lgkmcnt(3)\n"
    "v_mfma_f32_16x16x32_bf16 v[144:147], v[40:43], a[240:243], v[144:147]\n"
    "ds_read_b128 v[28:31], v3 offset:19712\n"
    "v_mfma_f32_16x16x32_bf16 v[152:155], v[40:43], a[244:247], v[152:155]\n"
    "buffer_load_dwordx4 a[84:87], v0, s[4:7], s29 offen offset:2048 sc1\n"
    "v_mfma_f32_16x16x32_bf16 v[160:163], v[40:43], a[248:251], v[160:163]\n"
    "v_mfma_f32_16x16x32_bf16 v[168:171], v[40:43], a[252:255], v[168:171]\n"
    "v_mfma_f32_16x16x32_bf16 v[148:151], v[44:47], a[240:243], v[148:151]\n"
    "s_barrier\n"
    "ds_read_b128 v[32:35], v0 offset:9216\n"
    "v_mfma_f32_16x16x32_bf16 v[156:159], v[44:47], a[244:247], v[156:159]\n"
    "v_mfma_f32_16x16x32_bf16 v[164:167], v[44:47], a[248:251], v[164:167]\n"
    "s_add_u32 m0, s17, 0\n"
    "v_add_u32 v222, 4096, v222\n"
    "v_mfma_f32_16x16x32_bf16 v[172:175], v[44:47], a[252:255], v[172:175]\n"
    "buffer_load_dwordx4 v222, s[4:7], s47 offen offset:0 lds\n"
    "s_waitcnt lgkmcnt(3)\n"
    "v_mfma_f32_16x16x32_bf16 v[144:147], v[16:19], v[224:227], v[144:147]\n"
    "ds_read_b128 v[36:39], v0 offset:10240\n"
    "v_mfma_f32_16x16x32_bf16 v[152:155], v[16:19], v[228:231], v[152:155]\n"
    "v_mfma_f32_16x16x32_bf16 v[160:163], v[16:19], v[232:235], v[160:163]\n"
    "v_mfma_f32_16x16x32_bf16 v[168:171], v[16:19], v[236:239], v[168:171]\n"
    "buffer_load_dwordx4 v222, s[4:7], s48 offen offset:1024 lds\n"
    "v_mfma_f32_16x16x32_bf16 v[148:151], v[20:23], v[224:227], v[148:151]\n"
    "ds_read_b128 v[40:43], v0 offset:11264\n"
    "v_mfma_f32_16x16x32_bf16 v[156:159], v[20:23], v[228:231], v[156:159]\n"
    "ds_read_b128 v[44:47], v0 offset:12288\n"
    "v_mfma_f32_16x16x32_bf16 v[164:167], v[20:23], v[232:235], v[164:167]\n"
    "ds_read_b128 v[16:19], v0 offset:13312\n"
    "s_waitcnt vmcnt(15)\n"
    "v_cmp_ne_u32 vcc, v218, v216\n"
    "s_cbranch_vccnz LP10%=\n"
    "LQ10%=:\n"
    "v_mfma_f32_16x16x32_bf16 v[172:175], v[20:23], v[236:239], v[172:175]\n"
    "s_waitcnt lgkmcnt(5)\n"
    "v_mfma_f32_16x16x32_bf16 v[144:147], v[24:27], v[240:243], v[144:147]\n"
    "ds_read_b128 v[20:23], v0 offset:14336\n"
    "v_mfma_f32_16x16x32_bf16 v[152:155], v[24:27], v[244:247], v[152:155]\n"
    "buffer_load_dwordx4 v222, s[4:7], s30 offen offset:2048 lds\n"
    "v_mfma_f32_16x16x32_bf16 v[160:163], v[24:27], v[248:251], v[160:163]\n"
    "v_mfma_f32_16x16x32_bf16 v[168:171], v[24:27], v[252:255], v[168:171]\n"
    "v_mfma_f32_16x16x32_bf16 v[148:151], v[28:31], v[240:243], v[148:151]\n"
    "ds_read_b128 v[24:27], v0 offset:15360\n"
    "v_mfma_f32_16x16x32_bf16 v[156:159], v[28:31], v[244:247], v[156:159]\n"
    "buffer_load_dwordx4 v222, s[4:7], s50 offen offset:3072 lds\n"
    "v_mfma_f32_16x16x32_bf16 v[164:167], v[28:31], v[248:251], v[164:167]\n"
    "v_mfma_f32_16x16x32_bf16 v[172:175], v[28:31], v[252:255], v[172:175]\n"
    "s_waitcnt lgkmcnt(3)\n"
    "v_mfma_f32_16x16x32_bf16 v[64:67], v[32:35], v[176:179], 0\n"
    "ds_read_b128 v[28:31], v0 offset:46080\n"
    "v_mfma_f32_16x16x32_bf16 v[68:71], v[32:35], v[180:183], 0\n"
    "v_mfma_f32_16x16x32_bf16 v[72:75], v[36:39], v[176:179], 0\n"
    "ds_read_b128 v[32:35], v0 offset:47104\n"
    "s_add_u32 m0, s17, 4096\n"
    "v_mfma_f32_16x16x32_bf16 v[76:79], v[36:39], v[180:183], 0\n"
    "v_mfma_f32_16x16x32_bf16 v[80:83], v[40:43], v[176:179], 0\n"
    "ds_read_b128 v[36:39], v0 offset:48128\n"
    "buffer_load_dwordx4 v222, s[4:7], s51 offen offset:0 lds\n"
    "v_mfma_f32_16x16x32_bf16 v[84:87], v[40:43], v[180:183], 0\n"
    "v_mul_f32 v148, v144, v148\n"
    "v_mul_f32 v149, v145, v149\n"
    "v_mfma_f32_16x16x32_bf16 v[48:51], v[44:47], v[176:179], 0\n"
    "ds_read_b128 v[40:43], v0 offset:49152\n"
    "v_mul_f32 v150, v146, v150\n"
    "v_mfma_f32_16x16x32_bf16 v[52:55], v[44:47], v[180:183], 0\n"
    "v_mul_f32 v151, v147, v151\n"
    "s_waitcnt lgkmcnt(3)\n"
    "v_mfma_f32_16x16x32_bf16 v[56:59], v[16:19], v[176:179], 0\n"
    "ds_read_b128 v[44:47], v0 offset:50176\n"
    "buffer_load_dwordx4 v222, s[4:7], s49 offen offset:1024 lds\n"
    "v_mfma_f32_16x16x32_bf16 v[60:63], v[16:19], v[180:183], 0\n"
    "v_exp_f32 v144, -v144\n"
    "v_mfma_f32_16x16x32_bf16 v[88:91], v[20:23], v[176:179], 0\n"
    "ds_read_b128 v[16:19], v0 offset:51200\n"
    "v_fma_f32 v144, v144, s36, s36\n"
    "v_mfma_f32_16x16x32_bf16 v[92:95], v[20:23], v[180:183], 0\n"
    "v_exp_f32 v145, -v145\n"
    "v_mfma_f32_16x16x32_bf16 v[96:99], v[24:27], v[176:179], 0\n"
    "ds_read_b128 v[20:23], v0 offset:52224\n"
    "buffer_load_dwordx4 v222, s[4:7], s31 offen offset:2048 lds\n"
    "v_mfma_f32_16x16x32_bf16 v[100:103], v[24:27], v[180:183], 0\n"
    "v_exp_f32 v146, -v146\n"
    "v_mfma_f32_16x16x32_bf16 v[64:67], v[28:31], v[184:187], v[64:67]\n"
    "ds_read_b128 v[24:27], v208 offset:17408\n"
    "v_fma_f32 v145, v145, s36, s36\n"
    "v_mfma_f32_16x16x32_bf16 v[68:71], v[28:31], v[188:191], v[68:71]\n"
    "v_fma_f32 v146, v146, s36, s36\n"
    "s_waitcnt lgkmcnt(3)\n"
    "v_mfma_f32_16x16x32_bf16 v[72:75], v[32:35], v[184:187], v[72:75]\n"
    "ds_read_b128 v[28:31], v208 offset:18432\n"
    "v_mul_f32 v156, v152, v156\n"
    "v_mfma_f32_16x16x32_bf16 v[76:79], v[32:35], v[188:191], v[76:79]\n"
    "v_exp_f32 v147, -v147\n"
    "v_mfma_f32_16x16x32_bf16 v[80:83], v[36:39], v[184:187], v[80:83]\n"
    "ds_read_b128 v[32:35], v208 offset:19456\n"
    "buffer_load_dwordx4 a[88:91], v0, s[4:7], s28 offen offset:3072 sc1\n"
    "v_mfma_f32_16x16x32_bf16 v[84:87], v[36:39], v[188:191], v[84:87]\n"
    "v_rcp_f32 v144, v144\n"
    "v_mfma_f32_16x16x32_bf16 v[48:51], v[40:43], v[184:187], v[48:51]\n"
    "ds_read_b128 v[36:39], v208 offset:20480\n"
    "v_fma_f32 v147, v147, s36, s36\n"
    "v_mfma_f32_16x16x32_bf16 v[52:55], v[40:43], v[188:191], v[52:55]\n"
    "v_rcp_f32 v145, v145\n"
    "v_mfma_f32_16x16x32_bf16 v[56:59], v[44:47], v[184:187], v[56:59]\n"
    "ds_read_b128 v[40:43], v208 offset:21504\n"
    "buffer_load_dwordx4 a[92:95], v0, s[4:7], s29 offen offset:3072 sc1\n"
    "v_mfma_f32_16x16x32_bf16 v[60:63], v[44:47], v[188:191], v[60:63]\n"
    "v_mul_f32 v148, v144, v148\n"
    "s_waitcnt lgkmcnt(3)\n"
    "v_mfma_f32_16x16x32_bf16 v[88:91], v[16:19], v[184:187], v[88:91]\n"
    "ds_read_b128 v[44:47], v208 offset:22528\n"
    "v_mul_f32 v149, v145, v149\n"
    "v_mfma_f32_16x16x32_bf16 v[92:95], v[16:19], v[188:191], v[92:95]\n"
    "v_rcp_f32 v146, v146\n"
    "v_mfma_f32_16x16x32_bf16 v[96:99], v[20:23], v[184:187], v[96:99]\n"
    "ds_read_b128 v[16:19], v208 offset:23552\n"
    "v_mul_f32 v150, v146, v150\n"
    "v_mfma_f32_16x16x32_bf16 v[100:103], v[20:23], v[188:191], v[100:103]\n"
    "v_rcp_f32 v147, v147\n"
    "v_mfma_f32_16x16x32_bf16 v[64:67], v[24:27], v[192:195], v[64:67]\n"
    "ds_read_b128 v[20:23], v208 offset:54272\n"
    "s_add_u32 s28, s28, 4096\n"
    "v_mfma_f32_16x16x32_bf16 v[68:71], v[24:27], v[196:199], v[68:71]\n"
    "v_exp_f32 v152, -v152\n"
    "v_mfma_f32_16x16x32_bf16 v[72:75], v[28:31], v[192:195], v[72:75]\n"
    "ds_read_b128 v[24:27], v208 offset:55296\n"
    "buffer_load_dwordx4 a[96:99], v0, s[4:7], s28 offen offset:0 sc1\n"
    "v_mfma_f32_16x16x32_bf16 v[76:79], v[28:31], v[196:199], v[76:79]\n"
    "v_mul_f32 v151, v147, v151\n"
    "s_waitcnt lgkmcnt(3)\n"
    "v_mfma_f32_16x16x32_bf16 v[80:83], v[32:35], v[192:195], v[80:83]\n"
    "ds_read_b128 v[28:31], v208 offset:56320\n"
    "s_add_u32 s29, s29, 4096\n"
    "v_mfma_f32_16x16x32_bf16 v[84:87], v[32:35], v[196:199], v[84:87]\n"
    "v_cvt_pk_bf16_f32 v9, v150, v151\n"
    "v_cvt_pk_bf16_f32 v8, v148, v149\n"
    "v_mfma_f32_16x16x32_bf16 v[48:51], v[36:39], v[192:195], v[48:51]\n"
    "ds_read_b128 v[32:35], v208 offset:57344\n"
    "buffer_load_dwordx4 a[100:103], v0, s[4:7], s29 offen offset:0 sc1\n"
    "v_mfma_f32_16x16x32_bf16 v[52:55], v[36:39], v[196:199], v[52:55]\n"
    "v_mul_f32 v157, v153, v157\n"
    "v_mul_f32 v158, v154, v158\n"
    "v_mfma_f32_16x16x32_bf16 v[56:59], v[40:43], v[192:195], v[56:59]\n"
    "ds_read_b128 v[36:39], v208 offset:58368\n"
    "v_mul_f32 v159, v155, v159\n"
    "v_mfma_f32_16x16x32_bf16 v[60:63], v[40:43], v[196:199], v[60:63]\n"
    "v_exp_f32 v153, -v153\n"
    "v_mfma_f32_16x16x32_bf16 v[88:91], v[44:47], v[192:195], v[88:91]\n"
    "ds_read_b128 v[40:43], v208 offset:59392\n"
    "buffer_load_dwordx4 a[104:107], v0, s[4:7], s28 offen offset:1024 sc1\n"
    "v_mfma_f32_16x16x32_bf16 v[92:95], v[44:47], v[196:199], v[92:95]\n"
    "v_fma_f32 v152, v152, s36, s36\n"
    "s_waitcnt lgkmcnt(3)\n"
    "v_mfma_f32_16x16x32_bf16 v[96:99], v[16:19], v[192:195], v[96:99]\n"
    "ds_read_b128 v[44:47], v208 offset:60416\n"
    "v_fma_f32 v153, v153, s36, s36\n"
    "v_mfma_f32_16x16x32_bf16 v[100:103], v[16:19], v[196:199], v[100:103]\n"
    "v_exp_f32 v154, -v154\n"
    "v_mfma_f32_16x16x32_bf16 v[64:67], v[20:23], v[200:203], v[64:67]\n"
    "s_waitcnt vmcnt(14)\n"
    "s_barrier\n"
    "s_cmp_lg_u32 s17, 0\n"
    "s_cbranch_scc1 LF4%=\n"
    "s_mov_b64 exec, 1\n"
    "buffer_store_dword v216, off, s[4:7], s20 offset:96 sc1\n"
    "s_mov_b64 exec, -1\n"
    "LF4%=:\n"
    "ds_read_b128 v[16:19], v0 offset:16384\n"
    "buffer_load_dwordx4 a[108:111], v0, s[4:7], s29 offen offset:1024 sc1\n"
    "v_mfma_f32_16x16x32_bf16 v[68:71], v[20:23], v[204:207], v[68:71]\n"
    "v_exp_f32 v155, -v155\n"
    "v_mfma_f32_16x16x32_bf16 v[72:75], v[24:27], v[200:203], v[72:75]\n"
    "ds_read_b128 v[20:23], v0 offset:17408\n"
    "v_fma_f32 v154, v154, s36, s36\n"
    "v_mfma_f32_16x16x32_bf16 v[76:79], v[24:27], v[204:207], v[76:79]\n"
    "v_rcp_f32 v152, v152\n"
    "v_mfma_f32_16x16x32_bf16 v[80:83], v[28:31], v[200:203], v[80:83]\n"
    "ds_read_b128 v[24:27], v0 offset:18432\n"
    "v_fma_f32 v155, v155, s36, s36\n"
    "v_mfma_f32_16x16x32_bf16 v[84:87], v[28:31], v[204:207], v[84:87]\n"
    "s_add_u32 m0, s17, 9216\n"
    "v_add_u32 v222, 4096, v222\n"
    "s_waitcnt lgkmcnt(3)\n"
    "v_mfma_f32_16x16x32_bf16 v[48:51], v[32:35], v[200:203], v[48:51]\n"
    "ds_read_b128 v[28:31], v0 offset:19456\n"
    "buffer_load_dwordx4 v222, s[4:7], s47 offen offset:0 lds\n"
    "v_mfma_f32_16x16x32_bf16 v[52:55], v[32:35], v[204:207], v[52:55]\n"
    "v_rcp_f32 v153, v153\n"
    "v_mfma_f32_16x16x32_bf16 v[56:59], v[36:39], v[200:203], v[56:59]\n"
    "ds_read_b128 v[32:35], v0 offset:20480\n"
    "v_mul_f32 v156, v152, v156\n"
    "v_mfma_f32_16x16x32_bf16 v[60:63], v[36:39], v[204:207], v[60:63]\n"
    "v_rcp_f32 v154, v154\n"
    "v_mfma_f32_16x16x32_bf16 v[88:91], v[40:43], v[200:203], v[88:91]\n"
    "ds_read_b128 v[36:39], v0 offset:21504\n"
    "buffer_load_dwordx4 v222, s[4:7], s48 offen offset:1024 lds\n"
    "v_mfma_f32_16x16x32_bf16 v[92:95], v[40:43], v[204:207], v[92:95]\n"
    "v_rcp_f32 v155, v155\n"
    "v_mfma_f32_16x16x32_bf16 v[96:99], v[44:47], v[200:203], v[96:99]\n"
    "ds_read_b128 v[40:43], v0 offset:22528\n"
    "v_mul_f32 v157, v153, v157\n"
    "v_mfma_f32_16x16x32_bf16 v[100:103], v[44:47], v[204:207], v[100:103]\n"
    "v_mul_f32 v158, v154, v158\n"
    "s_waitcnt lgkmcnt(3)\n"
    "v_mfma_f32_16x16x32_bf16 v[64:67], v[16:19], a[0:3], v[64:67]\n"
    "ds_read_b128 v[44:47], v0 offset:53248\n"
    "v_mul_f32 v159, v155, v159\n"
    "v_mfma_f32_16x16x32_bf16 v[68:71], v[16:19], a[4:7], v[68:71]\n"
    "v_cvt_pk_bf16_f32 v11, v158, v159\n"
    "v_cvt_pk_bf16_f32 v10, v156, v157\n"
    "v_mfma_f32_16x16x32_bf16 v[72:75], v[20:23], a[0:3], v[72:75]\n"
    "ds_read_b128 v[16:19], v0 offset:54272\n"
    "buffer_load_dwordx4 v222, s[4:7], s30 offen offset:2048 lds\n"
    "v_mfma_f32_16x16x32_bf16 v[76:79], v[20:23], a[4:7], v[76:79]\n"
    "v_permlane32_swap_b32 v9, v11\n"
    "v_permlane32_swap_b32 v8, v10\n"
    "v_mfma_f32_16x16x32_bf16 v[80:83], v[24:27], a[0:3], v[80:83]\n"
    "ds_read_b128 v[20:23], v0 offset:55296\n"
    "buffer_store_dwordx4 v[8:11], v6, s[4:7], s21 offen sc1\n"
    "v_mfma_f32_16x16x32_bf16 v[84:87], v[24:27], a[4:7], v[84:87]\n"
    "v_mul_f32 v164, v160, v164\n"
    "v_mul_f32 v165, v161, v165\n"
    "v_mfma_f32_16x16x32_bf16 v[48:51], v[28:31], a[0:3], v[48:51]\n"
    "ds_read_b128 v[24:27], v0 offset:56320\n"
    "buffer_load_dwordx4 v222, s[4:7], s50 offen offset:3072 lds\n"
    "v_mfma_f32_16x16x32_bf16 v[52:55], v[28:31], a[4:7], v[52:55]\n"
    "v_mul_f32 v166, v162, v166\n"
    "s_waitcnt lgkmcnt(3)\n"
    "v_mfma_f32_16x16x32_bf16 v[56:59], v[32:35], a[0:3], v[56:59]\n"
    "ds_read_b128 v[28:31], v0 offset:57344\n"
    "v_mul_f32 v167, v163, v167\n"
    "v_mfma_f32_16x16x32_bf16 v[60:63], v[32:35], a[4:7], v[60:63]\n"
    "v_exp_f32 v160, -v160\n"
    "v_mfma_f32_16x16x32_bf16 v[88:91], v[36:39], a[0:3], v[88:91]\n"
    "ds_read_b128 v[32:35], v0 offset:58368\n"
    "v_fma_f32 v160, v160, s36, s36\n"
    "v_mfma_f32_16x16x32_bf16 v[92:95], v[36:39], a[4:7], v[92:95]\n"
    "v_exp_f32 v161, -v161\n"
    "v_mfma_f32_16x16x32_bf16 v[96:99], v[40:43], a[0:3], v[96:99]\n"
    "ds_read_b128 v[36:39], v0 offset:59392\n"
    "s_add_u32 m0, s17, 13312\n"
    "v_mfma_f32_16x16x32_bf16 v[100:103], v[40:43], a[4:7], v[100:103]\n"
    "v_exp_f32 v162, -v162\n"
    "v_mfma_f32_16x16x32_bf16 v[64:67], v[44:47], a[8:11], v[64:67]\n"
    "ds_read_b128 v[40:43], v208 offset:24576\n"
    "buffer_load_dwordx4 v222, s[4:7], s51 offen offset:0 lds\n"
    "v_mfma_f32_16x16x32_bf16 v[68:71], v[44:47], a[12:15], v[68:71]\n"
    "v_fma_f32 v161, v161, s36, s36\n"
    "s_waitcnt lgkmcnt(3)\n"
    "v_mfma_f32_16x16x32_bf16 v[72:75], v[16:19], a[8:11], v[72:75]\n"
    "ds_read_b128 v[44:47], v208 offset:25600\n"
    "buffer_load_dwordx4 v222, s[4:7], s49 offen offset:1024 lds\n"
    "v_mfma_f32_16x16x32_bf16 v[76:79], v[16:19], a[12:15], v[76:79]\n"
    "v_exp_f32 v163, -v163\n"
    "v_mfma_f32_16x16x32_bf16 v[80:83], v[20:23], a[8:11], v[80:83]\n"
    "ds_read_b128 v[16:19], v208 offset:26624\n"
    "v_fma_f32 v162, v162, s36, s36\n"
    "v_mfma_f32_16x16x32_bf16 v[84:87], v[20:23], a[12:15], v[84:87]\n"
    "v_rcp_f32 v160, v160\n"
    "v_mfma_f32_16x16x32_bf16 v[48:51], v[24:27], a[8:11], v[48:51]\n"
    "ds_read_b128 v[20:23], v208 offset:27648\n"
    "v_fma_f32 v163, v163, s36, s36\n"
    "v_mfma_f32_16x16x32_bf16 v[52:55], v[24:27], a[12:15], v[52:55]\n"
    "v_rcp_f32 v161, v161\n"
    "v_mfma_f32_16x16x32_bf16 v[56:59], v[28:31], a[8:11], v[56:59]\n"
    "ds_read_b128 v[24:27], v208 offset:28672\n"
    "buffer_load_dwordx4 v222, s[4:7], s31 offen offset:2048 lds\n"
    "v_mfma_f32_16x16x32_bf16 v[60:63], v[28:31], a[12:15], v[60:63]\n"
    "v_mul_f32 v164, v160, v164\n"
    "s_waitcnt lgkmcnt(3)\n"
    "v_mfma_f32_16x16x32_bf16 v[88:91], v[32:35], a[8:11], v[88:91]\n"
    "ds_read_b128 v[28:31], v208 offset:29696\n"
    "v_mul_f32 v165, v161, v165\n"
    "v_mfma_f32_16x16x32_bf16 v[92:95], v[32:35], a[12:15], v[92:95]\n"
    "v_rcp_f32 v162, v162\n"
    "v_mfma_f32_16x16x32_bf16 v[96:99], v[36:39], a[8:11], v[96:99]\n"
    "ds_read_b128 v[32:35], v208 offset:30720\n"
    "buffer_load_dwordx4 a[112:115], v0, s[4:7], s28 offen offset:2048 sc1\n"
    "v_mfma_f32_16x16x32_bf16 v[100:103], v[36:39], a[12:15], v[100:103]\n"
    "v_rcp_f32 v163, v163\n"
    "v_mfma_f32_16x16x32_bf16 v[64:67], v[40:43], a[16:19], v[64:67]\n"
    "ds_read_b128 v[36:39], v208 offset:61440\n"
    "v_mul_f32 v166, v162, v166\n"
    "v_mfma_f32_16x16x32_bf16 v[68:71], v[40:43], a[20:23], v[68:71]\n"
    "v_mul_f32 v167, v163, v167\n"
    "v_cvt_pk_bf16_f32 v13, v166, v167\n"
    "v_mfma_f32_16x16x32_bf16 v[72:75], v[44:47], a[16:19], v[72:75]\n"
    "ds_read_b128 v[40:43], v208 offset:62464\n"
    "v_cvt_pk_bf16_f32 v12, v164, v165\n"
    "v_mfma_f32_16x16x32_bf16 v[76:79], v[44:47], a[20:23], v[76:79]\n"
    "v_mul_f32 v172, v168, v172\n"
    "s_waitcnt lgkmcnt(3)\n"
    "v_mfma_f32_16x16x32_bf16 v[80:83], v[16:19], a[16:19], v[80:83]\n"
    "ds_read_b128 v[44:47], v208 offset:63488\n"
    "buffer_load_dwordx4 a[116:119], v0, s[4:7], s29 offen offset:2048 sc1\n"
    "v_mfma_f32_16x16x32_bf16 v[84:87], v[16:19], a[20:23], v[84:87]\n"
    "v_exp_f32 v168, -v168\n"
    "v_mfma_f32_16x16x32_bf16 v[48:51], v[20:23], a[16:19], v[48:51]\n"
    "ds_read_b128 v[16:19], v208 offset:64512\n"
    "v_mul_f32 v173, v169, v173\n"
    "v_mfma_f32_16x16x32_bf16 v[52:55], v[20:23], a[20:23], v[52:55]\n"
    "v_exp_f32 v169, -v169\n"
    "v_mfma_f32_16x16x32_bf16 v[56:59], v[24:27], a[16:19], v[56:59]\n"
    "ds_read_b128 v[20:23], v209 offset:0\n"
    "buffer_load_dwordx4 a[120:123], v0, s[4:7], s28 offen offset:3072 sc1\n"
    "v_mfma_f32_16x16x32_bf16 v[60:63], v[24:27], a[20:23], v[60:63]\n"
    "v_mul_f32 v174, v170, v174\n"
    "v_mul_f32 v175, v171, v175\n"
    "v_mfma_f32_16x16x32_bf16 v[88:91], v[28:31], a[16:19], v[88:91]\n"
    "ds_read_b128 v[24:27], v209 offset:1024\n"
    "v_fma_f32 v168, v168, s36, s36\n"
    "v_mfma_f32_16x16x32_bf16 v[92:95], v[28:31], a[20:23], v[92:95]\n"
    "v_fma_f32 v169, v169, s36, s36\n"
    "s_waitcnt lgkmcnt(3)\n"
    "v_mfma_f32_16x16x32_bf16 v[96:99], v[32:35], a[16:19], v[96:99]\n"
    "ds_read_b128 v[28:31], v209 offset:2048\n"
    "v_mfma_f32_16x16x32_bf16 v[100:103], v[32:35], a[20:23], v[100:103]\n"
    "v_exp_f32 v170, -v170\n"
    "v_mfma_f32_16x16x32_bf16 v[64:67], v[36:39], a[24:27], v[64:67]\n"
    "s_barrier\n"
    "ds_read_b128 v[32:35], v0 offset:23552\n"
    "buffer_load_dwordx4 a[124:127], v0, s[4:7], s29 offen offset:3072 sc1\n"
    "v_mfma_f32_16x16x32_bf16 v[68:71], v[36:39], a[28:31], v[68:71]\n"
    "v_exp_f32 v171, -v171\n"
    "v_mfma_f32_16x16x32_bf16 v[72:75], v[40:43], a[24:27], v[72:75]\n"
    "ds_read_b128 v[36:39], v0 offset:24576\n"
    "v_fma_f32 v170, v170, s36, s36\n"
    "v_mfma_f32_16x16x32_bf16 v[76:79], v[40:43], a[28:31], v[76:79]\n"
    "v_rcp_f32 v168, v168\n"
    "v_mfma_f32_16x16x32_bf16 v[80:83], v[44:47], a[24:27], v[80:83]\n"
    "ds_read_b128 v[40:43], v0 offset:25600\n"
    "s_add_u32 s28, s28, 4096\n"
    "v_mfma_f32_16x16x32_bf16 v[84:87], v[44:47], a[28:31], v[84:87]\n"
    "v_fma_f32 v171, v171, s36, s36\n"
    "s_waitcnt lgkmcnt(3)\n"
    "v_mfma_f32_16x16x32_bf16 v[48:51], v[16:19], a[24:27], v[48:51]\n"
    "ds_read_b128 v[44:47], v0 offset:26624\n"
    "buffer_load_dwordx4 a[128:131], v0, s[4:7], s28 offen offset:0 sc1\n"
    "v_mfma_f32_16x16x32_bf16 v[52:55], v[16:19], a[28:31], v[52:55]\n"
    "v_rcp_f32 v169, v169\n"
    "v_mfma_f32_16x16x32_bf16 v[56:59], v[20:23], a[24:27], v[56:59]\n"
    "ds_read_b128 v[16:19], v209 offset:16384\n"
    "v_mul_f32 v172, v168, v172\n"
    "v_mfma_f32_16x16x32_bf16 v[60:63], v[20:23], a[28:31], v[60:63]\n"
    "v_rcp_f32 v170, v170\n"
    "v_mfma_f32_16x16x32_bf16 v[88:91], v[24:27], a[24:27], v[88:91]\n"
    "ds_read_b128 v[20:23], v209 offset:17408\n"
    "s_add_u32 s29, s29, 4096\n"
    "v_mfma_f32_16x16x32_bf16 v[92:95], v[24:27], a[28:31], v[92:95]\n"
    "v_rcp_f32 v171, v171\n"
    "v_mfma_f32_16x16x32_bf16 v[96:99], v[28:31], a[24:27], v[96:99]\n"
    "ds_read_b128 v[24:27], v209 offset:18432\n"
    "buffer_load_dword v218, v213, s[4:7], s40 offen offset:96 sc1\n"
    "v_mfma_f32_16x16x32_bf16 v[100:103], v[28:31], a[28:31], v[100:103]\n"
    "v_mul_f32 v173, v169, v173\n"
    "s_waitcnt lgkmcnt(3)\n"
    "v_mfma_f32_16x16x32_bf16 v[64:67], v[32:35], a[32:35], v[64:67]\n"
    "ds_read_b128 v[28:31], v0 offset:60416\n"
    "buffer_load_dwordx4 a[132:135], v0, s[4:7], s29 offen offset:0 sc1\n"
    "v_mfma_f32_16x16x32_bf16 v[68:71], v[32:35], a[36:39], v[68:71]\n"
    "v_mul_f32 v174, v170, v174\n"
    "v_mul_f32 v175, v171, v175\n"
    "v_mfma_f32_16x16x32_bf16 v[72:75], v[36:39], a[32:35], v[72:75]\n"
    "ds_read_b128 v[32:35], v0 offset:61440\n"
    "buffer_load_dwordx4 a[136:139], v0, s[4:7], s28 offen offset:1024 sc1\n"
    "v_mfma_f32_16x16x32_bf16 v[76:79], v[36:39], a[36:39], v[76:79]\n"
    "v_cvt_pk_bf16_f32 v15, v174, v175\n"
    "v_cvt_pk_bf16_f32 v14, v172, v173\n"
    "v_mfma_f32_16x16x32_bf16 v[80:83], v[40:43], a[32:35], v[80:83]\n"
    "ds_read_b128 v[36:39], v0 offset:62464\n"
    "v_permlane32_swap_b32 v13, v15\n"
    "v_mfma_f32_16x16x32_bf16 v[84:87], v[40:43], a[36:39], v[84:87]\n"
    "v_permlane32_swap_b32 v12, v14\n"
    "buffer_store_dwordx4 v[12:15], v7, s[4:7], s21 offen sc1\n"
    "v_mfma_f32_16x16x32_bf16 v[48:51], v[44:47], a[32:35], v[48:51]\n"
    "ds_read_b128 v[40:43], v0 offset:63488\n"
    "buffer_load_dwordx4 a[140:143], v0, s[4:7], s29 offen offset:1024 sc1\n"
    "v_mfma_f32_16x16x32_bf16 v[52:55], v[44:47], a[36:39], v[52:55]\n"
    "s_waitcnt lgkmcnt(3)\n"
    "v_mfma_f32_16x16x32_bf16 v[56:59], v[16:19], a[32:35], v[56:59]\n"
    "ds_read_b128 v[44:47], v209 offset:19456\n"
    "v_mfma_f32_16x16x32_bf16 v[60:63], v[16:19], a[36:39], v[60:63]\n"
    "v_mfma_f32_16x16x32_bf16 v[88:91], v[20:23], a[32:35], v[88:91]\n"
    "ds_read_b128 v[16:19], v209 offset:20480\n"
    "v_mfma_f32_16x16x32_bf16 v[92:95], v[20:23], a[36:39], v[92:95]\n"
    "s_add_u32 m0, s17, 16384\n"
    "v_add_u32 v222, 4096, v222\n"
    "v_mfma_f32_16x16x32_bf16 v[96:99], v[24:27], a[32:35], v[96:99]\n"
    "ds_read_b128 v[20:23], v209 offset:21504\n"
    "buffer_load_dwordx4 v222, s[4:7], s47 offen offset:0 lds\n"
    "v_mfma_f32_16x16x32_bf16 v[100:103], v[24:27], a[36:39], v[100:103]\n"
    "v_mfma_f32_16x16x32_bf16 v[64:67], v[28:31], a[40:43], v[64:67]\n"
    "ds_read_b128 v[24:27], v208 offset:31744\n"
    "v_mfma_f32_16x16x32_bf16 v[68:71], v[28:31], a[44:47], v[68:71]\n"
    "s_waitcnt lgkmcnt(3)\n"
    "v_mfma_f32_16x16x32_bf16 v[72:75], v[32:35], a[40:43], v[72:75]\n"
    "ds_read_b128 v[28:31], v208 offset:32768\n"
    "buffer_load_dwordx4 v222, s[4:7], s48 offen offset:1024 lds\n"
    "v_mfma_f32_16x16x32_bf16 v[76:79], v[32:35], a[44:47], v[76:79]\n"
    "v_mfma_f32_16x16x32_bf16 v[80:83], v[36:39], a[40:43], v[80:83]\n"
    "ds_read_b128 v[32:35], v208 offset:33792\n"
    "v_mfma_f32_16x16x32_bf16 v[84:87], v[36:39], a[44:47], v[84:87]\n"
    "v_mfma_f32_16x16x32_bf16 v[48:51], v[40:43], a[40:43], v[48:51]\n"
    "LPK6:\n"
    "s_cbranch_execz LTR\n"
    "ds_read_b128 v[36:39], v208 offset:34816\n"
    "v_mfma_f32_16x16x32_bf16 v[52:55], v[40:43], a[44:47], v[52:55]\n"
    "v_mfma_f32_16x16x32_bf16 v[56:59], v[44:47], a[40:43], v[56:59]\n"
    "ds_read_b128 v[40:43], v209 offset:22528\n"
    "buffer_load_dwordx4 v222, s[4:7], s30 offen offset:2048 lds\n"
    "v_mfma_f32_16x16x32_bf16 v[60:63], v[44:47], a[44:47], v[60:63]\n"
    "s_waitcnt lgkmcnt(3)\n"
    "v_mfma_f32_16x16x32_bf16 v[88:91], v[16:19], a[40:43], v[88:91]\n"
    "ds_read_b128 v[44:47], v209 offset:23552\n"
    "v_mfma_f32_16x16x32_bf16 v[92:95], v[16:19], a[44:47], v[92:95]\n"
    "v_mfma_f32_16x16x32_bf16 v[96:99], v[20:23], a[40:43], v[96:99]\n"
    "ds_read_b128 v[16:19], v209 offset:24576\n"
    "buffer_load_dwordx4 v222, s[4:7], s50 offen offset:3072 lds\n"
    "v_mfma_f32_16x16x32_bf16 v[100:103], v[20:23], a[44:47], v[100:103]\n"
    "v_mfma_f32_16x16x32_bf16 v[64:67], v[24:27], a[48:51], v[64:67]\n"
    "ds_read_b128 v[20:23], v209 offset:3072\n"
    "v_mfma_f32_16x16x32_bf16 v[68:71], v[24:27], a[52:55], v[68:71]\n"
    "v_mfma_f32_16x16x32_bf16 v[72:75], v[28:31], a[48:51], v[72:75]\n"
    "ds_read_b128 v[24:27], v209 offset:4096\n"
    "v_mfma_f32_16x16x32_bf16 v[76:79], v[28:31], a[52:55], v[76:79]\n"
    "s_waitcnt lgkmcnt(3)\n"
    "v_mfma_f32_16x16x32_bf16 v[80:83], v[32:35], a[48:51], v[80:83]\n"
    "ds_read_b128 v[28:31], v209 offset:5120\n"
    "s_add_u32 m0, s17, 20480\n"
    "v_mfma_f32_16x16x32_bf16 v[84:87], v[32:35], a[52:55], v[84:87]\n"
    "v_mfma_f32_16x16x32_bf16 v[48:51], v[36:39], a[48:51], v[48:51]\n"
    "ds_read_b128 v[32:35], v209 offset:6144\n"
    "buffer_load_dwordx4 v222, s[4:7], s51 offen offset:0 lds\n"
    "v_mfma_f32_16x16x32_bf16 v[52:55], v[36:39], a[52:55], v[52:55]\n"
    "v_mfma_f32_16x16x32_bf16 v[56:59], v[40:43], a[48:51], v[56:59]\n"
    "ds_read_b128 v[36:39], v209 offset:25600\n"
    "buffer_load_dwordx4 v222, s[4:7], s49 offen offset:1024 lds\n"
    "v_mfma_f32_16x16x32_bf16 v[60:63], v[40:43], a[52:55], v[60:63]\n"
    "v_mfma_f32_16x16x32_bf16 v[88:91], v[44:47], a[48:51], v[88:91]\n"
    "ds_read_b128 v[40:43], v209 offset:26624\n"
    "v_mfma_f32_16x16x32_bf16 v[92:95], v[44:47], a[52:55], v[92:95]\n"
    "s_waitcnt lgkmcnt(3)\n"
    "v_mfma_f32_16x16x32_bf16 v[96:99], v[16:19], a[48:51], v[96:99]\n"
    "ds_read_b128 v[44:47], v209 offset:27648\n"
    "v_mfma_f32_16x16x32_bf16 v[100:103], v[16:19], a[52:55], v[100:103]\n"
    "v_mfma_f32_16x16x32_bf16 v[64:67], v[20:23], a[56:59], v[64:67]\n"
    "s_barrier\n"
    "ds_read_b128 v[16:19], v0 offset:27648\n"
    "buffer_load_dwordx4 v222, s[4:7], s31 offen offset:2048 lds\n"
    "v_mfma_f32_16x16x32_bf16 v[68:71], v[20:23], a[60:63], v[68:71]\n"
    "v_mfma_f32_16x16x32_bf16 v[72:75], v[24:27], a[56:59], v[72:75]\n"
    "ds_read_b128 v[20:23], v0 offset:28672\n"
    "v_mfma_f32_16x16x32_bf16 v[76:79], v[24:27], a[60:63], v[76:79]\n"
    "v_mfma_f32_16x16x32_bf16 v[80:83], v[28:31], a[56:59], v[80:83]\n"
    "ds_read_b128 v[24:27], v0 offset:29696\n"
    "buffer_load_dwordx4 a[144:147], v0, s[4:7], s28 offen offset:2048 sc1\n"
    "v_mfma_f32_16x16x32_bf16 v[84:87], v[28:31], a[60:63], v[84:87]\n"
    "s_waitcnt lgkmcnt(3)\n"
    "v_mfma_f32_16x16x32_bf16 v[48:51], v[32:35], a[56:59], v[48:51]\n"
    "ds_read_b128 v[28:31], v0 offset:30720\n"
    "v_mfma_f32_16x16x32_bf16 v[52:55], v[32:35], a[60:63], v[52:55]\n"
    "v_mfma_f32_16x16x32_bf16 v[56:59], v[36:39], a[56:59], v[56:59]\n"
    "ds_read_b128 v[32:35], v0 offset:31744\n"
    "v_mfma_f32_16x16x32_bf16 v[60:63], v[36:39], a[60:63], v[60:63]\n"
    "v_mfma_f32_16x16x32_bf16 v[88:91], v[40:43], a[56:59], v[88:91]\n"
    "ds_read_b128 v[36:39], v0 offset:32768\n"
    "buffer_load_dwordx4 a[148:151], v0, s[4:7], s29 offen offset:2048 sc1\n"
    "v_mfma_f32_16x16x32_bf16 v[92:95], v[40:43], a[60:63], v[92:95]\n"
    "v_mfma_f32_16x16x32_bf16 v[96:99], v[44:47], a[56:59], v[96:99]\n"
    "ds_read_b128 v[40:43], v0 offset:33792\n"
    "v_mfma_f32_16x16x32_bf16 v[100:103], v[44:47], a[60:63], v[100:103]\n"
    "s_waitcnt lgkmcnt(3)\n"
    "v_mfma_f32_16x16x32_bf16 v[64:67], v[16:19], a[64:67], v[64:67]\n"
    "ds_read_b128 v[44:47], v0 offset:64512\n"
    "buffer_load_dwordx4 a[152:155], v0, s[4:7], s28 offen offset:3072 sc1\n"
    "v_mfma_f32_16x16x32_bf16 v[68:71], v[16:19], a[68:71], v[68:71]\n"
    "v_mfma_f32_16x16x32_bf16 v[72:75], v[20:23], a[64:67], v[72:75]\n"
    "ds_read_b128 v[16:19], v208 offset:0\n"
    "s_waitcnt vmcnt(14)\n"
    "v_cmp_ne_u32 vcc, v218, v216\n"
    "s_cbranch_vccnz LP11%=\n"
    "LQ11%=:\n"
    "v_mfma_f32_16x16x32_bf16 v[76:79], v[20:23], a[68:71], v[76:79]\n"
    "v_mfma_f32_16x16x32_bf16 v[80:83], v[24:27], a[64:67], v[80:83]\n"
    "ds_read_b128 v[20:23], v208 offset:1024\n"
    "v_mfma_f32_16x16x32_bf16 v[84:87], v[24:27], a[68:71], v[84:87]\n"
    "v_mfma_f32_16x16x32_bf16 v[48:51], v[28:31], a[64:67], v[48:51]\n"
    "ds_read_b128 v[24:27], v208 offset:2048\n"
    "buffer_load_dwordx4 a[156:159], v0, s[4:7], s29 offen offset:3072 sc1\n"
    "v_mfma_f32_16x16x32_bf16 v[52:55], v[28:31], a[68:71], v[52:55]\n"
    "s_waitcnt lgkmcnt(3)\n"
    "v_mfma_f32_16x16x32_bf16 v[56:59], v[32:35], a[64:67], v[56:59]\n"
    "ds_read_b128 v[28:31], v208 offset:3072\n"
    "v_mfma_f32_16x16x32_bf16 v[60:63], v[32:35], a[68:71], v[60:63]\n"
    "v_mfma_f32_16x16x32_bf16 v[88:91], v[36:39], a[64:67], v[88:91]\n"
    "ds_read_b128 v[32:35], v208 offset:4096\n"
    "s_add_u32 s28, s28, 4096\n"
    "v_mfma_f32_16x16x32_bf16 v[92:95], v[36:39], a[68:71], v[92:95]\n"
    "v_mfma_f32_16x16x32_bf16 v[96:99], v[40:43], a[64:67], v[96:99]\n"
    "ds_read_b128 v[36:39], v208 offset:5120\n"
    "buffer_load_dwordx4 a[160:163], v0, s[4:7], s28 offen offset:0 sc1\n"
    "v_mfma_f32_16x16x32_bf16 v[100:103], v[40:43], a[68:71], v[100:103]\n"
    "v_mfma_f32_16x16x32_bf16 v[64:67], v[44:47], a[72:75], v[64:67]\n"
    "ds_read_b128 v[40:43], v208 offset:35840\n"
    "v_mfma_f32_16x16x32_bf16 v[68:71], v[44:47], a[76:79], v[68:71]\n"
    "s_waitcnt lgkmcnt(3)\n"
    "v_mfma_f32_16x16x32_bf16 v[72:75], v[16:19], a[72:75], v[72:75]\n"
    "ds_read_b128 v[44:47], v208 offset:36864\n"
    "s_add_u32 s29, s29, 4096\n"
    "v_mfma_f32_16x16x32_bf16 v[76:79], v[16:19], a[76:79], v[76:79]\n"
    "v_mfma_f32_16x16x32_bf16 v[80:83], v[20:23], a[72:75], v[80:83]\n"
    "ds_read_b128 v[16:19], v208 offset:37888\n"
    "buffer_load_dwordx4 a[164:167], v0, s[4:7], s29 offen offset:0 sc1\n"
    "v_mfma_f32_16x16x32_bf16 v[84:87], v[20:23], a[76:79], v[84:87]\n"
    "v_mfma_f32_16x16x32_bf16 v[48:51], v[24:27], a[72:75], v[48:51]\n"
    "ds_read_b128 v[20:23], v208 offset:38912\n"
    "buffer_load_dwordx4 a[168:171], v0, s[4:7], s28 offen offset:1024 sc1\n"
    "v_mfma_f32_16x16x32_bf16 v[52:55], v[24:27], a[76:79], v[52:55]\n"
    "v_mfma_f32_16x16x32_bf16 v[56:59], v[28:31], a[72:75], v[56:59]\n"
    "ds_read_b128 v[24:27], v208 offset:39936\n"
    "v_mfma_f32_16x16x32_bf16 v[60:63], v[28:31], a[76:79], v[60:63]\n"
    "s_waitcnt lgkmcnt(3)\n"
    "v_mfma_f32_16x16x32_bf16 v[88:91], v[32:35], a[72:75], v[88:91]\n"
    "ds_read_b128 v[28:31], v208 offset:40960\n"
    "v_mfma_f32_16x16x32_bf16 v[92:95], v[32:35], a[76:79], v[92:95]\n"
    "v_mfma_f32_16x16x32_bf16 v[96:99], v[36:39], a[72:75], v[96:99]\n"
    "ds_read_b128 v[32:35], v208 offset:41984\n"
    "buffer_load_dwordx4 a[172:175], v0, s[4:7], s29 offen offset:1024 sc1\n"
    "v_mfma_f32_16x16x32_bf16 v[100:103], v[36:39], a[76:79], v[100:103]\n"
    "v_mfma_f32_16x16x32_bf16 v[64:67], v[40:43], a[80:83], v[64:67]\n"
    "ds_read_b128 v[36:39], v209 offset:7168\n"
    "v_mfma_f32_16x16x32_bf16 v[68:71], v[40:43], a[84:87], v[68:71]\n"
    "v_mfma_f32_16x16x32_bf16 v[72:75], v[44:47], a[80:83], v[72:75]\n"
    "ds_read_b128 v[40:43], v209 offset:8192\n"
    "v_mfma_f32_16x16x32_bf16 v[76:79], v[44:47], a[84:87], v[76:79]\n"
    "s_add_u32 m0, s17, 23552\n"
    "v_add_u32 v222, 4096, v222\n"
    "s_waitcnt lgkmcnt(3)\n"
    "v_mfma_f32_16x16x32_bf16 v[80:83], v[16:19], a[80:83], v[80:83]\n"
    "ds_read_b128 v[44:47], v209 offset:9216\n"
    "buffer_load_dwordx4 v222, s[4:7], s47 offen offset:0 lds\n"
    "v_mfma_f32_16x16x32_bf16 v[84:87], v[16:19], a[84:87], v[84:87]\n"
    "v_mfma_f32_16x16x32_bf16 v[48:51], v[20:23], a[80:83], v[48:51]\n"
    "ds_read_b128 v[16:19], v209 offset:10240\n"
    "v_mfma_f32_16x16x32_bf16 v[52:55], v[20:23], a[84:87], v[52:55]\n"
    "v_mfma_f32_16x16x32_bf16 v[56:59], v[24:27], a[80:83], v[56:59]\n"
    "ds_read_b128 v[20:23], v209 offset:11264\n"
    "buffer_load_dwordx4 v222, s[4:7], s48 offen offset:1024 lds\n"
    "v_mfma_f32_16x16x32_bf16 v[60:63], v[24:27], a[84:87], v[60:63]\n"
    "v_mfma_f32_16x16x32_bf16 v[88:91], v[28:31], a[80:83], v[88:91]\n"
    "ds_read_b128 v[24:27], v209 offset:12288\n"
    "v_mfma_f32_16x16x32_bf16 v[92:95], v[28:31], a[84:87], v[92:95]\n"
    "s_waitcnt lgkmcnt(3)\n"
    "v_mfma_f32_16x16x32_bf16 v[96:99], v[32:35], a[80:83], v[96:99]\n"
    "ds_read_b128 v[28:31], v209 offset:13312\n"
    "buffer_load_dwordx4 v222, s[4:7], s30 offen offset:2048 lds\n"
    "v_mfma_f32_16x16x32_bf16 v[100:103], v[32:35], a[84:87], v[100:103]\n"
    "v_mfma_f32_16x16x32_bf16 v[64:67], v[36:39], a[88:91], v[64:67]\n"
    "s_waitcnt vmcnt(19)\n"
    "s_barrier\n"
    "s_cmp_lg_u32 s17, 0\n"
    "s_cbranch_scc1 LF5%=\n"
    "s_mov_b64 exec, 1\n"
    "buffer_store_dword v216, off, s[4:7], s20 offset:128 sc1\n"
    "s_mov_b64 exec, -1\n"
    "LF5%=:\n"
    "ds_read_b128 v[32:35], v0 offset:0\n"
    "v_mfma_f32_16x16x32_bf16 v[68:71], v[36:39], a[92:95], v[68:71]\n"
    "v_mfma_f32_16x16x32_bf16 v[72:75], v[40:43], a[88:91], v[72:75]\n"
    "ds_read_b128 v[36:39], v0 offset:1024\n"
    "v_mfma_f32_16x16x32_bf16 v[76:79], v[40:43], a[92:95], v[76:79]\n"
    "v_mfma_f32_16x16x32_bf16 v[80:83], v[44:47], a[88:91], v[80:83]\n"
    "ds_read_b128 v[40:43], v0 offset:2048\n"
    "buffer_load_dwordx4 v222, s[4:7], s50 offen offset:3072 lds\n"
    "v_mfma_f32_16x16x32_bf16 v[84:87], v[44:47], a[92:95], v[84:87]\n"
    "s_waitcnt lgkmcnt(3)\n"
    "v_mfma_f32_16x16x32_bf16 v[48:51], v[16:19], a[88:91], v[48:51]\n"
    "ds_read_b128 v[44:47], v0 offset:3072\n"
    "v_mfma_f32_16x16x32_bf16 v[52:55], v[16:19], a[92:95], v[52:55]\n"
    "v_mfma_f32_16x16x32_bf16 v[56:59], v[20:23], a[88:91], v[56:59]\n"
    "ds_read_b128 v[16:19], v0 offset:4096\n"
    "s_mov_b32 m0, s11\n"
    "v_mfma_f32_16x16x32_bf16 v[60:63], v[20:23], a[92:95], v[60:63]\n"
    "v_mfma_f32_16x16x32_bf16 v[88:91], v[24:27], a[88:91], v[88:91]\n"
    "ds_read_b128 v[20:23], v0 offset:5120\n"
    "buffer_load_dwordx4 v222, s[4:7], s51 offen offset:0 lds\n"
    "v_mfma_f32_16x16x32_bf16 v[92:95], v[24:27], a[92:95], v[92:95]\n"
    "v_mfma_f32_16x16x32_bf16 v[96:99], v[28:31], a[88:91], v[96:99]\n"
    "ds_read_b128 v[24:27], v0 offset:6144\n"
    "v_mfma_f32_16x16x32_bf16 v[100:103], v[28:31], a[92:95], v[100:103]\n"
    "s_waitcnt lgkmcnt(3)\n"
    "v_mfma_f32_16x16x32_bf16 v[64:67], v[32:35], a[96:99], v[64:67]\n"
    "ds_read_b128 v[28:31], v0 offset:36864\n"
    "buffer_load_dwordx4 v222, s[4:7], s49 offen offset:1024 lds\n"
    "v_mfma_f32_16x16x32_bf16 v[68:71], v[32:35], a[100:103], v[68:71]\n"
    "v_mfma_f32_16x16x32_bf16 v[72:75], v[36:39], a[96:99], v[72:75]\n"
    "ds_read_b128 v[32:35], v0 offset:37888\n"
    "v_mfma_f32_16x16x32_bf16 v[76:79], v[36:39], a[100:103], v[76:79]\n"
    "v_mfma_f32_16x16x32_bf16 v[80:83], v[40:43], a[96:99], v[80:83]\n"
    "ds_read_b128 v[36:39], v0 offset:38912\n"
    "buffer_load_dwordx4 v222, s[4:7], s31 offen offset:2048 lds\n"
    "v_mfma_f32_16x16x32_bf16 v[84:87], v[40:43], a[100:103], v[84:87]\n"
    "v_mfma_f32_16x16x32_bf16 v[48:51], v[44:47], a[96:99], v[48:51]\n"
    "ds_read_b128 v[40:43], v0 offset:39936\n"
    "v_mfma_f32_16x16x32_bf16 v[52:55], v[44:47], a[100:103], v[52:55]\n"
    "s_waitcnt lgkmcnt(3)\n"
    "v_mfma_f32_16x16x32_bf16 v[56:59], v[16:19], a[96:99], v[56:59]\n"
    "ds_read_b128 v[44:47], v0 offset:40960\n"
    "v_mfma_f32_16x16x32_bf16 v[60:63], v[16:19], a[100:103], v[60:63]\n"
    "s_add_u32 m0, s17, 27648\n"
    "v_add_u32 v222, 4096, v222\n"
    "v_mfma_f32_16x16x32_bf16 v[88:91], v[20:23], a[96:99], v[88:91]\n"
    "ds_read_b128 v[16:19], v0 offset:41984\n"
    "buffer_load_dwordx4 v222, s[4:7], s47 offen offset:0 lds\n"
    "v_mfma_f32_16x16x32_bf16 v[92:95], v[20:23], a[100:103], v[92:95]\n"
    "v_mfma_f32_16x16x32_bf16 v[96:99], v[24:27], a[96:99], v[96:99]\n"
    "ds_read_b128 v[20:23], v0 offset:43008\n"
    "v_mfma_f32_16x16x32_bf16 v[100:103], v[24:27], a[100:103], v[100:103]\n"
    "v_mfma_f32_16x16x32_bf16 v[64:67], v[28:31], a[104:107], v[64:67]\n"
    "ds_read_b128 v[24:27], v208 offset:8192\n"
    "buffer_load_dwordx4 v222, s[4:7], s48 offen offset:1024 lds\n"
    "v_mfma_f32_16x16x32_bf16 v[68:71], v[28:31], a[108:111], v[68:71]\n"
    "s_waitcnt lgkmcnt(3)\n"
    "v_mfma_f32_16x16x32_bf16 v[72:75], v[32:35], a[104:107], v[72:75]\n"
    "ds_read_b128 v[28:31], v208 offset:9216\n"
    "v_mfma_f32_16x16x32_bf16 v[76:79], v[32:35], a[108:111], v[76:79]\n"
    "v_mfma_f32_16x16x32_bf16 v[80:83], v[36:39], a[104:107], v[80:83]\n"
    "ds_read_b128 v[32:35], v208 offset:10240\n"
    "v_mfma_f32_16x16x32_bf16 v[84:87], v[36:39], a[108:111], v[84:87]\n"
    "v_mfma_f32_16x16x32_bf16 v[48:51], v[40:43], a[104:107], v[48:51]\n"
    "ds_read_b128 v[36:39], v208 offset:11264\n"
    "buffer_load_dwordx4 v222, s[4:7], s30 offen offset:2048 lds\n"
    "v_mfma_f32_16x16x32_bf16 v[52:55], v[40:43], a[108:111], v[52:55]\n"
    "v_mfma_f32_16x16x32_bf16 v[56:59], v[44:47], a[104:107], v[56:59]\n"
    "ds_read_b128 v[40:43], v208 offset:12288\n"
    "v_mfma_f32_16x16x32_bf16 v[60:63], v[44:47], a[108:111], v[60:63]\n"
    "s_waitcnt lgkmcnt(3)\n"
    "v_mfma_f32_16x16x32_bf16 v[88:91], v[16:19], a[104:107], v[88:91]\n"
    "ds_read_b128 v[44:47], v208 offset:13312\n"
    "buffer_load_dwordx4 a[176:179], v0, s[4:7], s28 offen offset:2048 sc1\n"
    "v_mfma_f32_16x16x32_bf16 v[92:95], v[16:19], a[108:111], v[92:95]\n"
    "v_mfma_f32_16x16x32_bf16 v[96:99], v[20:23], a[104:107], v[96:99]\n"
    "ds_read_b128 v[16:19], v208 offset:14336\n"
    "v_mfma_f32_16x16x32_bf16 v[100:103], v[20:23], a[108:111], v[100:103]\n"
    "v_mfma_f32_16x16x32_bf16 v[64:67], v[24:27], a[112:115], v[64:67]\n"
    "ds_read_b128 v[20:23], v208 offset:45056\n"
    "v_mfma_f32_16x16x32_bf16 v[68:71], v[24:27], a[116:119], v[68:71]\n"
    "v_mfma_f32_16x16x32_bf16 v[72:75], v[28:31], a[112:115], v[72:75]\n"
    "ds_read_b128 v[24:27], v208 offset:46080\n"
    "buffer_load_dwordx4 a[180:183], v0, s[4:7], s29 offen offset:2048 sc1\n"
    "v_mfma_f32_16x16x32_bf16 v[76:79], v[28:31], a[116:119], v[76:79]\n"
    "s_waitcnt lgkmcnt(3)\n"
    "v_mfma_f32_16x16x32_bf16 v[80:83], v[32:35], a[112:115], v[80:83]\n"
    "ds_read_b128 v[28:31], v208 offset:47104\n"
    "v_mfma_f32_16x16x32_bf16 v[84:87], v[32:35], a[116:119], v[84:87]\n"
    "v_mfma_f32_16x16x32_bf16 v[48:51], v[36:39], a[112:115], v[48:51]\n"
    "ds_read_b128 v[32:35], v208 offset:48128\n"
    "buffer_load_dwordx4 a[184:187], v0, s[4:7], s28 offen offset:3072 sc1\n"
    "v_mfma_f32_16x16x32_bf16 v[52:55], v[36:39], a[116:119], v[52:55]\n"
    "v_mfma_f32_16x16x32_bf16 v[56:59], v[40:43], a[112:115], v[56:59]\n"
    "ds_read_b128 v[36:39], v208 offset:49152\n"
    "v_mfma_f32_16x16x32_bf16 v[60:63], v[40:43], a[116:119], v[60:63]\n"
    "v_mfma_f32_16x16x32_bf16 v[88:91], v[44:47], a[112:115], v[88:91]\n"
    "ds_read_b128 v[40:43], v208 offset:50176\n"
    "v_mfma_f32_16x16x32_bf16 v[92:95], v[44:47], a[116:119], v[92:95]\n"
    "s_waitcnt lgkmcnt(3)\n"
    "v_mfma_f32_16x16x32_bf16 v[96:99], v[16:19], a[112:115], v[96:99]\n"
    "ds_read_b128 v[44:47], v208 offset:51200\n"
    "buffer_load_dwordx4 a[188:191], v0, s[4:7], s29 offen offset:3072 sc1\n"
    "v_mfma_f32_16x16x32_bf16 v[100:103], v[16:19], a[116:119], v[100:103]\n"
    "v_mfma_f32_16x16x32_bf16 v[64:67], v[20:23], a[120:123], v[64:67]\n"
    "s_barrier\n"
    "ds_read_b128 v[16:19], v0 offset:9216\n"
    "v_mfma_f32_16x16x32_bf16 v[68:71], v[20:23], a[124:127], v[68:71]\n"
    "v_mfma_f32_16x16x32_bf16 v[72:75], v[24:27], a[120:123], v[72:75]\n"
    "ds_read_b128 v[20:23], v0 offset:10240\n"
    "s_add_u32 s28, s28, 4096\n"
    "v_mfma_f32_16x16x32_bf16 v[76:79], v[24:27], a[124:127], v[76:79]\n"
    "v_mfma_f32_16x16x32_bf16 v[80:83], v[28:31], a[120:123], v[80:83]\n"
    "ds_read_b128 v[24:27], v0 offset:11264\n"
    "buffer_load_dword v218, v213, s[4:7], s40 offen offset:128 sc1\n"
    "v_mfma_f32_16x16x32_bf16 v[84:87], v[28:31], a[124:127], v[84:87]\n"
    "s_waitcnt lgkmcnt(3)\n"
    "v_mfma_f32_16x16x32_bf16 v[48:51], v[32:35], a[120:123], v[48:51]\n"
    "ds_read_b128 v[28:31], v0 offset:12288\n"
    "buffer_load_dwordx4 a[192:195], v0, s[4:7], s28 offen offset:0 sc1\n"
    "v_mfma_f32_16x16x32_bf16 v[52:55], v[32:35], a[124:127], v[52:55]\n"
    "v_mfma_f32_16x16x32_bf16 v[56:59], v[36:39], a[120:123], v[56:59]\n"
    "ds_read_b128 v[32:35], v0 offset:13312\n"
    "s_add_u32 s29, s29, 4096\n"
    "v_mfma_f32_16x16x32_bf16 v[60:63], v[36:39], a[124:127], v[60:63]\n"
    "v_mfma_f32_16x16x32_bf16 v[88:91], v[40:43], a[120:123], v[88:91]\n"
    "ds_read_b128 v[36:39], v0 offset:14336\n"
    "buffer_load_dwordx4 a[196:199], v0, s[4:7], s29 offen offset:0 sc1\n"
    "v_mfma_f32_16x16x32_bf16 v[92:95], v[40:43], a[124:127], v[92:95]\n"
    "v_mfma_f32_16x16x32_bf16 v[96:99], v[44:47], a[120:123], v[96:99]\n"
    "ds_read_b128 v[40:43], v0 offset:15360\n"
    "buffer_load_dwordx4 a[200:203], v0, s[4:7], s28 offen offset:1024 sc1\n"
    "v_mfma_f32_16x16x32_bf16 v[100:103], v[44:47], a[124:127], v[100:103]\n"
    "s_waitcnt lgkmcnt(3)\n"
    "v_mfma_f32_16x16x32_bf16 v[64:67], v[16:19], a[128:131], v[64:67]\n"
    "ds_read_b128 v[44:47], v0 offset:46080\n"
    "v_mfma_f32_16x16x32_bf16 v[68:71], v[16:19], a[132:135], v[68:71]\n"
    "v_mfma_f32_16x16x32_bf16 v[72:75], v[20:23], a[128:131], v[72:75]\n"
    "ds_read_b128 v[16:19], v0 offset:47104\n"
    "v_mfma_f32_16x16x32_bf16 v[76:79], v[20:23], a[132:135], v[76:79]\n"
    "v_mfma_f32_16x16x32_bf16 v[80:83], v[24:27], a[128:131], v[80:83]\n"
    "ds_read_b128 v[20:23], v0 offset:48128\n"
    "buffer_load_dwordx4 a[204:207], v0, s[4:7], s29 offen offset:1024 sc1\n"
    "v_mfma_f32_16x16x32_bf16 v[84:87], v[24:27], a[132:135], v[84:87]\n"
    "v_mfma_f32_16x16x32_bf16 v[48:51], v[28:31], a[128:131], v[48:51]\n"
    "ds_read_b128 v[24:27], v0 offset:49152\n"
    "v_mfma_f32_16x16x32_bf16 v[52:55], v[28:31], a[132:135], v[52:55]\n"
    "s_waitcnt vmcnt(34) lgkmcnt(3)\n"
    "v_mfma_f32_16x16x32_bf16 v[56:59], v[32:35], a[128:131], v[56:59]\n"
    "ds_read_b128 v[28:31], v0 offset:50176\n"
    "buffer_load_dwordx4 a[208:211], v0, s[4:7], s28 offen offset:2048 sc1\n"
    "v_mfma_f32_16x16x32_bf16 v[60:63], v[32:35], a[132:135], v[60:63]\n"
    "v_mfma_f32_16x16x32_bf16 v[88:91], v[36:39], a[128:131], v[88:91]\n"
    "ds_read_b128 v[32:35], v0 offset:51200\n"
    "v_mfma_f32_16x16x32_bf16 v[92:95], v[36:39], a[132:135], v[92:95]\n"
    "v_mfma_f32_16x16x32_bf16 v[96:99], v[40:43], a[128:131], v[96:99]\n"
    "ds_read_b128 v[36:39], v0 offset:52224\n"
    "v_mfma_f32_16x16x32_bf16 v[100:103], v[40:43], a[132:135], v[100:103]\n"
    "v_mfma_f32_16x16x32_bf16 v[64:67], v[44:47], a[136:139], v[64:67]\n"
    "ds_read_b128 v[40:43], v208 offset:17408\n"
    "buffer_load_dwordx4 a[212:215], v0, s[4:7], s29 offen offset:2048 sc1\n"
    "v_mfma_f32_16x16x32_bf16 v[68:71], v[44:47], a[140:143], v[68:71]\n"
    "s_waitcnt lgkmcnt(3)\n"
    "v_mfma_f32_16x16x32_bf16 v[72:75], v[16:19], a[136:139], v[72:75]\n"
    "ds_read_b128 v[44:47], v208 offset:18432\n"
    "v_mfma_f32_16x16x32_bf16 v[76:79], v[16:19], a[140:143], v[76:79]\n"
    "v_mfma_f32_16x16x32_bf16 v[80:83], v[20:23], a[136:139], v[80:83]\n"
    "ds_read_b128 v[16:19], v208 offset:19456\n"
    "buffer_load_dwordx4 v222, s[4:7], s50 offen offset:3072 lds\n"
    "v_mfma_f32_16x16x32_bf16 v[84:87], v[20:23], a[140:143], v[84:87]\n"
    "v_mfma_f32_16x16x32_bf16 v[48:51], v[24:27], a[136:139], v[48:51]\n"
    "ds_read_b128 v[20:23], v208 offset:20480\n"
    "v_mfma_f32_16x16x32_bf16 v[52:55], v[24:27], a[140:143], v[52:55]\n"
    "v_mfma_f32_16x16x32_bf16 v[56:59], v[28:31], a[136:139], v[56:59]\n"
    "ds_read_b128 v[24:27], v208 offset:21504\n"
    "v_mfma_f32_16x16x32_bf16 v[60:63], v[28:31], a[140:143], v[60:63]\n"
    "s_waitcnt vmcnt(28) lgkmcnt(3)\n"
    "v_mfma_f32_16x16x32_bf16 v[88:91], v[32:35], a[136:139], v[88:91]\n"
    "ds_read_b128 v[28:31], v208 offset:22528\n"
    "s_add_u32 m0, s17, 31744\n"
    "v_mfma_f32_16x16x32_bf16 v[92:95], v[32:35], a[140:143], v[92:95]\n"
    "v_mfma_f32_16x16x32_bf16 v[96:99], v[36:39], a[136:139], v[96:99]\n"
    "ds_read_b128 v[32:35], v208 offset:23552\n"
    "buffer_load_dwordx4 v222, s[4:7], s51 offen offset:0 lds\n"
    "v_mfma_f32_16x16x32_bf16 v[100:103], v[36:39], a[140:143], v[100:103]\n"
    "v_mfma_f32_16x16x32_bf16 v[64:67], v[40:43], a[144:147], v[64:67]\n"
    "ds_read_b128 v[36:39], v208 offset:54272\n"
    "buffer_load_dwordx4 v222, s[4:7], s49 offen offset:1024 lds\n"
    "v_mfma_f32_16x16x32_bf16 v[68:71], v[40:43], a[148:151], v[68:71]\n"
    "v_mfma_f32_16x16x32_bf16 v[72:75], v[44:47], a[144:147], v[72:75]\n"
    "ds_read_b128 v[40:43], v208 offset:55296\n"
    "v_mfma_f32_16x16x32_bf16 v[76:79], v[44:47], a[148:151], v[76:79]\n"
    "s_waitcnt lgkmcnt(3)\n"
    "v_mfma_f32_16x16x32_bf16 v[80:83], v[16:19], a[144:147], v[80:83]\n"
    "ds_read_b128 v[44:47], v208 offset:56320\n"
    "v_mfma_f32_16x16x32_bf16 v[84:87], v[16:19], a[148:151], v[84:87]\n"
    "v_mfma_f32_16x16x32_bf16 v[48:51], v[20:23], a[144:147], v[48:51]\n"
    "ds_read_b128 v[16:19], v208 offset:57344\n"
    "buffer_load_dwordx4 v222, s[4:7], s31 offen offset:2048 lds\n"
    "v_mfma_f32_16x16x32_bf16 v[52:55], v[20:23], a[148:151], v[52:55]\n"
    "v_mfma_f32_16x16x32_bf16 v[56:59], v[24:27], a[144:147], v[56:59]\n"
    "ds_read_b128 v[20:23], v208 offset:58368\n"
    "v_mfma_f32_16x16x32_bf16 v[60:63], v[24:27], a[148:151], v[60:63]\n"
    "v_mfma_f32_16x16x32_bf16 v[88:91], v[28:31], a[144:147], v[88:91]\n"
    "ds_read_b128 v[24:27], v208 offset:59392\n"
    "buffer_load_dwordx4 a[216:219], v0, s[4:7], s28 offen offset:3072 sc1\n"
    "v_mfma_f32_16x16x32_bf16 v[92:95], v[28:31], a[148:151], v[92:95]\n"
    "s_waitcnt vmcnt(30) lgkmcnt(3)\n"
    "v_mfma_f32_16x16x32_bf16 v[96:99], v[32:35], a[144:147], v[96:99]\n"
    "ds_read_b128 v[28:31], v208 offset:60416\n"
    "v_mfma_f32_16x16x32_bf16 v[100:103], v[32:35], a[148:151], v[100:103]\n"
    "v_mfma_f32_16x16x32_bf16 v[64:67], v[36:39], a[152:155], v[64:67]\n"
    "s_barrier\n"
    "ds_read_b128 v[32:35], v0 offset:16384\n"
    "v_mfma_f32_16x16x32_bf16 v[68:71], v[36:39], a[156:159], v[68:71]\n"
    "v_mfma_f32_16x16x32_bf16 v[72:75], v[40:43], a[152:155], v[72:75]\n"
    "ds_read_b128 v[36:39], v0 offset:17408\n"
    "buffer_load_dwordx4 a[220:223], v0, s[4:7], s29 offen offset:3072 sc1\n"
    "v_mfma_f32_16x16x32_bf16 v[76:79], v[40:43], a[156:159], v[76:79]\n"
    "v_mfma_f32_16x16x32_bf16 v[80:83], v[44:47], a[152:155], v[80:83]\n"
    "ds_read_b128 v[40:43], v0 offset:18432\n"
    "v_mfma_f32_16x16x32_bf16 v[84:87], v[44:47], a[156:159], v[84:87]\n"
    "s_waitcnt lgkmcnt(3)\n"
    "v_mfma_f32_16x16x32_bf16 v[48:51], v[16:19], a[152:155], v[48:51]\n"
    "ds_read_b128 v[44:47], v0 offset:19456\n"
    "v_mfma_f32_16x16x32_bf16 v[52:55], v[16:19], a[156:159], v[52:55]\n"
    "s_add_u32 m0, s17, 0\n"
    "s_add_u32 s56, s10, 0\n"
    "v_mfma_f32_16x16x32_bf16 v[56:59], v[20:23], a[152:155], v[56:59]\n"
    "ds_read_b128 v[16:19], v0 offset:20480\n"
    "s_waitcnt vmcnt(12)\n"
    "v_cmp_ne_u32 vcc, v218, v216\n"
    "s_cbranch_vccnz LP12%=\n"
    "LQ12%=:\n"
    "buffer_load_dwordx4 v0, s[4:7], s56 offen offset:0 lds\n"
    "v_mfma_f32_16x16x32_bf16 v[60:63], v[20:23], a[156:159], v[60:63]\n"
    "v_mfma_f32_16x16x32_bf16 v[88:91], v[24:27], a[152:155], v[88:91]\n"
    "ds_read_b128 v[20:23], v0 offset:21504\n"
    "v_mfma_f32_16x16x32_bf16 v[92:95], v[24:27], a[156:159], v[92:95]\n"
    "v_mfma_f32_16x16x32_bf16 v[96:99], v[28:31], a[152:155], v[96:99]\n"
    "ds_read_b128 v[24:27], v0 offset:22528\n"
    "s_add_u32 s28, s28, 4096\n"
    "v_mfma_f32_16x16x32_bf16 v[100:103], v[28:31], a[156:159], v[100:103]\n"
    "s_waitcnt lgkmcnt(3)\n"
    "v_mfma_f32_16x16x32_bf16 v[64:67], v[32:35], a[160:163], v[64:67]\n"
    "ds_read_b128 v[28:31], v0 offset:53248\n"
    "buffer_load_dwordx4 a[224:227], v0, s[4:7], s28 offen offset:0 sc1\n"
    "v_mfma_f32_16x16x32_bf16 v[68:71], v[32:35], a[164:167], v[68:71]\n"
    "v_mfma_f32_16x16x32_bf16 v[72:75], v[36:39], a[160:163], v[72:75]\n"
    "ds_read_b128 v[32:35], v0 offset:54272\n"
    "s_add_u32 s29, s29, 4096\n"
    "v_mfma_f32_16x16x32_bf16 v[76:79], v[36:39], a[164:167], v[76:79]\n"
    "v_mfma_f32_16x16x32_bf16 v[80:83], v[40:43], a[160:163], v[80:83]\n"
    "ds_read_b128 v[36:39], v0 offset:55296\n"
    "buffer_load_dwordx4 a[228:231], v0, s[4:7], s29 offen offset:0 sc1\n"
    "v_mfma_f32_16x16x32_bf16 v[84:87], v[40:43], a[164:167], v[84:87]\n"
    "v_mfma_f32_16x16x32_bf16 v[48:51], v[44:47], a[160:163], v[48:51]\n"
    "ds_read_b128 v[40:43], v0 offset:56320\n"
    "v_mfma_f32_16x16x32_bf16 v[52:55], v[44:47], a[164:167], v[52:55]\n"
    "s_waitcnt lgkmcnt(3)\n"
    "v_mfma_f32_16x16x32_bf16 v[56:59], v[16:19], a[160:163], v[56:59]\n"
    "ds_read_b128 v[44:47], v0 offset:57344\n"
    "buffer_load_dwordx4 a[232:235], v0, s[4:7], s28 offen offset:1024 sc1\n"
    "v_mfma_f32_16x16x32_bf16 v[60:63], v[16:19], a[164:167], v[60:63]\n"
    "v_mfma_f32_16x16x32_bf16 v[88:91], v[20:23], a[160:163], v[88:91]\n"
    "ds_read_b128 v[16:19], v0 offset:58368\n"
    "v_mfma_f32_16x16x32_bf16 v[92:95], v[20:23], a[164:167], v[92:95]\n"
    "v_mfma_f32_16x16x32_bf16 v[96:99], v[24:27], a[160:163], v[96:99]\n"
    "ds_read_b128 v[20:23], v0 offset:59392\n"
    "buffer_load_dwordx4 a[236:239], v0, s[4:7], s29 offen offset:1024 sc1\n"
    "v_mfma_f32_16x16x32_bf16 v[100:103], v[24:27], a[164:167], v[100:103]\n"
    "v_mfma_f32_16x16x32_bf16 v[64:67], v[28:31], a[168:171], v[64:67]\n"
    "ds_read_b128 v[24:27], v208 offset:24576\n"
    "v_mfma_f32_16x16x32_bf16 v[68:71], v[28:31], a[172:175], v[68:71]\n"
    "s_waitcnt lgkmcnt(3)\n"
    "v_mfma_f32_16x16x32_bf16 v[72:75], v[32:35], a[168:171], v[72:75]\n"
    "ds_read_b128 v[28:31], v208 offset:25600\n"
    "v_mfma_f32_16x16x32_bf16 v[76:79], v[32:35], a[172:175], v[76:79]\n"
    "v_mfma_f32_16x16x32_bf16 v[80:83], v[36:39], a[168:171], v[80:83]\n"
    "ds_read_b128 v[32:35], v208 offset:26624\n"
    "buffer_load_dwordx4 a[240:243], v0, s[4:7], s28 offen offset:2048 sc1\n"
    "v_mfma_f32_16x16x32_bf16 v[84:87], v[36:39], a[172:175], v[84:87]\n"
    "v_mfma_f32_16x16x32_bf16 v[48:51], v[40:43], a[168:171], v[48:51]\n"
    "ds_read_b128 v[36:39], v208 offset:27648\n"
    "v_mfma_f32_16x16x32_bf16 v[52:55], v[40:43], a[172:175], v[52:55]\n"
    "v_mfma_f32_16x16x32_bf16 v[56:59], v[44:47], a[168:171], v[56:59]\n"
    "ds_read_b128 v[40:43], v208 offset:28672\n"
    "buffer_load_dwordx4 a[244:247], v0, s[4:7], s29 offen offset:2048 sc1\n"
    "v_mfma_f32_16x16x32_bf16 v[60:63], v[44:47], a[172:175], v[60:63]\n"
    "s_waitcnt lgkmcnt(3)\n"
    "v_mfma_f32_16x16x32_bf16 v[88:91], v[16:19], a[168:171], v[88:91]\n"
    "ds_read_b128 v[44:47], v208 offset:29696\n"
    "v_mfma_f32_16x16x32_bf16 v[92:95], v[16:19], a[172:175], v[92:95]\n"
    "v_mfma_f32_16x16x32_bf16 v[96:99], v[20:23], a[168:171], v[96:99]\n"
    "ds_read_b128 v[16:19], v208 offset:30720\n"
    "v_mfma_f32_16x16x32_bf16 v[100:103], v[20:23], a[172:175], v[100:103]\n"
    "v_mfma_f32_16x16x32_bf16 v[64:67], v[24:27], a[176:179], v[64:67]\n"
    "ds_read_b128 v[20:23], v208 offset:61440\n"
    "buffer_load_dwordx4 a[248:251], v0, s[4:7], s28 offen offset:3072 sc1\n"
    "v_mfma_f32_16x16x32_bf16 v[68:71], v[24:27], a[180:183], v[68:71]\n"
    "v_mfma_f32_16x16x32_bf16 v[72:75], v[28:31], a[176:179], v[72:75]\n"
    "ds_read_b128 v[24:27], v208 offset:62464\n"
    "v_mfma_f32_16x16x32_bf16 v[76:79], v[28:31], a[180:183], v[76:79]\n"
    "s_waitcnt lgkmcnt(3)\n"
    "v_mfma_f32_16x16x32_bf16 v[80:83], v[32:35], a[176:179], v[80:83]\n"
    "ds_read_b128 v[28:31], v208 offset:63488\n"
    "buffer_load_dwordx4 a[252:255], v0, s[4:7], s29 offen offset:3072 sc1\n"
    "v_mfma_f32_16x16x32_bf16 v[84:87], v[32:35], a[180:183], v[84:87]\n"
    "v_mfma_f32_16x16x32_bf16 v[48:51], v[36:39], a[176:179], v[48:51]\n"
    "ds_read_b128 v[32:35], v208 offset:64512\n"
    "v_mfma_f32_16x16x32_bf16 v[52:55], v[36:39], a[180:183], v[52:55]\n"
    "v_mfma_f32_16x16x32_bf16 v[56:59], v[40:43], a[176:179], v[56:59]\n"
    "ds_read_b128 v[36:39], v209 offset:0\n"
    "v_mfma_f32_16x16x32_bf16 v[60:63], v[40:43], a[180:183], v[60:63]\n"
    "v_mfma_f32_16x16x32_bf16 v[88:91], v[44:47], a[176:179], v[88:91]\n"
    "ds_read_b128 v[40:43], v209 offset:1024\n"
    "buffer_load_dwordx4 v0, s[4:7], s56 offen offset:1024 lds\n"
    "v_mfma_f32_16x16x32_bf16 v[92:95], v[44:47], a[180:183], v[92:95]\n"
    "s_waitcnt lgkmcnt(3)\n"
    "v_mfma_f32_16x16x32_bf16 v[96:99], v[16:19], a[176:179], v[96:99]\n"
    "ds_read_b128 v[44:47], v209 offset:2048\n"
    "v_mfma_f32_16x16x32_bf16 v[100:103], v[16:19], a[180:183], v[100:103]\n"
    "v_mfma_f32_16x16x32_bf16 v[64:67], v[20:23], a[184:187], v[64:67]\n"
    "s_barrier\n"
    "ds_read_b128 v[16:19], v0 offset:23552\n"
    "buffer_load_dwordx4 v0, s[4:7], s56 offen offset:2048 lds\n"
    "v_mfma_f32_16x16x32_bf16 v[68:71], v[20:23], a[188:191], v[68:71]\n"
    "v_mfma_f32_16x16x32_bf16 v[72:75], v[24:27], a[184:187], v[72:75]\n"
    "ds_read_b128 v[20:23], v0 offset:24576\n"
    "v_mfma_f32_16x16x32_bf16 v[76:79], v[24:27], a[188:191], v[76:79]\n"
    "v_mfma_f32_16x16x32_bf16 v[80:83], v[28:31], a[184:187], v[80:83]\n"
    "ds_read_b128 v[24:27], v0 offset:25600\n"
    "v_mfma_f32_16x16x32_bf16 v[84:87], v[28:31], a[188:191], v[84:87]\n"
    "s_add_u32 m0, s17, 4096\n"
    "s_add_u32 s56, s10, 147456\n"
    "s_waitcnt lgkmcnt(3)\n"
    "v_mfma_f32_16x16x32_bf16 v[48:51], v[32:35], a[184:187], v[48:51]\n"
    "ds_read_b128 v[28:31], v0 offset:60416\n"
    "buffer_load_dwordx4 v0, s[4:7], s56 offen offset:0 lds\n"
    "v_mfma_f32_16x16x32_bf16 v[52:55], v[32:35], a[188:191], v[52:55]\n"
    "v_mfma_f32_16x16x32_bf16 v[56:59], v[36:39], a[184:187], v[56:59]\n"
    "ds_read_b128 v[32:35], v0 offset:61440\n"
    "v_mfma_f32_16x16x32_bf16 v[60:63], v[36:39], a[188:191], v[60:63]\n"
    "v_mfma_f32_16x16x32_bf16 v[88:91], v[40:43], a[184:187], v[88:91]\n"
    "ds_read_b128 v[36:39], v0 offset:62464\n"
    "buffer_load_dwordx4 v0, s[4:7], s56 offen offset:1024 lds\n"
    "v_mfma_f32_16x16x32_bf16 v[92:95], v[40:43], a[188:191], v[92:95]\n"
    "v_mfma_f32_16x16x32_bf16 v[96:99], v[44:47], a[184:187], v[96:99]\n"
    "ds_read_b128 v[40:43], v208 offset:31744\n"
    "v_mfma_f32_16x16x32_bf16 v[100:103], v[44:47], a[188:191], v[100:103]\n"
    "s_waitcnt vmcnt(21) lgkmcnt(3)\n"
    "v_mfma_f32_16x16x32_bf16 v[64:67], v[16:19], a[192:195], v[64:67]\n"
    "ds_read_b128 v[44:47], v208 offset:32768\n"
    "v_mfma_f32_16x16x32_bf16 v[68:71], v[16:19], a[196:199], v[68:71]\n"
    "v_mfma_f32_16x16x32_bf16 v[72:75], v[20:23], a[192:195], v[72:75]\n"
    "ds_read_b128 v[16:19], v208 offset:33792\n"
    "buffer_load_dwordx4 v0, s[4:7], s56 offen offset:2048 lds\n"
    "v_mfma_f32_16x16x32_bf16 v[76:79], v[20:23], a[196:199], v[76:79]\n"
    "v_mfma_f32_16x16x32_bf16 v[80:83], v[24:27], a[192:195], v[80:83]\n"
    "ds_read_b128 v[20:23], v209 offset:3072\n"
    "v_mfma_f32_16x16x32_bf16 v[84:87], v[24:27], a[196:199], v[84:87]\n"
    "v_mfma_f32_16x16x32_bf16 v[64:67], v[28:31], a[200:203], v[64:67]\n"
    "ds_read_b128 v[24:27], v209 offset:4096\n"
    "v_mfma_f32_16x16x32_bf16 v[68:71], v[28:31], a[204:207], v[68:71]\n"
    "s_add_u32 m0, s17, 9216\n"
    "s_add_u32 s56, s10, 12288\n"
    "s_waitcnt vmcnt(20) lgkmcnt(3)\n"
    "v_mfma_f32_16x16x32_bf16 v[72:75], v[32:35], a[200:203], v[72:75]\n"
    "ds_read_b128 v[28:31], v209 offset:5120\n"
    "buffer_load_dwordx4 v0, s[4:7], s56 offen offset:0 lds\n"
    "v_mfma_f32_16x16x32_bf16 v[76:79], v[32:35], a[204:207], v[76:79]\n"
    "v_mfma_f32_16x16x32_bf16 v[80:83], v[36:39], a[200:203], v[80:83]\n"
    "ds_read_b128 v[32:35], v0 offset:27648\n"
    "buffer_load_dwordx2 v[160:161], v210, s[4:7], s39 offen offset:0\n"
    "v_mfma_f32_16x16x32_bf16 v[84:87], v[36:39], a[204:207], v[84:87]\n"
    "v_mfma_f32_16x16x32_bf16 v[64:67], v[40:43], a[208:211], v[64:67]\n"
    "ds_read_b128 v[36:39], v0 offset:28672\n"
    "buffer_load_dwordx4 v0, s[4:7], s56 offen offset:1024 lds\n"
    "v_mfma_f32_16x16x32_bf16 v[68:71], v[40:43], a[212:215], v[68:71]\n"
    "v_mfma_f32_16x16x32_bf16 v[72:75], v[44:47], a[208:211], v[72:75]\n"
    "LPK7:\n"
    "s_cbranch_execz LTR\n"
    "ds_read_b128 v[40:43], v0 offset:29696\n"
    "buffer_load_dwordx2 v[162:163], v210, s[4:7], s42 offen offset:0\n"
    "v_mfma_f32_16x16x32_bf16 v[76:79], v[44:47], a[212:215], v[76:79]\n"
    "s_waitcnt vmcnt(18) lgkmcnt(3)\n"
    "v_mfma_f32_16x16x32_bf16 v[80:83], v[16:19], a[208:211], v[80:83]\n"
    "ds_read_b128 v[44:47], v0 offset:64512\n"
    "buffer_load_dwordx4 v0, s[4:7], s56 offen offset:2048 lds\n"
    "v_mfma_f32_16x16x32_bf16 v[84:87], v[16:19], a[212:215], v[84:87]\n"
    "v_mfma_f32_16x16x32_bf16 v[64:67], v[20:23], a[216:219], v[64:67]\n"
    "ds_read_b128 v[16:19], v208 offset:0\n"
    "buffer_load_dwordx2 v[164:165], v210, s[4:7], s39 offen offset:512\n"
    "v_mfma_f32_16x16x32_bf16 v[68:71], v[20:23], a[220:223], v[68:71]\n"
    "v_mfma_f32_16x16x32_bf16 v[72:75], v[24:27], a[216:219], v[72:75]\n"
    "ds_read_b128 v[20:23], v208 offset:1024\n"
    "v_mfma_f32_16x16x32_bf16 v[76:79], v[24:27], a[220:223], v[76:79]\n"
    "s_add_u32 m0, s17, 13312\n"
    "s_add_u32 s56, s10, 159744\n"
    "v_mfma_f32_16x16x32_bf16 v[80:83], v[28:31], a[216:219], v[80:83]\n"
    "ds_read_b128 v[24:27], v208 offset:35840\n"
    "buffer_load_dwordx4 v0, s[4:7], s56 offen offset:0 lds\n"
    "v_mfma_f32_16x16x32_bf16 v[84:87], v[28:31], a[220:223], v[84:87]\n"
    "s_waitcnt vmcnt(16) lgkmcnt(3)\n"
    "v_mfma_f32_16x16x32_bf16 v[64:67], v[32:35], a[224:227], v[64:67]\n"
    "ds_read_b128 v[28:31], v208 offset:36864\n"
    "buffer_load_dwordx2 v[166:167], v210, s[4:7], s42 offen offset:512\n"
    "v_mfma_f32_16x16x32_bf16 v[68:71], v[32:35], a[228:231], v[68:71]\n"
    "v_mfma_f32_16x16x32_bf16 v[72:75], v[36:39], a[224:227], v[72:75]\n"
    "ds_read_b128 v[32:35], v208 offset:37888\n"
    "buffer_load_dwordx4 v0, s[4:7], s56 offen offset:1024 lds\n"
    "v_mfma_f32_16x16x32_bf16 v[76:79], v[36:39], a[228:231], v[76:79]\n"
    "v_mfma_f32_16x16x32_bf16 v[80:83], v[40:43], a[224:227], v[80:83]\n"
    "ds_read_b128 v[36:39], v209 offset:7168\n"
    "buffer_load_dword v168, v220, s[4:7], s2 offen\n"
    "v_mfma_f32_16x16x32_bf16 v[84:87], v[40:43], a[228:231], v[84:87]\n"
    "v_mfma_f32_16x16x32_bf16 v[64:67], v[44:47], a[232:235], v[64:67]\n"
    "ds_read_b128 v[40:43], v209 offset:8192\n"
    "v_mfma_f32_16x16x32_bf16 v[68:71], v[44:47], a[236:239], v[68:71]\n"
    "s_waitcnt vmcnt(17) lgkmcnt(3)\n"
    "v_mfma_f32_16x16x32_bf16 v[72:75], v[16:19], a[232:235], v[72:75]\n"
    "ds_read_b128 v[44:47], v209 offset:9216\n"
    "buffer_load_dwordx4 v0, s[4:7], s56 offen offset:2048 lds\n"
    "v_mfma_f32_16x16x32_bf16 v[76:79], v[16:19], a[236:239], v[76:79]\n"
    "v_mfma_f32_16x16x32_bf16 v[80:83], v[20:23], a[232:235], v[80:83]\n"
    "s_barrier\n"
    "ds_read_b128 v[16:19], v0 offset:26624\n"
    "buffer_load_dword v170, v212, s[4:7], s38 offen offset:0\n"
    "v_mfma_f32_16x16x32_bf16 v[84:87], v[20:23], a[236:239], v[84:87]\n"
    "v_mfma_f32_16x16x32_bf16 v[64:67], v[24:27], a[240:243], v[64:67]\n"
    "ds_read_b128 v[20:23], v209 offset:16384\n"
    "v_mfma_f32_16x16x32_bf16 v[68:71], v[24:27], a[244:247], v[68:71]\n"
    "s_add_u32 m0, s17, 16384\n"
    "s_add_u32 s56, s10, 24576\n"
    "v_mfma_f32_16x16x32_bf16 v[72:75], v[28:31], a[240:243], v[72:75]\n"
    "ds_read_b128 v[24:27], v0 offset:63488\n"
    "buffer_load_dwordx4 v0, s[4:7], s56 offen offset:0 lds\n"
    "v_mfma_f32_16x16x32_bf16 v[76:79], v[28:31], a[244:247], v[76:79]\n"
    "s_waitcnt vmcnt(18) lgkmcnt(3)\n"
    "v_mfma_f32_16x16x32_bf16 v[80:83], v[32:35], a[240:243], v[80:83]\n"
    "ds_read_b128 v[28:31], v209 offset:19456\n"
    "buffer_load_dword v171, v212, s[4:7], s38 offen offset:8\n"
    "v_mfma_f32_16x16x32_bf16 v[84:87], v[32:35], a[244:247], v[84:87]\n"
    "v_mfma_f32_16x16x32_bf16 v[64:67], v[36:39], a[248:251], v[64:67]\n"
    "ds_read_b128 v[32:35], v208 offset:34816\n"
    "buffer_load_dwordx4 v0, s[4:7], s56 offen offset:1024 lds\n"
    "v_mfma_f32_16x16x32_bf16 v[68:71], v[36:39], a[252:255], v[68:71]\n"
    "v_mfma_f32_16x16x32_bf16 v[72:75], v[40:43], a[248:251], v[72:75]\n"
    "ds_read_b128 v[36:39], v209 offset:22528\n"
    "buffer_load_dword v172, v212, s[4:7], s38 offen offset:64\n"
    "v_mfma_f32_16x16x32_bf16 v[76:79], v[40:43], a[252:255], v[76:79]\n"
    "v_mfma_f32_16x16x32_bf16 v[80:83], v[44:47], a[248:251], v[80:83]\n"
    "ds_read_b128 v[40:43], v209 offset:6144\n"
    "buffer_load_dwordx4 v0, s[4:7], s56 offen offset:2048 lds\n"
    "v_mfma_f32_16x16x32_bf16 v[84:87], v[44:47], a[252:255], v[84:87]\n"
    "s_waitcnt lgkmcnt(3)\n"
    "v_mfma_f32_16x16x32_bf16 v[48:51], v[16:19], a[192:195], v[48:51]\n"
    "ds_read_b128 v[44:47], v209 offset:25600\n"
    "buffer_load_dword v173, v212, s[4:7], s38 offen offset:72\n"
    "v_mfma_f32_16x16x32_bf16 v[52:55], v[16:19], a[196:199], v[52:55]\n"
    "v_cvt_pk_bf16_f32 v120, v64, v65\n"
    "v_cvt_pk_bf16_f32 v121, v66, v67\n"
    "v_mfma_f32_16x16x32_bf16 v[56:59], v[20:23], a[192:195], v[56:59]\n"
    "ds_read_b128 v[16:19], v0 offset:30720\n"
    "v_cvt_pk_bf16_f32 v122, v68, v69\n"
    "v_mfma_f32_16x16x32_bf16 v[60:63], v[20:23], a[196:199], v[60:63]\n"
    "s_add_u32 m0, s17, 20480\n"
    "s_add_u32 s56, s10, 172032\n"
    "v_mfma_f32_16x16x32_bf16 v[48:51], v[24:27], a[200:203], v[48:51]\n"
    "ds_read_b128 v[20:23], v0 offset:31744\n"
    "buffer_load_dwordx4 v0, s[4:7], s56 offen offset:0 lds\n"
    "v_mfma_f32_16x16x32_bf16 v[52:55], v[24:27], a[204:207], v[52:55]\n"
    "v_cvt_pk_bf16_f32 v123, v70, v71\n"
    "buffer_store_dwordx4 v[120:123], v0, s[4:7], s53 offen offset:0 sc1\n"
    "v_mfma_f32_16x16x32_bf16 v[56:59], v[28:31], a[200:203], v[56:59]\n"
    "ds_read_b128 v[24:27], v208 offset:2048\n"
    "buffer_load_dword v169, v219, s[4:7], s8 offen\n"
    "v_mfma_f32_16x16x32_bf16 v[60:63], v[28:31], a[204:207], v[60:63]\n"
    "v_cvt_pk_bf16_f32 v124, v72, v73\n"
    "s_waitcnt lgkmcnt(3)\n"
    "v_mfma_f32_16x16x32_bf16 v[48:51], v[32:35], a[208:211], v[48:51]\n"
    "ds_read_b128 v[28:31], v208 offset:3072\n"
    "buffer_load_dwordx4 v0, s[4:7], s56 offen offset:1024 lds\n"
    "v_mfma_f32_16x16x32_bf16 v[52:55], v[32:35], a[212:215], v[52:55]\n"
    "v_cvt_pk_bf16_f32 v125, v74, v75\n"
    "v_cvt_pk_bf16_f32 v126, v76, v77\n"
    "v_mfma_f32_16x16x32_bf16 v[56:59], v[36:39], a[208:211], v[56:59]\n"
    "ds_read_b128 v[32:35], v208 offset:38912\n"
    "v_cvt_pk_bf16_f32 v127, v78, v79\n"
    "v_mfma_f32_16x16x32_bf16 v[60:63], v[36:39], a[212:215], v[60:63]\n"
    "buffer_store_dwordx4 v[124:127], v0, s[4:7], s53 offen offset:1024 sc1\n"
    "v_cvt_pk_bf16_f32 v136, v80, v81\n"
    "v_mfma_f32_16x16x32_bf16 v[48:51], v[40:43], a[216:219], v[48:51]\n"
    "ds_read_b128 v[36:39], v208 offset:39936\n"
    "v_cvt_pk_bf16_f32 v137, v82, v83\n"
    "v_mfma_f32_16x16x32_bf16 v[52:55], v[40:43], a[220:223], v[52:55]\n"
    "v_cvt_pk_bf16_f32 v138, v84, v85\n"
    "v_cvt_pk_bf16_f32 v139, v86, v87\n"
    "v_mfma_f32_16x16x32_bf16 v[56:59], v[44:47], a[216:219], v[56:59]\n"
    "ds_read_b128 v[40:43], v209 offset:10240\n"
    "buffer_load_dwordx4 v0, s[4:7], s56 offen offset:2048 lds\n"
    "v_mfma_f32_16x16x32_bf16 v[60:63], v[44:47], a[220:223], v[60:63]\n"
    "buffer_store_dwordx4 v[136:139], v0, s[4:7], s55 offen sc1\n"
    "s_waitcnt lgkmcnt(3)\n"
    "v_mfma_f32_16x16x32_bf16 v[48:51], v[16:19], a[224:227], v[48:51]\n"
    "ds_read_b128 v[44:47], v209 offset:11264\n"
    "v_mfma_f32_16x16x32_bf16 v[52:55], v[16:19], a[228:231], v[52:55]\n"
    "v_mfma_f32_16x16x32_bf16 v[56:59], v[20:23], a[224:227], v[56:59]\n"
    "ds_read_b128 v[16:19], v209 offset:17408\n"
    "v_mfma_f32_16x16x32_bf16 v[60:63], v[20:23], a[228:231], v[60:63]\n"
    "v_mfma_f32_16x16x32_bf16 v[48:51], v[24:27], a[232:235], v[48:51]\n"
    "ds_read_b128 v[20:23], v209 offset:18432\n"
    "v_mfma_f32_16x16x32_bf16 v[52:55], v[24:27], a[236:239], v[52:55]\n"
    "v_mfma_f32_16x16x32_bf16 v[56:59], v[28:31], a[232:235], v[56:59]\n"
    "ds_read_b128 v[24:27], v209 offset:20480\n"
    "v_mfma_f32_16x16x32_bf16 v[60:63], v[28:31], a[236:239], v[60:63]\n"
    "s_waitcnt lgkmcnt(3)\n"
    "v_mfma_f32_16x16x32_bf16 v[48:51], v[32:35], a[240:243], v[48:51]\n"
    "ds_read_b128 v[28:31], v209 offset:21504\n"
    "v_mfma_f32_16x16x32_bf16 v[52:55], v[32:35], a[244:247], v[52:55]\n"
    "v_mfma_f32_16x16x32_bf16 v[56:59], v[36:39], a[240:243], v[56:59]\n"
    "ds_read_b128 v[32:35], v209 offset:23552\n"
    "v_mfma_f32_16x16x32_bf16 v[60:63], v[36:39], a[244:247], v[60:63]\n"
    "v_mfma_f32_16x16x32_bf16 v[48:51], v[40:43], a[248:251], v[48:51]\n"
    "ds_read_b128 v[36:39], v209 offset:24576\n"
    "v_mfma_f32_16x16x32_bf16 v[52:55], v[40:43], a[252:255], v[52:55]\n"
    "v_mfma_f32_16x16x32_bf16 v[56:59], v[44:47], a[248:251], v[56:59]\n"
    "ds_read_b128 v[40:43], v209 offset:26624\n"
    "v_mfma_f32_16x16x32_bf16 v[60:63], v[44:47], a[252:255], v[60:63]\n"
    "s_waitcnt lgkmcnt(3)\n"
    "v_mfma_f32_16x16x32_bf16 v[88:91], v[16:19], a[192:195], v[88:91]\n"
    "ds_read_b128 v[44:47], v209 offset:27648\n"
    "v_mfma_f32_16x16x32_bf16 v[92:95], v[16:19], a[196:199], v[92:95]\n"
    "v_mfma_f32_16x16x32_bf16 v[96:99], v[20:23], a[192:195], v[96:99]\n"
    "ds_read_b128 v[16:19], v0 offset:32768\n"
    "v_cvt_pk_bf16_f32 v112, v48, v49\n"
    "v_mfma_f32_16x16x32_bf16 v[100:103], v[20:23], a[196:199], v[100:103]\n"
    "v_cvt_pk_bf16_f32 v113, v50, v51\n"
    "v_cvt_pk_bf16_f32 v114, v52, v53\n"
    "v_mfma_f32_16x16x32_bf16 v[88:91], v[24:27], a[200:203], v[88:91]\n"
    "ds_read_b128 v[20:23], v0 offset:33792\n"
    "v_cvt_pk_bf16_f32 v115, v54, v55\n"
    "v_mfma_f32_16x16x32_bf16 v[92:95], v[24:27], a[204:207], v[92:95]\n"
    "buffer_store_dwordx4 v[112:115], v0, s[4:7], s52 offen offset:0 sc1\n"
    "v_cvt_pk_bf16_f32 v116, v56, v57\n"
    "v_mfma_f32_16x16x32_bf16 v[96:99], v[28:31], a[200:203], v[96:99]\n"
    "ds_read_b128 v[24:27], v208 offset:4096\n"
    "v_cvt_pk_bf16_f32 v117, v58, v59\n"
    "v_mfma_f32_16x16x32_bf16 v[100:103], v[28:31], a[204:207], v[100:103]\n"
    "v_cvt_pk_bf16_f32 v118, v60, v61\n"
    "s_waitcnt lgkmcnt(3)\n"
    "v_mfma_f32_16x16x32_bf16 v[88:91], v[32:35], a[208:211], v[88:91]\n"
    "ds_read_b128 v[28:31], v208 offset:5120\n"
    "v_cvt_pk_bf16_f32 v119, v62, v63\n"
    "v_mfma_f32_16x16x32_bf16 v[92:95], v[32:35], a[212:215], v[92:95]\n"
    "buffer_store_dwordx4 v[116:119], v0, s[4:7], s52 offen offset:1024 sc1\n"
    "v_mfma_f32_16x16x32_bf16 v[96:99], v[36:39], a[208:211], v[96:99]\n"
    "ds_read_b128 v[32:35], v208 offset:40960\n"
    "v_mfma_f32_16x16x32_bf16 v[100:103], v[36:39], a[212:215], v[100:103]\n"
    "v_mfma_f32_16x16x32_bf16 v[88:91], v[40:43], a[216:219], v[88:91]\n"
    "ds_read_b128 v[36:39], v208 offset:41984\n"
    "v_mfma_f32_16x16x32_bf16 v[92:95], v[40:43], a[220:223], v[92:95]\n"
    "v_mfma_f32_16x16x32_bf16 v[96:99], v[44:47], a[216:219], v[96:99]\n"
    "ds_read_b128 v[40:43], v209 offset:12288\n"
    "v_mfma_f32_16x16x32_bf16 v[100:103], v[44:47], a[220:223], v[100:103]\n"
    "s_waitcnt lgkmcnt(3)\n"
    "v_mfma_f32_16x16x32_bf16 v[88:91], v[16:19], a[224:227], v[88:91]\n"
    "ds_read_b128 v[44:47], v209 offset:13312\n"
    "v_mfma_f32_16x16x32_bf16 v[92:95], v[16:19], a[228:231], v[92:95]\n"
    "v_mfma_f32_16x16x32_bf16 v[96:99], v[20:23], a[224:227], v[96:99]\n"
    "s_waitcnt vmcnt(27)\n"
    "s_barrier\n"
    "ds_read_b128 v[16:19], v0 offset:0\n"
    "v_mfma_f32_16x16x32_bf16 v[100:103], v[20:23], a[228:231], v[100:103]\n"
    "v_mfma_f32_16x16x32_bf16 v[88:91], v[24:27], a[232:235], v[88:91]\n"
    "ds_read_b128 v[20:23], v0 offset:4096\n"
    "v_mfma_f32_16x16x32_bf16 v[92:95], v[24:27], a[236:239], v[92:95]\n"
    "v_mfma_f32_16x16x32_bf16 v[96:99], v[28:31], a[232:235], v[96:99]\n"
    "ds_read_b128 v[24:27], v0 offset:1024\n"
    "v_mfma_f32_16x16x32_bf16 v[100:103], v[28:31], a[236:239], v[100:103]\n"
    "s_waitcnt lgkmcnt(3)\n"
    "v_mfma_f32_16x16x32_bf16 v[88:91], v[32:35], a[240:243], v[88:91]\n"
    "ds_read_b128 v[28:31], v0 offset:5120\n"
    "v_mfma_f32_16x16x32_bf16 v[92:95], v[32:35], a[244:247], v[92:95]\n"
    "v_mfma_f32_16x16x32_bf16 v[96:99], v[36:39], a[240:243], v[96:99]\n"
    "ds_read_b128 v[32:35], v0 offset:2048\n"
    "v_mfma_f32_16x16x32_bf16 v[100:103], v[36:39], a[244:247], v[100:103]\n"
    "v_mfma_f32_16x16x32_bf16 v[88:91], v[40:43], a[248:251], v[88:91]\n"
    "ds_read_b128 v[36:39], v0 offset:6144\n"
    "v_mfma_f32_16x16x32_bf16 v[92:95], v[40:43], a[252:255], v[92:95]\n"
    "v_mfma_f32_16x16x32_bf16 v[96:99], v[44:47], a[248:251], v[96:99]\n"
    "ds_read_b128 v[40:43], v0 offset:36864\n"
    "v_mfma_f32_16x16x32_bf16 v[100:103], v[44:47], a[252:255], v[100:103]\n"
    "s_waitcnt lgkmcnt(3)\n"
    "v_mfma_f32_16x16x32_bf16 v[144:147], v[16:19], v[176:179], 0\n"
    "ds_read_b128 v[44:47], v0 offset:40960\n"
    "v_mfma_f32_16x16x32_bf16 v[148:151], v[16:19], v[180:183], 0\n"
    "v_mfma_f32_16x16x32_bf16 v[152:155], v[20:23], v[176:179], 0\n"
    "ds_read_b128 v[16:19], v0 offset:37888\n"
    "v_cvt_pk_bf16_f32 v128, v88, v89\n"
    "v_mfma_f32_16x16x32_bf16 v[156:159], v[20:23], v[180:183], 0\n"
    "v_cvt_pk_bf16_f32 v129, v90, v91\n"
    "v_cvt_pk_bf16_f32 v130, v92, v93\n"
    "v_mfma_f32_16x16x32_bf16 v[144:147], v[24:27], v[184:187], v[144:147]\n"
    "ds_read_b128 v[20:23], v0 offset:41984\n"
    "v_cvt_pk_bf16_f32 v131, v94, v95\n"
    "v_mfma_f32_16x16x32_bf16 v[148:151], v[24:27], v[188:191], v[148:151]\n"
    "buffer_store_dwordx4 v[128:131], v0, s[4:7], s45 offen offset:0 sc1\n"
    "v_cvt_pk_bf16_f32 v132, v96, v97\n"
    "v_mfma_f32_16x16x32_bf16 v[152:155], v[28:31], v[184:187], v[152:155]\n"
    "ds_read_b128 v[24:27], v0 offset:38912\n"
    "v_cvt_pk_bf16_f32 v133, v98, v99\n"
    "v_mfma_f32_16x16x32_bf16 v[156:159], v[28:31], v[188:191], v[156:159]\n"
    "v_cvt_pk_bf16_f32 v134, v100, v101\n"
    "s_waitcnt lgkmcnt(3)\n"
    "v_mfma_f32_16x16x32_bf16 v[144:147], v[32:35], v[192:195], v[144:147]\n"
    "ds_read_b128 v[28:31], v0 offset:43008\n"
    "v_cvt_pk_bf16_f32 v135, v102, v103\n"
    "v_mfma_f32_16x16x32_bf16 v[148:151], v[32:35], v[196:199], v[148:151]\n"
    "buffer_store_dwordx4 v[132:135], v0, s[4:7], s45 offen offset:1024 sc1\n"
    "v_mfma_f32_16x16x32_bf16 v[152:155], v[36:39], v[192:195], v[152:155]\n"
    "ds_read_b128 v[32:35], v208 offset:8192\n"
    "v_mfma_f32_16x16x32_bf16 v[156:159], v[36:39], v[196:199], v[156:159]\n"
    "v_mfma_f32_16x16x32_bf16 v[144:147], v[40:43], v[200:203], v[144:147]\n"
    "ds_read_b128 v[36:39], v208 offset:12288\n"
    "v_mfma_f32_16x16x32_bf16 v[148:151], v[40:43], v[204:207], v[148:151]\n"
    "v_mfma_f32_16x16x32_bf16 v[152:155], v[44:47], v[200:203], v[152:155]\n"
    "ds_read_b128 v[40:43], v208 offset:9216\n"
    "v_mfma_f32_16x16x32_bf16 v[156:159], v[44:47], v[204:207], v[156:159]\n"
    "s_waitcnt lgkmcnt(3)\n"
    "v_mfma_f32_16x16x32_bf16 v[144:147], v[16:19], a[0:3], v[144:147]\n"
    "ds_read_b128 v[44:47], v208 offset:13312\n"
    "v_mfma_f32_16x16x32_bf16 v[148:151], v[16:19], a[4:7], v[148:151]\n"
    "v_mfma_f32_16x16x32_bf16 v[152:155], v[20:23], a[0:3], v[152:155]\n"
    "ds_read_b128 v[16:19], v208 offset:10240\n"
    "v_mfma_f32_16x16x32_bf16 v[156:159], v[20:23], a[4:7], v[156:159]\n"
    "v_mfma_f32_16x16x32_bf16 v[144:147], v[24:27], a[8:11], v[144:147]\n"
    "ds_read_b128 v[20:23], v208 offset:14336\n"
    "v_mfma_f32_16x16x32_bf16 v[148:151], v[24:27], a[12:15], v[148:151]\n"
    "v_mfma_f32_16x16x32_bf16 v[152:155], v[28:31], a[8:11], v[152:155]\n"
    "ds_read_b128 v[24:27], v208 offset:45056\n"
    "v_mfma_f32_16x16x32_bf16 v[156:159], v[28:31], a[12:15], v[156:159]\n"
    "s_waitcnt lgkmcnt(3)\n"
    "v_mfma_f32_16x16x32_bf16 v[144:147], v[32:35], a[16:19], v[144:147]\n"
    "ds_read_b128 v[28:31], v208 offset:49152\n"
    "v_mfma_f32_16x16x32_bf16 v[148:151], v[32:35], a[20:23], v[148:151]\n"
    "v_mfma_f32_16x16x32_bf16 v[152:155], v[36:39], a[16:19], v[152:155]\n"
    "ds_read_b128 v[32:35], v208 offset:46080\n"
    "v_mfma_f32_16x16x32_bf16 v[156:159], v[36:39], a[20:23], v[156:159]\n"
    "v_mfma_f32_16x16x32_bf16 v[144:147], v[40:43], a[24:27], v[144:147]\n"
    "ds_read_b128 v[36:39], v208 offset:50176\n"
    "v_mfma_f32_16x16x32_bf16 v[148:151], v[40:43], a[28:31], v[148:151]\n"
    "v_mfma_f32_16x16x32_bf16 v[152:155], v[44:47], a[24:27], v[152:155]\n"
    "ds_read_b128 v[40:43], v208 offset:47104\n"
    "v_mfma_f32_16x16x32_bf16 v[156:159], v[44:47], a[28:31], v[156:159]\n"
    "s_waitcnt lgkmcnt(3)\n"
    "v_mfma_f32_16x16x32_bf16 v[144:147], v[16:19], a[32:35], v[144:147]\n"
    "ds_read_b128 v[44:47], v208 offset:51200\n"
    "v_mfma_f32_16x16x32_bf16 v[148:151], v[16:19], a[36:39], v[148:151]\n"
    "v_mfma_f32_16x16x32_bf16 v[152:155], v[20:23], a[32:35], v[152:155]\n"
    "s_waitcnt vmcnt(2)\n"
    "s_barrier\n"
    "s_cmp_lg_u32 s17, 0\n"
    "s_cbranch_scc1 LF6%=\n"
    "s_mov_b64 exec, 1\n"
    "buffer_store_dword v216, off, s[4:7], s43 offset:0 sc1\n"
    "s_mov_b64 exec, -1\n"
    "LF6%=:\n"
    "ds_read_b128 v[16:19], v0 offset:9216\n"
    "v_mfma_f32_16x16x32_bf16 v[156:159], v[20:23], a[36:39], v[156:159]\n"
    "v_mfma_f32_16x16x32_bf16 v[144:147], v[24:27], a[40:43], v[144:147]\n"
    "ds_read_b128 v[20:23], v0 offset:13312\n"
    "v_lshlrev_b32 v48, 16, v160\n"
    "v_mfma_f32_16x16x32_bf16 v[148:151], v[24:27], a[44:47], v[148:151]\n"
    "v_and_b32 v49, 0xffff0000, v160\n"
    "v_lshlrev_b32 v50, 16, v161\n"
    "v_mfma_f32_16x16x32_bf16 v[152:155], v[28:31], a[40:43], v[152:155]\n"
    "ds_read_b128 v[24:27], v0 offset:10240\n"
    "v_and_b32 v51, 0xffff0000, v161\n"
    "v_mfma_f32_16x16x32_bf16 v[156:159], v[28:31], a[44:47], v[156:159]\n"
    "v_lshlrev_b32 v52, 16, v162\n"
    "s_waitcnt lgkmcnt(3)\n"
    "v_mfma_f32_16x16x32_bf16 v[144:147], v[32:35], a[48:51], v[144:147]\n"
    "ds_read_b128 v[28:31], v0 offset:14336\n"
    "v_and_b32 v53, 0xffff0000, v162\n"
    "v_mfma_f32_16x16x32_bf16 v[148:151], v[32:35], a[52:55], v[148:151]\n"
    "v_lshlrev_b32 v54, 16, v163\n"
    "v_and_b32 v55, 0xffff0000, v163\n"
    "v_mfma_f32_16x16x32_bf16 v[152:155], v[36:39], a[48:51], v[152:155]\n"
    "ds_read_b128 v[32:35], v0 offset:11264\n"
    "v_lshlrev_b32 v56, 16, v164\n"
    "v_mfma_f32_16x16x32_bf16 v[156:159], v[36:39], a[52:55], v[156:159]\n"
    "v_and_b32 v57, 0xffff0000, v164\n"
    "v_lshlrev_b32 v58, 16, v165\n"
    "v_mfma_f32_16x16x32_bf16 v[144:147], v[40:43], a[56:59], v[144:147]\n"
    "ds_read_b128 v[36:39], v0 offset:15360\n"
    "v_and_b32 v59, 0xffff0000, v165\n"
    "v_mfma_f32_16x16x32_bf16 v[148:151], v[40:43], a[60:63], v[148:151]\n"
    "v_lshlrev_b32 v60, 16, v166\n"
    "v_and_b32 v61, 0xffff0000, v166\n"
    "v_mfma_f32_16x16x32_bf16 v[152:155], v[44:47], a[56:59], v[152:155]\n"
    "ds_read_b128 v[40:43], v0 offset:46080\n"
    "v_lshlrev_b32 v62, 16, v167\n"
    "v_mfma_f32_16x16x32_bf16 v[156:159], v[44:47], a[60:63], v[156:159]\n"
    "v_and_b32 v63, 0xffff0000, v167\n"
    "s_waitcnt lgkmcnt(3)\n"
    "v_mfma_f32_16x16x32_bf16 v[144:147], v[16:19], a[64:67], v[144:147]\n"
    "ds_read_b128 v[44:47], v0 offset:50176\n"
    "v_lshlrev_b32 v64, 16, v168\n"
    "v_mfma_f32_16x16x32_bf16 v[148:151], v[16:19], a[68:71], v[148:151]\n"
    "v_and_b32 v65, 0xffff0000, v168\n"
    "v_lshlrev_b32 v66, 16, v170\n"
    "v_mfma_f32_16x16x32_bf16 v[152:155], v[20:23], a[64:67], v[152:155]\n"
    "ds_read_b128 v[16:19], v0 offset:47104\n"
    "v_and_b32 v67, 0xffff0000, v170\n"
    "v_mfma_f32_16x16x32_bf16 v[156:159], v[20:23], a[68:71], v[156:159]\n"
    "v_lshlrev_b32 v68, 16, v171\n"
    "v_and_b32 v69, 0xffff0000, v171\n"
    "v_mfma_f32_16x16x32_bf16 v[144:147], v[24:27], a[72:75], v[144:147]\n"
    "ds_read_b128 v[20:23], v0 offset:51200\n"
    "v_lshlrev_b32 v70, 16, v172\n"
    "v_mfma_f32_16x16x32_bf16 v[148:151], v[24:27], a[76:79], v[148:151]\n"
    "v_and_b32 v71, 0xffff0000, v172\n"
    "v_lshlrev_b32 v72, 16, v173\n"
    "v_mfma_f32_16x16x32_bf16 v[152:155], v[28:31], a[72:75], v[152:155]\n"
    "ds_read_b128 v[24:27], v0 offset:48128\n"
    "v_and_b32 v73, 0xffff0000, v173\n"
    "v_mfma_f32_16x16x32_bf16 v[156:159], v[28:31], a[76:79], v[156:159]\n"
    "v_lshlrev_b32 v74, 16, v169\n"
    "s_waitcnt lgkmcnt(3)\n"
    "v_mfma_f32_16x16x32_bf16 v[144:147], v[32:35], a[80:83], v[144:147]\n"
    "ds_read_b128 v[28:31], v0 offset:52224\n"
    "v_and_b32 v75, 0xffff0000, v169\n"
    "v_mfma_f32_16x16x32_bf16 v[148:151], v[32:35], a[84:87], v[148:151]\n"
    "v_mfma_f32_16x16x32_bf16 v[152:155], v[36:39], a[80:83], v[152:155]\n"
    "ds_read_b128 v[32:35], v208 offset:17408\n"
    "v_mfma_f32_16x16x32_bf16 v[156:159], v[36:39], a[84:87], v[156:159]\n"
    "v_mfma_f32_16x16x32_bf16 v[144:147], v[40:43], a[88:91], v[144:147]\n"
    "ds_read_b128 v[36:39], v208 offset:21504\n"
    "v_mfma_f32_16x16x32_bf16 v[148:151], v[40:43], a[92:95], v[148:151]\n"
    "v_mfma_f32_16x16x32_bf16 v[152:155], v[44:47], a[88:91], v[152:155]\n"
    "ds_read_b128 v[40:43], v208 offset:18432\n"
    "v_mfma_f32_16x16x32_bf16 v[156:159], v[44:47], a[92:95], v[156:159]\n"
    "s_waitcnt lgkmcnt(3)\n"
    "v_mfma_f32_16x16x32_bf16 v[144:147], v[16:19], a[96:99], v[144:147]\n"
    "ds_read_b128 v[44:47], v208 offset:22528\n"
    "v_mfma_f32_16x16x32_bf16 v[148:151], v[16:19], a[100:103], v[148:151]\n"
    "v_mfma_f32_16x16x32_bf16 v[152:155], v[20:23], a[96:99], v[152:155]\n"
    "ds_read_b128 v[16:19], v208 offset:19456\n"
    "v_mfma_f32_16x16x32_bf16 v[156:159], v[20:23], a[100:103], v[156:159]\n"
    "v_mfma_f32_16x16x32_bf16 v[144:147], v[24:27], a[104:107], v[144:147]\n"
    "ds_read_b128 v[20:23], v208 offset:23552\n"
    "v_mfma_f32_16x16x32_bf16 v[148:151], v[24:27], a[108:111], v[148:151]\n"
    "v_mfma_f32_16x16x32_bf16 v[152:155], v[28:31], a[104:107], v[152:155]\n"
    "ds_read_b128 v[24:27], v208 offset:54272\n"
    "v_mfma_f32_16x16x32_bf16 v[156:159], v[28:31], a[108:111], v[156:159]\n"
    "s_waitcnt lgkmcnt(3)\n"
    "v_mfma_f32_16x16x32_bf16 v[144:147], v[32:35], a[112:115], v[144:147]\n"
    "ds_read_b128 v[28:31], v208 offset:58368\n"
    "v_mfma_f32_16x16x32_bf16 v[148:151], v[32:35], a[116:119], v[148:151]\n"
    "v_mfma_f32_16x16x32_bf16 v[152:155], v[36:39], a[112:115], v[152:155]\n"
    "ds_read_b128 v[32:35], v208 offset:55296\n"
    "v_mfma_f32_16x16x32_bf16 v[156:159], v[36:39], a[116:119], v[156:159]\n"
    "v_mfma_f32_16x16x32_bf16 v[144:147], v[40:43], a[120:123], v[144:147]\n"
    "ds_read_b128 v[36:39], v208 offset:59392\n"
    "buffer_load_dword v218, v221, s[4:7], s41 offen offset:0 sc1\n"
    "v_mfma_f32_16x16x32_bf16 v[148:151], v[40:43], a[124:127], v[148:151]\n"
    "v_mfma_f32_16x16x32_bf16 v[152:155], v[44:47], a[120:123], v[152:155]\n"
    "ds_read_b128 v[40:43], v208 offset:56320\n"
    "v_mfma_f32_16x16x32_bf16 v[156:159], v[44:47], a[124:127], v[156:159]\n"
    "s_waitcnt lgkmcnt(3)\n"
    "v_mfma_f32_16x16x32_bf16 v[144:147], v[16:19], a[128:131], v[144:147]\n"
    "ds_read_b128 v[44:47], v208 offset:60416\n"
    "v_mfma_f32_16x16x32_bf16 v[148:151], v[16:19], a[132:135], v[148:151]\n"
    "v_mfma_f32_16x16x32_bf16 v[152:155], v[20:23], a[128:131], v[152:155]\n"
    "s_waitcnt vmcnt(1)\n"
    "s_barrier\n"
    "s_cmp_lg_u32 s17, 0\n"
    "s_cbranch_scc1 LF7%=\n"
    "s_mov_b64 exec, 1\n"
    "buffer_store_dword v216, off, s[4:7], s43 offset:16 sc1\n"
    "s_mov_b64 exec, -1\n"
    "LF7%=:\n"
    "ds_read_b128 v[16:19], v0 offset:16384\n"
    "v_mfma_f32_16x16x32_bf16 v[156:159], v[20:23], a[132:135], v[156:159]\n"
    "v_mfma_f32_16x16x32_bf16 v[144:147], v[24:27], a[136:139], v[144:147]\n"
    "ds_read_b128 v[20:23], v0 offset:20480\n"
    "v_mfma_f32_16x16x32_bf16 v[148:151], v[24:27], a[140:143], v[148:151]\n"
    "v_mfma_f32_16x16x32_bf16 v[152:155], v[28:31], a[136:139], v[152:155]\n"
    "ds_read_b128 v[24:27], v0 offset:17408\n"
    "v_mfma_f32_16x16x32_bf16 v[156:159], v[28:31], a[140:143], v[156:159]\n"
    "s_waitcnt lgkmcnt(3)\n"
    "v_mfma_f32_16x16x32_bf16 v[144:147], v[32:35], a[144:147], v[144:147]\n"
    "ds_read_b128 v[28:31], v0 offset:21504\n"
    "v_mfma_f32_16x16x32_bf16 v[148:151], v[32:35], a[148:151], v[148:151]\n"
    "v_mfma_f32_16x16x32_bf16 v[152:155], v[36:39], a[144:147], v[152:155]\n"
    "ds_read_b128 v[32:35], v0 offset:18432\n"
    "v_mfma_f32_16x16x32_bf16 v[156:159], v[36:39], a[148:151], v[156:159]\n"
    "v_mfma_f32_16x16x32_bf16 v[144:147], v[40:43], a[152:155], v[144:147]\n"
    "ds_read_b128 v[36:39], v0 offset:22528\n"
    "v_mfma_f32_16x16x32_bf16 v[148:151], v[40:43], a[156:159], v[148:151]\n"
    "v_mfma_f32_16x16x32_bf16 v[152:155], v[44:47], a[152:155], v[152:155]\n"
    "ds_read_b128 v[40:43], v0 offset:53248\n"
    "v_mfma_f32_16x16x32_bf16 v[156:159], v[44:47], a[156:159], v[156:159]\n"
    "s_waitcnt lgkmcnt(3)\n"
    "v_mfma_f32_16x16x32_bf16 v[144:147], v[16:19], a[160:163], v[144:147]\n"
    "ds_read_b128 v[44:47], v0 offset:57344\n"
    "v_mfma_f32_16x16x32_bf16 v[148:151], v[16:19], a[164:167], v[148:151]\n"
    "v_mfma_f32_16x16x32_bf16 v[152:155], v[20:23], a[160:163], v[152:155]\n"
    "ds_read_b128 v[16:19], v0 offset:54272\n"
    "v_mfma_f32_16x16x32_bf16 v[156:159], v[20:23], a[164:167], v[156:159]\n"
    "v_mfma_f32_16x16x32_bf16 v[144:147], v[24:27], a[168:171], v[144:147]\n"
    "ds_read_b128 v[20:23], v0 offset:58368\n"
    "v_mfma_f32_16x16x32_bf16 v[148:151], v[24:27], a[172:175], v[148:151]\n"
    "v_mfma_f32_16x16x32_bf16 v[152:155], v[28:31], a[168:171], v[152:155]\n"
    "ds_read_b128 v[24:27], v0 offset:55296\n"
    "v_mfma_f32_16x16x32_bf16 v[156:159], v[28:31], a[172:175], v[156:159]\n"
    "s_waitcnt lgkmcnt(3)\n"
    "v_mfma_f32_16x16x32_bf16 v[144:147], v[32:35], a[176:179], v[144:147]\n"
    "ds_read_b128 v[28:31], v0 offset:59392\n"
    "v_mfma_f32_16x16x32_bf16 v[148:151], v[32:35], a[180:183], v[148:151]\n"
    "v_mfma_f32_16x16x32_bf16 v[152:155], v[36:39], a[176:179], v[152:155]\n"
    "ds_read_b128 v[32:35], v208 offset:24576\n"
    "v_mfma_f32_16x16x32_bf16 v[156:159], v[36:39], a[180:183], v[156:159]\n"
    "v_mfma_f32_16x16x32_bf16 v[144:147], v[40:43], a[184:187], v[144:147]\n"
    "ds_read_b128 v[36:39], v208 offset:28672\n"
    "v_mfma_f32_16x16x32_bf16 v[148:151], v[40:43], a[188:191], v[148:151]\n"
    "v_mfma_f32_16x16x32_bf16 v[152:155], v[44:47], a[184:187], v[152:155]\n"
    "ds_read_b128 v[40:43], v208 offset:25600\n"
    "v_mfma_f32_16x16x32_bf16 v[156:159], v[44:47], a[188:191], v[156:159]\n"
    "s_waitcnt lgkmcnt(3)\n"
    "v_mfma_f32_16x16x32_bf16 v[144:147], v[16:19], a[192:195], v[144:147]\n"
    "ds_read_b128 v[44:47], v208 offset:29696\n"
    "v_mfma_f32_16x16x32_bf16 v[148:151], v[16:19], a[196:199], v[148:151]\n"
    "v_mfma_f32_16x16x32_bf16 v[152:155], v[20:23], a[192:195], v[152:155]\n"
    "ds_read_b128 v[16:19], v208 offset:26624\n"
    "v_mfma_f32_16x16x32_bf16 v[156:159], v[20:23], a[196:199], v[156:159]\n"
    "v_mfma_f32_16x16x32_bf16 v[144:147], v[24:27], a[200:203], v[144:147]\n"
    "ds_read_b128 v[20:23], v208 offset:30720\n"
    "v_mfma_f32_16x16x32_bf16 v[148:151], v[24:27], a[204:207], v[148:151]\n"
    "v_mfma_f32_16x16x32_bf16 v[152:155], v[28:31], a[200:203], v[152:155]\n"
    "ds_read_b128 v[24:27], v208 offset:61440\n"
    "v_mfma_f32_16x16x32_bf16 v[156:159], v[28:31], a[204:207], v[156:159]\n"
    "s_waitcnt lgkmcnt(3)\n"
    "v_mfma_f32_16x16x32_bf16 v[144:147], v[32:35], a[208:211], v[144:147]\n"
    "ds_read_b128 v[28:31], v209 offset:0\n"
    "v_mfma_f32_16x16x32_bf16 v[148:151], v[32:35], a[212:215], v[148:151]\n"
    "v_mfma_f32_16x16x32_bf16 v[152:155], v[36:39], a[208:211], v[152:155]\n"
    "ds_read_b128 v[32:35], v208 offset:62464\n"
    "v_mfma_f32_16x16x32_bf16 v[156:159], v[36:39], a[212:215], v[156:159]\n"
    "v_mfma_f32_16x16x32_bf16 v[144:147], v[40:43], a[216:219], v[144:147]\n"
    "ds_read_b128 v[36:39], v209 offset:1024\n"
    "v_mfma_f32_16x16x32_bf16 v[148:151], v[40:43], a[220:223], v[148:151]\n"
    "v_mfma_f32_16x16x32_bf16 v[152:155], v[44:47], a[216:219], v[152:155]\n"
    "ds_read_b128 v[40:43], v208 offset:63488\n"
    "s_waitcnt vmcnt(0)\n"
    "v_cmp_ne_u32 vcc, v218, v216\n"
    "s_cbranch_vccnz LP13%=\n"
    "LQ13%=:\n"
    "s_add_u32 s57, s27, 8192\n"
    "v_add_u32 v175, 4096, v214\n"
    "buffer_load_dwordx4 v[224:227], v0, s[4:7], s46 offen offset:0 sc1\n"
    "buffer_load_dwordx4 v[228:231], v0, s[4:7], s46 offen offset:1024 sc1\n"
    "buffer_load_dwordx4 v[232:235], v0, s[4:7], s54 offen offset:0 sc1\n"
    "buffer_load_dwordx4 v[236:239], v0, s[4:7], s54 offen offset:1024 sc1\n"
    "buffer_load_dword v248, v214, s[4:7], s27 offen sc1\n"
    "buffer_load_dword v249, v214, s[4:7], s57 offen sc1\n"
    "buffer_load_dword v250, v175, s[4:7], s27 offen sc1\n"
    "buffer_load_dword v251, v175, s[4:7], s57 offen sc1\n"
    "v_mfma_f32_16x16x32_bf16 v[156:159], v[44:47], a[220:223], v[156:159]\n"
    "s_waitcnt lgkmcnt(3)\n"
    "v_mfma_f32_16x16x32_bf16 v[144:147], v[16:19], a[224:227], v[144:147]\n"
    "ds_read_b128 v[44:47], v209 offset:2048\n"
    "v_mfma_f32_16x16x32_bf16 v[148:151], v[16:19], a[228:231], v[148:151]\n"
    "v_mfma_f32_16x16x32_bf16 v[152:155], v[20:23], a[224:227], v[152:155]\n"
    "v_mfma_f32_16x16x32_bf16 v[156:159], v[20:23], a[228:231], v[156:159]\n"
    "v_mfma_f32_16x16x32_bf16 v[144:147], v[24:27], a[232:235], v[144:147]\n"
    "v_mfma_f32_16x16x32_bf16 v[148:151], v[24:27], a[236:239], v[148:151]\n"
    "v_mfma_f32_16x16x32_bf16 v[152:155], v[28:31], a[232:235], v[152:155]\n"
    "v_mfma_f32_16x16x32_bf16 v[156:159], v[28:31], a[236:239], v[156:159]\n"
    "s_waitcnt lgkmcnt(0)\n"
    "v_mfma_f32_16x16x32_bf16 v[144:147], v[32:35], a[240:243], v[144:147]\n"
    "buffer_load_dword v174, off, s[4:7], s26 offset:0 sc1\n"
    "v_mfma_f32_16x16x32_bf16 v[148:151], v[32:35], a[244:247], v[148:151]\n"
    "v_mfma_f32_16x16x32_bf16 v[152:155], v[36:39], a[240:243], v[152:155]\n"
    "v_mfma_f32_16x16x32_bf16 v[156:159], v[36:39], a[244:247], v[156:159]\n"
    "v_mfma_f32_16x16x32_bf16 v[144:147], v[40:43], a[248:251], v[144:147]\n"
    "v_mfma_f32_16x16x32_bf16 v[148:151], v[40:43], a[252:255], v[148:151]\n"
    "v_mfma_f32_16x16x32_bf16 v[152:155], v[44:47], a[248:251], v[152:155]\n"
    "s_waitcnt vmcnt(8)\n"
    "v_lshlrev_b32 v76, 16, v224\n"
    "v_and_b32 v77, 0xffff0000, v224\n"
    "v_mfma_f32_16x16x32_bf16 v[156:159], v[44:47], a[252:255], v[156:159]\n"
    "v_lshlrev_b32 v78, 16, v225\n"
    "s_waitcnt vmcnt(0)\n"
    "v_cmp_ne_u32 vcc, v174, v216\n"
    "s_cbranch_vccnz LP14%=\n"
    "LQ14%=:\n"
    "buffer_load_dwordx4 v[240:243], v0, s[4:7], s33 offen offset:0 sc1\n"
    "buffer_load_dwordx4 v[244:247], v0, s[4:7], s33 offen offset:1024 sc1\n"
    "v_and_b32 v79, 0xffff0000, v225\n"
    "v_lshlrev_b32 v80, 16, v226\n"
    "v_and_b32 v81, 0xffff0000, v226\n"
    "v_lshlrev_b32 v82, 16, v227\n"
    "v_and_b32 v83, 0xffff0000, v227\n"
    "v_lshlrev_b32 v84, 16, v228\n"
    "v_and_b32 v85, 0xffff0000, v228\n"
    "v_lshlrev_b32 v86, 16, v229\n"
    "v_and_b32 v87, 0xffff0000, v229\n"
    "v_lshlrev_b32 v88, 16, v230\n"
    "v_and_b32 v89, 0xffff0000, v230\n"
    "v_lshlrev_b32 v90, 16, v231\n"
    "v_and_b32 v91, 0xffff0000, v231\n"
    "v_dot2c_f32_bf16 v76, v232, v216\n"
    "v_dot2c_f32_bf16 v77, v232, v217\n"
    "v_dot2c_f32_bf16 v78, v233, v216\n"
    "v_dot2c_f32_bf16 v79, v233, v217\n"
    "v_dot2c_f32_bf16 v80, v234, v216\n"
    "v_dot2c_f32_bf16 v81, v234, v217\n"
    "v_dot2c_f32_bf16 v82, v235, v216\n"
    "v_dot2c_f32_bf16 v83, v235, v217\n"
    "v_dot2c_f32_bf16 v84, v236, v216\n"
    "v_dot2c_f32_bf16 v85, v236, v217\n"
    "v_dot2c_f32_bf16 v86, v237, v216\n"
    "v_dot2c_f32_bf16 v87, v237, v217\n"
    "v_dot2c_f32_bf16 v88, v238, v216\n"
    "v_dot2c_f32_bf16 v89, v238, v217\n"
    "v_dot2c_f32_bf16 v90, v239, v216\n"
    "v_dot2c_f32_bf16 v91, v239, v217\n"
    "v_lshlrev_b32 v92, 16, v248\n"
    "v_and_b32 v93, 0xffff0000, v248\n"
    "v_dot2c_f32_bf16 v92, v249, v216\n"
    "v_dot2c_f32_bf16 v93, v249, v217\n"
    "v_dot2c_f32_bf16 v92, v250, v216\n"
    "v_dot2c_f32_bf16 v93, v250, v217\n"
    "v_dot2c_f32_bf16 v92, v251, v216\n"
    "v_dot2c_f32_bf16 v93, v251, v217\n"
    "v_fmac_f32 v48, v76, v66\n"
    "v_fmac_f32 v49, v77, v67\n"
    "v_fmac_f32 v50, v78, v68\n"
    "v_fmac_f32 v51, v79, v69\n"
    "v_fmac_f32 v52, v80, v66\n"
    "v_fmac_f32 v53, v81, v67\n"
    "v_fmac_f32 v54, v82, v68\n"
    "v_fmac_f32 v55, v83, v69\n"
    "v_fmac_f32 v56, v84, v70\n"
    "v_fmac_f32 v57, v85, v71\n"
    "v_fmac_f32 v58, v86, v72\n"
    "v_fmac_f32 v59, v87, v73\n"
    "v_fmac_f32 v60, v88, v70\n"
    "v_fmac_f32 v61, v89, v71\n"
    "v_fmac_f32 v62, v90, v72\n"
    "v_fmac_f32 v63, v91, v73\n"
    "v_fmac_f32 v64, v92, v74\n"
    "LPK8:\n"
    "s_cbranch_execz LTR\n"
    "v_fmac_f32 v65, v93, v75\n"
    "v_cvt_pk_bf16_f32 v92, v64, v65\n"
    "buffer_store_dword v92, v215, s[12:15], s35 offen sc0 sc1\n"
    "s_nop 7\n"
    "s_waitcnt vmcnt(2)\n"
    "v_dot2c_f32_bf16 v144, v240, v216\n"
    "v_dot2c_f32_bf16 v145, v240, v217\n"
    "v_dot2c_f32_bf16 v146, v241, v216\n"
    "v_dot2c_f32_bf16 v147, v241, v217\n"
    "v_dot2c_f32_bf16 v148, v242, v216\n"
    "v_dot2c_f32_bf16 v149, v242, v217\n"
    "v_dot2c_f32_bf16 v150, v243, v216\n"
    "v_dot2c_f32_bf16 v151, v243, v217\n"
    "s_waitcnt vmcnt(1)\n"
    "v_dot2c_f32_bf16 v152, v244, v216\n"
    "v_dot2c_f32_bf16 v153, v244, v217\n"
    "v_dot2c_f32_bf16 v154, v245, v216\n"
    "v_dot2c_f32_bf16 v155, v245, v217\n"
    "v_dot2c_f32_bf16 v156, v246, v216\n"
    "v_dot2c_f32_bf16 v157, v246, v217\n"
    "v_dot2c_f32_bf16 v158, v247, v216\n"
    "v_dot2c_f32_bf16 v159, v247, v217\n"
    "v_fmac_f32 v48, v144, v66\n"
    "v_fmac_f32 v49, v145, v67\n"
    "v_fmac_f32 v50, v146, v68\n"
    "v_fmac_f32 v51, v147, v69\n"
    "v_fmac_f32 v56, v152, v70\n"
    "v_fmac_f32 v57, v153, v71\n"
    "v_fmac_f32 v58, v154, v72\n"
    "v_fmac_f32 v59, v155, v73\n"
    "v_fmac_f32 v52, v148, v66\n"
    "v_fmac_f32 v53, v149, v67\n"
    "v_fmac_f32 v54, v150, v68\n"
    "v_fmac_f32 v55, v151, v69\n"
    "v_fmac_f32 v60, v156, v70\n"
    "v_fmac_f32 v61, v157, v71\n"
    "v_fmac_f32 v62, v158, v72\n"
    "v_fmac_f32 v63, v159, v73\n"
    "v_cvt_pk_bf16_f32 v112, v48, v49\n"
    "v_cvt_pk_bf16_f32 v113, v50, v51\n"
    "v_cvt_pk_bf16_f32 v114, v56, v57\n"
    "v_cvt_pk_bf16_f32 v115, v58, v59\n"
    "v_cvt_pk_bf16_f32 v116, v52, v53\n"
    "v_cvt_pk_bf16_f32 v117, v54, v55\n"
    "v_cvt_pk_bf16_f32 v118, v60, v61\n"
    "v_cvt_pk_bf16_f32 v119, v62, v63\n"
    "v_permlane16_swap_b32 v112, v114\n"
    "v_permlane16_swap_b32 v113, v115\n"
    "buffer_store_dwordx4 v[112:115], v211, s[12:15], s34 offen sc0 sc1\n"
    "v_permlane16_swap_b32 v116, v118\n"
    "v_permlane16_swap_b32 v117, v119\n"
    "buffer_store_dwordx4 v[116:119], v211, s[12:15], s9 offen sc0 sc1\n"
    "s_branch LE%=\n"
    "LP8%=:\n"
    "s_mov_b32 s57, 0x8000\n"
    "LR8%=:\n"
    "buffer_load_dword v218, v213, s[4:7], s40 offen offset:0 sc1\n"
    "s_sub_u32 s57, s57, 1\n"
    "s_waitcnt vmcnt(0)\n"
    "s_cbranch_scc1 LQ8%=\n"
    "v_cmp_ne_u32 vcc, v218, v216\n"
    "s_cbranch_vccnz LR8%=\n"
    "s_branch LQ8%=\n"
    "LP9%=:\n"
    "s_mov_b32 s57, 0x8000\n"
    "LR9%=:\n"
    "buffer_load_dword v218, v213, s[4:7], s40 offen offset:32 sc1\n"
    "s_sub_u32 s57, s57, 1\n"
    "s_waitcnt vmcnt(0)\n"
    "s_cbranch_scc1 LQ9%=\n"
    "v_cmp_ne_u32 vcc, v218, v216\n"
    "s_cbranch_vccnz LR9%=\n"
    "s_branch LQ9%=\n"
    "LP10%=:\n"
    "s_mov_b32 s57, 0x8000\n"
    "LR10%=:\n"
    "buffer_load_dword v218, v213, s[4:7], s40 offen offset:64 sc1\n"
    "s_sub_u32 s57, s57, 1\n"
    "s_waitcnt vmcnt(0)\n"
    "s_cbranch_scc1 LQ10%=\n"
    "v_cmp_ne_u32 vcc, v218, v216\n"
    "s_cbranch_vccnz LR10%=\n"
    "s_branch LQ10%=\n"
    "LP11%=:\n"
    "s_mov_b32 s57, 0x8000\n"
    "LR11%=:\n"
    "buffer_load_dword v218, v213, s[4:7], s40 offen offset:96 sc1\n"
    "s_sub_u32 s57, s57, 1\n"
    "s_waitcnt vmcnt(0)\n"
    "s_cbranch_scc1 LQ11%=\n"
    "v_cmp_ne_u32 vcc, v218, v216\n"
    "s_cbranch_vccnz LR11%=\n"
    "s_branch LQ11%=\n"
    "LP12%=:\n"
    "s_mov_b32 s57, 0x8000\n"
    "LR12%=:\n"
    "buffer_load_dword v218, v213, s[4:7], s40 offen offset:128 sc1\n"
    "s_sub_u32 s57, s57, 1\n"
    "s_waitcnt vmcnt(0)\n"
    "s_cbranch_scc1 LQ12%=\n"
    "v_cmp_ne_u32 vcc, v218, v216\n"
    "s_cbranch_vccnz LR12%=\n"
    "s_branch LQ12%=\n"
    "LP13%=:\n"
    "s_mov_b32 s57, 0x8000\n"
    "LR13%=:\n"
    "buffer_load_dword v218, v221, s[4:7], s41 offen offset:0 sc1\n"
    "s_sub_u32 s57, s57, 1\n"
    "s_waitcnt vmcnt(0)\n"
    "s_cbranch_scc1 LQ13%=\n"
    "v_cmp_ne_u32 vcc, v218, v216\n"
    "s_cbranch_vccnz LR13%=\n"
    "s_branch LQ13%=\n"
    "LP14%=:\n"
    "s_mov_b32 s57, 0x8000\n"
    "LR14%=:\n"
    "buffer_load_dword v174, off, s[4:7], s26 offset:0 sc1\n"
    "s_sub_u32 s57, s57, 1\n"
    "s_waitcnt vmcnt(0)\n"
    "s_cbranch_scc1 LQ14%=\n"
    "v_cmp_ne_u32 vcc, v174, v216\n"
    "s_cbranch_vccnz LR14%=\n"
    "s_branch LQ14%=\n"
    "LE%=:\n"
    "LCODE_END:\n"
      : "+{v0}"(tid), "+{s2}"(bx), "+{s[0:1]}"(ka), "={v223}"(stv)
      :
      : "v1", "v2", "v3", "v4", "v5", "v6", "v7", "v8", "v9", "v10", "v11", "v12", "v13", "v14", "v15", "v16", "v17", "v18", "v19", "v20", "v21", "v22", "v23", "v24", "v25", "v26", "v27", "v28", "v29", "v30", "v31", "v32", "v33", "v34", "v35", "v36", "v37", "v38", "v39", "v40", "v41", "v42", "v43", "v44", "v45", "v46", "v47", "v48", "v49", "v50", "v51", "v52", "v53", "v54", "v55", "v56", "v57", "v58", "v59", "v60", "v61", "v62", "v63", "v64", "v65", "v66", "v67", "v68", "v69", "v70", "v71", "v72", "v73", "v74", "v75", "v76", "v77", "v78", "v79", "v80", "v81", "v82", "v83", "v84", "v85", "v86", "v87", "v88", "v89", "v90", "v91", "v92", "v93", "v94", "v95", "v96", "v97", "v98", "v99", "v100", "v101", "v102", "v103", "v104", "v105", "v106", "v107", "v108", "v109", "v110", "v111", "v112", "v113", "v114", "v115", "v116", "v117", "v118", "v119", "v120", "v121", "v122", "v123", "v124", "v125", "v126", "v127", "v128", "v129", "v130", "v131", "v132", "v133", "v134", "v135", "v136", "v137", "v138", "v139", "v140", "v141", "v142", "v143", "v144", "v145", "v146", "v147", "v148", "v149", "v150", "v151", "v152", "v153", "v154", "v155", "v156", "v157", "v158", "v159", "v160", "v161", "v162", "v163", "v164", "v165", "v166", "v167", "v168", "v169", "v170", "v171", "v172", "v173", "v174", "v175", "v176", "v177", "v178", "v179", "v180", "v181", "v182", "v183", "v184", "v185", "v186", "v187", "v188", "v189", "v190", "v191", "v192", "v193", "v194", "v195", "v196", "v197", "v198", "v199", "v200", "v201", "v202", "v203", "v204", "v205", "v206", "v207", "v208", "v209", "v210", "v211", "v212", "v213", "v214", "v215", "v216", "v217", "v218", "v219", "v220", "v221", "v222", "v224", "v225", "v226", "v227", "v228", "v229", "v230", "v231", "v232", "v233", "v234", "v235", "v236", "v237", "v238", "v239", "v240", "v241", "v242", "v243", "v244", "v245", "v246", "v247", "v248", "v249", "v250", "v251", "v252", "v253", "v254", "v255", "a0", "a1", "a2", "a3", "a4", "a5", "a6", "a7", "a8", "a9", "a10", "a11", "a12", "a13", "a14", "a15", "a16", "a17", "a18", "a19", "a20", "a21", "a22", "a23", "a24", "a25", "a26", "a27", "a28", "a29", "a30", "a31", "a32", "a33", "a34", "a35", "a36", "a37", "a38", "a39", "a40", "a41", "a42", "a43", "a44", "a45", "a46", "a47", "a48", "a49", "a50", "a51", "a52", "a53", "a54", "a55", "a56", "a57", "a58", "a59", "a60", "a61", "a62", "a63", "a64", "a65", "a66", "a67", "a68", "a69", "a70", "a71", "a72", "a73", "a74", "a75", "a76", "a77", "a78", "a79", "a80", "a81", "a82", "a83", "a84", "a85", "a86", "a87", "a88", "a89", "a90", "a91", "a92", "a93", "a94", "a95", "a96", "a97", "a98", "a99", "a100", "a101", "a102", "a103", "a104", "a105", "a106", "a107", "a108", "a109", "a110", "a111", "a112", "a113", "a114", "a115", "a116", "a117", "a118", "a119", "a120", "a121", "a122", "a123", "a124", "a125", "a126", "a127", "a128", "a129", "a130", "a131", "a132", "a133", "a134", "a135", "a136", "a137", "a138", "a139", "a140", "a141", "a142", "a143", "a144", "a145", "a146", "a147", "a148", "a149", "a150", "a151", "a152", "a153", "a154", "a155", "a156", "a157", "a158", "a159", "a160", "a161", "a162", "a163", "a164", "a165", "a166", "a167", "a168", "a169", "a170", "a171", "a172", "a173", "a174", "a175", "a176", "a177", "a178", "a179", "a180", "a181", "a182", "a183", "a184", "a185", "a186", "a187", "a188", "a189", "a190", "a191", "a192", "a193", "a194", "a195", "a196", "a197", "a198", "a199", "a200", "a201", "a202", "a203", "a204", "a205", "a206", "a207", "a208", "a209", "a210", "a211", "a212", "a213", "a214", "a215", "a216", "a217", "a218", "a219", "a220", "a221", "a222", "a223", "a224", "a225", "a226", "a227", "a228", "a229", "a230", "a231", "a232", "a233", "a234", "a235", "a236", "a237", "a238", "a239", "a240", "a241", "a242", "a243", "a244", "a245", "a246", "a247", "a248", "a249", "a250", "a251", "a252", "a253", "a254", "a255", "s3", "s4", "s5", "s6", "s7", "s8", "s9", "s10", "s11", "s12", "s13", "s14", "s15", "s16", "s17", "s18", "s19", "s20", "s21", "s22", "s23", "s24", "s25", "s26", "s27", "s28", "s29", "s30", "s31", "s33", "s34", "s35", "s36", "s37", "s38", "s39", "s40", "s41", "s42", "s43", "s44", "s45", "s46", "s47", "s48", "s49", "s50", "s51", "s52", "s53", "s54", "s55", "s56", "s57", "s58", "s59", "vcc", "scc", "memory");
  STAMP(13); STAMP_DUMP(3);
}

extern "C" void kernel_launch(void* const* d_in, const int* in_sizes, int n_in, void* d_out, int out_size, void* d_ws,
                              size_t ws_size, hipStream_t stream) {
  const u16* x = (const u16*)d_in[0];
  const u16* wq = (const u16*)d_in[1];
  const u16* wk = (const u16*)d_in[2];
  const u16* wv = (const u16*)d_in[3];
  const u16* wo = (const u16*)d_in[4];
  const u16* wg = (const u16*)d_in[6];
  const u16* wu = (const u16*)d_in[7];
  const u16* wd = (const u16*)d_in[8];
  const u16* c = (const u16*)d_in[9];
  const u16* wmod = (const u16*)d_in[10];
  char* ws = (char*)d_ws;
  constexpr unsigned DF = 0x00020000u;
  (void)hipFuncSetAttribute((const void*)k3_attn, hipFuncAttributeMaxDynamicSharedMemorySize, LDSZ(L3_SIZE));
  (void)hipFuncSetAttribute((const void*)k5_ffn, hipFuncAttributeMaxDynamicSharedMemorySize, LDSZ(159744));
  k3_attn<<<256, 512, LDSZ(L3_SIZE), stream>>>(ws, 0x10000000u, DF, x, S_ * D_ * 2, DF, wq, D_ * D_ * 2, DF, wmod, D_ * D_ * 12, DF,
                                                   wk, D_ * 36, DF, wv, D_ * 36, DF, wo, D_ * D_ * 2, DF, c, D_ * 2, DF, wg, wu, wd, d_out);
  k5_ffn<<<256, 256, LDSZ(159744), stream>>>(ws, (u16*)d_out);
}
```
